```python
import torch

def _softmax(x, axis=-1):
    return torch.softmax(x, dim=axis)
def _gelu(x):
    return torch.nn.functional.gelu(x, approximate="tanh")
def _silu(x):
    return torch.nn.functional.silu(x)
def _sigmoid(x):
    return torch.sigmoid(x)
def _layernorm(x, axis=-1, eps=1e-5):
    m = x.mean(dim=axis, keepdim=True)
    v = x.var(dim=axis, keepdim=True, unbiased=False)
    return (x - m) / torch.sqrt(v + eps)
def _scatter_add(tgt, idx, src, axis=0):
    return tgt.clone().index_add_(axis, idx, src)
def _segment_sum(src, seg, n):
    out = torch.zeros((n,) + src.shape[1:], dtype=src.dtype)
    return out.index_add_(0, seg, src)
def _pad(x, pw):
    flat = tuple(v for pair in reversed(pw) for v in pair)
    return torch.nn.functional.pad(x, flat)

kernel_name = 'gated_mix__ssm_conv_s4d__moe_arch_mod__prompt'

def setup_inputs():

    _w = (lambda a: (lambda t: t.to(torch.bfloat16) if t.is_floating_point() else t)(torch.as_tensor(a)))
    def _A_setup():
        Bsz, L, D, N = 1, 2048, 1024, 128
        return {
            "u": _w((lambda *s: torch.randn(*s, dtype=torch.bfloat16) * 0.05)(Bsz, L, D)),
            "log_A_real": _w((lambda *s: torch.randn(*s, dtype=torch.bfloat16) * 6.4)(D, N) * 0.5),
            "A_imag": _w((lambda *s: torch.randn(*s, dtype=torch.bfloat16) * 0.1)(D, N) * 3.14),
            "C_real": _w((lambda *s: torch.randn(*s, dtype=torch.bfloat16) * 0.4)(D, N) / N ** 0.5),
            "C_imag": _w((lambda *s: torch.randn(*s, dtype=torch.bfloat16) * 0.4)(D, N) / N ** 0.5),
            "log_dt": _w((lambda *s: torch.randn(*s, dtype=torch.bfloat16) * 0.1)(D) * 0.5 - 4.0),
            "D_skip": _w((lambda *s: torch.randn(*s, dtype=torch.bfloat16) * 0.1)(D)),
        }
    _A_in = _A_setup()

    def _B_setup():
        B, S, D, F, C = 1, 2048, 1024, 3584, 8
        return {
            "x": _w((lambda *s: torch.randn(*s, dtype=torch.bfloat16) * 0.05)(B, S, D)),
            "w_r": _w((lambda *s: torch.randn(*s, dtype=torch.bfloat16) * 6.4)(D)),
            "w_gate": _w((lambda *s: torch.randn(*s, dtype=torch.bfloat16) * 0.1)(D, F)),
            "w_up": _w((lambda *s: torch.randn(*s, dtype=torch.bfloat16) * 0.1)(D, F)),
            "w_down": _w((lambda *s: torch.randn(*s, dtype=torch.bfloat16) * 0.1)(F, D)),
        }
    _B_in = _B_setup()

    return {
        "A_u": _A_in["u"],
        "A_log_A_real": _A_in["log_A_real"],
        "A_A_imag": _A_in["A_imag"],
        "A_C_real": _A_in["C_real"],
        "A_C_imag": _A_in["C_imag"],
        "A_log_dt": _A_in["log_dt"],
        "A_D_skip": _A_in["D_skip"],
        "B_x": _B_in["x"],
        "B_w_r": _B_in["w_r"],
        "B_w_gate": _B_in["w_gate"],
        "B_w_up": _B_in["w_up"],
        "B_w_down": _B_in["w_down"],
        "gate_w": _w((lambda *s: torch.randn(*s, dtype=torch.bfloat16) * 0.05)(1024) * 16.0),
    }

def reference(A_u, A_log_A_real, A_A_imag, A_C_real, A_C_imag, A_log_dt, A_D_skip, B_x, B_w_r, B_w_gate, B_w_up, B_w_down, gate_w):
    def _A_ref(u, log_A_real, A_imag, C_real, C_imag, log_dt, D_skip):
        L = u.shape[1]
        dt = torch.exp(log_dt)
        A = -torch.exp(log_A_real) + 1j * A_imag
        C = (C_real + 1j * C_imag) * dt[:, None]
        pos = (lambda x, dt: x.to(dt))(torch.arange(L), log_dt.dtype)
        P = torch.exp((A * dt[:, None])[:, :, None] * pos[None, None, :])
        K = 2.0 * torch.einsum("dn,dnl->dl", C, P).real
        L2 = 2 * L
        u_t = (lambda x, *d: x.permute(*d))(u, 0, 2, 1)
        Kf = (lambda x, *a, **k: torch.fft.rfft(x.float(), *a, **k))(K, L2)
        Uf = (lambda x, *a, **k: torch.fft.rfft(x.float(), *a, **k))(u_t, L2)
        y = (lambda x, *a, **k: torch.fft.irfft(x, *a, **k).to(torch.bfloat16))(Uf * Kf[None, :, :], L2)[:, :, :L]
        y = (lambda x, *d: x.permute(*d))(y, 0, 2, 1)
        return y + u * D_skip[None, None, :]

    def _B_ref(x, w_r, w_gate, w_up, w_down):
        B, S, D = x.shape
        C = 8
        r = x @ w_r
        idx = torch.argsort(-r, axis=-1)[:, :C]
        idx_e = (lambda x, n, axis: torch.repeat_interleave(x, n, dim=axis))(idx.reshape(B, C, 1), D, axis=2)
        x_sel = (lambda x, i, axis: torch.take_along_dim(x, i, dim=axis))(x, idx_e, axis=1)
        g = _silu(x_sel @ w_gate)
        u = x_sel @ w_up
        y = (g * u) @ w_down
        r_sel = _sigmoid((lambda x, i, axis: torch.take_along_dim(x, i, dim=axis))(r, idx, axis=1))
        y = y * r_sel.reshape(B, C, 1)
        b_off = (torch.arange(B) * S).reshape(B, 1)
        flat_idx = (idx + b_off).reshape(B * C)
        zero = (x * 0.0).reshape(B * S, D)
        upd = _scatter_add(zero, flat_idx, y.reshape(B * C, D), axis=0)
        return x + upd.reshape(B, S, D)

    x = A_u
    g = _sigmoid((x * gate_w).sum(-1, keepdims=True))
    a = _A_ref(x, A_log_A_real, A_A_imag, A_C_real, A_C_imag, A_log_dt, A_D_skip)
    b = _B_ref(x, B_w_r, B_w_gate, B_w_up, B_w_down)
    return g * a + (1 - g) * b
```

```cpp
#include <hip/hip_runtime.h>
#include <stdint.h>

typedef unsigned short u16;
typedef unsigned u32;
typedef _Float16 h2 __attribute__((ext_vector_type(2)));
typedef _Float16 h8 __attribute__((ext_vector_type(8)));
typedef __bf16 b2 __attribute__((ext_vector_type(2)));
typedef __bf16 b8 __attribute__((ext_vector_type(8)));
typedef short s4 __attribute__((ext_vector_type(4)));
typedef float f2 __attribute__((ext_vector_type(2)));
typedef float f4 __attribute__((ext_vector_type(4)));
typedef u32 u2v __attribute__((ext_vector_type(2)));
typedef u32 u3v __attribute__((ext_vector_type(3)));
typedef u32 u4v __attribute__((ext_vector_type(4)));
typedef __attribute__((address_space(3))) s4* lds_s4p;

constexpr int L = 2048;
constexpr int D = 1024;
constexpr int NM = 128;
constexpr int F = 3584;
constexpr int NTAP = 1024;

constexpr int SEG_I = 576, SEG_O0 = 800, SEG_O1 = 880, SEG_SP = 896;

#define AI __attribute__((always_inline))
#define SB() __builtin_amdgcn_sched_barrier(0)
#define OPQ(x) asm volatile("" : "+v"(x))
#define DIAG_K 0
#define DIAG_M 1
#define STAMP0() u32 dg_ = 0; const bool dgw_ = (DIAG_M >> w) & 1; if (DIAG_K == 0 && dgw_) dg_ = (u32)kt[0] | 1u
#define STAMP(i) do { if (i == DIAG_K && dgw_) { SB(); dg_ = (u32)__builtin_amdgcn_s_memrealtime() | 1u; SB(); } } while (0)
#define STAMP_END() do { if (DIAG_K == 99 && dgw_) { asm volatile("s_waitcnt vmcnt(0)" ::: "memory"); dg_ = (u32)__builtin_amdgcn_s_memrealtime() | 1u; } if (dg_) { while ((int)((u32)__builtin_amdgcn_s_memrealtime() - (dg_ + 4000u)) < 0) __builtin_amdgcn_s_sleep(2); } } while (0)

#define STAMP_T0

__device__ __forceinline__ float bflo(u32 v) { return __uint_as_float(v << 16); }
__device__ __forceinline__ float bfhi(u32 v) { return __uint_as_float(v & 0xffff0000u); }
__device__ __forceinline__ u32 pk_bf(float a, float b) { f2 v = {a, b}; b2 r = __builtin_convertvector(v, b2); return __builtin_bit_cast(u32, r); }
__device__ __forceinline__ float rb1(float v) { return __uint_as_float(pk_bf(0.f, v)); }
__device__ __forceinline__ h8 as_h8(u4v v) { return __builtin_bit_cast(h8, v); }
__device__ __forceinline__ b8 as_b8(u4v v) { return __builtin_bit_cast(b8, v); }
__device__ __forceinline__ u2v tr_read(const char* p) {
  s4 t = __builtin_amdgcn_ds_read_tr16_b64_v4i16((lds_s4p)p);
  return __builtin_bit_cast(u2v, t);
}
template <int CTRL> __device__ __forceinline__ u32 dpp_u(u32 v) { return (u32)__builtin_amdgcn_update_dpp(0, (int)v, CTRL, 0xf, 0xf, false); }
template <int CTRL> __device__ __forceinline__ float dpp_f(float v) { return __uint_as_float(dpp_u<CTRL>(__float_as_uint(v))); }
__device__ __forceinline__ u32 wave_max_u32(u32 v) {
  v = max(v, dpp_u<0x111>(v)); v = max(v, dpp_u<0x112>(v)); v = max(v, dpp_u<0x114>(v)); v = max(v, dpp_u<0x118>(v));
  v = max(v, dpp_u<0x142>(v)); v = max(v, dpp_u<0x143>(v));
  return (u32)__builtin_amdgcn_readlane((int)v, 63);
}
#define DPM " row_mask:0xf bank_mask:0xf bound_ctrl:1\n\t"
#define T2S(HO, HI, LI, C) "v_max_u32_dpp " HO ", " HI ", " HI " " C DPM "v_min_u32_dpp %8, " HI ", " HI " " C DPM "v_max_u32_dpp %7, " LI ", " LI " " C DPM "v_max_u32 %7, %7, %8\n\t"
#define T2R(RM, REST) T2S("%5", "%0", "%1", "row_shr:1") T2S("%6", "%5", "%7", "row_shr:2") T2S("%5", "%6", "%7", "row_shr:4") T2S("%6", "%5", "%7", "row_shr:8") \
    T2S("%5", "%6", "%7", "row_bcast:15") T2S("%6", "%5", "%7", "row_bcast:31") \
    "v_readlane_b32 %9, %6, 63\n\tv_readlane_b32 %10, %7, 63\n\ts_nop 0\n\tv_mov_b32 %5, %9\n\tv_cmp_le_u32 %11, %10, %0\n\tv_cmp_le_u32 %12, %10, %1\n\tv_mov_b32 %6, %10\n\t" \
    "v_cndmask_b32 %0, %0, %1, %11\n\tv_mov_b32_dpp %4, %5 quad_perm:[0,1,2,3] row_mask:" RM " bank_mask:0x3\n\tv_cndmask_b32 %0, %0, %2, %12\n\t" \
    "v_mov_b32_dpp %4, %6 quad_perm:[0,1,2,3] row_mask:" RM " bank_mask:0xc\n\t" REST
#define T2P "v_cndmask_b32 %1, %1, %2, %11\n\tv_cndmask_b32 %2, %2, %3, %11\n\tv_cndmask_b32 %1, %1, %3, %12\n\tv_cndmask_b32 %3, %3, 0, %11\n\tv_cndmask_b32 %2, %2, 0, %12\n\t"
__device__ __forceinline__ u32 wave_top8(u32& m1, u32 m2, u32 m3, u32 m4, u32& w2) {
  u32 key, ta, tb, lo, t, w1;
  unsigned long long c1, c2;
  asm("s_nop 1\n\t" T2R("0x1", T2P) T2R("0x2", T2P) T2R("0x4", T2P) T2R("0x8", "") "s_nop 0"
      : "+v"(m1), "+v"(m2), "+v"(m3), "+v"(m4), "=&v"(key), "=&v"(ta), "=&v"(tb), "=&v"(lo), "=&v"(t), "=&s"(w1), "=&s"(w2), "=&s"(c1), "=&s"(c2));
  return key;
}

__device__ __forceinline__ float exp_bf(float v) {
  const float e = __builtin_amdgcn_exp2f(v * 1.44268798828125f);
  return fmaf(e, v * (7.0526077e-06f * 0.69314718f), e);
}
struct cf { f2 r, i; };
struct ch { h2 r, i; };
__device__ __forceinline__ cf cmul(cf a, cf b) { cf c; c.r = a.r * b.r - a.i * b.i; c.i = a.r * b.i + a.i * b.r; return c; }
__device__ __forceinline__ ch hmul(ch a, ch b) {
  ch c;
  asm("v_pk_mul_f16 %0, %2, %4\n\tv_pk_mul_f16 %1, %2, %5\n\tv_pk_fma_f16 %0, %3, %5, %0 neg_lo:[1,0,0] neg_hi:[1,0,0]\n\tv_pk_fma_f16 %1, %3, %4, %1"
      : "=&v"(c.r), "=&v"(c.i) : "v"(a.r), "v"(a.i), "v"(b.r), "v"(b.i));
  return c;
}
__device__ __forceinline__ cf csq(cf a) { cf c; c.r = a.r * a.r - a.i * a.i; c.i = (a.r + a.r) * a.i; return c; }
__device__ __forceinline__ ch toh(cf a) { ch c; c.r = __builtin_convertvector(a.r, h2); c.i = __builtin_convertvector(a.i, h2); return c; }
__device__ __forceinline__ ch tohc(cf a) { ch c; c.r = __builtin_convertvector(a.r, h2); c.i = __builtin_convertvector(-a.i, h2); return c; }
__device__ __forceinline__ h2 ash(u32 v) { return __builtin_bit_cast(h2, v); }
__device__ __forceinline__ cf cexp_p(f2 a2, f2 brev, float p) {
  f2 e = a2 * p, t = brev * p;
  f2 mag = {__builtin_amdgcn_exp2f(e[0]), __builtin_amdgcn_exp2f(e[1])};
  float r0 = __builtin_amdgcn_fractf(t[0]), r1 = __builtin_amdgcn_fractf(t[1]);
  cf c;
  c.r = mag * f2{__builtin_amdgcn_cosf(r0), __builtin_amdgcn_cosf(r1)};
  c.i = mag * f2{__builtin_amdgcn_sinf(r0), __builtin_amdgcn_sinf(r1)};
  return c;
}
__device__ __forceinline__ u32 med3u(u32 a, u32 b, u32 c) { u32 r; asm("v_med3_u32 %0, %1, %2, %3" : "=v"(r) : "v"(a), "v"(b), "v"(c)); return r; }
__device__ __forceinline__ f2 unbf(u32 v) { return f2{bflo(v), bfhi(v)}; }
__device__ __forceinline__ float sigm(float v) { return __builtin_amdgcn_rcpf(1.f + __builtin_amdgcn_exp2f(-1.44269504088896341f * v)); }

#define TS2(A, B, C, D) "v_xor_b32 %5, " A ", %9\n\tv_xor_b32 %6, " B ", %9\n\tv_xor_b32 %7, " C ", %9\n\tv_xor_b32 %8, " D ", %9\n\t" \
    "v_fma_f32 %1, %5, 1.0, %1\n\tv_fma_f32 %2, %6, 1.0, %2\n\tv_or3_b32 %0, %0, %5, %6\n\tv_fma_f32 %3, %7, 1.0, %3\n\tv_fma_f32 %4, %8, 1.0, %4\n\tv_or3_b32 %0, %0, %7, %8\n\t"
#define TSR TS2("%10", "%11", "%12", "%13") TS2("%14", "%15", "%16", "%17") TS2("%18", "%19", "%20", "%21")
#define TSI "=&v"(t0), "=&v"(t1), "=&v"(t2), "=&v"(t3) : "v"(t10), "v"(p[0]), "v"(p[1]), "v"(p[2]), "v"(p[3]), "v"(q[0]), "v"(q[1]), "v"(q[2]), "v"(q[3]), \
    "v"(r[0]), "v"(r[1]), "v"(r[2]), "v"(r[3])
__device__ __forceinline__ f4 tsum4(u32& bad, u32 t10, u4v s, u4v p, u4v q, u4v r) {
  float s0, s1, s2, s3; u32 t0, t1, t2, t3;
  asm volatile("v_xor_b32 %1, %22, %9\n\tv_xor_b32 %2, %23, %9\n\tv_xor_b32 %3, %24, %9\n\tv_xor_b32 %4, %25, %9\n\tv_or3_b32 %0, %0, %1, %2\n\tv_or3_b32 %0, %0, %3, %4\n\t" TSR
               : "+v"(bad), "=&v"(s0), "=&v"(s1), "=&v"(s2), "=&v"(s3), TSI, "v"(s[0]), "v"(s[1]), "v"(s[2]), "v"(s[3]));
  return f4{s0, s1, s2, s3};
}
__device__ __forceinline__ f4 tsum3(u32& bad, u32 t10, f4 s, u4v p, u4v q, u4v r) {
  float s0 = s[0], s1 = s[1], s2 = s[2], s3 = s[3]; u32 t0, t1, t2, t3;
  asm volatile(TSR : "+v"(bad), "+v"(s0), "+v"(s1), "+v"(s2), "+v"(s3), TSI);
  return f4{s0, s1, s2, s3};
}
__device__ __forceinline__ void silu4(f4& g, float nl2e) {
  float g0 = g[0], g1 = g[1], g2 = g[2], g3 = g[3], t0, t1, t2, t3;
  asm volatile("v_fma_f32 %4, %0, %8, 0\n\tv_fma_f32 %5, %1, %8, 0\n\tv_fma_f32 %6, %2, %8, 0\n\tv_fma_f32 %7, %3, %8, 0\n\t"
               "v_exp_f32 %4, %4\n\tv_exp_f32 %5, %5\n\tv_exp_f32 %6, %6\n\tv_exp_f32 %7, %7\n\t"
               "v_fma_f32 %4, %4, 1.0, 1.0\n\tv_fma_f32 %5, %5, 1.0, 1.0\n\tv_fma_f32 %6, %6, 1.0, 1.0\n\tv_fma_f32 %7, %7, 1.0, 1.0\n\t"
               "v_rcp_f32 %4, %4\n\tv_rcp_f32 %5, %5\n\tv_rcp_f32 %6, %6\n\tv_rcp_f32 %7, %7\n\t"
               "v_fma_f32 %0, %0, %4, 0\n\tv_fma_f32 %1, %1, %5, 0\n\tv_fma_f32 %2, %2, %6, 0\n\tv_fma_f32 %3, %3, %7, 0"
               : "+v"(g0), "+v"(g1), "+v"(g2), "+v"(g3), "=&v"(t0), "=&v"(t1), "=&v"(t2), "=&v"(t3) : "v"(nl2e));
  g = f4{g0, g1, g2, g3};
}

constexpr int KT_PITCH = 52;
constexpr int kt_off(int p) {
  int i = 20, j = 0;
  if (p < 256) { i = p & 7; j = p >> 3; }
  else if (p < 512) { int q = (p - 256) >> 1; i = 8 + (q & 3); j = q >> 2; }
  else if (p < 1024) { int q = (p - 512) >> 2; i = 12 + (q & 1) + 2 * (q >> 6); j = (q >> 1) & 31; }
  else if (p < 2048) { int q = (p - 1024) >> 3; i = 16 + (q >> 5); j = q & 31; }
  return j * KT_PITCH + i * 2;
}
constexpr int KT_ZERO = 32 * KT_PITCH + 16;
constexpr int rnd_lag(int m) {
  const int st = m < 256 ? 1 : m < 512 ? 2 : m < 1024 ? 4 : 8;
  int q = m / st;
  const int rem = m - q * st;
  if (2 * rem > st || (2 * rem == st && (q & 1))) ++q;
  return q * st;
}
constexpr int tap_entry(int e) {
  if (e < 544) {
    if (543 - e <= 525) return kt_off(rnd_lag(543 - e));
  } else if (e >= 560 && e < 752) {
    const int q = (e - 560) / 48, x = (e - 560) - q * 48;
    const int p = 512 + 16 * (31 - x) + 4 * (q + 1);
    if (x < 32 && p > 524) return kt_off(p);
  } else if (e >= 752 && e < 816) {
    return kt_off(1032 + 16 * (815 - e));
  } else if (e >= 832 && e < 896) {
    return kt_off(1040 + 16 * (895 - e));
  } else if (e == SEG_SP + 27 || e == SEG_SP + 28) {
    return kt_off(1024);
  }
  return KT_ZERO;
}
struct TapIdx { u16 v[NTAP]; };
constexpr TapIdx make_tapidx() {
  TapIdx t{};
  for (int e = 0; e < NTAP; ++e) t.v[e] = (u16)tap_entry(2 * ((e >> 4) + 64 * ((e >> 1) & 7)) + (e & 1));
  return t;
}
__device__ __attribute__((aligned(16))) const TapIdx g_tapidx = make_tapidx();

constexpr int TROW = 544;
constexpr int O_OUT = 0;
constexpr int OT = 576;
constexpr int O_U = O_OUT + 8 * OT;
constexpr int O_TAP = O_U + 4608;
constexpr int TAP_PITCH = 2 * NTAP + 64;
constexpr int CHB = O_TAP + 16 * TROW + 32;
static_assert(16 * TROW >= 4 * TAP_PITCH, "lds");
static_assert((CHB / 4) % 32 == 8, "banks");
constexpr int NSLOT = 5, WWIN = 4, DWIN = 8;
constexpr int O_GT = 4 * CHB;
constexpr int O_RING = O_GT + 8192;
constexpr int O_WD = O_RING + 2 * NSLOT * 2048;
constexpr int O_FAR = O_WD + 32768;
constexpr int O_XS = O_FAR + 16384;
constexpr int XS_PITCH = 544;
constexpr int O_HB = O_XS + 8 * XS_PITCH;
constexpr int O_PART = O_HB + 2 * 528;
constexpr int O_SEL = O_PART + 512;
constexpr int O_PAR = O_SEL + 32;
constexpr int O_CTL = O_PAR + 16;
constexpr int O_JUNK = O_CTL + 512;
constexpr int K_LDS = O_JUNK + 1024;
static_assert(K_LDS <= 163840 && O_RING % 16 == 0, "lds");
enum { C_PART = 0, C_GP, C_PUB, C_SEL, C_XS, C_HRDY, C_WGU, C_WD, C_TL, C_XL, C_TAPS = 12, C_Q = 16, C_FAR = 32, C_TILE = 48 };

#define CTLA(word) (lbase + O_CTL + (word) * 4)
#define ARRIVE(word) asm volatile("s_mov_b64 exec, 1\n\tds_add_u32 %0, %1\n\ts_mov_b64 exec, -1" :: "v"(CTLA(word)), "v"(1) : "memory")
#define WAITQ(word, target) do { u32 t_; int n_ = 1 << 20; asm volatile( \
    "1:\n\tds_read_b32 %0, %2\n\ts_sub_u32 %1, %1, 1\n\ts_cbranch_scc1 2f\n\ts_waitcnt lgkmcnt(0)\n\tv_cmp_gt_u32 vcc, %3, %0\n\ts_cbranch_vccnz 1b\n2:\n\ts_waitcnt lgkmcnt(0)" \
    : "=&v"(t_), "+s"(n_) : "v"(CTLA(word)), "n"(target) : "vcc", "scc", "memory"); } while (0)
#define WAITS(word, target) do { u32 t_; int n_ = 1 << 20; asm volatile( \
    "1:\n\tds_read_b32 %0, %2\n\ts_sub_u32 %1, %1, 1\n\ts_cbranch_scc1 2f\n\ts_waitcnt lgkmcnt(0)\n\tv_cmp_le_u32 vcc, %3, %0\n\ts_cbranch_vccnz 2f\n\ts_sleep 2\n\ts_branch 1b\n2:\n\ts_waitcnt lgkmcnt(0)" \
    : "=&v"(t_), "+s"(n_) : "v"(CTLA(word)), "n"(target) : "vcc", "scc", "memory"); } while (0)
#define BAR() asm volatile("s_waitcnt lgkmcnt(0)\n\ts_barrier" ::: "memory")
__device__ __forceinline__ u2v lds8(const char* p) { return __builtin_bit_cast(u2v, __hip_atomic_load((const unsigned long long*)p, __ATOMIC_RELAXED, __HIP_MEMORY_SCOPE_WORKGROUP)); }

constexpr int WS_EPOCH = 0;
constexpr int WS_HEAD = 1024;
constexpr int WS_REC = 4096;
constexpr int WS_FLG = 40960;
constexpr int WS_KALL = WS_REC + 8192;
constexpr int WS_KEY = 32768;
constexpr int WS_P1 = 65536;
constexpr int WS_P2 = WS_P1 + 56 * 16384;
constexpr int WS_XT = WS_P2 + 256 * 7168;
constexpr int XT_PITCH = 33280;
constexpr int WS_END = WS_XT + 128 * XT_PITCH;
constexpr int SPIN_MAX = 1 << 16;
constexpr u32 TMASK = 0x3ffu;

#define DT_HEAD 150
#define WDELAY 60
#define DT_KEY 270
#define DT_REC 290
#define DT_FLG 400
#define DT_VIS 40
#define DT_LATE 50
#define DE_P1 425
#define DE_P2 590
#define DE_FAST 575
#define DT_MIN 25
#define DT_P1 100
#define DT_P2 130
#define SLEEP_AG1 12
#define SLEEP_G 6
#define STSLP 2
#define STWIN 4
#define H2_AT 9
#define CODE_B 28400

#define ROLE_END() do { STAMP_END(); asm volatile("s_endpgm" ::: "memory"); } while (0)
__device__ __forceinline__ u32 now32() { return (u32)__builtin_amdgcn_s_memrealtime(); }
__device__ __forceinline__ void pace(u32 t) {
#pragma unroll 1
  for (int i = 0; i < 1024 && (int)(now32() - t) < 0; ++i) __builtin_amdgcn_s_sleep(1);
}
__device__ __forceinline__ void pace2(u32 n, u32 t, int dmax) {
  int d = (int)(t - n);
  d = d < DT_MIN ? DT_MIN : (d > dmax ? dmax : d);
  pace(n + (u32)d);
}

#define ALL_OTHER "vcc", "scc", "memory", \
    "s0","s1","s2","s5","s6","s7","s33","s34","s35","s44","s45","s46","s47","s48","s49","s50","s51","s52","s53","s54","s55","s56","s57","s58","s59", \
    "s60","s61","s62","s63","s64","s65","s66","s67","s68","s69","s70","s71","s72","s73","s74","s75","s76","s77","s78","s79","s80","s81","s82","s83","s84", \
    "s85","s86","s87","s88","s89","s90","s91","s92","s93","s94","s95","s96","s97","s98","s99", \
    "v0","v1","v2","v3","v4","v5","v6","v7","v8","v9","v10","v11","v12","v13","v14","v15","v16","v17","v18","v19","v20","v21","v22","v23","v24","v25", \
    "v26","v27","v28","v29","v30","v31","v32","v33","v34","v35","v36","v37","v38","v39","v40","v41","v42","v43","v44","v45","v46","v47","v48","v49","v50", \
    "v51","v52","v53","v54","v55","v56","v57","v58","v59","v60","v61","v62","v63","v64","v65","v66","v67","v68","v69","v70","v71","v72","v73","v74","v75", \
    "v76","v77","v78","v79","v80","v81","v82","v83","v84","v85","v86","v87","v88","v89","v90","v91","v92","v93","v94","v95","v96","v97","v98","v99", \
    "v100","v101","v102","v103","v104","v105","v106","v107","v108","v109","v110","v111","v112","v113","v114","v115","v116","v117","v118","v119","v120", \
    "v121","v122","v123","v124","v125","v126","v127","v128","v129","v130","v131","v132","v133","v134","v135","v136","v137","v138","v139","v140","v141", \
    "v142","v143","v144","v145","v146","v147","v148","v149","v150","v151","v152","v153","v154","v155","v156","v157","v158","v159","v160","v161","v162", \
    "v163","v164","v165","v166","v167"
typedef __attribute__((address_space(1))) const u16* gp16;
typedef __attribute__((address_space(1))) const u32* gp32;
typedef __attribute__((address_space(1))) const u4v* gp128;
typedef unsigned long long u64;
typedef u64 q2 __attribute__((ext_vector_type(2)));
typedef u64 q4 __attribute__((ext_vector_type(4)));
typedef u64 q8 __attribute__((ext_vector_type(8)));

__global__ void __launch_bounds__(768, 3) k_main(
    const u16* x_, const u16* w_r_, const u16* gate_w_, const u16* logA_, const u16* Aim_, const u16* Cre_, const u16* Cim_,
    const u16* log_dt_, const u16* Dskip_, const u16* w_gate_, const u16* w_up_, const u16* w_down_, char* wsp_, u16* out_) {
  __shared__ __attribute__((aligned(256))) char smem[K_LDS];
  const u32 lbase = (u32)(uintptr_t)(__attribute__((address_space(3))) char*)smem;
#define XDMA(OFF) "global_load_lds_dwordx4 v1, s[8:9] offset:" #OFF " nt\n\ts_addk_i32 m0, 896\n\t"
  asm volatile("s_load_dwordx16 s[8:23], %0, 0\n\tv_readfirstlane_b32 s3, %2\n\ts_load_dwordx8 s[24:31], %0, 64\n\ts_load_dwordx4 s[36:39], %0, 96\n\t"
               "s_memrealtime s[40:41]\n\t" STAMP_T0 "s_mov_b32 s4, %1\n\ts_lshr_b32 s3, s3, 6\n\t"
               "s_cmp_ge_u32 s3, 8\n\ts_cbranch_scc1 .Lrole_g\n\ts_cmp_eq_u32 s3, 7\n\ts_cbranch_scc1 .Lrole_d\n\t"
               "s_cmp_ge_u32 s3, 5\n\ts_cbranch_scc1 .Lrole_h\n\ts_cmp_eq_u32 s3, 4\n\ts_cbranch_scc1 .Lrole_c\n\t"
               "s_lshr_b32 s5, s4, 3\n\tv_lshlrev_b32 v3, 2, %2\n\ts_mov_b32 m0, %4\n\tv_lshl_or_b32 v3, s5, 10, v3\n\ts_getpc_b64 s[6:7]\n\t"
               "v_min_u32 v3, %3, v3\n\tv_lshrrev_b32 v1, 3, %2\n\tglobal_load_lds_dword v3, s[6:7]\n\t"
               "v_and_b32 v2, 6, v1\n\ts_and_b32 s34, s4, 7\n\tv_xor_b32 v2, v2, %2\n\ts_lshl_b32 s34, s34, 5\n\tv_and_b32 v2, 7, v2\n\ts_add_u32 s5, s5, s34\n\t"
               "v_and_b32 v1, 7, v1\n\ts_lshl_b32 s5, s5, 5\n\tv_lshlrev_b32 v1, 11, v1\n\ts_add_u32 s5, s5, s3\n\tv_lshl_or_b32 v1, v2, 4, v1\n\t"
               "s_mul_i32 s34, s3, %5\n\tv_lshl_add_u32 v1, s5, 9, v1\n\ts_add_u32 m0, s34, %6\n\tv_and_b32 v2, 31, %2\n\ts_waitcnt lgkmcnt(0)\n\t"
               XDMA(0) "s_lshl_b32 s5, s3, 5\n\t" XDMA(128) "v_add_lshl_u32 v2, s5, v2, 4\n\t" XDMA(256) "s_lshl_b32 s5, s3, 10\n\t"
               "global_load_lds_dwordx4 v1, s[8:9] offset:384 nt\n\ts_add_u32 m0, s5, %7\n\ts_mov_b32 exec_hi, 0\n\t"
               "global_load_lds_dwordx4 v2, s[10:11]\n\ts_addk_i32 m0, 512\n\ts_nop 0\n\tglobal_load_lds_dwordx4 v2, s[12:13]\n\ts_mov_b32 exec_hi, -1"
               :: "s"(__builtin_amdgcn_kernarg_segment_ptr()), "s"(blockIdx.x), "v"(threadIdx.x), "n"(CODE_B - 256), "n"(O_JUNK), "n"(CHB), "n"(O_U + 512), "n"(O_FAR)
               : "s3", "s4", "s5", "s6", "s7", "s8", "s9", "s10", "s11", "s12", "s13", "s14", "s15", "s16", "s17", "s18", "s19", "s20", "s21", "s22", "s23",
                 "s24", "s25", "s26", "s27", "s28", "s29", "s30", "s31", "s34", "s36", "s37", "s38", "s39", "s40", "s41", "s42", "s43", "v1", "v2", "v3", "scc", "memory");
  int lane, c16, g, w, b, xg, xi, cg, cc, c, d;
  char* ch_;
  q8 ka; q4 kb; q2 kc, kt;
  u32* ctl = (u32*)(smem + O_CTL);
  u32* par = (u32*)(smem + O_PAR);
#define IDS() do { int z_ = 0; OPQ(z_); lane = __builtin_amdgcn_mbcnt_hi(~0u, __builtin_amdgcn_mbcnt_lo(~0u, z_)); c16 = lane & 15; g = lane >> 4; } while (0)
#define ROLE(name) \
  asm volatile(".Lrole_" name ":\n\ts_waitcnt lgkmcnt(0)" : "={s[8:23]}"(ka), "={s[24:31]}"(kb), "={s[36:39]}"(kc), "={s[40:43]}"(kt), "={s3}"(w), "={s4}"(b) :: ALL_OTHER); \
  if (__builtin_expect(w > 11, 0)) asm volatile("s_endpgm" ::: "memory");        \
  IDS(); \
  xg = b & 7; xi = b >> 3;                          \
  cg = xg * 32 + xi;                                \
  cc = (xi >> 4) * 128 + xg * 16 + (xi & 15);       \
  c = w & 3; ch_ = smem + c * CHB; d = cc * 4 + c; \
  const u32 ts0 = (u32)kt[0]; \
  auto wrs = __builtin_amdgcn_make_buffer_rsrc((void*)kc[0], 0, WS_END, 0x00020000); \
  STAMP0()

  const char* tapb;
  const char* ub;
  char* erp;
  const char* eup;
  const char* egp;
  u4v At[17], band[4];
  float dm1;
  f4 acc[8];
  auto conv_setup = [&]() AI {
    tapb = ch_ + O_TAP + ((15 - c16) & 3) * TAP_PITCH + ((15 - c16) & ~3) * 2;
    ub = ch_ + O_U - 32 + c16 * 32 + g * 16;
    erp = ch_ + O_OUT + g * 144 + c16 * 8;
    eup = ch_ + O_U + 512 + c16 * 32 + g * 8;
    egp = smem + O_GT + lane * 16;
  };
  auto band_setup = [&]() AI {
#pragma unroll
    for (int k = 0; k < 4; ++k) {
      const int ctr = k == 0 ? 12 : 8, hw = k + 1;
      const u32 m = ((1u << (2 * hw + 1)) - 1u) << (ctr - hw);
      const u32 bits = (m << c16) >> (8 * g);
#pragma unroll
      for (int j = 0; j < 4; ++j)
        band[k][j] = (__builtin_amdgcn_sbfe(bits, 2 * j, 1) & 0x3f80u) | (__builtin_amdgcn_sbfe(bits, 2 * j + 1, 1) & 0x3f800000u);
      OPQ(band[k]);
    }
  };
  auto frag2 = [&](const char* pa, const char* pb) AI { const u2v lo = lds8(pa), hi = lds8(pb); return u4v{lo[0], lo[1], hi[0], hi[1]}; };
  auto atload = [&](int s0, int s1) AI {
#pragma unroll
    for (int s = s0; s < s1; ++s) At[s] = frag2(tapb + 64 * s + g * 16, tapb + 64 * s + g * 16 + 8);
  };
  auto bin = [&](int pair, int be) AI { const char* p = tapb + g * 8 + (SEG_I + 96 * pair - 16 * be) * 2; return frag2(p, p + 96); };
  auto bout = [&](int be) AI { const char* p = tapb + g * 8 + (SEG_O0 - 16 * be) * 2; return frag2(p, p + (SEG_O1 - SEG_O0) * 2); };
  auto asp = [&]() AI { return frag2(tapb + SEG_SP * 2 + g * 16, tapb + SEG_SP * 2 + g * 16 + 8); };
  auto mma = [&](u4v a, u4v bq, f4 cin) AI { return __builtin_amdgcn_mfma_f32_16x16x32_bf16(as_b8(a), as_b8(bq), cin, 0, 0, 0); };
  auto uld = [&](int sg) AI { return *(const u4v*)(ub + 512 + sg * 64); };
  auto uld24 = [&](int un) AI { return *(const u4v*)(ub + 512 + un * 512 - 16); };
  u2v euw[8], eo[8]; f4 egv[8], efv[8];
  float eu[8][4], ev[8][4], ea[8][4];
  u4v Fq[57], ru[6], wa_[6][3];
  f4 wq[6][6];
  u32 pk[12], one, qa, fa;
  char* tfp; char* fp_;
#define LOPQ(p_) do { u32 a_ = (u32)(uintptr_t)(__attribute__((address_space(3))) const char*)p_; OPQ(a_); p_ = (decltype(p_))(__attribute__((address_space(3))) char*)(uintptr_t)a_; } while (0)
  auto m_setup = [&]() AI {
    tfp = smem + (lane == 0 ? O_CTL + c * 4 : O_JUNK + lane * 4);
    fp_ = smem + O_FAR + c * 4096 + lane * 16;
    qa = CTLA(C_Q) + (lane & 3) * 4; fa = CTLA(C_FAR + c); one = 1u;
    OPQ(qa); OPQ(fa); OPQ(one);
    LOPQ(tfp); LOPQ(fp_); LOPQ(erp); LOPQ(eup); LOPQ(egp); LOPQ(ub);
  };
#define WAITA(addr) do { u32 t_; int n_ = 1 << 20; asm volatile( \
    "1:\n\tds_read_b32 %0, %2\n\ts_sub_u32 %1, %1, 1\n\ts_cbranch_scc1 2f\n\ts_waitcnt lgkmcnt(0)\n\tv_cmp_eq_u32 vcc, 0, %0\n\ts_cbranch_vccnz 1b\n2:\n\ts_waitcnt lgkmcnt(0)" \
    : "=&v"(t_), "+s"(n_) : "v"(addr) : "vcc", "scc", "memory"); } while (0)
#define MF(d_, a_, b_) asm volatile("v_mfma_f32_16x16x32_bf16 %0, %1, %2, %0" : "+v"(d_) : "v"(a_), "v"(b_)); SB()
#define MF0(d_, a_, b_) asm volatile("v_mfma_f32_16x16x32_bf16 %0, %1, %2, 0" : "=&v"(d_) : "v"(a_), "v"(b_)); SB()
#define NOP(n_) asm volatile("s_nop " #n_); SB()
#define FL(i_, sg_) Fq[i_] = uld(sg_); SB()
#define RL(un_) ru[un_] = uld24(un_); SB()
#define AL(s_) atload(s_, s_ + 1); SB()
#define ULD(t_) euw[t_] = *(const u2v*)(eup + t_ * 512); SB()
#define GLD(t_) egv[t_] = *(const f4*)(egp + t_ * 1024); SB()
#define FLD(t_) efv[t_] = *(const f4*)(fp_ + (t_ - 2) * 1024); SB()
#define FST(un_) *(f4*)(fp_ + un_ * 1024) = acc[un_ + 2]; SB()
#define EUN(t_, e_) eu[t_][e_] = (e_ & 1) ? bfhi(euw[t_][e_ >> 1]) : bflo(euw[t_][e_ >> 1]); OPQ(eu[t_][e_]); ev[t_][e_] = eu[t_][e_]; SB()
#define EA1(t_, e_) ea[t_][e_] = fmaf(eu[t_][e_], dm1, acc[t_][e_]); OPQ(ea[t_][e_]); SB()
#define EFA(t_, e_) ev[t_][e_] = fmaf(egv[t_][e_], efv[t_][e_], eu[t_][e_]); OPQ(ev[t_][e_]); SB()
#define EA2(t_, e_) ea[t_][e_] = fmaf(egv[t_][e_], ea[t_][e_], ev[t_][e_]); OPQ(ea[t_][e_]); SB()
#define EPK(t_, k_) { u32 v_ = pk_bf(ea[t_][2 * k_], ea[t_][2 * k_ + 1]); OPQ(v_); eo[t_][k_] = v_; } SB()
#define OST(t_) *(u2v*)(erp + t_ * OT) = eo[t_]; SB()
#define WPK(un_, p_, j_) { u32 v_ = pk_bf(wq[un_][2 * p_ + j_ / 2][2 * (j_ & 1)], wq[un_][2 * p_ + j_ / 2][2 * (j_ & 1) + 1]); OPQ(v_); wa_[un_][p_][j_] = v_; } SB()
#define FLAG(w_) asm volatile("" ::: "memory"); *(u32*)(tfp + (w_) * 4) = one; SB()
#define PEEK(i_, a_) pk[i_] = __hip_atomic_load((__attribute__((address_space(3))) u32*)(uintptr_t)(a_), __ATOMIC_RELAXED, __HIP_MEMORY_SCOPE_WORKGROUP); SB()
#define CHECK(i_, a_) if (__builtin_expect(__builtin_amdgcn_ballot_w64(pk[i_] == 0u) != 0ull, 0)) WAITA(a_); asm volatile("" ::: "memory"); SB()
#define MIX2(nm, nd, nv) _Pragma("unroll") for (int r_ = 0; r_ < (nm); ++r_) { __builtin_amdgcn_sched_group_barrier(0x008, 1, 0); \
    if (nd) __builtin_amdgcn_sched_group_barrier(0x100, (nd), 0); if (nv) __builtin_amdgcn_sched_group_barrier(0x002, (nv), 0); }

  {
    ROLE("x");
    const int xw = w;
    char* img = ch_ + O_U + 512;
    STAMP(11);
    __builtin_amdgcn_s_setprio(2);
    if (xi == 31 && w == 0) { const u32 th = ts0; if (lane == 0) __builtin_amdgcn_raw_buffer_store_b64(u2v{th, ~th}, wrs, WS_HEAD + xg * 8, 0, 16); }
    const char* wp; const char* fp; const char* tp0; const char* tp1; char* pp;
    {
      const int r = lane & 7, q = (lane >> 2) & 3;
      fp = img + r * 128 + ((((lane & 8) / 2 + g) ^ (r & 6)) * 16);
      wp = smem + O_FAR + xw * 1024 + (lane & 1) * 512 + (lane & 4) * 16 + g * 16;
      tp0 = img + q * 128 + (((2 * g + ((lane >> 1) & 1)) ^ (q & 2)) * 16) + (lane & 1) * 8;
      tp1 = img + 512 + q * 128 + (((2 * g + ((lane >> 1) & 1)) ^ ((q & 2) + 4)) * 16) + (lane & 1) * 8;
      pp = smem + ((lane >> 3) == 0 || (lane >> 3) == 3 ? O_PART + r * 64 + xw * 16 + g * 8 : O_JUNK + lane * 8);
    }
    const int so = WS_XT + (32 * xw + (lane >> 3)) * XT_PITCH + cg * 128 + (lane & 7) * 16, sstep = 8 * XT_PITCH;
    u32 lo_ = WS_XT + (cc >> 1) * XT_PITCH + (16 * xw + (lane >> 2)) * 128 + (cc & 1) * 64 + (lane & 3) * 16;
    OPQ(lo_);
    SB();
    BAR();
    STAMP(12);
    asm volatile("s_waitcnt vmcnt(0)" ::: "memory");
    STAMP(1);
    {
      u4v wa[4], fb[4]; u2v ta[4], tb[4];
#pragma unroll
      for (int s = 0; s < 4; ++s) { wa[s] = *(const u4v*)(wp + s * 128); fb[s] = *(const u4v*)(fp + s * 1024); }
      SB();
      f4 r = {0.f, 0.f, 0.f, 0.f};
#pragma unroll
      for (int s = 0; s < 4; ++s) r = mma(wa[s], fb[s], r);
      *(f2*)pp = f2{r[0], r[1]};
      ARRIVE(C_PART);
      SB();
#pragma unroll
      for (int s = 0; s < 4; ++s) { ta[s] = tr_read(tp0 + s * 1024); tb[s] = tr_read(tp1 + s * 1024); }
      SB();
      WAITQ(C_GP, 1);
#pragma unroll
      for (int s = 0; s < 4; ++s) __builtin_amdgcn_raw_buffer_store_b128(u4v{ta[s][0], ta[s][1], tb[s][0], tb[s][1]}, wrs, so + s * sstep, 0, 16);
    }
    STAMP(2);
    const u32 dskw = ((gp16)kb[0])[d];
    conv_setup();
    band_setup();
    const u32 tag = par[0];
    ARRIVE(C_PUB);
    asm volatile("s_waitcnt vmcnt(0)" ::: "memory");
    if (lane == 0) __builtin_amdgcn_raw_buffer_store_b32(tag, wrs, WS_FLG + xw * 1024 + cg * 4, 0, 16);
    STAMP(3);
    dm1 = bflo(dskw) - 1.f;
    WAITS(C_TL, 1);
    const u32 tl0 = (u32)__builtin_amdgcn_readfirstlane((int)par[1]);
    u3v fq[2];
    auto rec_ld = [&]() AI {
#pragma unroll
      for (int k = 0; k < 2; ++k) fq[k] = __builtin_amdgcn_raw_buffer_load_b96(wrs, WS_REC + xw * 2048 + k * 1024 + lane * 16, 0, 16);
    };
    pace(tl0 + DT_REC);
    rec_ld();
    SB();
    {
#pragma unroll 1
      for (int spin = 0; spin < SPIN_MAX && __builtin_expect(__any(fq[0][2] != tag || fq[1][2] != tag), 0); ++spin) {
        __builtin_amdgcn_s_sleep(SLEEP_G);
        rec_ld();
        asm volatile("" ::: "memory");
      }
      char* gp = smem + O_GT + xw * 2048 + (lane & 3) * 256 + (lane >> 2) * 16;
#pragma unroll
      for (int k = 0; k < 2; ++k) *(f4*)(gp + k * 1024) = f4{bflo(fq[k][0]), bfhi(fq[k][0]), bflo(fq[k][1]), bfhi(fq[k][1])};
    }
    pace(tl0 + DT_FLG);
    u4v xq[4];
    bool late[4];
    {
      const u32 fl = __builtin_amdgcn_raw_buffer_load_b32(wrs, WS_FLG + (cc >> 6) * 1024 + xw * 64 + g * 256 + c16 * 4, 0, 16);
      const u64 bad = __builtin_amdgcn_ballot_w64(fl != tag);
      asm volatile("" ::: "memory");
#pragma unroll
      for (int k = 0; k < 4; ++k) {
        late[k] = ((bad >> (16 * k)) & 0xffffull) != 0ull;
        xq[k] = __builtin_amdgcn_raw_buffer_load_b128(wrs, lo_ | (late[k] ? 1u << 30 : 0u), k * 8192, 16);
        SB();
      }
    }
    ARRIVE(C_XL);
    STAMP(4);
    WAITQ(C_TAPS + c, 1);
    u4v Bi[2][3], Bo[4], As;
#pragma unroll
    for (int be = 0; be < 3; ++be) { Bi[0][be] = bin(0, be); Bi[1][be] = bin(1, be); }
#pragma unroll
    for (int be = 0; be < 4; ++be) Bo[be] = bout(be);
    As = asp();
    WAITQ(C_PUB, 4);
    char* dp = smem + (lane & 3) * CHB + O_U + 512 + xw * 256 + (lane >> 2) * 16;
    m_setup();
    auto stage = [&](int k) AI {
      SB();
      if (__builtin_expect(late[k], 0)) {
#pragma unroll 1
        for (int spin = 0; spin < SPIN_MAX; ++spin) {
          STAMP(14);
          const u32 f1 = __builtin_amdgcn_raw_buffer_load_b32(wrs, WS_FLG + (cc >> 6) * 1024 + xw * 64 + k * 256 + c16 * 4, 0, 16);
          if (!__any(f1 != tag)) break;
          __builtin_amdgcn_s_sleep(SLEEP_G);
          asm volatile("" ::: "memory");
        }
        asm volatile("" ::: "memory");
        xq[k] = __builtin_amdgcn_raw_buffer_load_b128(wrs, lo_, k * 8192, 16);
        asm volatile("s_waitcnt vmcnt(0)" : "+v"(xq[k]) :: "memory");
      }
      *(u4v*)(dp + k * 1024) = xq[k];
      FLAG(C_Q + k * 4);
    };
    stage(0);
    stage(1);
    STAMP(5);
    __builtin_amdgcn_s_setprio(0);
    WAITA(qa);
    SB();
    FL(0, 0); RL(0); FL(8, 8); RL(1);
    MF0(wq[0][0], Fq[0], band[0]);
    MF0(wq[0][1], Fq[0], band[1]);
    MF0(wq[0][2], ru[0], band[0]);
    MF0(wq[0][3], ru[0], band[1]);
    MF0(wq[0][4], Fq[0], band[2]);
    MF0(wq[0][5], ru[0], band[3]);
    MF0(acc[4], As, Fq[0]); PEEK(6, qa + 16);
    MF0(wq[1][0], Fq[8], band[0]); WPK(0, 0, 0); WPK(0, 0, 1);
    MF0(wq[1][1], Fq[8], band[1]); WPK(0, 0, 2); WPK(0, 0, 3);
    MF0(wq[1][2], ru[1], band[0]); WPK(0, 1, 0); WPK(0, 1, 1);
    MF0(wq[1][3], ru[1], band[1]); WPK(0, 1, 2); WPK(0, 1, 3);
    MF0(wq[1][4], Fq[8], band[2]); CHECK(6, qa + 16);
    MF0(wq[1][5], ru[1], band[3]); WPK(0, 2, 0); WPK(0, 2, 1);
    MF0(acc[5], As, Fq[8]); WPK(0, 2, 2); WPK(0, 2, 3);
    MF0(acc[2], wa_[0][0], Bi[0][0]); FL(16, 16); RL(2);
    MF(acc[2], wa_[0][1], Bi[1][0]); WPK(1, 0, 0); WPK(1, 0, 1);
    MF0(acc[3], wa_[0][0], Bi[0][1]); WPK(1, 0, 2); WPK(1, 0, 3);
    MF(acc[3], wa_[0][1], Bi[1][1]); WPK(1, 1, 0); WPK(1, 1, 1);
    MF(acc[4], wa_[0][0], Bi[0][2]); FST(0); FLAG(C_FAR + 0 * 4);
    MF(acc[4], wa_[0][1], Bi[1][2]); WPK(1, 1, 2); WPK(1, 1, 3);
    MF(acc[4], wa_[0][2], Bo[0]); WPK(1, 2, 0); WPK(1, 2, 1);
    MF(acc[5], wa_[0][2], Bo[1]); WPK(1, 2, 2); WPK(1, 2, 3);
    MF0(acc[6], wa_[0][2], Bo[2]);
    MF0(acc[7], wa_[0][2], Bo[3]); stage(2);
    MF0(wq[2][0], Fq[16], band[0]);
    MF0(wq[2][1], Fq[16], band[1]); stage(3);
    MF0(wq[2][2], ru[2], band[0]);
    MF0(wq[2][3], ru[2], band[1]);
    MF0(wq[2][4], Fq[16], band[2]); FL(24, 24); RL(3);
    MF0(wq[2][5], ru[2], band[3]); WPK(2, 0, 0); WPK(2, 0, 1);
    MF(acc[6], As, Fq[16]); WPK(2, 0, 2); WPK(2, 0, 3);
    MF(acc[3], wa_[1][0], Bi[0][0]); WPK(2, 1, 0); WPK(2, 1, 1);
    MF(acc[3], wa_[1][1], Bi[1][0]); WPK(2, 1, 2); WPK(2, 1, 3);
    MF(acc[4], wa_[1][0], Bi[0][1]); WPK(2, 2, 0); WPK(2, 2, 1);
    MF(acc[4], wa_[1][1], Bi[1][1]); WPK(2, 2, 2); WPK(2, 2, 3);
    MF(acc[5], wa_[1][0], Bi[0][2]); FST(1); FLAG(C_FAR + 1 * 4);
    MF(acc[5], wa_[1][1], Bi[1][2]); PEEK(7, qa + 32);
    MF(acc[5], wa_[1][2], Bo[0]);
    MF(acc[6], wa_[1][2], Bo[1]);
    MF(acc[7], wa_[1][2], Bo[2]);
    MF0(wq[3][0], Fq[24], band[0]);
    MF0(wq[3][1], Fq[24], band[1]); CHECK(7, qa + 32);
    MF0(wq[3][2], ru[3], band[0]);
    MF0(wq[3][3], ru[3], band[1]);
    MF0(wq[3][4], Fq[24], band[2]); FL(32, 32); RL(4);
    MF0(wq[3][5], ru[3], band[3]); WPK(3, 0, 0); WPK(3, 0, 1);
    MF(acc[7], As, Fq[24]); WPK(3, 0, 2); WPK(3, 0, 3);
    MF(acc[4], wa_[2][0], Bi[0][0]); WPK(3, 1, 0); WPK(3, 1, 1);
    MF(acc[4], wa_[2][1], Bi[1][0]); WPK(3, 1, 2); WPK(3, 1, 3);
    MF(acc[5], wa_[2][0], Bi[0][1]); WPK(3, 2, 0); WPK(3, 2, 1);
    MF(acc[5], wa_[2][1], Bi[1][1]); FL(40, 40); RL(5);
    MF(acc[6], wa_[2][0], Bi[0][2]); FST(2); FLAG(C_FAR + 2 * 4);
    MF(acc[6], wa_[2][1], Bi[1][2]); WPK(3, 2, 2); WPK(3, 2, 3);
    MF(acc[6], wa_[2][2], Bo[0]); AL(0);
    MF(acc[7], wa_[2][2], Bo[1]); AL(1);
    MF0(wq[4][0], Fq[32], band[0]); AL(2);
    MF0(wq[4][1], Fq[32], band[1]); AL(3);
    MF0(wq[4][2], ru[4], band[0]); AL(4);
    MF0(wq[4][3], ru[4], band[1]); WPK(4, 0, 0); WPK(4, 0, 1);
    MF(acc[5], wa_[3][0], Bi[0][0]); WPK(4, 0, 2); WPK(4, 0, 3);
    MF(acc[5], wa_[3][1], Bi[1][0]); WPK(4, 1, 0); WPK(4, 1, 1);
    MF(acc[6], wa_[3][0], Bi[0][1]); WPK(4, 1, 2); WPK(4, 1, 3);
    MF(acc[6], wa_[3][1], Bi[1][1]); AL(5);
    MF(acc[7], wa_[3][0], Bi[0][2]); FST(3); FLAG(C_FAR + 3 * 4);
    MF(acc[7], wa_[3][1], Bi[1][2]); AL(6);
    MF(acc[7], wa_[3][2], Bo[0]); FL(33, 33); AL(7);
    MF0(wq[5][0], Fq[40], band[0]); FL(34, 34); AL(8);
    MF0(wq[5][1], Fq[40], band[1]); FL(35, 35); AL(9);
    MF0(wq[5][2], ru[5], band[0]); WPK(5, 0, 0); WPK(5, 0, 1);
    MF0(wq[5][3], ru[5], band[1]); AL(10);
    MF(acc[6], wa_[4][0], Bi[0][0]); PEEK(8, qa + 48); FL(36, 36);
    MF(acc[6], wa_[4][1], Bi[1][0]); FL(37, 37); WPK(5, 0, 2);
    MF(acc[7], wa_[4][0], Bi[0][1]); FL(38, 38); WPK(5, 0, 3);
    MF(acc[7], wa_[4][1], Bi[1][1]); FL(39, 39); WPK(5, 1, 0);
    MF(acc[6], At[0], Fq[32]); WPK(5, 1, 1); WPK(5, 1, 2);
    MF(acc[6], At[1], Fq[33]); CHECK(8, qa + 48);
    MF(acc[6], At[2], Fq[34]); WPK(5, 1, 3); FL(41, 41);
    MF(acc[6], At[3], Fq[35]); AL(11);
    MF(acc[7], wa_[5][0], Bi[0][0]); FL(42, 42); AL(12);
    MF(acc[7], wa_[5][1], Bi[1][0]); AL(13);
    MF(acc[6], At[4], Fq[36]); FL(43, 43); AL(14);
    MF(acc[6], At[5], Fq[37]); AL(15);
    MF(acc[6], At[6], Fq[38]); FL(44, 44); AL(16);
    MF(acc[6], At[7], Fq[39]);
    MF(acc[6], At[8], Fq[40]); FL(45, 45);
    MF(acc[7], At[0], Fq[40]);
    MF(acc[6], At[9], Fq[41]); FL(46, 46);
    MF(acc[7], At[1], Fq[41]);
    MF(acc[6], At[10], Fq[42]); FL(47, 47);
    MF(acc[7], At[2], Fq[42]);
    MF(acc[6], At[11], Fq[43]); FL(48, 48);
    MF(acc[7], At[3], Fq[43]);
    MF(acc[6], At[12], Fq[44]); FL(49, 49); ULD(6);
    MF(acc[7], At[4], Fq[44]); FL(50, 50); GLD(6);
    MF(acc[6], At[13], Fq[45]); FL(51, 51);
    MF(acc[7], At[5], Fq[45]); FL(52, 52);
    MF(acc[6], At[14], Fq[46]); FL(53, 53);
    MF(acc[7], At[6], Fq[46]); FL(54, 54); EUN(6, 0);
    MF(acc[6], At[15], Fq[47]); FL(55, 55); EUN(6, 1);
    MF(acc[7], At[7], Fq[47]); FL(56, 56); EUN(6, 2);
    MF(acc[6], At[16], Fq[48]); EUN(6, 3);
    MF(acc[7], At[8], Fq[48]); ULD(7); GLD(7);
    MF(acc[7], At[9], Fq[49]);
    MF(acc[7], At[10], Fq[50]);
    MF(acc[7], At[11], Fq[51]);
    MF(acc[7], At[12], Fq[52]); EA1(6, 0); EA1(6, 1); EA1(6, 2); EA1(6, 3);
    MF(acc[7], At[13], Fq[53]); EA2(6, 0); EA2(6, 1); EA2(6, 2); EA2(6, 3);
    MF(acc[7], At[14], Fq[54]); EPK(6, 0); EPK(6, 1);
    MF(acc[7], At[15], Fq[55]); OST(6); FLAG(C_TILE + 6 * 4);
    MF(acc[7], At[16], Fq[56]); EUN(7, 0); EUN(7, 1); EUN(7, 2); EUN(7, 3); NOP(3); EA1(7, 0); EA1(7, 1); EA1(7, 2); EA1(7, 3); EA2(7, 0); EA2(7, 1); EA2(7, 2); EA2(7, 3); EPK(7, 0); EPK(7, 1); OST(7); FLAG(C_TILE + 7 * 4);
    STAMP(9);
    ROLE_END();
  }
  {
    ROLE("h");
    const int hw = w - 5;
    const int j = xg * 28 + xi;
    const int s1 = j >> 2, kq = j & 3;
    const bool own = xi < 28;
    const u64 wbase = hw == 0 ? kb[1] : kb[2];
    const int pitch = F * 2;
    auto hrs = __builtin_amdgcn_make_buffer_rsrc((void*)wbase, 0, own ? D * F * 2 : 0, 0x00020000);
    const int r8 = lane >> 3;
    const int vo = (256 * kq * F + 64 * s1) * 2 + (8 * (r8 >> 2) + (r8 & 3)) * pitch + (((lane & 7) * 16) ^ (((r8 >> 1) & 3) * 32));
    auto dma = [&](int h, int sB, int pt, u32 dst) AI {
      const int m = h >> 1;
#pragma unroll
      for (int i = 0; i < 2; ++i)
        asm volatile("s_mov_b32 m0, %3\n\ts_nop 0\n\tbuffer_load_dwordx4 %0, %1, %2 offen nt lds"
                     :: "v"(vo), "s"(hrs), "s"((m & 1) * 32 * pt + (m >> 1) * sB + (16 * i + 4 * (h & 1)) * pt), "s"(dst + i * 1024) : "memory");
    };
    const int q = c16 >> 2, p = c16 & 3;
    int tro[4];
#pragma unroll
    for (int ct = 0; ct < 4; ++ct) tro[ct] = (4 * g + q) * 128 + ((ct ^ (((4 * g + q) >> 1) & 3)) * 32) + p * 8;
    const int tok = lane & 7;
    u4v wd[2][8];
    auto wd_ld = [&](int k, int cb) AI {
#pragma unroll
      for (int i = 0; i < 8; ++i) {
        const char* pp = smem + O_WD + cb * 8192 + (i >> 2) * 4096 + tro[i & 3];
        const u2v lo = tr_read(pp), hi = tr_read(pp + 2048);
        wd[k][i] = u4v{lo[0], lo[1], hi[0], hi[1]};
      }
    };
    u32 p2o, p2k[4], hlo, hhi;
    auto f2_setup = [&](int cb0) AI {
      p2o = WS_P2 + ((64 * kq + 16 * cb0 + (lane & 8) / 2 + g) * 56 + s1) * 128 + tok * 16;
      hlo = lbase + O_HB + ((lane & 8) ? 512 : g * 128 + tok * 16);
      hhi = lbase + O_HB + ((lane & 8) ? g * 128 + tok * 16 : 512);
#pragma unroll
      for (int i = 0; i < (cb0 == 2 ? 4 : 2); ++i) { p2k[i] = p2o + i * 8 * 7168; OPQ(p2k[i]); }
      OPQ(hlo); OPQ(hhi);
    };
    auto f2_job = [&](int nb, u32 t10) AI {
      u4v hb[4];
      {
        u32 cnt; int n_ = 1 << 20;
        asm volatile("1:\n\tds_read_b32 %4, %6\n\tds_read_b128 %0, %7\n\tds_read_b128 %1, %8\n\tds_read_b128 %2, %7 offset:528\n\tds_read_b128 %3, %8 offset:528\n\t"
                     "s_sub_u32 %5, %5, 1\n\ts_cbranch_scc1 2f\n\ts_waitcnt lgkmcnt(0)\n\tv_cmp_gt_u32 vcc, 2, %4\n\ts_cbranch_vccnz 1b\n2:\n\ts_waitcnt lgkmcnt(0)"
                     : "=&v"(hb[0]), "=&v"(hb[1]), "=&v"(hb[2]), "=&v"(hb[3]), "=&v"(cnt), "+s"(n_) : "v"(CTLA(C_HRDY)), "v"(hlo), "v"(hhi) : "vcc", "scc", "memory");
      }
      STAMP(11);
      f4 r[4];
#pragma unroll
      for (int i = 0; i < 2 * nb; ++i) {
        const u4v* wp = &wd[i >> 1][(i & 1) * 2];
        r[i] = mma(wp[0], hb[0], f4{0.f, 0.f, 0.f, 0.f});
        r[i] = mma(wp[1], hb[1], r[i]);
        r[i] = mma(wp[4], hb[2], r[i]);
        r[i] = mma(wp[5], hb[3], r[i]);
      }
      SB();
#pragma unroll
      for (int i = 0; i < 2 * nb; ++i) {
        u4v v = __builtin_bit_cast(u4v, r[i]);
#pragma unroll
        for (int e = 0; e < 4; ++e) v[e] = __builtin_amdgcn_bitop3_b32(v[e], t10, TMASK, 0xd8);
        __builtin_amdgcn_raw_buffer_store_b128(v, wrs, p2k[i], 0, 16);
      }
      STAMP(12);
    };
    const u32 rb = lbase + O_RING + hw * (NSLOT * 2048);
    pace(ts0 + WDELAY);
    STAMP(2);
#pragma unroll
    for (int h = 0; h < WWIN; ++h) dma(h, 64 * F * 2, F * 2, rb + h * 2048);
    __builtin_amdgcn_s_setprio(3);
    BAR();
    STAMP(3);
    if (__builtin_expect(own, 1)) {
      u4v wv[32];
      f4 a1[4] = {f4{0.f, 0.f, 0.f, 0.f}, f4{0.f, 0.f, 0.f, 0.f}, f4{0.f, 0.f, 0.f, 0.f}, f4{0.f, 0.f, 0.f, 0.f}};
      u4v xb[8];
      auto f1 = [&](int k0, int k1) AI {
#pragma unroll
        for (int k = k0; k < k1; ++k)
#pragma unroll
          for (int ct = 0; ct < 4; ++ct) a1[ct] = mma(wv[4 * k + ct], xb[k], a1[ct]);
      };
      u32 xo = lbase + O_XS + tok * XS_PITCH + g * 16;
      u32 p1o = WS_P1 + s1 * 16384 + kq * 4096 + hw * 2048 + (lane & 8) * 64 + g * 128 + tok * 16;
      u32 t10, tpub;
      OPQ(xo); OPQ(p1o);
      constexpr int HX = (16 - WWIN + 1) & ~1;
#pragma unroll
      for (int h = 0; h < 16; ++h) {
        const int left = (h + WWIN < 16 ? WWIN : 16 - h) - 1;
        asm volatile("s_waitcnt vmcnt(%0)" :: "n"(2 * left) : "memory");
        if (h + WWIN < 16) {
          asm volatile("s_waitcnt lgkmcnt(0)" ::: "memory");
          dma(h + WWIN, 64 * F * 2, F * 2, rb + ((h + WWIN) % NSLOT) * 2048);
        }
#pragma unroll
        for (int ct = 0; ct < 4; ++ct) {
          const u2v t = tr_read(smem + O_RING + hw * (NSLOT * 2048) + (h % NSLOT) * 2048 + tro[ct]);
          wv[4 * (h >> 1) + ct][2 * (h & 1)] = t[0]; wv[4 * (h >> 1) + ct][2 * (h & 1) + 1] = t[1];
        }
        if (h == H2_AT) ARRIVE(C_WGU);
        if (h == HX - 1) {
          STAMP(4);
          WAITQ(C_XS, 1);
          STAMP(1);
#pragma unroll
          for (int k = 0; k < 8; ++k) xb[k] = *(const __attribute__((address_space(3))) u4v*)(uintptr_t)(xo + k * 64);
          t10 = par[0]; tpub = par[2];
          SB();
          f1(0, 2);
          SB();
        }
        if (h >= HX) { f1(h - HX + 2, h - HX + 3); SB(); }
      }
      STAMP(5);
      f1(18 - HX, 8);
      t10 &= TMASK;
#pragma unroll
      for (int a = 0; a < 2; ++a) {
        u4v v;
#pragma unroll
        for (int e = 0; e < 4; ++e)
          v[e] = ((u32)__builtin_amdgcn_update_dpp(__float_as_int(a1[2 * a][e]), __float_as_int(a1[2 * a + 1][e]), 0xe4, 0xf, 0xc, false) & ~TMASK) | t10;
        __builtin_amdgcn_raw_buffer_store_b128(v, wrs, p1o + a * 1024, 0, 16);
      }
      STAMP(6);
      const u32 tn = now32();
      u32 voff = WS_P1 + s1 * 16384 + hw * 1024 + lane * 16;
      u32 ho = lbase + O_HB + hw * 528 + g * 128 + tok * 16 + (lane & 8);
      float nl2e = -1.44269504088896341f;
      OPQ(voff); OPQ(ho); OPQ(nl2e);
      f2_setup(hw & 1);
      pace2(tn, (u32)__builtin_amdgcn_readfirstlane((int)tpub) + DE_P1, DT_P1);
      u4v pb[8];
      auto p1_ld = [&]() AI {
#pragma unroll
        for (int i = 0; i < 8; ++i) { pb[i] = __builtin_amdgcn_raw_buffer_load_b128(wrs, voff + (i >> 2) * 2048, (i & 3) * 4096, 16); SB(); }
      };
      p1_ld();
      SB();
      f4 G, U;
#pragma unroll 1
      for (int spin = 0; spin < SPIN_MAX; ++spin) {
        u32 bad = 0;
        OPQ(bad);
        asm volatile("" :: "v"(pb[0]));
        const u32 cw = __hip_atomic_load(ctl + C_WD, __ATOMIC_RELAXED, __HIP_MEMORY_SCOPE_WORKGROUP);
        G = tsum4(bad, t10, pb[0], pb[1], pb[2], pb[3]);
        silu4(G, nl2e);
        if (__builtin_expect(__builtin_amdgcn_ballot_w64(cw == 0u) != 0ull, 0)) WAITQ(C_WD, 1);
        asm volatile("" ::: "memory");
        wd_ld(0, hw);
        SB();
        U = tsum4(bad, t10, pb[4], pb[5], pb[6], pb[7]);
        if (__builtin_expect(!__any((bad & TMASK) != 0), 1)) break;
        STAMP(14);
        p1_ld();
        asm volatile("" ::: "memory");
      }
      STAMP(7);
      {
        float hv[4];
#pragma unroll
        for (int e = 0; e < 4; ++e) { hv[e] = G[e] * U[e]; asm("" : "+v"(hv[e])); }
        *(__attribute__((address_space(3))) u2v*)(uintptr_t)ho = u2v{pk_bf(hv[0], hv[1]), pk_bf(hv[2], hv[3])};
      }
      ARRIVE(C_HRDY);
      STAMP(8);
      f2_job(1, t10);
    }
    if (hw == 0) {
      WAITS(C_SEL, 1);
      const u32 t10 = par[0] & TMASK;
      const u32 tpub = (u32)__builtin_amdgcn_readfirstlane((int)par[2]);
      const u32 mykey = ((const u32*)(smem + O_SEL))[lane & 7];
      if (own) pace2(now32(), tpub + DE_P2, DT_P2); else pace(tpub + DE_P2 - 40);
      u4v pa[7];
      const int voff = WS_P2 + cc * 7168 + lane * 16;
#pragma unroll
      for (int k = 0; k < 7; ++k) pa[k] = __builtin_amdgcn_raw_buffer_load_b128(wrs, voff + (k & 3) * 1024, (k >> 2) * 4096, 16);
      SB();
      const int row = (int)(~mykey & 2047u);
      const float rsig = rb1(sigm(__uint_as_float((mykey ^ (~(u32)((int)mykey >> 31) | 0x80000000u)) & 0xffff0000u)));
      WAITA(CTLA(C_TILE) + (lane & 31) * 4);
      float cf_ = rsig * (1.f - *(const float*)(smem + O_GT + (row >> 8) * 1024 + ((row >> 2) & 3) * 256 + ((row >> 4) & 15) * 16 + (row & 3) * 4));
      float base[4];
#pragma unroll
      for (int e = 0; e < 4; ++e) base[e] = bflo(*(const u16*)(smem + e * CHB + O_OUT + (row >> 8) * OT + ((row >> 2) & 3) * 144 + ((row >> 4) & 15) * 8 + (row & 3) * 2));
      u64 op = kc[1] + (row * D + cc * 4) * 2;
      { float cq = cf_; OPQ(cq); cf_ = cq; OPQ(op); }
#pragma unroll
      for (int e = 0; e < 4; ++e) OPQ(base[e]);
      STAMP(13);
      f4 y;
#pragma unroll 1
      for (int spin = 0; spin < SPIN_MAX; ++spin) {
        u32 bad = 0;
        OPQ(bad);
        y = tsum4(bad, t10, pa[0], pa[1], pa[2], pa[3]);
        y = tsum3(bad, t10, y, pa[4], pa[5], pa[6]);
        if (__builtin_expect(!__any((bad & TMASK) != 0), 1)) break;
        STAMP(15);
#pragma unroll
        for (int k = 0; k < 7; ++k) pa[k] = __builtin_amdgcn_raw_buffer_load_b128(wrs, voff + (k & 3) * 1024, (k >> 2) * 4096, 16);
        asm volatile("" ::: "memory");
      }
      STAMP(9);
      float o[4];
#pragma unroll
      for (int e = 0; e < 4; ++e) {
        float v = y[e] + dpp_f<0x128>(y[e]);
        auto s16 = __builtin_amdgcn_permlane16_swap(__float_as_uint(v), __float_as_uint(v), false, false);
        v = __uint_as_float(s16[0]) + __uint_as_float(s16[1]);
        auto s32 = __builtin_amdgcn_permlane32_swap(__float_as_uint(v), __float_as_uint(v), false, false);
        o[e] = fmaf(__uint_as_float(s32[0]) + __uint_as_float(s32[1]), cf_, base[e]);
      }
      if (lane < 8) *(__attribute__((address_space(1))) u2v*)op = u2v{pk_bf(o[0], o[1]), pk_bf(o[2], o[3])};
    }
    ROLE_END();
  }
  {
    ROLE("g");
    const u32 pLA = __builtin_nontemporal_load((gp32)ka[3] + d * (NM / 2) + lane);
    const u32 pAI = __builtin_nontemporal_load((gp32)ka[4] + d * (NM / 2) + lane);
    const u32 pCR = __builtin_nontemporal_load((gp32)ka[5] + d * (NM / 2) + lane);
    const u32 pCI = __builtin_nontemporal_load((gp32)ka[6] + d * (NM / 2) + lane);
    const u32 ldt = ((gp16)ka[7])[d], dskw = ((gp16)kb[0])[d];
    u4v ixa[2];
#pragma unroll
    for (int r = 0; r < 2; ++r) ixa[r] = *(const u4v*)(g_tapidx.v + lane * 16 + r * 8);
    BAR();
    asm volatile("s_mov_b32 m0, %0\n\ts_nop 0" :: "s"(lbase + c * CHB) : "memory");
#define PUT1(off, val) asm volatile("ds_write_addtid_b32 %0 offset:%1" :: "v"(val), "n"(off) : "memory")
#define PUTR(off, val) do { const ch v_ = (val); asm volatile("ds_write_addtid_b32 %0 offset:%2\n\tds_write_addtid_b32 %1 offset:%3" :: "v"(v_.r), "v"(v_.i), "n"(off), "n"((off) + 256) : "memory"); } while (0)
    const char* pf = ch_ + c16 * TROW + g * 16;
    PUT1(O_TAP, 0x3c003c00u); PUT1(O_TAP + 256, 0u);
    PUT1(O_U, 0u); PUT1(O_U + 256, 0u);
    SB();
    const float dt = rb1(exp_bf(bflo(ldt)));
    const f2 a2 = f2{-rb1(exp_bf(bflo(pLA))), -rb1(exp_bf(bfhi(pLA)))} * dt * 1.44269504088896341f;
    const f2 br = unbf(pAI) * dt * 0.15915494309189535f;
    STAMP(1);
    u4v Lf[2][8], Wf[8];
    f4 kk[2][2];
    float fk;
    {
      const cf z1 = cexp_p(a2, br, 1.f);
      const cf z2 = csq(z1), z4 = csq(z2), z8 = csq(z4), z16 = csq(z8), z32 = csq(z16), z64 = csq(z32);
      ch base[4], anc[4];
      base[1] = tohc(z8); base[2] = tohc(z16); base[3] = tohc(cmul(z16, z8));
      anc[1] = tohc(z32); anc[2] = tohc(z64); anc[3] = tohc(cmul(z64, z32));
      PUTR(O_OUT + 5 * TROW, tohc(csq(z64)));
#pragma unroll
      for (int j = 1; j < 16; ++j) {
        const int q = j >> 2, r = j & 3;
        PUTR(O_TAP + j * TROW, q == 0 ? base[r] : r == 0 ? anc[q] : hmul(anc[q], base[r]));
      }
#pragma unroll
      for (int s = 0; s < 8; ++s) Lf[0][s] = *(const u4v*)(pf + O_TAP + s * 64);
#pragma unroll
      for (int s = 0; s < 8; ++s) Lf[1][s] = *(const u4v*)(ch_ + O_OUT + 5 * TROW + g * 16 + s * 64);
      SB();
      STAMP(11);
      u32 mx = max(max(pCR << 17, pCR << 1), max(pCI << 17, pCI << 1));
      mx = wave_max_u32(mx);
      int ex = (int)(mx >> 24);
      ex = ex < 1 ? 1 : (ex > 253 ? 253 : ex);
      const float sdn = __uint_as_float((u32)(254 - ex) << 23);
      fk = 2.f * dt * __uint_as_float((u32)ex << 23);
      cf C; C.r = unbf(pCR) * sdn; C.i = unbf(pCI) * sdn;
      const cf E = cexp_p(a2, br, 256.f);
      const ch y1 = toh(z1), y2 = toh(z2), y4 = toh(z4), y6 = toh(cmul(z4, z2));
      {
        const ch R0 = toh(C), R2 = hmul(R0, y2), R4 = hmul(R0, y4), R6 = hmul(R0, y6);
        PUTR(O_TAP, R0); PUTR(O_TAP + 2 * TROW, R2); PUTR(O_TAP + 4 * TROW, R4); PUTR(O_TAP + 6 * TROW, R6);
        PUTR(O_TAP + 1 * TROW, hmul(R0, y1)); PUTR(O_TAP + 3 * TROW, hmul(R2, y1)); PUTR(O_TAP + 5 * TROW, hmul(R4, y1)); PUTR(O_TAP + 7 * TROW, hmul(R6, y1));
        C = cmul(C, E);
        const ch R8 = toh(C);
        PUTR(O_TAP + 8 * TROW, R8); PUTR(O_TAP + 9 * TROW, hmul(R8, y2)); PUTR(O_TAP + 10 * TROW, hmul(R8, y4)); PUTR(O_TAP + 11 * TROW, hmul(R8, y6));
        C = cmul(C, E);
        const ch R12 = toh(C);
        PUTR(O_TAP + 12 * TROW, R12); PUTR(O_TAP + 13 * TROW, hmul(R12, y4));
        C = cmul(C, E);
        const ch R14 = toh(C);
        PUTR(O_TAP + 14 * TROW, R14); PUTR(O_TAP + 15 * TROW, hmul(R14, y4));
      }
#pragma unroll
      for (int s = 0; s < 8; ++s) Wf[s] = *(const u4v*)(pf + O_TAP + s * 64);
      SB();
      STAMP(12);
#pragma unroll
      for (int i = 0; i < 5; ++i) { C = cmul(C, E); PUTR(O_OUT + i * TROW, toh(C)); }
      SB();
      auto mh = [&](u4v a, u4v bq, f4 cin) AI { return __builtin_amdgcn_mfma_f32_16x16x32_f16(as_h8(a), as_h8(bq), cin, 0, 0, 0); };
      kk[0][0] = f4{0.f, 0.f, 0.f, 0.f}; kk[0][1] = kk[0][0]; kk[1][0] = kk[0][0]; kk[1][1] = kk[0][0];
#pragma unroll
      for (int s = 0; s < 5; ++s) {
#pragma unroll
        for (int k = 0; k < 4; ++k) {
          if (k < 2) { if (s < 4) kk[0][0] = mh(Wf[s + 4 * k], Lf[0][s + 4 * k], kk[0][0]); }
          else if (s > 0) { const int u = s - 1 + 4 * (k - 2); kk[0][1] = mh(Wf[u], Lf[1][u], kk[0][1]); Wf[u] = *(const u4v*)(pf + O_OUT + u * 64); }
          SB();
          if (s < 4) {
            const ch o = hmul(ch{ash(Lf[0][s][k]), ash(Lf[0][s + 4][k])}, ch{ash(Lf[1][s][k]), ash(Lf[1][s + 4][k])});
            Lf[1][s][k] = __builtin_bit_cast(u32, o.r); Lf[1][s + 4][k] = __builtin_bit_cast(u32, o.i);
          }
          if (s == 3 && k == 3) asm volatile("s_nop 1");
          SB();
        }
      }
#pragma unroll
      for (int s = 0; s < 8; ++s) { kk[1][0] = mh(Wf[s], Lf[0][s], kk[1][0]); kk[1][1] = mh(Wf[s], Lf[1][s], kk[1][1]); }
    }
    {
      u32 ia[16];
      const u32 kb_ = lbase + c * CHB + O_OUT;
#pragma unroll
      for (int e = 0; e < 16; ++e) { const u32 iw = ixa[e >> 3][(e >> 1) & 3]; ia[e] = kb_ + ((e & 1) ? (iw >> 16) : (iw & 0xffffu)); }
      MIX2(16, 0, 1)
      SB();
      STAMP(2);
      char* ktp = ch_ + O_OUT + c16 * KT_PITCH + g * 8;
#pragma unroll
      for (int hf = 1; hf >= 0; --hf) {
#pragma unroll
        for (int ct = 0; ct < 2; ++ct) {
          const f4 v = kk[hf][ct] * fk;
          *(u32*)(ktp + ct * 16 * KT_PITCH + hf * 32) = pk_bf(v[0], v[1]);
          *(u32*)(ktp + ct * 16 * KT_PITCH + hf * 32 + 4) = pk_bf(v[2], v[3]);
        }
        asm volatile("" ::: "memory");
      }
      u32 zz = 0u; OPQ(zz);
      *(u32*)(ch_ + O_OUT + KT_ZERO + (lane & 7) * 4) = zz;
      asm volatile("" ::: "memory");
      u32 v[16], pe[9];
#pragma unroll
      for (int e = 15; e >= 0; --e) v[e] = *(const __attribute__((address_space(3))) u16*)(uintptr_t)ia[e];
      SB();
      pe[8] = zz;
#pragma unroll
      for (int i = 7; i >= 0; --i) {
        pe[i] = v[2 * i] | (v[2 * i + 1] << 16);
        const u32 nx = (u32)__builtin_amdgcn_update_dpp((int)dpp_u<0x134>(pe[i + 1]), (int)pe[i], 0x130, 0xf, 0xf, false);
        const u32 po = __builtin_amdgcn_alignbyte(nx, pe[i], 2);
        PUT1(O_TAP + i * 256, pe[i]); PUT1(O_TAP + 2 * TAP_PITCH - 4 + i * 256, pe[i]);
        PUT1(O_TAP + TAP_PITCH + i * 256, po); PUT1(O_TAP + 3 * TAP_PITCH - 4 + i * 256, po);
      }
      ARRIVE(C_TAPS + c);
    }
    STAMP(3);
    IDS();
    conv_setup();
    dm1 = bflo(dskw) - 1.f;
    atload(0, 17);
    m_setup();
    __builtin_amdgcn_s_setprio(1);
    SB();
    WAITA(qa);
    STAMP(4);
    SB();
    FL(0, -7); FL(1, -6); FL(2, -5); FL(3, -4); FL(4, -3); FL(5, -2); FL(6, -1); FL(7, 0); ULD(0); GLD(0);
    MF0(acc[0], At[9], Fq[0]); EUN(0, 0); EUN(0, 1);
    MF(acc[0], At[10], Fq[1]); EUN(0, 2); EUN(0, 3);
    MF(acc[0], At[11], Fq[2]);
    MF(acc[0], At[12], Fq[3]);
    MF(acc[0], At[13], Fq[4]);
    MF(acc[0], At[14], Fq[5]);
    MF(acc[0], At[15], Fq[6]); FL(8, 1);
    MF(acc[0], At[16], Fq[7]);
    MF0(acc[1], At[1], Fq[0]); FL(9, 2);
    MF(acc[1], At[2], Fq[1]);
    MF(acc[1], At[3], Fq[2]); FL(10, 3);
    MF(acc[1], At[4], Fq[3]);
    MF(acc[1], At[5], Fq[4]); FL(11, 4); EA1(0, 0);
    MF(acc[1], At[6], Fq[5]); EA1(0, 1); EA1(0, 2);
    MF(acc[1], At[7], Fq[6]); EA1(0, 3); PEEK(0, qa + 16);
    MF(acc[1], At[8], Fq[7]); FL(12, 5); EA2(0, 0);
    MF0(acc[2], At[0], Fq[7]); EA2(0, 1); EA2(0, 2);
    MF(acc[1], At[9], Fq[8]); EA2(0, 3); EPK(0, 0);
    MF(acc[2], At[1], Fq[8]); EPK(0, 1); FL(13, 6);
    MF(acc[1], At[10], Fq[9]); OST(0); FLAG(C_TILE + 0 * 4);
    MF(acc[2], At[2], Fq[9]); FL(14, 7); FL(15, 8);
    MF(acc[1], At[11], Fq[10]); CHECK(0, qa + 16);
    MF(acc[2], At[3], Fq[10]);
    MF(acc[1], At[12], Fq[11]); FL(16, 9);
    MF(acc[2], At[4], Fq[11]); ULD(1); GLD(1);
    MF(acc[1], At[13], Fq[12]); FL(17, 10);
    MF(acc[2], At[5], Fq[12]);
    MF(acc[1], At[14], Fq[13]); FL(18, 11);
    MF(acc[2], At[6], Fq[13]);
    MF(acc[1], At[15], Fq[14]); FL(19, 12); EUN(1, 0);
    MF(acc[2], At[7], Fq[14]); EUN(1, 1); EUN(1, 2);
    MF(acc[1], At[16], Fq[15]); FL(20, 13); EUN(1, 3);
    MF(acc[2], At[8], Fq[15]);
    MF0(acc[3], At[0], Fq[15]); FL(21, 14);
    MF(acc[2], At[9], Fq[16]);
    MF(acc[3], At[1], Fq[16]); FL(22, 15); EA1(1, 0);
    MF(acc[2], At[10], Fq[17]); EA1(1, 1); EA1(1, 2);
    MF(acc[3], At[2], Fq[17]); FL(23, 16); EA1(1, 3);
    MF(acc[2], At[11], Fq[18]); PEEK(2, fa + 0); EA2(1, 0);
    MF(acc[3], At[3], Fq[18]); EA2(1, 1); EA2(1, 2);
    MF(acc[2], At[12], Fq[19]); EA2(1, 3); EPK(1, 0);
    MF(acc[3], At[4], Fq[19]); EPK(1, 1); FL(24, 17);
    MF(acc[2], At[13], Fq[20]); OST(1); FLAG(C_TILE + 1 * 4);
    MF(acc[3], At[5], Fq[20]); FL(25, 18); CHECK(2, fa + 0);
    MF(acc[2], At[14], Fq[21]); FL(26, 19); ULD(2);
    MF(acc[3], At[6], Fq[21]); FLD(2); GLD(2);
    MF(acc[2], At[15], Fq[22]); FL(27, 20); PEEK(1, qa + 32);
    MF(acc[3], At[7], Fq[22]);
    MF(acc[2], At[16], Fq[23]); FL(28, 21);
    MF(acc[3], At[8], Fq[23]); EUN(2, 0); EUN(2, 1);
    MF0(acc[4], At[0], Fq[23]); FL(29, 22); EUN(2, 2);
    MF(acc[3], At[9], Fq[24]); EUN(2, 3); EA1(2, 0);
    MF(acc[4], At[1], Fq[24]); FL(30, 23); EA1(2, 1);
    MF(acc[3], At[10], Fq[25]); EA1(2, 2); EA1(2, 3);
    MF(acc[4], At[2], Fq[25]); FL(31, 24); EFA(2, 0);
    MF(acc[3], At[11], Fq[26]); EFA(2, 1); EFA(2, 2);
    MF(acc[4], At[3], Fq[26]); EFA(2, 3); PEEK(3, fa + 16);
    MF(acc[3], At[12], Fq[27]); EA2(2, 0); EA2(2, 1);
    MF(acc[4], At[4], Fq[27]); EA2(2, 2); EA2(2, 3);
    MF(acc[3], At[13], Fq[28]); CHECK(1, qa + 32); EPK(2, 0); EPK(2, 1);
    MF(acc[4], At[5], Fq[28]); FL(32, 25); OST(2);
    MF(acc[3], At[14], Fq[29]); FLAG(C_TILE + 2 * 4); FL(33, 26);
    MF(acc[4], At[6], Fq[29]); FL(34, 27); CHECK(3, fa + 16);
    MF(acc[3], At[15], Fq[30]); ULD(3); FL(35, 28);
    MF(acc[4], At[7], Fq[30]); FLD(3); GLD(3);
    MF(acc[3], At[16], Fq[31]); FL(36, 29);
    MF(acc[4], At[8], Fq[31]);
    MF0(acc[5], At[0], Fq[31]); FL(37, 30);
    MF(acc[4], At[9], Fq[32]); EUN(3, 0); EUN(3, 1);
    MF(acc[5], At[1], Fq[32]); PEEK(4, fa + 32); FL(38, 31);
    MF(acc[4], At[10], Fq[33]); EUN(3, 2); EUN(3, 3);
    MF(acc[5], At[2], Fq[33]); FL(39, 32); EA1(3, 0);
    MF(acc[4], At[11], Fq[34]); EA1(3, 1); EA1(3, 2);
    MF(acc[5], At[3], Fq[34]); EA1(3, 3); EFA(3, 0);
    MF(acc[4], At[12], Fq[35]); EFA(3, 1); EFA(3, 2); EFA(3, 3);
    MF(acc[5], At[4], Fq[35]); EA2(3, 0); EA2(3, 1); EA2(3, 2); EA2(3, 3);
    MF(acc[4], At[13], Fq[36]); FL(40, 33); EPK(3, 0); EPK(3, 1);
    MF(acc[5], At[5], Fq[36]); FL(41, 34); OST(3);
    MF(acc[4], At[14], Fq[37]); FL(42, 35); FLAG(C_TILE + 3 * 4); CHECK(4, fa + 32); ULD(4);
    MF(acc[5], At[6], Fq[37]); FL(43, 36); FLD(4); GLD(4); PEEK(5, fa + 48);
    MF(acc[4], At[15], Fq[38]); FL(44, 37); FL(45, 38);
    MF(acc[5], At[7], Fq[38]); FL(46, 39); FL(47, 40);
    MF(acc[4], At[16], Fq[39]);
    MF(acc[5], At[8], Fq[39]); EUN(4, 0); EUN(4, 1);
    MF(acc[5], At[9], Fq[40]); EUN(4, 2); EUN(4, 3);
    MF(acc[5], At[10], Fq[41]); EFA(4, 0); EA1(4, 0);
    MF(acc[5], At[11], Fq[42]); EA1(4, 1); EA1(4, 2);
    MF(acc[5], At[12], Fq[43]); EA1(4, 3); EFA(4, 1); EFA(4, 2); EFA(4, 3); CHECK(5, fa + 48); ULD(5);
    MF(acc[5], At[13], Fq[44]); EA2(4, 0); EA2(4, 1); EA2(4, 2); EA2(4, 3); FLD(5); GLD(5);
    MF(acc[5], At[14], Fq[45]); EPK(4, 0); EPK(4, 1);
    MF(acc[5], At[15], Fq[46]); OST(4); FLAG(C_TILE + 4 * 4);
    MF(acc[5], At[16], Fq[47]); EUN(5, 0); EUN(5, 1); EUN(5, 2); EUN(5, 3); EFA(5, 0); EFA(5, 1); EFA(5, 2); EFA(5, 3); EA1(5, 0); EA1(5, 1); EA1(5, 2); EA1(5, 3); EA2(5, 0); EA2(5, 1); EA2(5, 2); EA2(5, 3); EPK(5, 0); EPK(5, 1); OST(5); FLAG(C_TILE + 5 * 4);
    STAMP(8);
    ROLE_END();
  }
  {
    ROLE("c");
    __builtin_amdgcn_s_setprio(3);
    const u32 ep = *(gp32)(kc[0] + WS_EPOCH);
    {
      u32 zz = 0u; OPQ(zz);
      *(u2v*)(ctl + 2 * lane) = u2v{zz, zz};
      if (lane < 2) *(u4v*)(smem + O_HB + 512 + lane * 528) = u4v{zz, zz, zz, zz};
    }
    BAR();
    u32* selw = (u32*)(smem + O_SEL);
    u32 tag = (u32)__builtin_amdgcn_readfirstlane((int)ep) + 1u;
    tag += tag == 0u;
    if (lane == 0) par[0] = tag;
    {
      const u32 pa = lbase + O_PART + (lane & 7) * 64;
      const u32 ib = 2047u - (u32)(cg * 8 + (lane & 7));
      f4 p0, p1, p2, p3; u32 cnt; int n_ = 1 << 20; unsigned long long tk;
      asm volatile("1:\n\tds_read_b32 %4, %7\n\tds_read_b128 %0, %8\n\tds_read_b128 %1, %8 offset:16\n\tds_read_b128 %2, %8 offset:32\n\tds_read_b128 %3, %8 offset:48\n\t"
                   "s_memrealtime %6\n\ts_sub_u32 %5, %5, 1\n\ts_cbranch_scc1 2f\n\ts_waitcnt lgkmcnt(0)\n\tv_cmp_gt_u32 vcc, 4, %4\n\ts_cbranch_vccnz 1b\n2:\n\ts_waitcnt lgkmcnt(0)"
                   : "=&v"(p0), "=&v"(p1), "=&v"(p2), "=&v"(p3), "=&v"(cnt), "+s"(n_), "=&s"(tk) : "v"(CTLA(C_PART)), "v"(pa) : "vcc", "scc", "memory");
      const f4 s4_ = (p0 + p1) + (p2 + p3);
      const u32 rp = pk_bf(s4_[1] + s4_[3], s4_[0] + s4_[2]);
      const u32 key = ((rp ^ ((u32)((int)rp >> 31) | 0x80000000u)) & 0xffff0000u) | ib;
      const u32 kn = dpp_u<0xb1>(key);
      u32 m1 = max(key, kn), m2 = min(key, kn), m3;
      { const u32 o1 = dpp_u<0x4e>(m1), o2 = dpp_u<0x4e>(m2); const u32 lo = min(m1, o1), hb = max(m2, o2); m1 = max(m1, o1); m2 = max(lo, hb); m3 = min(lo, hb); }
      { const u32 o1 = dpp_u<0x141>(m1), o2 = dpp_u<0x141>(m2), o3 = dpp_u<0x141>(m3); const u32 lo = min(m1, o1), hb = max(m2, o2);
        m1 = max(m1, o1); m2 = max(lo, hb); m3 = max(min(lo, hb), max(m3, o3)); }
      const u32 wm = (((u32)tk + 8u) >> 2) & 0xffu;
      if (lane == 0) __builtin_amdgcn_raw_buffer_store_b128(u4v{m1, m2, (m3 & 0xffff0000u) | wm, tag}, wrs, WS_KEY + cg * 16, 0, 16);
      SB();
      STAMP(1);
      ARRIVE(C_GP);
      {
        const float gv = sigm(bflo(rp));
        const u32 g2 = pk_bf(gv, dpp_f<0xb1>(gv));
        u32 g3 = dpp_u<0x4e>(g2);
        OPQ(g3);
        if ((lane & 59) == 0) __builtin_amdgcn_raw_buffer_store_b128(u4v{g2, g3, tag, tag}, wrs, WS_REC + cg * 32 + lane * 4, 0, 16);
      }
      if ((lane & 57) == 0) __builtin_amdgcn_raw_buffer_store_b128(u4v{key, kn, 0u, tag}, wrs, WS_KALL + cg * 64 + lane * 8, 0, 16);
    }
    u32 l7 = (lane & 7) * 16, ko = WS_KEY + lane * 16;
    OPQ(l7); OPQ(ko);
    WAITQ(C_TL, 1);
    STAMP(5);
    const u32 tkey = (u32)__builtin_amdgcn_readfirstlane((int)par[1]) + DT_KEY;
    pace(tkey);
    u4v gr[4];
    u32 mykey;
    u4v xs[4];
    auto xrs = __builtin_amdgcn_make_buffer_rsrc((void*)(ka[0] + (xi & 3) * 512), 0, L * D * 2 - 1536, 0x00020000);
    asm volatile("" : "+s"(xrs));
    auto xs_ld = [&]() AI {
      const u32 vo = ((~mykey & 2047u) << 11) | l7;
#pragma unroll
      for (int k = 0; k < 4; ++k) xs[k] = __builtin_amdgcn_raw_buffer_load_b128(xrs, vo + k * 128, 0, 0);
    };
#pragma unroll
    for (int i = 0; i < 4; ++i) gr[i] = __builtin_amdgcn_raw_buffer_load_b128(wrs, ko + i * 1024, 0, 16);
#pragma unroll 1
    for (int spin = 0; spin < SPIN_MAX; ++spin) {
      const u32 bad = (gr[0][3] ^ tag) | (gr[1][3] ^ tag) | (gr[2][3] ^ tag) | (gr[3][3] ^ tag);
      if (__builtin_expect(!__any(bad != 0), 1)) break;
      STAMP(13);
      __builtin_amdgcn_s_sleep(SLEEP_AG1);
#pragma unroll
      for (int i = 0; i < 4; ++i)
        if (gr[i][3] != tag) gr[i] = __builtin_amdgcn_raw_buffer_load_b128(wrs, ko + i * 1024, 0, 16);
      asm volatile("" ::: "memory");
    }
    STAMP(2);
    {
      u32 m1 = gr[0][0], m2 = gr[0][1], m3 = 0, m4 = 0;
#pragma unroll
      for (int i = 2; i < 8; ++i) {
        const u32 k = gr[i >> 1][i & 1];
        if (i > 2) m4 = med3u(m3, m4, k);
        m3 = med3u(m2, m3, k);
        m2 = (i & 1) ? max(m2, k) : med3u(m1, m2, k);
        if (!(i & 1)) m1 = max(m1, k);
      }
      const u32 third = max(max(gr[0][2], gr[1][2]), max(gr[2][2], gr[3][2]));
      u32 W2;
      mykey = wave_top8(m1, m2, m3, m4, W2);
      xs_ld();
      SB();
      if (__builtin_expect(__any(m1 == 0u || (third | 0xffffu) > W2), 0)) {
        u4v ak[16];
#pragma unroll 1
        for (int spin = 0; spin < SPIN_MAX; ++spin) {
          u32 bad = 0;
#pragma unroll
          for (int i = 0; i < 16; ++i) {
            ak[i] = __builtin_amdgcn_raw_buffer_load_b128(wrs, WS_KALL + (i & 3) * 1024 + lane * 16, (i >> 2) * 4096, 16);
            bad |= ak[i][3] ^ tag;
          }
          if (!__any(bad != 0)) break;
          __builtin_amdgcn_s_sleep(8);
          asm volatile("" ::: "memory");
        }
        u32 prev = 0xffffffffu;
#pragma unroll 1
        for (int it = 0; it < 8; ++it) {
          u32 mm_ = 0;
#pragma unroll
          for (int i = 0; i < 32; ++i) {
            const u32 k = ak[i >> 1][i & 1];
            mm_ = max(mm_, k < prev ? k : 0u);
          }
          OPQ(mm_);
          prev = wave_max_u32(mm_);
          mykey = (lane >> 3) == it ? prev : mykey;
        }
        xs_ld();
      }
      OPQ(mykey);
    }
    SB();
    STAMP(3);
    if (l7 == 0) selw[lane >> 3] = mykey;
    {
      const u32 tn = now32();
      u32 yg = 0;
#pragma unroll
      for (int i = 0; i < 4; ++i) yg = max(yg, (gr[i][2] - (tn >> 2) - 1u) & 0xffu);
      const u32 tpub = tn - 4u * (255u - wave_max_u32(yg)) + DT_VIS;
      if (lane == 0) par[2] = (int)(tpub - tkey) > 0 ? tpub + DT_LATE : tkey;
    }
    ARRIVE(C_SEL);
    if (b == 255 && lane == 0) __builtin_amdgcn_raw_buffer_store_b32(tag, wrs, WS_EPOCH, 0, 16);
    SB();
    {
      char* xp = smem + O_XS + (lane >> 3) * XS_PITCH + l7;
#pragma unroll
      for (int k = 0; k < 4; ++k) *(u4v*)(xp + k * 128) = xs[k];
      ARRIVE(C_XS);
      STAMP(6);
    }
    __builtin_amdgcn_s_setprio(2);
    auto ors = __builtin_amdgcn_make_buffer_rsrc((void*)kc[1], 0, L * D * 2, 0x00020000);
    const u32 rl = (lane + (xi & 15) * 4) & 63, un = (g * 4 + (lane & 3) + xi) & 15;
    u32 skip = 0;
#pragma unroll
    for (int k = 0; k < 8; ++k) {
      const u32 r = ~(u32)__builtin_amdgcn_readlane((int)mykey, 8 * k) & 2047u;
      skip |= (r & 63u) == rl ? 1u << (r >> 6) : 0u;
    }
    const char* op = smem + ((lane >> 2) & 3) * CHB + O_OUT + (un & 3) * 144 + (un >> 2) * 8;
    const u32 ob = rl * 2048 + cc * 8;
    const u32 tfast = (u32)__builtin_amdgcn_readfirstlane((int)par[2]) + DE_FAST;
#pragma unroll 1
    for (int t = 0; t < 8; ++t) {
      WAITA(CTLA(C_TILE) + t * 16 + (lane & 3) * 4);
      u2v yv[4];
#pragma unroll
      for (int e = 0; e < 4; ++e) yv[e] = tr_read(op + t * OT + e * 32);
      const bool slow = (int)(now32() - tfast) < 0;
#pragma unroll
      for (int e = 0; e < 4; ++e) {
        __builtin_amdgcn_raw_buffer_store_b64(yv[e], ors, ob | (((skip >> e) & 1u) << 30), t * 524288 + e * 131072, 0);
        if (slow) {
          SB(); __builtin_amdgcn_s_sleep(STSLP); SB();
          asm volatile("s_waitcnt vmcnt(%0)" :: "n"(STWIN - 1) : "memory");
        }
      }
      skip >>= 4;
    }
    STAMP(9);
    ROLE_END();
  }
  {
    ROLE("d");
        const int j = xg * 28 + xi;
    const int s1 = j >> 2, kq = j & 3;
    const bool own = xi < 28;
    const u64 wbase = kb[3];
    const int pitch = D * 2;
    auto hrs = __builtin_amdgcn_make_buffer_rsrc((void*)wbase, 0, own ? D * F * 2 : 0, 0x00020000);
    const int r8 = lane >> 3;
    const int vo = (64 * s1 * D + 256 * kq) * 2 + (8 * (r8 >> 2) + (r8 & 3)) * pitch + (((lane & 7) * 16) ^ (((r8 >> 1) & 3) * 32));
    auto dma = [&](int h, int sB, int pt, u32 dst) AI {
      const int m = h >> 1;
#pragma unroll
      for (int i = 0; i < 2; ++i)
        asm volatile("s_mov_b32 m0, %3\n\ts_nop 0\n\tbuffer_load_dwordx4 %0, %1, %2 offen nt lds"
                     :: "v"(vo), "s"(hrs), "s"((m & 1) * 32 * pt + (m >> 1) * sB + (16 * i + 4 * (h & 1)) * pt), "s"(dst + i * 1024) : "memory");
    };
    const int q = c16 >> 2, p = c16 & 3;
    int tro[4];
#pragma unroll
    for (int ct = 0; ct < 4; ++ct) tro[ct] = (4 * g + q) * 128 + ((ct ^ (((4 * g + q) >> 1) & 3)) * 32) + p * 8;
    const int tok = lane & 7;
    u4v wd[2][8];
    auto wd_ld = [&](int k, int cb) AI {
#pragma unroll
      for (int i = 0; i < 8; ++i) {
        const char* pp = smem + O_WD + cb * 8192 + (i >> 2) * 4096 + tro[i & 3];
        const u2v lo = tr_read(pp), hi = tr_read(pp + 2048);
        wd[k][i] = u4v{lo[0], lo[1], hi[0], hi[1]};
      }
    };
    u32 p2o, p2k[4], hlo, hhi;
    auto f2_setup = [&](int cb0) AI {
      p2o = WS_P2 + ((64 * kq + 16 * cb0 + (lane & 8) / 2 + g) * 56 + s1) * 128 + tok * 16;
      hlo = lbase + O_HB + ((lane & 8) ? 512 : g * 128 + tok * 16);
      hhi = lbase + O_HB + ((lane & 8) ? g * 128 + tok * 16 : 512);
#pragma unroll
      for (int i = 0; i < (cb0 == 2 ? 4 : 2); ++i) { p2k[i] = p2o + i * 8 * 7168; OPQ(p2k[i]); }
      OPQ(hlo); OPQ(hhi);
    };
    auto f2_job = [&](int nb, u32 t10) AI {
      u4v hb[4];
      {
        u32 cnt; int n_ = 1 << 20;
        asm volatile("1:\n\tds_read_b32 %4, %6\n\tds_read_b128 %0, %7\n\tds_read_b128 %1, %8\n\tds_read_b128 %2, %7 offset:528\n\tds_read_b128 %3, %8 offset:528\n\t"
                     "s_sub_u32 %5, %5, 1\n\ts_cbranch_scc1 2f\n\ts_waitcnt lgkmcnt(0)\n\tv_cmp_gt_u32 vcc, 2, %4\n\ts_cbranch_vccnz 1b\n2:\n\ts_waitcnt lgkmcnt(0)"
                     : "=&v"(hb[0]), "=&v"(hb[1]), "=&v"(hb[2]), "=&v"(hb[3]), "=&v"(cnt), "+s"(n_) : "v"(CTLA(C_HRDY)), "v"(hlo), "v"(hhi) : "vcc", "scc", "memory");
      }
      STAMP(11);
      f4 r[4];
#pragma unroll
      for (int i = 0; i < 2 * nb; ++i) {
        const u4v* wp = &wd[i >> 1][(i & 1) * 2];
        r[i] = mma(wp[0], hb[0], f4{0.f, 0.f, 0.f, 0.f});
        r[i] = mma(wp[1], hb[1], r[i]);
        r[i] = mma(wp[4], hb[2], r[i]);
        r[i] = mma(wp[5], hb[3], r[i]);
      }
      SB();
#pragma unroll
      for (int i = 0; i < 2 * nb; ++i) {
        u4v v = __builtin_bit_cast(u4v, r[i]);
#pragma unroll
        for (int e = 0; e < 4; ++e) v[e] = __builtin_amdgcn_bitop3_b32(v[e], t10, TMASK, 0xd8);
        __builtin_amdgcn_raw_buffer_store_b128(v, wrs, p2k[i], 0, 16);
      }
      STAMP(12);
    };
    {
      __builtin_amdgcn_s_setprio(3);
      BAR();
      const u32 ts = ts0 - 500u;
      u32 tl = ~0u;
      pace(ts0 + DT_HEAD);
      STAMP(3);
#pragma unroll 1
      for (int i = 0; i < 8 && tl >= 1500u; ++i) {
        const u2v hd = __builtin_amdgcn_raw_buffer_load_b64(wrs, WS_HEAD + (lane & 7) * 8, 0, 16);
        tl = wave_max_u32(hd[0] == ~hd[1] ? hd[0] - ts : ~0u);
        STAMP(14);
        asm volatile("" ::: "memory");
      }
      tl = tl < 1500u ? tl + ts : ts0;
      if (lane == 0) par[1] = tl;
      ARRIVE(C_TL);
      STAMP(2);
      if (own) {
        WAITS(C_WGU, 1);
        WAITS(C_SEL, 1);
#pragma unroll
        for (int h = 0; h < 16; ++h) {
          if (h == 3) WAITS(C_XL, 4);
          if (h >= DWIN) asm volatile("s_waitcnt vmcnt(%0)" :: "n"(2 * (DWIN - 1)) : "memory");
          dma(h, 128, D * 2, lbase + O_WD + h * 2048);
        }
        asm volatile("s_waitcnt vmcnt(0)" ::: "memory");
        ARRIVE(C_WD);
        STAMP(5);
        wd_ld(0, 2); wd_ld(1, 3);
        f2_setup(2);
        WAITS(C_HRDY, 1);
        f2_job(2, par[0] & TMASK);
      }
      ROLE_END();
    }
  }
}

extern "C" void kernel_launch(void* const* d_in, const int* in_sizes, int n_in,
                              void* d_out, int out_size, void* d_ws, size_t ws_size,
                              hipStream_t stream) {
  k_main<<<256, 768, 0, stream>>>((const u16*)d_in[0], (const u16*)d_in[8], (const u16*)d_in[12],
                                  (const u16*)d_in[1], (const u16*)d_in[2], (const u16*)d_in[3],
                                  (const u16*)d_in[4], (const u16*)d_in[5], (const u16*)d_in[6],
                                  (const u16*)d_in[9], (const u16*)d_in[10], (const u16*)d_in[11],
                                  (char*)d_ws, (u16*)d_out);
}
```

```cpp
#include <hip/hip_runtime.h>
#include <stdint.h>

typedef unsigned short u16;
typedef unsigned u32;
typedef _Float16 h2 __attribute__((ext_vector_type(2)));
typedef _Float16 h8 __attribute__((ext_vector_type(8)));
typedef __bf16 b2 __attribute__((ext_vector_type(2)));
typedef __bf16 b8 __attribute__((ext_vector_type(8)));
typedef short s4 __attribute__((ext_vector_type(4)));
typedef float f2 __attribute__((ext_vector_type(2)));
typedef float f4 __attribute__((ext_vector_type(4)));
typedef u32 u2v __attribute__((ext_vector_type(2)));
typedef u32 u3v __attribute__((ext_vector_type(3)));
typedef u32 u4v __attribute__((ext_vector_type(4)));
typedef __attribute__((address_space(3))) s4* lds_s4p;

constexpr int L = 2048;
constexpr int D = 1024;
constexpr int NM = 128;
constexpr int F = 3584;
constexpr int NTAP = 1024;

constexpr int SEG_I = 576, SEG_O0 = 800, SEG_O1 = 880, SEG_SP = 896;

#define AI __attribute__((always_inline))
#define SB() __builtin_amdgcn_sched_barrier(0)
#define OPQ(x) asm volatile("" : "+v"(x))
#define STAMP0() do {} while (0)
#define STAMP(i)
#define STAMP_END()
#define STAMP_T0

__device__ __forceinline__ float bflo(u32 v) { return __uint_as_float(v << 16); }
__device__ __forceinline__ float bfhi(u32 v) { return __uint_as_float(v & 0xffff0000u); }
__device__ __forceinline__ u32 pk_bf(float a, float b) { f2 v = {a, b}; b2 r = __builtin_convertvector(v, b2); return __builtin_bit_cast(u32, r); }
__device__ __forceinline__ float rb1(float v) { return __uint_as_float(pk_bf(0.f, v)); }
__device__ __forceinline__ h8 as_h8(u4v v) { return __builtin_bit_cast(h8, v); }
__device__ __forceinline__ b8 as_b8(u4v v) { return __builtin_bit_cast(b8, v); }
__device__ __forceinline__ u2v tr_read(const char* p) {
  s4 t = __builtin_amdgcn_ds_read_tr16_b64_v4i16((lds_s4p)p);
  return __builtin_bit_cast(u2v, t);
}
template <int CTRL> __device__ __forceinline__ u32 dpp_u(u32 v) { return (u32)__builtin_amdgcn_update_dpp(0, (int)v, CTRL, 0xf, 0xf, false); }
template <int CTRL> __device__ __forceinline__ float dpp_f(float v) { return __uint_as_float(dpp_u<CTRL>(__float_as_uint(v))); }
__device__ __forceinline__ u32 wave_max_u32(u32 v) {
  v = max(v, dpp_u<0x111>(v)); v = max(v, dpp_u<0x112>(v)); v = max(v, dpp_u<0x114>(v)); v = max(v, dpp_u<0x118>(v));
  v = max(v, dpp_u<0x142>(v)); v = max(v, dpp_u<0x143>(v));
  return (u32)__builtin_amdgcn_readlane((int)v, 63);
}
#define DPM " row_mask:0xf bank_mask:0xf bound_ctrl:1\n\t"
#define T2S(HO, HI, LI, C) "v_max_u32_dpp " HO ", " HI ", " HI " " C DPM "v_min_u32_dpp %8, " HI ", " HI " " C DPM "v_max_u32_dpp %7, " LI ", " LI " " C DPM "v_max_u32 %7, %7, %8\n\t"
#define T2R(RM, REST) T2S("%5", "%0", "%1", "row_shr:1") T2S("%6", "%5", "%7", "row_shr:2") T2S("%5", "%6", "%7", "row_shr:4") T2S("%6", "%5", "%7", "row_shr:8") \
    T2S("%5", "%6", "%7", "row_bcast:15") T2S("%6", "%5", "%7", "row_bcast:31") \
    "v_readlane_b32 %9, %6, 63\n\tv_readlane_b32 %10, %7, 63\n\ts_nop 0\n\tv_mov_b32 %5, %9\n\tv_cmp_le_u32 %11, %10, %0\n\tv_cmp_le_u32 %12, %10, %1\n\tv_mov_b32 %6, %10\n\t" \
    "v_cndmask_b32 %0, %0, %1, %11\n\tv_mov_b32_dpp %4, %5 quad_perm:[0,1,2,3] row_mask:" RM " bank_mask:0x3\n\tv_cndmask_b32 %0, %0, %2, %12\n\t" \
    "v_mov_b32_dpp %4, %6 quad_perm:[0,1,2,3] row_mask:" RM " bank_mask:0xc\n\t" REST
#define T2P "v_cndmask_b32 %1, %1, %2, %11\n\tv_cndmask_b32 %2, %2, %3, %11\n\tv_cndmask_b32 %1, %1, %3, %12\n\tv_cndmask_b32 %3, %3, 0, %11\n\tv_cndmask_b32 %2, %2, 0, %12\n\t"
__device__ __forceinline__ u32 wave_top8(u32& m1, u32 m2, u32 m3, u32 m4, u32& w2) {
  u32 key, ta, tb, lo, t, w1;
  unsigned long long c1, c2;
  asm("s_nop 1\n\t" T2R("0x1", T2P) T2R("0x2", T2P) T2R("0x4", T2P) T2R("0x8", "") "s_nop 0"
      : "+v"(m1), "+v"(m2), "+v"(m3), "+v"(m4), "=&v"(key), "=&v"(ta), "=&v"(tb), "=&v"(lo), "=&v"(t), "=&s"(w1), "=&s"(w2), "=&s"(c1), "=&s"(c2));
  return key;
}

__device__ __forceinline__ float exp_bf(float v) {
  const float e = __builtin_amdgcn_exp2f(v * 1.44268798828125f);
  return fmaf(e, v * (7.0526077e-06f * 0.69314718f), e);
}
struct cf { f2 r, i; };
struct ch { h2 r, i; };
__device__ __forceinline__ cf cmul(cf a, cf b) { cf c; c.r = a.r * b.r - a.i * b.i; c.i = a.r * b.i + a.i * b.r; return c; }
__device__ __forceinline__ ch hmul(ch a, ch b) {
  ch c;
  asm("v_pk_mul_f16 %0, %2, %4\n\tv_pk_mul_f16 %1, %2, %5\n\tv_pk_fma_f16 %0, %3, %5, %0 neg_lo:[1,0,0] neg_hi:[1,0,0]\n\tv_pk_fma_f16 %1, %3, %4, %1"
      : "=&v"(c.r), "=&v"(c.i) : "v"(a.r), "v"(a.i), "v"(b.r), "v"(b.i));
  return c;
}
__device__ __forceinline__ cf csq(cf a) { cf c; c.r = a.r * a.r - a.i * a.i; c.i = (a.r + a.r) * a.i; return c; }
__device__ __forceinline__ ch toh(cf a) { ch c; c.r = __builtin_convertvector(a.r, h2); c.i = __builtin_convertvector(a.i, h2); return c; }
__device__ __forceinline__ ch tohc(cf a) { ch c; c.r = __builtin_convertvector(a.r, h2); c.i = __builtin_convertvector(-a.i, h2); return c; }
__device__ __forceinline__ h2 ash(u32 v) { return __builtin_bit_cast(h2, v); }
__device__ __forceinline__ cf cexp_p(f2 a2, f2 brev, float p) {
  f2 e = a2 * p, t = brev * p;
  f2 mag = {__builtin_amdgcn_exp2f(e[0]), __builtin_amdgcn_exp2f(e[1])};
  float r0 = __builtin_amdgcn_fractf(t[0]), r1 = __builtin_amdgcn_fractf(t[1]);
  cf c;
  c.r = mag * f2{__builtin_amdgcn_cosf(r0), __builtin_amdgcn_cosf(r1)};
  c.i = mag * f2{__builtin_amdgcn_sinf(r0), __builtin_amdgcn_sinf(r1)};
  return c;
}
__device__ __forceinline__ u32 med3u(u32 a, u32 b, u32 c) { u32 r; asm("v_med3_u32 %0, %1, %2, %3" : "=v"(r) : "v"(a), "v"(b), "v"(c)); return r; }
__device__ __forceinline__ f2 unbf(u32 v) { return f2{bflo(v), bfhi(v)}; }
__device__ __forceinline__ float sigm(float v) { return __builtin_amdgcn_rcpf(1.f + __builtin_amdgcn_exp2f(-1.44269504088896341f * v)); }

#define TS2(A, B, C, D) "v_xor_b32 %5, " A ", %9\n\tv_xor_b32 %6, " B ", %9\n\tv_xor_b32 %7, " C ", %9\n\tv_xor_b32 %8, " D ", %9\n\t" \
    "v_fma_f32 %1, %5, 1.0, %1\n\tv_fma_f32 %2, %6, 1.0, %2\n\tv_or3_b32 %0, %0, %5, %6\n\tv_fma_f32 %3, %7, 1.0, %3\n\tv_fma_f32 %4, %8, 1.0, %4\n\tv_or3_b32 %0, %0, %7, %8\n\t"
#define TSR TS2("%10", "%11", "%12", "%13") TS2("%14", "%15", "%16", "%17") TS2("%18", "%19", "%20", "%21")
#define TSI "=&v"(t0), "=&v"(t1), "=&v"(t2), "=&v"(t3) : "v"(t10), "v"(p[0]), "v"(p[1]), "v"(p[2]), "v"(p[3]), "v"(q[0]), "v"(q[1]), "v"(q[2]), "v"(q[3]), \
    "v"(r[0]), "v"(r[1]), "v"(r[2]), "v"(r[3])
__device__ __forceinline__ f4 tsum4(u32& bad, u32 t10, u4v s, u4v p, u4v q, u4v r) {
  float s0, s1, s2, s3; u32 t0, t1, t2, t3;
  asm volatile("v_xor_b32 %1, %22, %9\n\tv_xor_b32 %2, %23, %9\n\tv_xor_b32 %3, %24, %9\n\tv_xor_b32 %4, %25, %9\n\tv_or3_b32 %0, %0, %1, %2\n\tv_or3_b32 %0, %0, %3, %4\n\t" TSR
               : "+v"(bad), "=&v"(s0), "=&v"(s1), "=&v"(s2), "=&v"(s3), TSI, "v"(s[0]), "v"(s[1]), "v"(s[2]), "v"(s[3]));
  return f4{s0, s1, s2, s3};
}
__device__ __forceinline__ f4 tsum3(u32& bad, u32 t10, f4 s, u4v p, u4v q, u4v r) {
  float s0 = s[0], s1 = s[1], s2 = s[2], s3 = s[3]; u32 t0, t1, t2, t3;
  asm volatile(TSR : "+v"(bad), "+v"(s0), "+v"(s1), "+v"(s2), "+v"(s3), TSI);
  return f4{s0, s1, s2, s3};
}
__device__ __forceinline__ void silu4(f4& g, float nl2e) {
  float g0 = g[0], g1 = g[1], g2 = g[2], g3 = g[3], t0, t1, t2, t3;
  asm volatile("v_fma_f32 %4, %0, %8, 0\n\tv_fma_f32 %5, %1, %8, 0\n\tv_fma_f32 %6, %2, %8, 0\n\tv_fma_f32 %7, %3, %8, 0\n\t"
               "v_exp_f32 %4, %4\n\tv_exp_f32 %5, %5\n\tv_exp_f32 %6, %6\n\tv_exp_f32 %7, %7\n\t"
               "v_fma_f32 %4, %4, 1.0, 1.0\n\tv_fma_f32 %5, %5, 1.0, 1.0\n\tv_fma_f32 %6, %6, 1.0, 1.0\n\tv_fma_f32 %7, %7, 1.0, 1.0\n\t"
               "v_rcp_f32 %4, %4\n\tv_rcp_f32 %5, %5\n\tv_rcp_f32 %6, %6\n\tv_rcp_f32 %7, %7\n\t"
               "v_fma_f32 %0, %0, %4, 0\n\tv_fma_f32 %1, %1, %5, 0\n\tv_fma_f32 %2, %2, %6, 0\n\tv_fma_f32 %3, %3, %7, 0"
               : "+v"(g0), "+v"(g1), "+v"(g2), "+v"(g3), "=&v"(t0), "=&v"(t1), "=&v"(t2), "=&v"(t3) : "v"(nl2e));
  g = f4{g0, g1, g2, g3};
}

constexpr int KT_PITCH = 52;
constexpr int kt_off(int p) {
  int i = 20, j = 0;
  if (p < 256) { i = p & 7; j = p >> 3; }
  else if (p < 512) { int q = (p - 256) >> 1; i = 8 + (q & 3); j = q >> 2; }
  else if (p < 1024) { int q = (p - 512) >> 2; i = 12 + (q & 1) + 2 * (q >> 6); j = (q >> 1) & 31; }
  else if (p < 2048) { int q = (p - 1024) >> 3; i = 16 + (q >> 5); j = q & 31; }
  return j * KT_PITCH + i * 2;
}
constexpr int KT_ZERO = 32 * KT_PITCH + 16;
constexpr int rnd_lag(int m) {
  const int st = m < 256 ? 1 : m < 512 ? 2 : m < 1024 ? 4 : 8;
  int q = m / st;
  const int rem = m - q * st;
  if (2 * rem > st || (2 * rem == st && (q & 1))) ++q;
  return q * st;
}
constexpr int tap_entry(int e) {
  if (e < 544) {
    if (543 - e <= 525) return kt_off(rnd_lag(543 - e));
  } else if (e >= 560 && e < 752) {
    const int q = (e - 560) / 48, x = (e - 560) - q * 48;
    const int p = 512 + 16 * (31 - x) + 4 * (q + 1);
    if (x < 32 && p > 524) return kt_off(p);
  } else if (e >= 752 && e < 816) {
    return kt_off(1032 + 16 * (815 - e));
  } else if (e >= 832 && e < 896) {
    return kt_off(1040 + 16 * (895 - e));
  } else if (e == SEG_SP + 27 || e == SEG_SP + 28) {
    return kt_off(1024);
  }
  return KT_ZERO;
}
struct TapIdx { u16 v[NTAP]; };
constexpr TapIdx make_tapidx() {
  TapIdx t{};
  for (int e = 0; e < NTAP; ++e) t.v[e] = (u16)tap_entry(2 * ((e >> 4) + 64 * ((e >> 1) & 7)) + (e & 1));
  return t;
}
__device__ __attribute__((aligned(16))) const TapIdx g_tapidx = make_tapidx();

constexpr int TROW = 544;
constexpr int O_OUT = 0;
constexpr int OT = 576;
constexpr int O_U = O_OUT + 8 * OT;
constexpr int O_TAP = O_U + 4608;
constexpr int TAP_PITCH = 2 * NTAP + 64;
constexpr int CHB = O_TAP + 16 * TROW + 32;
static_assert(16 * TROW >= 4 * TAP_PITCH, "lds");
static_assert((CHB / 4) % 32 == 8, "banks");
#define DEV 3
#define DWIN 8
#define DSEL 0
constexpr int NSLOT = 5, WWIN = 4;
constexpr int O_GT = 4 * CHB;
constexpr int O_RING = O_GT + 8192;
constexpr int O_WD = O_RING + 2 * NSLOT * 2048;
constexpr int O_FAR = O_WD + 32768;
constexpr int O_XS = O_FAR + 16384;
constexpr int XS_PITCH = 544;
constexpr int O_HB = O_XS + 8 * XS_PITCH;
constexpr int O_PART = O_HB + 2 * 528;
constexpr int O_SEL = O_PART + 512;
constexpr int O_PAR = O_SEL + 32;
constexpr int O_CTL = O_PAR + 16;
constexpr int O_JUNK = O_CTL + 512;
constexpr int K_LDS = O_JUNK + 1024;
static_assert(DWIN == 8, "flags of the down tile");
static_assert(K_LDS <= 163840 && O_RING % 16 == 0, "lds");
enum { C_PART = 0, C_GP, C_PUB, C_SEL, C_XS, C_HRDY, C_WGU, C_TL, C_XL, C_WD = 10, C_TAPS = 12, C_Q = 16, C_FAR = 32, C_TILE = 48 };

#define CTLA(word) (lbase + O_CTL + (word) * 4)
#define ARRIVE(word) asm volatile("s_mov_b64 exec, 1\n\tds_add_u32 %0, %1\n\ts_mov_b64 exec, -1" :: "v"(CTLA(word)), "v"(1) : "memory")
#define WAITQ(word, target) do { u32 t_; int n_ = 1 << 20; asm volatile( \
    "1:\n\tds_read_b32 %0, %2\n\ts_sub_u32 %1, %1, 1\n\ts_cbranch_scc1 2f\n\ts_waitcnt lgkmcnt(0)\n\tv_cmp_gt_u32 vcc, %3, %0\n\ts_cbranch_vccnz 1b\n2:\n\ts_waitcnt lgkmcnt(0)" \
    : "=&v"(t_), "+s"(n_) : "v"(CTLA(word)), "n"(target) : "vcc", "scc", "memory"); } while (0)
#define WAITS(word, target) do { u32 t_; int n_ = 1 << 20; asm volatile( \
    "1:\n\tds_read_b32 %0, %2\n\ts_sub_u32 %1, %1, 1\n\ts_cbranch_scc1 2f\n\ts_waitcnt lgkmcnt(0)\n\tv_cmp_le_u32 vcc, %3, %0\n\ts_cbranch_vccnz 2f\n\ts_sleep 2\n\ts_branch 1b\n2:\n\ts_waitcnt lgkmcnt(0)" \
    : "=&v"(t_), "+s"(n_) : "v"(CTLA(word)), "n"(target) : "vcc", "scc", "memory"); } while (0)
#define BAR() asm volatile("s_waitcnt lgkmcnt(0)\n\ts_barrier" ::: "memory")
__device__ __forceinline__ u2v lds8(const char* p) { return __builtin_bit_cast(u2v, __hip_atomic_load((const unsigned long long*)p, __ATOMIC_RELAXED, __HIP_MEMORY_SCOPE_WORKGROUP)); }

constexpr int WS_EPOCH = 0;
constexpr int WS_HEAD = 1024;
constexpr int WS_REC = 4096;
constexpr int WS_FLG = 40960;
constexpr int WS_KALL = WS_REC + 8192;
constexpr int WS_KEY = 32768;
constexpr int WS_P1 = 65536;
constexpr int WS_P2 = WS_P1 + 56 * 16384;
constexpr int WS_XT = WS_P2 + 256 * 7168;
constexpr int XT_PITCH = 33280;
constexpr int WS_END = WS_XT + 128 * XT_PITCH;
constexpr int SPIN_MAX = 1 << 16;
constexpr u32 TMASK = 0x3ffu;

#define DT_HEAD 110
#define WDELAY 60
#define DT_KEY 240
#define DT_REC 270
#define DT_FLG 380
#define DT_VIS 40
#define DT_LATE 50
#define DE_P1 350
#define DE_P2 590
#define DE_FAST 575
#define DT_MIN 25
#define DT_P1 100
#define DT_P2 130
#define SLEEP_AG1 12
#define SLEEP_G 6
#define STSLP 2
#define STWIN 4
#define H2_AT 9
#define CODE_B 28400

#define ROLE_END() do { STAMP_END(); asm volatile("s_endpgm" ::: "memory"); } while (0)
__device__ __forceinline__ u32 now32() { return (u32)__builtin_amdgcn_s_memrealtime(); }
__device__ __forceinline__ void pace(u32 t) {
#pragma unroll 1
  for (int i = 0; i < 1024 && (int)(now32() - t) < 0; ++i) __builtin_amdgcn_s_sleep(1);
}
__device__ __forceinline__ void pace2(u32 n, u32 t, int dmax) {
  int d = (int)(t - n);
  d = d < DT_MIN ? DT_MIN : (d > dmax ? dmax : d);
  pace(n + (u32)d);
}

#define ALL_OTHER "vcc", "scc", "memory", \
    "s0","s1","s2","s5","s6","s7","s33","s34","s35","s44","s45","s46","s47","s48","s49","s50","s51","s52","s53","s54","s55","s56","s57","s58","s59", \
    "s60","s61","s62","s63","s64","s65","s66","s67","s68","s69","s70","s71","s72","s73","s74","s75","s76","s77","s78","s79","s80","s81","s82","s83","s84", \
    "s85","s86","s87","s88","s89","s90","s91","s92","s93","s94","s95","s96","s97","s98","s99", \
    "v0","v1","v2","v3","v4","v5","v6","v7","v8","v9","v10","v11","v12","v13","v14","v15","v16","v17","v18","v19","v20","v21","v22","v23","v24","v25", \
    "v26","v27","v28","v29","v30","v31","v32","v33","v34","v35","v36","v37","v38","v39","v40","v41","v42","v43","v44","v45","v46","v47","v48","v49","v50", \
    "v51","v52","v53","v54","v55","v56","v57","v58","v59","v60","v61","v62","v63","v64","v65","v66","v67","v68","v69","v70","v71","v72","v73","v74","v75", \
    "v76","v77","v78","v79","v80","v81","v82","v83","v84","v85","v86","v87","v88","v89","v90","v91","v92","v93","v94","v95","v96","v97","v98","v99", \
    "v100","v101","v102","v103","v104","v105","v106","v107","v108","v109","v110","v111","v112","v113","v114","v115","v116","v117","v118","v119","v120", \
    "v121","v122","v123","v124","v125","v126","v127","v128","v129","v130","v131","v132","v133","v134","v135","v136","v137","v138","v139","v140","v141", \
    "v142","v143","v144","v145","v146","v147","v148","v149","v150","v151","v152","v153","v154","v155","v156","v157","v158","v159","v160","v161","v162", \
    "v163","v164","v165","v166","v167"
typedef __attribute__((address_space(1))) const u16* gp16;
typedef __attribute__((address_space(1))) const u32* gp32;
typedef __attribute__((address_space(1))) const u4v* gp128;
typedef unsigned long long u64;
typedef u64 q2 __attribute__((ext_vector_type(2)));
typedef u64 q4 __attribute__((ext_vector_type(4)));
typedef u64 q8 __attribute__((ext_vector_type(8)));

__global__ void __launch_bounds__(768, 3) k_main(
    const u16* x_, const u16* w_r_, const u16* gate_w_, const u16* logA_, const u16* Aim_, const u16* Cre_, const u16* Cim_,
    const u16* log_dt_, const u16* Dskip_, const u16* w_gate_, const u16* w_up_, const u16* w_down_, char* wsp_, u16* out_) {
  __shared__ __attribute__((aligned(256))) char smem[K_LDS];
  const u32 lbase = (u32)(uintptr_t)(__attribute__((address_space(3))) char*)smem;
#define XDMA(OFF) "global_load_lds_dwordx4 v1, s[8:9] offset:" #OFF " nt\n\ts_addk_i32 m0, 896\n\t"
  asm volatile("s_load_dwordx16 s[8:23], %0, 0\n\tv_readfirstlane_b32 s3, %2\n\ts_load_dwordx8 s[24:31], %0, 64\n\ts_load_dwordx4 s[36:39], %0, 96\n\t"
               "s_memrealtime s[40:41]\n\t" STAMP_T0 "s_mov_b32 s4, %1\n\ts_lshr_b32 s3, s3, 6\n\t"
               "s_cmp_ge_u32 s3, 8\n\ts_cbranch_scc1 .Lrole_g\n\ts_cmp_eq_u32 s3, 7\n\ts_cbranch_scc1 .Lrole_d\n\t"
               "s_cmp_ge_u32 s3, 5\n\ts_cbranch_scc1 .Lrole_h\n\ts_cmp_eq_u32 s3, 4\n\ts_cbranch_scc1 .Lrole_c\n\t"
               "s_lshr_b32 s5, s4, 3\n\tv_lshlrev_b32 v3, 2, %2\n\ts_mov_b32 m0, %4\n\tv_lshl_or_b32 v3, s5, 10, v3\n\ts_getpc_b64 s[6:7]\n\t"
               "v_min_u32 v3, %3, v3\n\tv_lshrrev_b32 v1, 3, %2\n\tglobal_load_lds_dword v3, s[6:7]\n\t"
               "v_and_b32 v2, 6, v1\n\ts_and_b32 s34, s4, 7\n\tv_xor_b32 v2, v2, %2\n\ts_lshl_b32 s34, s34, 5\n\tv_and_b32 v2, 7, v2\n\ts_add_u32 s5, s5, s34\n\t"
               "v_and_b32 v1, 7, v1\n\ts_lshl_b32 s5, s5, 5\n\tv_lshlrev_b32 v1, 11, v1\n\ts_add_u32 s5, s5, s3\n\tv_lshl_or_b32 v1, v2, 4, v1\n\t"
               "s_mul_i32 s34, s3, %5\n\tv_lshl_add_u32 v1, s5, 9, v1\n\ts_add_u32 m0, s34, %6\n\tv_and_b32 v2, 31, %2\n\ts_waitcnt lgkmcnt(0)\n\t"
               XDMA(0) "s_lshl_b32 s5, s3, 5\n\t" XDMA(128) "v_add_lshl_u32 v2, s5, v2, 4\n\t" XDMA(256) "s_lshl_b32 s5, s3, 10\n\t"
               "global_load_lds_dwordx4 v1, s[8:9] offset:384 nt\n\ts_add_u32 m0, s5, %7\n\ts_mov_b32 exec_hi, 0\n\t"
               "global_load_lds_dwordx4 v2, s[10:11]\n\ts_addk_i32 m0, 512\n\ts_nop 0\n\tglobal_load_lds_dwordx4 v2, s[12:13]\n\ts_mov_b32 exec_hi, -1"
               :: "s"(__builtin_amdgcn_kernarg_segment_ptr()), "s"(blockIdx.x), "v"(threadIdx.x), "n"(CODE_B - 256), "n"(O_JUNK), "n"(CHB), "n"(O_U + 512), "n"(O_FAR)
               : "s3", "s4", "s5", "s6", "s7", "s8", "s9", "s10", "s11", "s12", "s13", "s14", "s15", "s16", "s17", "s18", "s19", "s20", "s21", "s22", "s23",
                 "s24", "s25", "s26", "s27", "s28", "s29", "s30", "s31", "s34", "s36", "s37", "s38", "s39", "s40", "s41", "s42", "s43", "v1", "v2", "v3", "scc", "memory");
  int lane, c16, g, w, b, xg, xi, cg, cc, c, d;
  char* ch_;
  q8 ka; q4 kb; q2 kc, kt;
  u32* ctl = (u32*)(smem + O_CTL);
  u32* par = (u32*)(smem + O_PAR);
#define IDS() do { int z_ = 0; OPQ(z_); lane = __builtin_amdgcn_mbcnt_hi(~0u, __builtin_amdgcn_mbcnt_lo(~0u, z_)); c16 = lane & 15; g = lane >> 4; } while (0)
#define ROLE(name) \
  asm volatile(".Lrole_" name ":\n\ts_waitcnt lgkmcnt(0)" : "={s[8:23]}"(ka), "={s[24:31]}"(kb), "={s[36:39]}"(kc), "={s[40:43]}"(kt), "={s3}"(w), "={s4}"(b) :: ALL_OTHER); \
  if (__builtin_expect(w > 11, 0)) asm volatile("s_endpgm" ::: "memory");        \
  IDS(); \
  xg = b & 7; xi = b >> 3;                          \
  cg = xg * 32 + xi;                                \
  cc = (xi >> 4) * 128 + xg * 16 + (xi & 15);       \
  c = w & 3; ch_ = smem + c * CHB; d = cc * 4 + c; \
  const u32 ts0 = (u32)kt[0]; \
  auto wrs = __builtin_amdgcn_make_buffer_rsrc((void*)kc[0], 0, WS_END, 0x00020000); \
  STAMP0()

  const char* tapb;
  const char* ub;
  char* erp;
  const char* eup;
  const char* egp;
  u4v At[17], band[4];
  float dm1;
  f4 acc[8];
  auto conv_setup = [&]() AI {
    tapb = ch_ + O_TAP + ((15 - c16) & 3) * TAP_PITCH + ((15 - c16) & ~3) * 2;
    ub = ch_ + O_U - 32 + c16 * 32 + g * 16;
    erp = ch_ + O_OUT + g * 144 + c16 * 8;
    eup = ch_ + O_U + 512 + c16 * 32 + g * 8;
    egp = smem + O_GT + lane * 16;
  };
  auto band_setup = [&]() AI {
#pragma unroll
    for (int k = 0; k < 4; ++k) {
      const int ctr = k == 0 ? 12 : 8, hw = k + 1;
      const u32 m = ((1u << (2 * hw + 1)) - 1u) << (ctr - hw);
      const u32 bits = (m << c16) >> (8 * g);
#pragma unroll
      for (int j = 0; j < 4; ++j)
        band[k][j] = (__builtin_amdgcn_sbfe(bits, 2 * j, 1) & 0x3f80u) | (__builtin_amdgcn_sbfe(bits, 2 * j + 1, 1) & 0x3f800000u);
      OPQ(band[k]);
    }
  };
  auto frag2 = [&](const char* pa, const char* pb) AI { const u2v lo = lds8(pa), hi = lds8(pb); return u4v{lo[0], lo[1], hi[0], hi[1]}; };
  auto atload = [&](int s0, int s1) AI {
#pragma unroll
    for (int s = s0; s < s1; ++s) At[s] = frag2(tapb + 64 * s + g * 16, tapb + 64 * s + g * 16 + 8);
  };
  auto bin = [&](int pair, int be) AI { const char* p = tapb + g * 8 + (SEG_I + 96 * pair - 16 * be) * 2; return frag2(p, p + 96); };
  auto bout = [&](int be) AI { const char* p = tapb + g * 8 + (SEG_O0 - 16 * be) * 2; return frag2(p, p + (SEG_O1 - SEG_O0) * 2); };
  auto asp = [&]() AI { return frag2(tapb + SEG_SP * 2 + g * 16, tapb + SEG_SP * 2 + g * 16 + 8); };
  auto mma = [&](u4v a, u4v bq, f4 cin) AI { return __builtin_amdgcn_mfma_f32_16x16x32_bf16(as_b8(a), as_b8(bq), cin, 0, 0, 0); };
  auto uld = [&](int sg) AI { return *(const u4v*)(ub + 512 + sg * 64); };
  auto uld24 = [&](int un) AI { return *(const u4v*)(ub + 512 + un * 512 - 16); };
  u2v euw[8], eo[8]; f4 egv[8], efv[8];
  float eu[8][4], ev[8][4], ea[8][4];
  u4v Fq[57], ru[6], wa_[6][3];
  f4 wq[6][6];
  u32 pk[12], one, qa, fa;
  char* tfp; char* fp_;
#define LOPQ(p_) do { u32 a_ = (u32)(uintptr_t)(__attribute__((address_space(3))) const char*)p_; OPQ(a_); p_ = (decltype(p_))(__attribute__((address_space(3))) char*)(uintptr_t)a_; } while (0)
  auto m_setup = [&]() AI {
    tfp = smem + (lane == 0 ? O_CTL + c * 4 : O_JUNK + lane * 4);
    fp_ = smem + O_FAR + c * 4096 + lane * 16;
    qa = CTLA(C_Q) + (lane & 3) * 4; fa = CTLA(C_FAR + c); one = 1u;
    OPQ(qa); OPQ(fa); OPQ(one);
    LOPQ(tfp); LOPQ(fp_); LOPQ(erp); LOPQ(eup); LOPQ(egp); LOPQ(ub);
  };
#define WAITA(addr) do { u32 t_; int n_ = 1 << 20; asm volatile( \
    "1:\n\tds_read_b32 %0, %2\n\ts_sub_u32 %1, %1, 1\n\ts_cbranch_scc1 2f\n\ts_waitcnt lgkmcnt(0)\n\tv_cmp_eq_u32 vcc, 0, %0\n\ts_cbranch_vccnz 1b\n2:\n\ts_waitcnt lgkmcnt(0)" \
    : "=&v"(t_), "+s"(n_) : "v"(addr) : "vcc", "scc", "memory"); } while (0)
#define MF(d_, a_, b_) asm volatile("v_mfma_f32_16x16x32_bf16 %0, %1, %2, %0" : "+v"(d_) : "v"(a_), "v"(b_)); SB()
#define MF0(d_, a_, b_) asm volatile("v_mfma_f32_16x16x32_bf16 %0, %1, %2, 0" : "=&v"(d_) : "v"(a_), "v"(b_)); SB()
#define NOP(n_) asm volatile("s_nop " #n_); SB()
#define FL(i_, sg_) Fq[i_] = uld(sg_); SB()
#define RL(un_) ru[un_] = uld24(un_); SB()
#define AL(s_) atload(s_, s_ + 1); SB()
#define ULD(t_) euw[t_] = *(const u2v*)(eup + t_ * 512); SB()
#define GLD(t_) egv[t_] = *(const f4*)(egp + t_ * 1024); SB()
#define FLD(t_) efv[t_] = *(const f4*)(fp_ + (t_ - 2) * 1024); SB()
#define FST(un_) *(f4*)(fp_ + un_ * 1024) = acc[un_ + 2]; SB()
#define EUN(t_, e_) eu[t_][e_] = (e_ & 1) ? bfhi(euw[t_][e_ >> 1]) : bflo(euw[t_][e_ >> 1]); OPQ(eu[t_][e_]); ev[t_][e_] = eu[t_][e_]; SB()
#define EA1(t_, e_) ea[t_][e_] = fmaf(eu[t_][e_], dm1, acc[t_][e_]); OPQ(ea[t_][e_]); SB()
#define EFA(t_, e_) ev[t_][e_] = fmaf(egv[t_][e_], efv[t_][e_], eu[t_][e_]); OPQ(ev[t_][e_]); SB()
#define EA2(t_, e_) ea[t_][e_] = fmaf(egv[t_][e_], ea[t_][e_], ev[t_][e_]); OPQ(ea[t_][e_]); SB()
#define EPK(t_, k_) { u32 v_ = pk_bf(ea[t_][2 * k_], ea[t_][2 * k_ + 1]); OPQ(v_); eo[t_][k_] = v_; } SB()
#define OST(t_) *(u2v*)(erp + t_ * OT) = eo[t_]; SB()
#define WPK(un_, p_, j_) { u32 v_ = pk_bf(wq[un_][2 * p_ + j_ / 2][2 * (j_ & 1)], wq[un_][2 * p_ + j_ / 2][2 * (j_ & 1) + 1]); OPQ(v_); wa_[un_][p_][j_] = v_; } SB()
#define FLAG(w_) asm volatile("" ::: "memory"); *(u32*)(tfp + (w_) * 4) = one; SB()
#define PEEK(i_, a_) pk[i_] = __hip_atomic_load((__attribute__((address_space(3))) u32*)(uintptr_t)(a_), __ATOMIC_RELAXED, __HIP_MEMORY_SCOPE_WORKGROUP); SB()
#define CHECK(i_, a_) if (__builtin_expect(__builtin_amdgcn_ballot_w64(pk[i_] == 0u) != 0ull, 0)) WAITA(a_); asm volatile("" ::: "memory"); SB()
#define MIX2(nm, nd, nv) _Pragma("unroll") for (int r_ = 0; r_ < (nm); ++r_) { __builtin_amdgcn_sched_group_barrier(0x008, 1, 0); \
    if (nd) __builtin_amdgcn_sched_group_barrier(0x100, (nd), 0); if (nv) __builtin_amdgcn_sched_group_barrier(0x002, (nv), 0); }

  {
    ROLE("x");
    const int xw = w;
    char* img = ch_ + O_U + 512;
    STAMP(11);
    __builtin_amdgcn_s_setprio(2);
    if (xi == 31 && w == 0) { const u32 th = ts0; if (lane == 0) __builtin_amdgcn_raw_buffer_store_b64(u2v{th, ~th}, wrs, WS_HEAD + xg * 8, 0, 16); }
    const char* wp; const char* fp; const char* tp0; const char* tp1; char* pp;
    {
      const int r = lane & 7, q = (lane >> 2) & 3;
      fp = img + r * 128 + ((((lane & 8) / 2 + g) ^ (r & 6)) * 16);
      wp = smem + O_FAR + xw * 1024 + (lane & 1) * 512 + (lane & 4) * 16 + g * 16;
      tp0 = img + q * 128 + (((2 * g + ((lane >> 1) & 1)) ^ (q & 2)) * 16) + (lane & 1) * 8;
      tp1 = img + 512 + q * 128 + (((2 * g + ((lane >> 1) & 1)) ^ ((q & 2) + 4)) * 16) + (lane & 1) * 8;
      pp = smem + ((lane >> 3) == 0 || (lane >> 3) == 3 ? O_PART + r * 64 + xw * 16 + g * 8 : O_JUNK + lane * 8);
    }
    const int so = WS_XT + (32 * xw + (lane >> 3)) * XT_PITCH + cg * 128 + (lane & 7) * 16, sstep = 8 * XT_PITCH;
    u32 lo_ = WS_XT + (cc >> 1) * XT_PITCH + (16 * xw + (lane >> 2)) * 128 + (cc & 1) * 64 + (lane & 3) * 16;
    OPQ(lo_);
    SB();
    BAR();
    STAMP(12);
    asm volatile("s_waitcnt vmcnt(0)" ::: "memory");
    STAMP(1);
    {
      u4v wa[4], fb[4]; u2v ta[4], tb[4];
#pragma unroll
      for (int s = 0; s < 4; ++s) { wa[s] = *(const u4v*)(wp + s * 128); fb[s] = *(const u4v*)(fp + s * 1024); }
      SB();
      f4 r = {0.f, 0.f, 0.f, 0.f};
#pragma unroll
      for (int s = 0; s < 4; ++s) r = mma(wa[s], fb[s], r);
      *(f2*)pp = f2{r[0], r[1]};
      ARRIVE(C_PART);
      SB();
#pragma unroll
      for (int s = 0; s < 4; ++s) { ta[s] = tr_read(tp0 + s * 1024); tb[s] = tr_read(tp1 + s * 1024); }
      SB();
      WAITQ(C_GP, 1);
#pragma unroll
      for (int s = 0; s < 4; ++s) __builtin_amdgcn_raw_buffer_store_b128(u4v{ta[s][0], ta[s][1], tb[s][0], tb[s][1]}, wrs, so + s * sstep, 0, 16);
    }
    STAMP(2);
    const u32 dskw = ((gp16)kb[0])[d];
    conv_setup();
    band_setup();
    const u32 tag = par[0];
    ARRIVE(C_PUB);
    asm volatile("s_waitcnt vmcnt(0)" ::: "memory");
    if (lane == 0) __builtin_amdgcn_raw_buffer_store_b32(tag, wrs, WS_FLG + xw * 1024 + cg * 4, 0, 16);
    STAMP(3);
    dm1 = bflo(dskw) - 1.f;
    WAITS(C_TL, 1);
    const u32 tl0 = (u32)__builtin_amdgcn_readfirstlane((int)par[1]);
    u3v fq[2];
    auto rec_ld = [&]() AI {
#pragma unroll
      for (int k = 0; k < 2; ++k) fq[k] = __builtin_amdgcn_raw_buffer_load_b96(wrs, WS_REC + xw * 2048 + k * 1024 + lane * 16, 0, 16);
    };
    pace(tl0 + DT_REC);
    rec_ld();
    SB();
    {
#pragma unroll 1
      for (int spin = 0; spin < SPIN_MAX && __builtin_expect(__any(fq[0][2] != tag || fq[1][2] != tag), 0); ++spin) {
        __builtin_amdgcn_s_sleep(SLEEP_G);
        rec_ld();
        asm volatile("" ::: "memory");
      }
      char* gp = smem + O_GT + xw * 2048 + (lane & 3) * 256 + (lane >> 2) * 16;
#pragma unroll
      for (int k = 0; k < 2; ++k) *(f4*)(gp + k * 1024) = f4{bflo(fq[k][0]), bfhi(fq[k][0]), bflo(fq[k][1]), bfhi(fq[k][1])};
    }
    pace(tl0 + DT_FLG);
    u4v xq[4];
    bool late[4];
    {
      const u32 fl = __builtin_amdgcn_raw_buffer_load_b32(wrs, WS_FLG + (cc >> 6) * 1024 + xw * 64 + g * 256 + c16 * 4, 0, 16);
      const u64 bad = __builtin_amdgcn_ballot_w64(fl != tag);
      asm volatile("" ::: "memory");
#pragma unroll
      for (int k = 0; k < 4; ++k) {
        late[k] = ((bad >> (16 * k)) & 0xffffull) != 0ull;
        xq[k] = __builtin_amdgcn_raw_buffer_load_b128(wrs, lo_ | (late[k] ? 1u << 30 : 0u), k * 8192, 16);
        SB();
      }
    }
    ARRIVE(C_XL);
    STAMP(4);
    WAITQ(C_TAPS + c, 1);
    u4v Bi[2][3], Bo[4], As;
#pragma unroll
    for (int be = 0; be < 3; ++be) { Bi[0][be] = bin(0, be); Bi[1][be] = bin(1, be); }
#pragma unroll
    for (int be = 0; be < 4; ++be) Bo[be] = bout(be);
    As = asp();
    WAITQ(C_PUB, 4);
    char* dp = smem + (lane & 3) * CHB + O_U + 512 + xw * 256 + (lane >> 2) * 16;
    m_setup();
    auto stage = [&](int k) AI {
      SB();
      if (__builtin_expect(late[k], 0)) {
#pragma unroll 1
        for (int spin = 0; spin < SPIN_MAX; ++spin) {
          STAMP(14);
          const u32 f1 = __builtin_amdgcn_raw_buffer_load_b32(wrs, WS_FLG + (cc >> 6) * 1024 + xw * 64 + k * 256 + c16 * 4, 0, 16);
          if (!__any(f1 != tag)) break;
          __builtin_amdgcn_s_sleep(SLEEP_G);
          asm volatile("" ::: "memory");
        }
        asm volatile("" ::: "memory");
        xq[k] = __builtin_amdgcn_raw_buffer_load_b128(wrs, lo_, k * 8192, 16);
        asm volatile("s_waitcnt vmcnt(0)" : "+v"(xq[k]) :: "memory");
      }
      *(u4v*)(dp + k * 1024) = xq[k];
      FLAG(C_Q + k * 4);
    };
    stage(0);
    stage(1);
    STAMP(5);
    __builtin_amdgcn_s_setprio(0);
    WAITA(qa);
    SB();
    FL(0, 0); RL(0); FL(8, 8); RL(1);
    MF0(wq[0][0], Fq[0], band[0]);
    MF0(wq[0][1], Fq[0], band[1]);
    MF0(wq[0][2], ru[0], band[0]);
    MF0(wq[0][3], ru[0], band[1]);
    MF0(wq[0][4], Fq[0], band[2]);
    MF0(wq[0][5], ru[0], band[3]);
    MF0(acc[4], As, Fq[0]); PEEK(6, qa + 16);
    MF0(wq[1][0], Fq[8], band[0]); WPK(0, 0, 0); WPK(0, 0, 1);
    MF0(wq[1][1], Fq[8], band[1]); WPK(0, 0, 2); WPK(0, 0, 3);
    MF0(wq[1][2], ru[1], band[0]); WPK(0, 1, 0); WPK(0, 1, 1);
    MF0(wq[1][3], ru[1], band[1]); WPK(0, 1, 2); WPK(0, 1, 3);
    MF0(wq[1][4], Fq[8], band[2]); CHECK(6, qa + 16);
    MF0(wq[1][5], ru[1], band[3]); WPK(0, 2, 0); WPK(0, 2, 1);
    MF0(acc[5], As, Fq[8]); WPK(0, 2, 2); WPK(0, 2, 3);
    MF0(acc[2], wa_[0][0], Bi[0][0]); FL(16, 16); RL(2);
    MF(acc[2], wa_[0][1], Bi[1][0]); WPK(1, 0, 0); WPK(1, 0, 1);
    MF0(acc[3], wa_[0][0], Bi[0][1]); WPK(1, 0, 2); WPK(1, 0, 3);
    MF(acc[3], wa_[0][1], Bi[1][1]); WPK(1, 1, 0); WPK(1, 1, 1);
    MF(acc[4], wa_[0][0], Bi[0][2]); FST(0); FLAG(C_FAR + 0 * 4);
    MF(acc[4], wa_[0][1], Bi[1][2]); WPK(1, 1, 2); WPK(1, 1, 3);
    MF(acc[4], wa_[0][2], Bo[0]); WPK(1, 2, 0); WPK(1, 2, 1);
    MF(acc[5], wa_[0][2], Bo[1]); WPK(1, 2, 2); WPK(1, 2, 3);
    MF0(acc[6], wa_[0][2], Bo[2]);
    MF0(acc[7], wa_[0][2], Bo[3]); stage(2);
    MF0(wq[2][0], Fq[16], band[0]);
    MF0(wq[2][1], Fq[16], band[1]); stage(3);
    MF0(wq[2][2], ru[2], band[0]);
    MF0(wq[2][3], ru[2], band[1]);
    MF0(wq[2][4], Fq[16], band[2]); FL(24, 24); RL(3);
    MF0(wq[2][5], ru[2], band[3]); WPK(2, 0, 0); WPK(2, 0, 1);
    MF(acc[6], As, Fq[16]); WPK(2, 0, 2); WPK(2, 0, 3);
    MF(acc[3], wa_[1][0], Bi[0][0]); WPK(2, 1, 0); WPK(2, 1, 1);
    MF(acc[3], wa_[1][1], Bi[1][0]); WPK(2, 1, 2); WPK(2, 1, 3);
    MF(acc[4], wa_[1][0], Bi[0][1]); WPK(2, 2, 0); WPK(2, 2, 1);
    MF(acc[4], wa_[1][1], Bi[1][1]); WPK(2, 2, 2); WPK(2, 2, 3);
    MF(acc[5], wa_[1][0], Bi[0][2]); FST(1); FLAG(C_FAR + 1 * 4);
    MF(acc[5], wa_[1][1], Bi[1][2]); PEEK(7, qa + 32);
    MF(acc[5], wa_[1][2], Bo[0]);
    MF(acc[6], wa_[1][2], Bo[1]);
    MF(acc[7], wa_[1][2], Bo[2]);
    MF0(wq[3][0], Fq[24], band[0]);
    MF0(wq[3][1], Fq[24], band[1]); CHECK(7, qa + 32);
    MF0(wq[3][2], ru[3], band[0]);
    MF0(wq[3][3], ru[3], band[1]);
    MF0(wq[3][4], Fq[24], band[2]); FL(32, 32); RL(4);
    MF0(wq[3][5], ru[3], band[3]); WPK(3, 0, 0); WPK(3, 0, 1);
    MF(acc[7], As, Fq[24]); WPK(3, 0, 2); WPK(3, 0, 3);
    MF(acc[4], wa_[2][0], Bi[0][0]); WPK(3, 1, 0); WPK(3, 1, 1);
    MF(acc[4], wa_[2][1], Bi[1][0]); WPK(3, 1, 2); WPK(3, 1, 3);
    MF(acc[5], wa_[2][0], Bi[0][1]); WPK(3, 2, 0); WPK(3, 2, 1);
    MF(acc[5], wa_[2][1], Bi[1][1]); FL(40, 40); RL(5);
    MF(acc[6], wa_[2][0], Bi[0][2]); FST(2); FLAG(C_FAR + 2 * 4);
    MF(acc[6], wa_[2][1], Bi[1][2]); WPK(3, 2, 2); WPK(3, 2, 3);
    MF(acc[6], wa_[2][2], Bo[0]); AL(0);
    MF(acc[7], wa_[2][2], Bo[1]); AL(1);
    MF0(wq[4][0], Fq[32], band[0]); AL(2);
    MF0(wq[4][1], Fq[32], band[1]); AL(3);
    MF0(wq[4][2], ru[4], band[0]); AL(4);
    MF0(wq[4][3], ru[4], band[1]); WPK(4, 0, 0); WPK(4, 0, 1);
    MF(acc[5], wa_[3][0], Bi[0][0]); WPK(4, 0, 2); WPK(4, 0, 3);
    MF(acc[5], wa_[3][1], Bi[1][0]); WPK(4, 1, 0); WPK(4, 1, 1);
    MF(acc[6], wa_[3][0], Bi[0][1]); WPK(4, 1, 2); WPK(4, 1, 3);
    MF(acc[6], wa_[3][1], Bi[1][1]); AL(5);
    MF(acc[7], wa_[3][0], Bi[0][2]); FST(3); FLAG(C_FAR + 3 * 4);
    MF(acc[7], wa_[3][1], Bi[1][2]); AL(6);
    MF(acc[7], wa_[3][2], Bo[0]); FL(33, 33); AL(7);
    MF0(wq[5][0], Fq[40], band[0]); FL(34, 34); AL(8);
    MF0(wq[5][1], Fq[40], band[1]); FL(35, 35); AL(9);
    MF0(wq[5][2], ru[5], band[0]); WPK(5, 0, 0); WPK(5, 0, 1);
    MF0(wq[5][3], ru[5], band[1]); AL(10);
    MF(acc[6], wa_[4][0], Bi[0][0]); PEEK(8, qa + 48); FL(36, 36);
    MF(acc[6], wa_[4][1], Bi[1][0]); FL(37, 37); WPK(5, 0, 2);
    MF(acc[7], wa_[4][0], Bi[0][1]); FL(38, 38); WPK(5, 0, 3);
    MF(acc[7], wa_[4][1], Bi[1][1]); FL(39, 39); WPK(5, 1, 0);
    MF(acc[6], At[0], Fq[32]); WPK(5, 1, 1); WPK(5, 1, 2);
    MF(acc[6], At[1], Fq[33]); CHECK(8, qa + 48);
    MF(acc[6], At[2], Fq[34]); WPK(5, 1, 3); FL(41, 41);
    MF(acc[6], At[3], Fq[35]); AL(11);
    MF(acc[7], wa_[5][0], Bi[0][0]); FL(42, 42); AL(12);
    MF(acc[7], wa_[5][1], Bi[1][0]); AL(13);
    MF(acc[6], At[4], Fq[36]); FL(43, 43); AL(14);
    MF(acc[6], At[5], Fq[37]); AL(15);
    MF(acc[6], At[6], Fq[38]); FL(44, 44); AL(16);
    MF(acc[6], At[7], Fq[39]);
    MF(acc[6], At[8], Fq[40]); FL(45, 45);
    MF(acc[7], At[0], Fq[40]);
    MF(acc[6], At[9], Fq[41]); FL(46, 46);
    MF(acc[7], At[1], Fq[41]);
    MF(acc[6], At[10], Fq[42]); FL(47, 47);
    MF(acc[7], At[2], Fq[42]);
    MF(acc[6], At[11], Fq[43]); FL(48, 48);
    MF(acc[7], At[3], Fq[43]);
    MF(acc[6], At[12], Fq[44]); FL(49, 49); ULD(6);
    MF(acc[7], At[4], Fq[44]); FL(50, 50); GLD(6);
    MF(acc[6], At[13], Fq[45]); FL(51, 51);
    MF(acc[7], At[5], Fq[45]); FL(52, 52);
    MF(acc[6], At[14], Fq[46]); FL(53, 53);
    MF(acc[7], At[6], Fq[46]); FL(54, 54); EUN(6, 0);
    MF(acc[6], At[15], Fq[47]); FL(55, 55); EUN(6, 1);
    MF(acc[7], At[7], Fq[47]); FL(56, 56); EUN(6, 2);
    MF(acc[6], At[16], Fq[48]); EUN(6, 3);
    MF(acc[7], At[8], Fq[48]); ULD(7); GLD(7);
    MF(acc[7], At[9], Fq[49]);
    MF(acc[7], At[10], Fq[50]);
    MF(acc[7], At[11], Fq[51]);
    MF(acc[7], At[12], Fq[52]); EA1(6, 0); EA1(6, 1); EA1(6, 2); EA1(6, 3);
    MF(acc[7], At[13], Fq[53]); EA2(6, 0); EA2(6, 1); EA2(6, 2); EA2(6, 3);
    MF(acc[7], At[14], Fq[54]); EPK(6, 0); EPK(6, 1);
    MF(acc[7], At[15], Fq[55]); OST(6); FLAG(C_TILE + 6 * 4);
    MF(acc[7], At[16], Fq[56]); EUN(7, 0); EUN(7, 1); EUN(7, 2); EUN(7, 3); NOP(3); EA1(7, 0); EA1(7, 1); EA1(7, 2); EA1(7, 3); EA2(7, 0); EA2(7, 1); EA2(7, 2); EA2(7, 3); EPK(7, 0); EPK(7, 1); OST(7); FLAG(C_TILE + 7 * 4);
    STAMP(9);
    ROLE_END();
  }
  {
    ROLE("h");
    const int hw = w - 5;
    const int j = xg * 28 + xi;
    const int s1 = j >> 2, kq = j & 3;
    const bool own = xi < 28;
    const u64 wbase = hw == 0 ? kb[1] : kb[2];
    const int pitch = F * 2;
    auto hrs = __builtin_amdgcn_make_buffer_rsrc((void*)wbase, 0, own ? D * F * 2 : 0, 0x00020000);
    const int r8 = lane >> 3;
    const int vo = (256 * kq * F + 64 * s1) * 2 + (8 * (r8 >> 2) + (r8 & 3)) * pitch + (((lane & 7) * 16) ^ (((r8 >> 1) & 3) * 32));
    auto dma = [&](int h, int sB, int pt, u32 dst) AI {
      const int m = h >> 1;
#pragma unroll
      for (int i = 0; i < 2; ++i)
        asm volatile("s_mov_b32 m0, %3\n\ts_nop 0\n\tbuffer_load_dwordx4 %0, %1, %2 offen nt lds"
                     :: "v"(vo), "s"(hrs), "s"((m & 1) * 32 * pt + (m >> 1) * sB + (16 * i + 4 * (h & 1)) * pt), "s"(dst + i * 1024) : "memory");
    };
    const int q = c16 >> 2, p = c16 & 3;
    int tro[4];
#pragma unroll
    for (int ct = 0; ct < 4; ++ct) tro[ct] = (4 * g + q) * 128 + ((ct ^ (((4 * g + q) >> 1) & 3)) * 32) + p * 8;
    const int tok = lane & 7;
    u4v wd[2][8];
    auto wd_ld = [&](int k, int cb) AI {
#pragma unroll
      for (int i = 0; i < 8; ++i) {
        const char* pp = smem + O_WD + cb * 8192 + (i >> 2) * 4096 + tro[i & 3];
        const u2v lo = tr_read(pp), hi = tr_read(pp + 2048);
        wd[k][i] = u4v{lo[0], lo[1], hi[0], hi[1]};
      }
    };
    u32 p2o, p2k[4], hlo, hhi;
    auto f2_setup = [&](int cb0) AI {
      p2o = WS_P2 + ((64 * kq + 16 * cb0 + (lane & 8) / 2 + g) * 56 + s1) * 128 + tok * 16;
      hlo = lbase + O_HB + ((lane & 8) ? 512 : g * 128 + tok * 16);
      hhi = lbase + O_HB + ((lane & 8) ? g * 128 + tok * 16 : 512);
#pragma unroll
      for (int i = 0; i < (cb0 == 2 ? 4 : 2); ++i) { p2k[i] = p2o + i * 8 * 7168; OPQ(p2k[i]); }
      OPQ(hlo); OPQ(hhi);
    };
    auto f2_job = [&](int nb, u32 t10) AI {
      u4v hb[4];
      {
        u32 cnt; int n_ = 1 << 20;
        asm volatile("1:\n\tds_read_b32 %4, %6\n\tds_read_b128 %0, %7\n\tds_read_b128 %1, %8\n\tds_read_b128 %2, %7 offset:528\n\tds_read_b128 %3, %8 offset:528\n\t"
                     "s_sub_u32 %5, %5, 1\n\ts_cbranch_scc1 2f\n\ts_waitcnt lgkmcnt(0)\n\tv_cmp_gt_u32 vcc, 2, %4\n\ts_cbranch_vccnz 1b\n2:\n\ts_waitcnt lgkmcnt(0)"
                     : "=&v"(hb[0]), "=&v"(hb[1]), "=&v"(hb[2]), "=&v"(hb[3]), "=&v"(cnt), "+s"(n_) : "v"(CTLA(C_HRDY)), "v"(hlo), "v"(hhi) : "vcc", "scc", "memory");
      }
      STAMP(11);
      f4 r[4];
#pragma unroll
      for (int i = 0; i < 2 * nb; ++i) {
        const u4v* wp = &wd[i >> 1][(i & 1) * 2];
        r[i] = mma(wp[0], hb[0], f4{0.f, 0.f, 0.f, 0.f});
        r[i] = mma(wp[1], hb[1], r[i]);
        r[i] = mma(wp[4], hb[2], r[i]);
        r[i] = mma(wp[5], hb[3], r[i]);
      }
      SB();
#pragma unroll
      for (int i = 0; i < 2 * nb; ++i) {
        u4v v = __builtin_bit_cast(u4v, r[i]);
#pragma unroll
        for (int e = 0; e < 4; ++e) v[e] = __builtin_amdgcn_bitop3_b32(v[e], t10, TMASK, 0xd8);
        __builtin_amdgcn_raw_buffer_store_b128(v, wrs, p2k[i], 0, 16);
      }
      STAMP(12);
    };
    const u32 rb = lbase + O_RING + hw * (NSLOT * 2048);
    pace(ts0 + WDELAY);
    STAMP(2);
#pragma unroll
    for (int h = 0; h < WWIN; ++h) dma(h, 64 * F * 2, F * 2, rb + h * 2048);
    __builtin_amdgcn_s_setprio(3);
    BAR();
    STAMP(3);
    if (__builtin_expect(own, 1)) {
      u4v wv[32];
      f4 a1[4] = {f4{0.f, 0.f, 0.f, 0.f}, f4{0.f, 0.f, 0.f, 0.f}, f4{0.f, 0.f, 0.f, 0.f}, f4{0.f, 0.f, 0.f, 0.f}};
      u4v xb[8];
      auto f1 = [&](int k0, int k1) AI {
#pragma unroll
        for (int k = k0; k < k1; ++k)
#pragma unroll
          for (int ct = 0; ct < 4; ++ct) a1[ct] = mma(wv[4 * k + ct], xb[k], a1[ct]);
      };
      u32 xo = lbase + O_XS + tok * XS_PITCH + g * 16;
      u32 p1o = WS_P1 + s1 * 16384 + kq * 4096 + hw * 2048 + (lane & 8) * 64 + g * 128 + tok * 16;
      u32 t10, tpub;
      OPQ(xo); OPQ(p1o);
      constexpr int HX = (16 - WWIN + 1) & ~1;
#pragma unroll
      for (int h = 0; h < 16; ++h) {
        const int left = (h + WWIN < 16 ? WWIN : 16 - h) - 1;
        asm volatile("s_waitcnt vmcnt(%0)" :: "n"(2 * left) : "memory");
        if (h + WWIN < 16) {
          asm volatile("s_waitcnt lgkmcnt(0)" ::: "memory");
          dma(h + WWIN, 64 * F * 2, F * 2, rb + ((h + WWIN) % NSLOT) * 2048);
        }
#pragma unroll
        for (int ct = 0; ct < 4; ++ct) {
          const u2v t = tr_read(smem + O_RING + hw * (NSLOT * 2048) + (h % NSLOT) * 2048 + tro[ct]);
          wv[4 * (h >> 1) + ct][2 * (h & 1)] = t[0]; wv[4 * (h >> 1) + ct][2 * (h & 1) + 1] = t[1];
        }
        if (h == H2_AT) ARRIVE(C_WGU);
        if (h == HX - 1) {
          STAMP(4);
          WAITQ(C_XS, 1);
          STAMP(1);
#pragma unroll
          for (int k = 0; k < 8; ++k) xb[k] = *(const __attribute__((address_space(3))) u4v*)(uintptr_t)(xo + k * 64);
          t10 = par[0]; tpub = par[2];
          SB();
          f1(0, 2);
          SB();
        }
        if (h >= HX) { f1(h - HX + 2, h - HX + 3); SB(); }
      }
      STAMP(5);
      f1(18 - HX, 8);
      t10 &= TMASK;
#pragma unroll
      for (int a = 0; a < 2; ++a) {
        u4v v;
#pragma unroll
        for (int e = 0; e < 4; ++e)
          v[e] = ((u32)__builtin_amdgcn_update_dpp(__float_as_int(a1[2 * a][e]), __float_as_int(a1[2 * a + 1][e]), 0xe4, 0xf, 0xc, false) & ~TMASK) | t10;
        __builtin_amdgcn_raw_buffer_store_b128(v, wrs, p1o + a * 1024, 0, 16);
      }
      STAMP(6);
      const u32 tn = now32();
      u32 voff = WS_P1 + s1 * 16384 + hw * 1024 + lane * 16;
      u32 ho = lbase + O_HB + hw * 528 + g * 128 + tok * 16 + (lane & 8);
      float nl2e = -1.44269504088896341f;
      u32 wda = CTLA(C_WD) + hw * 4;
      OPQ(voff); OPQ(ho); OPQ(nl2e); OPQ(wda);
      f2_setup(hw & 1);
      pace2(tn, (u32)__builtin_amdgcn_readfirstlane((int)tpub) + DE_P1, DT_P1);
      u4v pb[8];
      auto p1_ld = [&]() AI {
#pragma unroll
        for (int i = 0; i < 8; ++i) { pb[i] = __builtin_amdgcn_raw_buffer_load_b128(wrs, voff + (i >> 2) * 2048, (i & 3) * 4096, 16); SB(); }
      };
      p1_ld();
      SB();
      f4 G, U;
      bool early = false;
#pragma unroll 1
      for (int spin = 0; spin < SPIN_MAX; ++spin) {
        u32 bad = 0;
        OPQ(bad);
        asm volatile("" :: "v"(pb[0]));
        const u32 cw = __hip_atomic_load((__attribute__((address_space(3))) u32*)(uintptr_t)wda, __ATOMIC_RELAXED, __HIP_MEMORY_SCOPE_WORKGROUP);
        G = tsum4(bad, t10, pb[0], pb[1], pb[2], pb[3]);
        early = __builtin_amdgcn_ballot_w64(cw == 0u) == 0ull;
        asm volatile("" ::: "memory");
        if (__builtin_expect(early, 1)) wd_ld(0, hw);
        SB();
        silu4(G, nl2e);
        U = tsum4(bad, t10, pb[4], pb[5], pb[6], pb[7]);
        if (__builtin_expect(!__any((bad & TMASK) != 0), 1)) break;
        STAMP(14);
        p1_ld();
        asm volatile("" ::: "memory");
      }
      STAMP(7);
      {
        float hv[4];
#pragma unroll
        for (int e = 0; e < 4; ++e) { hv[e] = G[e] * U[e]; asm("" : "+v"(hv[e])); }
        *(__attribute__((address_space(3))) u2v*)(uintptr_t)ho = u2v{pk_bf(hv[0], hv[1]), pk_bf(hv[2], hv[3])};
      }
      ARRIVE(C_HRDY);
      STAMP(8);
      if (__builtin_expect(!early, 0)) { WAITA(wda); wd_ld(0, hw); }
      f2_job(1, t10);
    }
    if (hw == 0) {
      WAITS(C_SEL, 1);
      const u32 t10 = par[0] & TMASK;
      const u32 tpub = (u32)__builtin_amdgcn_readfirstlane((int)par[2]);
      const u32 mykey = ((const u32*)(smem + O_SEL))[lane & 7];
      if (own) pace2(now32(), tpub + DE_P2, DT_P2); else pace(tpub + DE_P2 - 40);
      u4v pa[7];
      const int voff = WS_P2 + cc * 7168 + lane * 16;
#pragma unroll
      for (int k = 0; k < 7; ++k) pa[k] = __builtin_amdgcn_raw_buffer_load_b128(wrs, voff + (k & 3) * 1024, (k >> 2) * 4096, 16);
      SB();
      const int row = (int)(~mykey & 2047u);
      const float rsig = rb1(sigm(__uint_as_float((mykey ^ (~(u32)((int)mykey >> 31) | 0x80000000u)) & 0xffff0000u)));
      WAITA(CTLA(C_TILE) + (lane & 31) * 4);
      float cf_ = rsig * (1.f - *(const float*)(smem + O_GT + (row >> 8) * 1024 + ((row >> 2) & 3) * 256 + ((row >> 4) & 15) * 16 + (row & 3) * 4));
      float base[4];
#pragma unroll
      for (int e = 0; e < 4; ++e) base[e] = bflo(*(const u16*)(smem + e * CHB + O_OUT + (row >> 8) * OT + ((row >> 2) & 3) * 144 + ((row >> 4) & 15) * 8 + (row & 3) * 2));
      u64 op = kc[1] + (row * D + cc * 4) * 2;
      { float cq = cf_; OPQ(cq); cf_ = cq; OPQ(op); }
#pragma unroll
      for (int e = 0; e < 4; ++e) OPQ(base[e]);
      STAMP(13);
      f4 y;
#pragma unroll 1
      for (int spin = 0; spin < SPIN_MAX; ++spin) {
        u32 bad = 0;
        OPQ(bad);
        y = tsum4(bad, t10, pa[0], pa[1], pa[2], pa[3]);
        y = tsum3(bad, t10, y, pa[4], pa[5], pa[6]);
        if (__builtin_expect(!__any((bad & TMASK) != 0), 1)) break;
        STAMP(15);
#pragma unroll
        for (int k = 0; k < 7; ++k) pa[k] = __builtin_amdgcn_raw_buffer_load_b128(wrs, voff + (k & 3) * 1024, (k >> 2) * 4096, 16);
        asm volatile("" ::: "memory");
      }
      STAMP(9);
      float o[4];
#pragma unroll
      for (int e = 0; e < 4; ++e) {
        float v = y[e] + dpp_f<0x128>(y[e]);
        auto s16 = __builtin_amdgcn_permlane16_swap(__float_as_uint(v), __float_as_uint(v), false, false);
        v = __uint_as_float(s16[0]) + __uint_as_float(s16[1]);
        auto s32 = __builtin_amdgcn_permlane32_swap(__float_as_uint(v), __float_as_uint(v), false, false);
        o[e] = fmaf(__uint_as_float(s32[0]) + __uint_as_float(s32[1]), cf_, base[e]);
      }
      if (lane < 8) *(__attribute__((address_space(1))) u2v*)op = u2v{pk_bf(o[0], o[1]), pk_bf(o[2], o[3])};
    }
    ROLE_END();
  }
  {
    ROLE("g");
    const u32 pLA = __builtin_nontemporal_load((gp32)ka[3] + d * (NM / 2) + lane);
    const u32 pAI = __builtin_nontemporal_load((gp32)ka[4] + d * (NM / 2) + lane);
    const u32 pCR = __builtin_nontemporal_load((gp32)ka[5] + d * (NM / 2) + lane);
    const u32 pCI = __builtin_nontemporal_load((gp32)ka[6] + d * (NM / 2) + lane);
    const u32 ldt = ((gp16)ka[7])[d], dskw = ((gp16)kb[0])[d];
    u4v ixa[2];
#pragma unroll
    for (int r = 0; r < 2; ++r) ixa[r] = *(const u4v*)(g_tapidx.v + lane * 16 + r * 8);
    BAR();
    asm volatile("s_mov_b32 m0, %0\n\ts_nop 0" :: "s"(lbase + c * CHB) : "memory");
#define PUT1(off, val) asm volatile("ds_write_addtid_b32 %0 offset:%1" :: "v"(val), "n"(off) : "memory")
#define PUTR(off, val) do { const ch v_ = (val); asm volatile("ds_write_addtid_b32 %0 offset:%2\n\tds_write_addtid_b32 %1 offset:%3" :: "v"(v_.r), "v"(v_.i), "n"(off), "n"((off) + 256) : "memory"); } while (0)
    const char* pf = ch_ + c16 * TROW + g * 16;
    PUT1(O_TAP, 0x3c003c00u); PUT1(O_TAP + 256, 0u);
    PUT1(O_U, 0u); PUT1(O_U + 256, 0u);
    SB();
    const float dt = rb1(exp_bf(bflo(ldt)));
    const f2 a2 = f2{-rb1(exp_bf(bflo(pLA))), -rb1(exp_bf(bfhi(pLA)))} * dt * 1.44269504088896341f;
    const f2 br = unbf(pAI) * dt * 0.15915494309189535f;
    STAMP(1);
    u4v Lf[2][8], Wf[8];
    f4 kk[2][2];
    float fk;
    {
      const cf z1 = cexp_p(a2, br, 1.f);
      const cf z2 = csq(z1), z4 = csq(z2), z8 = csq(z4), z16 = csq(z8), z32 = csq(z16), z64 = csq(z32);
      ch base[4], anc[4];
      base[1] = tohc(z8); base[2] = tohc(z16); base[3] = tohc(cmul(z16, z8));
      anc[1] = tohc(z32); anc[2] = tohc(z64); anc[3] = tohc(cmul(z64, z32));
      PUTR(O_OUT + 5 * TROW, tohc(csq(z64)));
#pragma unroll
      for (int j = 1; j < 16; ++j) {
        const int q = j >> 2, r = j & 3;
        PUTR(O_TAP + j * TROW, q == 0 ? base[r] : r == 0 ? anc[q] : hmul(anc[q], base[r]));
      }
#pragma unroll
      for (int s = 0; s < 8; ++s) Lf[0][s] = *(const u4v*)(pf + O_TAP + s * 64);
#pragma unroll
      for (int s = 0; s < 8; ++s) Lf[1][s] = *(const u4v*)(ch_ + O_OUT + 5 * TROW + g * 16 + s * 64);
      SB();
      STAMP(11);
      u32 mx = max(max(pCR << 17, pCR << 1), max(pCI << 17, pCI << 1));
      mx = wave_max_u32(mx);
      int ex = (int)(mx >> 24);
      ex = ex < 1 ? 1 : (ex > 253 ? 253 : ex);
      const float sdn = __uint_as_float((u32)(254 - ex) << 23);
      fk = 2.f * dt * __uint_as_float((u32)ex << 23);
      cf C; C.r = unbf(pCR) * sdn; C.i = unbf(pCI) * sdn;
      const cf E = cexp_p(a2, br, 256.f);
      const ch y1 = toh(z1), y2 = toh(z2), y4 = toh(z4), y6 = toh(cmul(z4, z2));
      {
        const ch R0 = toh(C), R2 = hmul(R0, y2), R4 = hmul(R0, y4), R6 = hmul(R0, y6);
        PUTR(O_TAP, R0); PUTR(O_TAP + 2 * TROW, R2); PUTR(O_TAP + 4 * TROW, R4); PUTR(O_TAP + 6 * TROW, R6);
        PUTR(O_TAP + 1 * TROW, hmul(R0, y1)); PUTR(O_TAP + 3 * TROW, hmul(R2, y1)); PUTR(O_TAP + 5 * TROW, hmul(R4, y1)); PUTR(O_TAP + 7 * TROW, hmul(R6, y1));
        C = cmul(C, E);
        const ch R8 = toh(C);
        PUTR(O_TAP + 8 * TROW, R8); PUTR(O_TAP + 9 * TROW, hmul(R8, y2)); PUTR(O_TAP + 10 * TROW, hmul(R8, y4)); PUTR(O_TAP + 11 * TROW, hmul(R8, y6));
        C = cmul(C, E);
        const ch R12 = toh(C);
        PUTR(O_TAP + 12 * TROW, R12); PUTR(O_TAP + 13 * TROW, hmul(R12, y4));
        C = cmul(C, E);
        const ch R14 = toh(C);
        PUTR(O_TAP + 14 * TROW, R14); PUTR(O_TAP + 15 * TROW, hmul(R14, y4));
      }
#pragma unroll
      for (int s = 0; s < 8; ++s) Wf[s] = *(const u4v*)(pf + O_TAP + s * 64);
      SB();
      STAMP(12);
#pragma unroll
      for (int i = 0; i < 5; ++i) { C = cmul(C, E); PUTR(O_OUT + i * TROW, toh(C)); }
      SB();
      auto mh = [&](u4v a, u4v bq, f4 cin) AI { return __builtin_amdgcn_mfma_f32_16x16x32_f16(as_h8(a), as_h8(bq), cin, 0, 0, 0); };
      kk[0][0] = f4{0.f, 0.f, 0.f, 0.f}; kk[0][1] = kk[0][0]; kk[1][0] = kk[0][0]; kk[1][1] = kk[0][0];
#pragma unroll
      for (int s = 0; s < 5; ++s) {
#pragma unroll
        for (int k = 0; k < 4; ++k) {
          if (k < 2) { if (s < 4) kk[0][0] = mh(Wf[s + 4 * k], Lf[0][s + 4 * k], kk[0][0]); }
          else if (s > 0) { const int u = s - 1 + 4 * (k - 2); kk[0][1] = mh(Wf[u], Lf[1][u], kk[0][1]); Wf[u] = *(const u4v*)(pf + O_OUT + u * 64); }
          SB();
          if (s < 4) {
            const ch o = hmul(ch{ash(Lf[0][s][k]), ash(Lf[0][s + 4][k])}, ch{ash(Lf[1][s][k]), ash(Lf[1][s + 4][k])});
            Lf[1][s][k] = __builtin_bit_cast(u32, o.r); Lf[1][s + 4][k] = __builtin_bit_cast(u32, o.i);
          }
          if (s == 3 && k == 3) asm volatile("s_nop 1");
          SB();
        }
      }
#pragma unroll
      for (int s = 0; s < 8; ++s) { kk[1][0] = mh(Wf[s], Lf[0][s], kk[1][0]); kk[1][1] = mh(Wf[s], Lf[1][s], kk[1][1]); }
    }
    {
      u32 ia[16];
      const u32 kb_ = lbase + c * CHB + O_OUT;
#pragma unroll
      for (int e = 0; e < 16; ++e) { const u32 iw = ixa[e >> 3][(e >> 1) & 3]; ia[e] = kb_ + ((e & 1) ? (iw >> 16) : (iw & 0xffffu)); }
      MIX2(16, 0, 1)
      SB();
      STAMP(2);
      char* ktp = ch_ + O_OUT + c16 * KT_PITCH + g * 8;
#pragma unroll
      for (int hf = 1; hf >= 0; --hf) {
#pragma unroll
        for (int ct = 0; ct < 2; ++ct) {
          const f4 v = kk[hf][ct] * fk;
          *(u32*)(ktp + ct * 16 * KT_PITCH + hf * 32) = pk_bf(v[0], v[1]);
          *(u32*)(ktp + ct * 16 * KT_PITCH + hf * 32 + 4) = pk_bf(v[2], v[3]);
        }
        asm volatile("" ::: "memory");
      }
      u32 zz = 0u; OPQ(zz);
      *(u32*)(ch_ + O_OUT + KT_ZERO + (lane & 7) * 4) = zz;
      asm volatile("" ::: "memory");
      u32 v[16], pe[9];
#pragma unroll
      for (int e = 15; e >= 0; --e) v[e] = *(const __attribute__((address_space(3))) u16*)(uintptr_t)ia[e];
      SB();
      pe[8] = zz;
#pragma unroll
      for (int i = 7; i >= 0; --i) {
        pe[i] = v[2 * i] | (v[2 * i + 1] << 16);
        const u32 nx = (u32)__builtin_amdgcn_update_dpp((int)dpp_u<0x134>(pe[i + 1]), (int)pe[i], 0x130, 0xf, 0xf, false);
        const u32 po = __builtin_amdgcn_alignbyte(nx, pe[i], 2);
        PUT1(O_TAP + i * 256, pe[i]); PUT1(O_TAP + 2 * TAP_PITCH - 4 + i * 256, pe[i]);
        PUT1(O_TAP + TAP_PITCH + i * 256, po); PUT1(O_TAP + 3 * TAP_PITCH - 4 + i * 256, po);
      }
      ARRIVE(C_TAPS + c);
    }
    STAMP(3);
    IDS();
    conv_setup();
    dm1 = bflo(dskw) - 1.f;
    atload(0, 17);
    m_setup();
    __builtin_amdgcn_s_setprio(1);
    SB();
    WAITA(qa);
    STAMP(4);
    SB();
    FL(0, -7); FL(1, -6); FL(2, -5); FL(3, -4); FL(4, -3); FL(5, -2); FL(6, -1); FL(7, 0); ULD(0); GLD(0);
    MF0(acc[0], At[9], Fq[0]); EUN(0, 0); EUN(0, 1);
    MF(acc[0], At[10], Fq[1]); EUN(0, 2); EUN(0, 3);
    MF(acc[0], At[11], Fq[2]);
    MF(acc[0], At[12], Fq[3]);
    MF(acc[0], At[13], Fq[4]);
    MF(acc[0], At[14], Fq[5]);
    MF(acc[0], At[15], Fq[6]); FL(8, 1);
    MF(acc[0], At[16], Fq[7]);
    MF0(acc[1], At[1], Fq[0]); FL(9, 2);
    MF(acc[1], At[2], Fq[1]);
    MF(acc[1], At[3], Fq[2]); FL(10, 3);
    MF(acc[1], At[4], Fq[3]);
    MF(acc[1], At[5], Fq[4]); FL(11, 4); EA1(0, 0);
    MF(acc[1], At[6], Fq[5]); EA1(0, 1); EA1(0, 2);
    MF(acc[1], At[7], Fq[6]); EA1(0, 3); PEEK(0, qa + 16);
    MF(acc[1], At[8], Fq[7]); FL(12, 5); EA2(0, 0);
    MF0(acc[2], At[0], Fq[7]); EA2(0, 1); EA2(0, 2);
    MF(acc[1], At[9], Fq[8]); EA2(0, 3); EPK(0, 0);
    MF(acc[2], At[1], Fq[8]); EPK(0, 1); FL(13, 6);
    MF(acc[1], At[10], Fq[9]); OST(0); FLAG(C_TILE + 0 * 4);
    MF(acc[2], At[2], Fq[9]); FL(14, 7); FL(15, 8);
    MF(acc[1], At[11], Fq[10]); CHECK(0, qa + 16);
    MF(acc[2], At[3], Fq[10]);
    MF(acc[1], At[12], Fq[11]); FL(16, 9);
    MF(acc[2], At[4], Fq[11]); ULD(1); GLD(1);
    MF(acc[1], At[13], Fq[12]); FL(17, 10);
    MF(acc[2], At[5], Fq[12]);
    MF(acc[1], At[14], Fq[13]); FL(18, 11);
    MF(acc[2], At[6], Fq[13]);
    MF(acc[1], At[15], Fq[14]); FL(19, 12); EUN(1, 0);
    MF(acc[2], At[7], Fq[14]); EUN(1, 1); EUN(1, 2);
    MF(acc[1], At[16], Fq[15]); FL(20, 13); EUN(1, 3);
    MF(acc[2], At[8], Fq[15]);
    MF0(acc[3], At[0], Fq[15]); FL(21, 14);
    MF(acc[2], At[9], Fq[16]);
    MF(acc[3], At[1], Fq[16]); FL(22, 15); EA1(1, 0);
    MF(acc[2], At[10], Fq[17]); EA1(1, 1); EA1(1, 2);
    MF(acc[3], At[2], Fq[17]); FL(23, 16); EA1(1, 3);
    MF(acc[2], At[11], Fq[18]); PEEK(2, fa + 0); EA2(1, 0);
    MF(acc[3], At[3], Fq[18]); EA2(1, 1); EA2(1, 2);
    MF(acc[2], At[12], Fq[19]); EA2(1, 3); EPK(1, 0);
    MF(acc[3], At[4], Fq[19]); EPK(1, 1); FL(24, 17);
    MF(acc[2], At[13], Fq[20]); OST(1); FLAG(C_TILE + 1 * 4);
    MF(acc[3], At[5], Fq[20]); FL(25, 18); CHECK(2, fa + 0);
    MF(acc[2], At[14], Fq[21]); FL(26, 19); ULD(2);
    MF(acc[3], At[6], Fq[21]); FLD(2); GLD(2);
    MF(acc[2], At[15], Fq[22]); FL(27, 20); PEEK(1, qa + 32);
    MF(acc[3], At[7], Fq[22]);
    MF(acc[2], At[16], Fq[23]); FL(28, 21);
    MF(acc[3], At[8], Fq[23]); EUN(2, 0); EUN(2, 1);
    MF0(acc[4], At[0], Fq[23]); FL(29, 22); EUN(2, 2);
    MF(acc[3], At[9], Fq[24]); EUN(2, 3); EA1(2, 0);
    MF(acc[4], At[1], Fq[24]); FL(30, 23); EA1(2, 1);
    MF(acc[3], At[10], Fq[25]); EA1(2, 2); EA1(2, 3);
    MF(acc[4], At[2], Fq[25]); FL(31, 24); EFA(2, 0);
    MF(acc[3], At[11], Fq[26]); EFA(2, 1); EFA(2, 2);
    MF(acc[4], At[3], Fq[26]); EFA(2, 3); PEEK(3, fa + 16);
    MF(acc[3], At[12], Fq[27]); EA2(2, 0); EA2(2, 1);
    MF(acc[4], At[4], Fq[27]); EA2(2, 2); EA2(2, 3);
    MF(acc[3], At[13], Fq[28]); CHECK(1, qa + 32); EPK(2, 0); EPK(2, 1);
    MF(acc[4], At[5], Fq[28]); FL(32, 25); OST(2);
    MF(acc[3], At[14], Fq[29]); FLAG(C_TILE + 2 * 4); FL(33, 26);
    MF(acc[4], At[6], Fq[29]); FL(34, 27); CHECK(3, fa + 16);
    MF(acc[3], At[15], Fq[30]); ULD(3); FL(35, 28);
    MF(acc[4], At[7], Fq[30]); FLD(3); GLD(3);
    MF(acc[3], At[16], Fq[31]); FL(36, 29);
    MF(acc[4], At[8], Fq[31]);
    MF0(acc[5], At[0], Fq[31]); FL(37, 30);
    MF(acc[4], At[9], Fq[32]); EUN(3, 0); EUN(3, 1);
    MF(acc[5], At[1], Fq[32]); PEEK(4, fa + 32); FL(38, 31);
    MF(acc[4], At[10], Fq[33]); EUN(3, 2); EUN(3, 3);
    MF(acc[5], At[2], Fq[33]); FL(39, 32); EA1(3, 0);
    MF(acc[4], At[11], Fq[34]); EA1(3, 1); EA1(3, 2);
    MF(acc[5], At[3], Fq[34]); EA1(3, 3); EFA(3, 0);
    MF(acc[4], At[12], Fq[35]); EFA(3, 1); EFA(3, 2); EFA(3, 3);
    MF(acc[5], At[4], Fq[35]); EA2(3, 0); EA2(3, 1); EA2(3, 2); EA2(3, 3);
    MF(acc[4], At[13], Fq[36]); FL(40, 33); EPK(3, 0); EPK(3, 1);
    MF(acc[5], At[5], Fq[36]); FL(41, 34); OST(3);
    MF(acc[4], At[14], Fq[37]); FL(42, 35); FLAG(C_TILE + 3 * 4); CHECK(4, fa + 32); ULD(4);
    MF(acc[5], At[6], Fq[37]); FL(43, 36); FLD(4); GLD(4); PEEK(5, fa + 48);
    MF(acc[4], At[15], Fq[38]); FL(44, 37); FL(45, 38);
    MF(acc[5], At[7], Fq[38]); FL(46, 39); FL(47, 40);
    MF(acc[4], At[16], Fq[39]);
    MF(acc[5], At[8], Fq[39]); EUN(4, 0); EUN(4, 1);
    MF(acc[5], At[9], Fq[40]); EUN(4, 2); EUN(4, 3);
    MF(acc[5], At[10], Fq[41]); EFA(4, 0); EA1(4, 0);
    MF(acc[5], At[11], Fq[42]); EA1(4, 1); EA1(4, 2);
    MF(acc[5], At[12], Fq[43]); EA1(4, 3); EFA(4, 1); EFA(4, 2); EFA(4, 3); CHECK(5, fa + 48); ULD(5);
    MF(acc[5], At[13], Fq[44]); EA2(4, 0); EA2(4, 1); EA2(4, 2); EA2(4, 3); FLD(5); GLD(5);
    MF(acc[5], At[14], Fq[45]); EPK(4, 0); EPK(4, 1);
    MF(acc[5], At[15], Fq[46]); OST(4); FLAG(C_TILE + 4 * 4);
    MF(acc[5], At[16], Fq[47]); EUN(5, 0); EUN(5, 1); EUN(5, 2); EUN(5, 3); EFA(5, 0); EFA(5, 1); EFA(5, 2); EFA(5, 3); EA1(5, 0); EA1(5, 1); EA1(5, 2); EA1(5, 3); EA2(5, 0); EA2(5, 1); EA2(5, 2); EA2(5, 3); EPK(5, 0); EPK(5, 1); OST(5); FLAG(C_TILE + 5 * 4);
    STAMP(8);
    ROLE_END();
  }
  {
    ROLE("c");
    __builtin_amdgcn_s_setprio(3);
    const u32 ep = *(gp32)(kc[0] + WS_EPOCH);
    {
      u32 zz = 0u; OPQ(zz);
      *(u2v*)(ctl + 2 * lane) = u2v{zz, zz};
      if (lane < 2) *(u4v*)(smem + O_HB + 512 + lane * 528) = u4v{zz, zz, zz, zz};
    }
    BAR();
    u32* selw = (u32*)(smem + O_SEL);
    u32 tag = (u32)__builtin_amdgcn_readfirstlane((int)ep) + 1u;
    tag += tag == 0u;
    if (lane == 0) par[0] = tag;
    {
      const u32 pa = lbase + O_PART + (lane & 7) * 64;
      const u32 ib = 2047u - (u32)(cg * 8 + (lane & 7));
      f4 p0, p1, p2, p3; u32 cnt; int n_ = 1 << 20; unsigned long long tk;
      asm volatile("1:\n\tds_read_b32 %4, %7\n\tds_read_b128 %0, %8\n\tds_read_b128 %1, %8 offset:16\n\tds_read_b128 %2, %8 offset:32\n\tds_read_b128 %3, %8 offset:48\n\t"
                   "s_memrealtime %6\n\ts_sub_u32 %5, %5, 1\n\ts_cbranch_scc1 2f\n\ts_waitcnt lgkmcnt(0)\n\tv_cmp_gt_u32 vcc, 4, %4\n\ts_cbranch_vccnz 1b\n2:\n\ts_waitcnt lgkmcnt(0)"
                   : "=&v"(p0), "=&v"(p1), "=&v"(p2), "=&v"(p3), "=&v"(cnt), "+s"(n_), "=&s"(tk) : "v"(CTLA(C_PART)), "v"(pa) : "vcc", "scc", "memory");
      const f4 s4_ = (p0 + p1) + (p2 + p3);
      const u32 rp = pk_bf(s4_[1] + s4_[3], s4_[0] + s4_[2]);
      const u32 key = ((rp ^ ((u32)((int)rp >> 31) | 0x80000000u)) & 0xffff0000u) | ib;
      const u32 kn = dpp_u<0xb1>(key);
      u32 m1 = max(key, kn), m2 = min(key, kn), m3;
      { const u32 o1 = dpp_u<0x4e>(m1), o2 = dpp_u<0x4e>(m2); const u32 lo = min(m1, o1), hb = max(m2, o2); m1 = max(m1, o1); m2 = max(lo, hb); m3 = min(lo, hb); }
      { const u32 o1 = dpp_u<0x141>(m1), o2 = dpp_u<0x141>(m2), o3 = dpp_u<0x141>(m3); const u32 lo = min(m1, o1), hb = max(m2, o2);
        m1 = max(m1, o1); m2 = max(lo, hb); m3 = max(min(lo, hb), max(m3, o3)); }
      const u32 wm = (((u32)tk + 8u) >> 2) & 0xffu;
      if (lane == 0) __builtin_amdgcn_raw_buffer_store_b128(u4v{m1, m2, (m3 & 0xffff0000u) | wm, tag}, wrs, WS_KEY + cg * 16, 0, 16);
      SB();
      STAMP(1);
      ARRIVE(C_GP);
      {
        const float gv = sigm(bflo(rp));
        const u32 g2 = pk_bf(gv, dpp_f<0xb1>(gv));
        u32 g3 = dpp_u<0x4e>(g2);
        OPQ(g3);
        if ((lane & 59) == 0) __builtin_amdgcn_raw_buffer_store_b128(u4v{g2, g3, tag, tag}, wrs, WS_REC + cg * 32 + lane * 4, 0, 16);
      }
      if ((lane & 57) == 0) __builtin_amdgcn_raw_buffer_store_b128(u4v{key, kn, 0u, tag}, wrs, WS_KALL + cg * 64 + lane * 8, 0, 16);
    }
    u32 l7 = (lane & 7) * 16, ko = WS_KEY + lane * 16;
    OPQ(l7); OPQ(ko);
    WAITQ(C_TL, 1);
    STAMP(5);
    const u32 tkey = (u32)__builtin_amdgcn_readfirstlane((int)par[1]) + DT_KEY;
    pace(tkey);
    u4v gr[4];
    u32 mykey;
    u4v xs[4];
    auto xrs = __builtin_amdgcn_make_buffer_rsrc((void*)(ka[0] + (xi & 3) * 512), 0, L * D * 2 - 1536, 0x00020000);
    asm volatile("" : "+s"(xrs));
    auto xs_ld = [&]() AI {
      const u32 vo = ((~mykey & 2047u) << 11) | l7;
#pragma unroll
      for (int k = 0; k < 4; ++k) xs[k] = __builtin_amdgcn_raw_buffer_load_b128(xrs, vo + k * 128, 0, 0);
    };
#pragma unroll
    for (int i = 0; i < 4; ++i) gr[i] = __builtin_amdgcn_raw_buffer_load_b128(wrs, ko + i * 1024, 0, 16);
#pragma unroll 1
    for (int spin = 0; spin < SPIN_MAX; ++spin) {
      const u32 bad = (gr[0][3] ^ tag) | (gr[1][3] ^ tag) | (gr[2][3] ^ tag) | (gr[3][3] ^ tag);
      if (__builtin_expect(!__any(bad != 0), 1)) break;
      STAMP(13);
      __builtin_amdgcn_s_sleep(SLEEP_AG1);
#pragma unroll
      for (int i = 0; i < 4; ++i)
        if (gr[i][3] != tag) gr[i] = __builtin_amdgcn_raw_buffer_load_b128(wrs, ko + i * 1024, 0, 16);
      asm volatile("" ::: "memory");
    }
    STAMP(2);
    {
      u32 m1 = gr[0][0], m2 = gr[0][1], m3 = 0, m4 = 0;
#pragma unroll
      for (int i = 2; i < 8; ++i) {
        const u32 k = gr[i >> 1][i & 1];
        if (i > 2) m4 = med3u(m3, m4, k);
        m3 = med3u(m2, m3, k);
        m2 = (i & 1) ? max(m2, k) : med3u(m1, m2, k);
        if (!(i & 1)) m1 = max(m1, k);
      }
      const u32 third = max(max(gr[0][2], gr[1][2]), max(gr[2][2], gr[3][2]));
      u32 W2;
      mykey = wave_top8(m1, m2, m3, m4, W2);
      xs_ld();
      SB();
      if (__builtin_expect(__any(m1 == 0u || (third | 0xffffu) > W2), 0)) {
        u4v ak[16];
#pragma unroll 1
        for (int spin = 0; spin < SPIN_MAX; ++spin) {
          u32 bad = 0;
#pragma unroll
          for (int i = 0; i < 16; ++i) {
            ak[i] = __builtin_amdgcn_raw_buffer_load_b128(wrs, WS_KALL + (i & 3) * 1024 + lane * 16, (i >> 2) * 4096, 16);
            bad |= ak[i][3] ^ tag;
          }
          if (!__any(bad != 0)) break;
          __builtin_amdgcn_s_sleep(8);
          asm volatile("" ::: "memory");
        }
        u32 prev = 0xffffffffu;
#pragma unroll 1
        for (int it = 0; it < 8; ++it) {
          u32 mm_ = 0;
#pragma unroll
          for (int i = 0; i < 32; ++i) {
            const u32 k = ak[i >> 1][i & 1];
            mm_ = max(mm_, k < prev ? k : 0u);
          }
          OPQ(mm_);
          prev = wave_max_u32(mm_);
          mykey = (lane >> 3) == it ? prev : mykey;
        }
        xs_ld();
      }
      OPQ(mykey);
    }
    SB();
    STAMP(3);
    if (l7 == 0) selw[lane >> 3] = mykey;
    {
      const u32 tn = now32();
      u32 yg = 0;
#pragma unroll
      for (int i = 0; i < 4; ++i) yg = max(yg, (gr[i][2] - (tn >> 2) - 1u) & 0xffu);
      const u32 tpub = tn - 4u * (255u - wave_max_u32(yg)) + DT_VIS;
      if (lane == 0) par[2] = (int)(tpub - tkey) > 0 ? tpub + DT_LATE : tkey;
    }
    ARRIVE(C_SEL);
    if (b == 255 && lane == 0) __builtin_amdgcn_raw_buffer_store_b32(tag, wrs, WS_EPOCH, 0, 16);
    SB();
    {
      char* xp = smem + O_XS + (lane >> 3) * XS_PITCH + l7;
#pragma unroll
      for (int k = 0; k < 4; ++k) *(u4v*)(xp + k * 128) = xs[k];
      ARRIVE(C_XS);
      STAMP(6);
    }
    __builtin_amdgcn_s_setprio(2);
    auto ors = __builtin_amdgcn_make_buffer_rsrc((void*)kc[1], 0, L * D * 2, 0x00020000);
    const u32 rl = (lane + (xi & 15) * 4) & 63, un = (g * 4 + (lane & 3) + xi) & 15;
    u32 skip = 0;
#pragma unroll
    for (int k = 0; k < 8; ++k) {
      const u32 r = ~(u32)__builtin_amdgcn_readlane((int)mykey, 8 * k) & 2047u;
      skip |= (r & 63u) == rl ? 1u << (r >> 6) : 0u;
    }
    const char* op = smem + ((lane >> 2) & 3) * CHB + O_OUT + (un & 3) * 144 + (un >> 2) * 8;
    const u32 ob = rl * 2048 + cc * 8;
    const u32 tfast = (u32)__builtin_amdgcn_readfirstlane((int)par[2]) + DE_FAST;
#pragma unroll 1
    for (int t = 0; t < 8; ++t) {
      WAITA(CTLA(C_TILE) + t * 16 + (lane & 3) * 4);
      u2v yv[4];
#pragma unroll
      for (int e = 0; e < 4; ++e) yv[e] = tr_read(op + t * OT + e * 32);
      const bool slow = (int)(now32() - tfast) < 0;
#pragma unroll
      for (int e = 0; e < 4; ++e) {
        __builtin_amdgcn_raw_buffer_store_b64(yv[e], ors, ob | (((skip >> e) & 1u) << 30), t * 524288 + e * 131072, 0);
        if (slow) {
          SB(); __builtin_amdgcn_s_sleep(STSLP); SB();
          asm volatile("s_waitcnt vmcnt(%0)" :: "n"(STWIN - 1) : "memory");
        }
      }
      skip >>= 4;
    }
    STAMP(9);
    ROLE_END();
  }
  {
    ROLE("d");
        const int j = xg * 28 + xi;
    const int s1 = j >> 2, kq = j & 3;
    const bool own = xi < 28;
    const u64 wbase = kb[3];
    const int pitch = D * 2;
    auto hrs = __builtin_amdgcn_make_buffer_rsrc((void*)wbase, 0, own ? D * F * 2 : 0, 0x00020000);
    const int r8 = lane >> 3;
    const int vo = (64 * s1 * D + 256 * kq) * 2 + (8 * (r8 >> 2) + (r8 & 3)) * pitch + (((lane & 7) * 16) ^ (((r8 >> 1) & 3) * 32));
    auto dma = [&](int h, int sB, int pt, u32 dst) AI {
      const int m = h >> 1;
#pragma unroll
      for (int i = 0; i < 2; ++i)
        asm volatile("s_mov_b32 m0, %3\n\ts_nop 0\n\tbuffer_load_dwordx4 %0, %1, %2 offen nt lds"
                     :: "v"(vo), "s"(hrs), "s"((m & 1) * 32 * pt + (m >> 1) * sB + (16 * i + 4 * (h & 1)) * pt), "s"(dst + i * 1024) : "memory");
    };
    const int q = c16 >> 2, p = c16 & 3;
    int tro[4];
#pragma unroll
    for (int ct = 0; ct < 4; ++ct) tro[ct] = (4 * g + q) * 128 + ((ct ^ (((4 * g + q) >> 1) & 3)) * 32) + p * 8;
    const int tok = lane & 7;
    u4v wd[2][8];
    auto wd_ld = [&](int k, int cb) AI {
#pragma unroll
      for (int i = 0; i < 8; ++i) {
        const char* pp = smem + O_WD + cb * 8192 + (i >> 2) * 4096 + tro[i & 3];
        const u2v lo = tr_read(pp), hi = tr_read(pp + 2048);
        wd[k][i] = u4v{lo[0], lo[1], hi[0], hi[1]};
      }
    };
    u32 p2o, p2k[4], hlo, hhi;
    auto f2_setup = [&](int cb0) AI {
      p2o = WS_P2 + ((64 * kq + 16 * cb0 + (lane & 8) / 2 + g) * 56 + s1) * 128 + tok * 16;
      hlo = lbase + O_HB + ((lane & 8) ? 512 : g * 128 + tok * 16);
      hhi = lbase + O_HB + ((lane & 8) ? g * 128 + tok * 16 : 512);
#pragma unroll
      for (int i = 0; i < (cb0 == 2 ? 4 : 2); ++i) { p2k[i] = p2o + i * 8 * 7168; OPQ(p2k[i]); }
      OPQ(hlo); OPQ(hhi);
    };
    u4v hb[4];
    f4 r[4];
    auto hb_ld = [&]() AI {
      u32 cnt; int n_ = 1 << 20;
      asm volatile("1:\n\tds_read_b32 %4, %6\n\tds_read_b128 %0, %7\n\tds_read_b128 %1, %8\n\tds_read_b128 %2, %7 offset:528\n\tds_read_b128 %3, %8 offset:528\n\t"
                   "s_sub_u32 %5, %5, 1\n\ts_cbranch_scc1 2f\n\ts_waitcnt lgkmcnt(0)\n\tv_cmp_gt_u32 vcc, 2, %4\n\ts_cbranch_vccnz 1b\n2:\n\ts_waitcnt lgkmcnt(0)"
                   : "=&v"(hb[0]), "=&v"(hb[1]), "=&v"(hb[2]), "=&v"(hb[3]), "=&v"(cnt), "+s"(n_) : "v"(CTLA(C_HRDY)), "v"(hlo), "v"(hhi) : "vcc", "scc", "memory");
    };
    auto f2_mm = [&](int k) AI {
      const u4v* wp = wd[k];
      r[2 * k] = mma(wp[0], hb[0], f4{0.f, 0.f, 0.f, 0.f}); r[2 * k + 1] = mma(wp[2], hb[0], f4{0.f, 0.f, 0.f, 0.f});
      r[2 * k] = mma(wp[1], hb[1], r[2 * k]); r[2 * k + 1] = mma(wp[3], hb[1], r[2 * k + 1]);
      r[2 * k] = mma(wp[4], hb[2], r[2 * k]); r[2 * k + 1] = mma(wp[6], hb[2], r[2 * k + 1]);
      r[2 * k] = mma(wp[5], hb[3], r[2 * k]); r[2 * k + 1] = mma(wp[7], hb[3], r[2 * k + 1]);
    };
    auto f2_st = [&](int k, u32 t10) AI {
#pragma unroll
      for (int i = 2 * k; i < 2 * k + 2; ++i) {
        u4v v = __builtin_bit_cast(u4v, r[i]);
#pragma unroll
        for (int e = 0; e < 4; ++e) v[e] = __builtin_amdgcn_bitop3_b32(v[e], t10, TMASK, 0xd8);
        __builtin_amdgcn_raw_buffer_store_b128(v, wrs, p2k[i], 0, 16);
      }
    };
    {
      __builtin_amdgcn_s_setprio(3);
      BAR();
      const u32 ts = ts0 - 500u;
      u32 tl = ~0u;
      pace(ts0 + DT_HEAD);
      STAMP(3);
#pragma unroll 1
      for (int i = 0; i < 8 && tl >= 1500u; ++i) {
        const u2v hd = __builtin_amdgcn_raw_buffer_load_b64(wrs, WS_HEAD + (lane & 7) * 8, 0, 16);
        tl = wave_max_u32(hd[0] == ~hd[1] ? hd[0] - ts : ~0u);
        STAMP(14);
        asm volatile("" ::: "memory");
      }
      tl = tl < 1500u ? tl + ts : ts0;
      if (lane == 0) par[1] = tl;
      ARRIVE(C_TL);
      STAMP(2);
      if (own) {
        WAITS(C_WGU, 1);
#pragma unroll
        for (int h = 0; h < 16; ++h) {
          if (h == DSEL) WAITS(C_SEL, 1);
          if (h == DEV) WAITS(C_XL, 4);
          if (h >= DWIN) asm volatile("s_waitcnt vmcnt(%0)" :: "n"(2 * (DWIN - 1)) : "memory");
          if (h == 3 + DWIN) ARRIVE(C_WD);
          if (h == 7 + DWIN) ARRIVE(C_WD + 1);
          dma(h, 128, D * 2, lbase + O_WD + h * 2048);
        }
        f2_setup(2);
        const u32 t10 = par[0] & TMASK;
        asm volatile("s_waitcnt vmcnt(8)" ::: "memory");
        wd_ld(0, 2);
        const u32 hc = __hip_atomic_load(ctl + C_HRDY, __ATOMIC_RELAXED, __HIP_MEMORY_SCOPE_WORKGROUP);
        const bool early = __builtin_amdgcn_ballot_w64(hc < 2u) == 0ull;
        if (early) { hb_ld(); f2_mm(0); }
        asm volatile("s_waitcnt vmcnt(0)" ::: "memory");
        STAMP(5);
        wd_ld(1, 3);
        if (!early) { WAITS(C_HRDY, 1); hb_ld(); STAMP(11); f2_mm(0); }
        SB();
        f2_mm(1);
        f2_st(0, t10);
        SB();
        f2_st(1, t10);
        STAMP(12);
      }
      ROLE_END();
    }
  }
}

extern "C" void kernel_launch(void* const* d_in, const int* in_sizes, int n_in,
                              void* d_out, int out_size, void* d_ws, size_t ws_size,
                              hipStream_t stream) {
  k_main<<<256, 768, 0, stream>>>((const u16*)d_in[0], (const u16*)d_in[8], (const u16*)d_in[12],
                                  (const u16*)d_in[1], (const u16*)d_in[2], (const u16*)d_in[3],
                                  (const u16*)d_in[4], (const u16*)d_in[5], (const u16*)d_in[6],
                                  (const u16*)d_in[9], (const u16*)d_in[10], (const u16*)d_in[11],
                                  (char*)d_ws, (u16*)d_out);
}
```

```cpp
#include <hip/hip_runtime.h>
#include <stdint.h>

typedef unsigned short u16;
typedef unsigned u32;
typedef _Float16 h2 __attribute__((ext_vector_type(2)));
typedef _Float16 h8 __attribute__((ext_vector_type(8)));
typedef __bf16 b2 __attribute__((ext_vector_type(2)));
typedef __bf16 b8 __attribute__((ext_vector_type(8)));
typedef short s4 __attribute__((ext_vector_type(4)));
typedef float f2 __attribute__((ext_vector_type(2)));
typedef float f4 __attribute__((ext_vector_type(4)));
typedef u32 u2v __attribute__((ext_vector_type(2)));
typedef u32 u3v __attribute__((ext_vector_type(3)));
typedef u32 u4v __attribute__((ext_vector_type(4)));
typedef __attribute__((address_space(3))) s4* lds_s4p;

constexpr int L = 2048;
constexpr int D = 1024;
constexpr int NM = 128;
constexpr int F = 3584;
constexpr int NTAP = 1024;

constexpr int SEG_I = 576, SEG_O0 = 800, SEG_O1 = 880, SEG_SP = 896;

#define AI __attribute__((always_inline))
#define SB() __builtin_amdgcn_sched_barrier(0)
#define OPQ(x) asm volatile("" : "+v"(x))
#define DIAG_K 14
#define DIAG_M 96
#define STAMP0() u32 dg_ = 0; const bool dgw_ = (DIAG_M >> w) & 1; if (DIAG_K == 0 && dgw_) dg_ = (u32)kt[0] | 1u
#define STAMP(i) do { if (i == DIAG_K && dgw_) { SB(); dg_ = (u32)__builtin_amdgcn_s_memrealtime() | 1u; SB(); } } while (0)
#define STAMP_END() do { if (DIAG_K == 99 && dgw_) { asm volatile("s_waitcnt vmcnt(0)" ::: "memory"); dg_ = (u32)__builtin_amdgcn_s_memrealtime() | 1u; } if (dg_) { while ((int)((u32)__builtin_amdgcn_s_memrealtime() - (dg_ + 4000u)) < 0) __builtin_amdgcn_s_sleep(2); } } while (0)

#define STAMP_T0

__device__ __forceinline__ float bflo(u32 v) { return __uint_as_float(v << 16); }
__device__ __forceinline__ float bfhi(u32 v) { return __uint_as_float(v & 0xffff0000u); }
__device__ __forceinline__ u32 pk_bf(float a, float b) { f2 v = {a, b}; b2 r = __builtin_convertvector(v, b2); return __builtin_bit_cast(u32, r); }
__device__ __forceinline__ float rb1(float v) { return __uint_as_float(pk_bf(0.f, v)); }
__device__ __forceinline__ h8 as_h8(u4v v) { return __builtin_bit_cast(h8, v); }
__device__ __forceinline__ b8 as_b8(u4v v) { return __builtin_bit_cast(b8, v); }
__device__ __forceinline__ u2v tr_read(const char* p) {
  s4 t = __builtin_amdgcn_ds_read_tr16_b64_v4i16((lds_s4p)p);
  return __builtin_bit_cast(u2v, t);
}
template <int CTRL> __device__ __forceinline__ u32 dpp_u(u32 v) { return (u32)__builtin_amdgcn_update_dpp(0, (int)v, CTRL, 0xf, 0xf, false); }
template <int CTRL> __device__ __forceinline__ float dpp_f(float v) { return __uint_as_float(dpp_u<CTRL>(__float_as_uint(v))); }
__device__ __forceinline__ u32 wave_max_u32(u32 v) {
  v = max(v, dpp_u<0x111>(v)); v = max(v, dpp_u<0x112>(v)); v = max(v, dpp_u<0x114>(v)); v = max(v, dpp_u<0x118>(v));
  v = max(v, dpp_u<0x142>(v)); v = max(v, dpp_u<0x143>(v));
  return (u32)__builtin_amdgcn_readlane((int)v, 63);
}
#define DPM " row_mask:0xf bank_mask:0xf bound_ctrl:1\n\t"
#define T2S(HO, HI, LI, C) "v_max_u32_dpp " HO ", " HI ", " HI " " C DPM "v_min_u32_dpp %8, " HI ", " HI " " C DPM "v_max_u32_dpp %7, " LI ", " LI " " C DPM "v_max_u32 %7, %7, %8\n\t"
#define T2R(RM, REST) T2S("%5", "%0", "%1", "row_shr:1") T2S("%6", "%5", "%7", "row_shr:2") T2S("%5", "%6", "%7", "row_shr:4") T2S("%6", "%5", "%7", "row_shr:8") \
    T2S("%5", "%6", "%7", "row_bcast:15") T2S("%6", "%5", "%7", "row_bcast:31") \
    "v_readlane_b32 %9, %6, 63\n\tv_readlane_b32 %10, %7, 63\n\ts_nop 0\n\tv_mov_b32 %5, %9\n\tv_cmp_le_u32 %11, %10, %0\n\tv_cmp_le_u32 %12, %10, %1\n\tv_mov_b32 %6, %10\n\t" \
    "v_cndmask_b32 %0, %0, %1, %11\n\tv_mov_b32_dpp %4, %5 quad_perm:[0,1,2,3] row_mask:" RM " bank_mask:0x3\n\tv_cndmask_b32 %0, %0, %2, %12\n\t" \
    "v_mov_b32_dpp %4, %6 quad_perm:[0,1,2,3] row_mask:" RM " bank_mask:0xc\n\t" REST
#define T2P "v_cndmask_b32 %1, %1, %2, %11\n\tv_cndmask_b32 %2, %2, %3, %11\n\tv_cndmask_b32 %1, %1, %3, %12\n\tv_cndmask_b32 %3, %3, 0, %11\n\tv_cndmask_b32 %2, %2, 0, %12\n\t"
__device__ __forceinline__ u32 wave_top8(u32& m1, u32 m2, u32 m3, u32 m4, u32& w2) {
  u32 key, ta, tb, lo, t, w1;
  unsigned long long c1, c2;
  asm("s_nop 1\n\t" T2R("0x1", T2P) T2R("0x2", T2P) T2R("0x4", T2P) T2R("0x8", "") "s_nop 0"
      : "+v"(m1), "+v"(m2), "+v"(m3), "+v"(m4), "=&v"(key), "=&v"(ta), "=&v"(tb), "=&v"(lo), "=&v"(t), "=&s"(w1), "=&s"(w2), "=&s"(c1), "=&s"(c2));
  return key;
}

__device__ __forceinline__ float exp_bf(float v) {
  const float e = __builtin_amdgcn_exp2f(v * 1.44268798828125f);
  return fmaf(e, v * (7.0526077e-06f * 0.69314718f), e);
}
struct cf { f2 r, i; };
struct ch { h2 r, i; };
__device__ __forceinline__ cf cmul(cf a, cf b) { cf c; c.r = a.r * b.r - a.i * b.i; c.i = a.r * b.i + a.i * b.r; return c; }
__device__ __forceinline__ ch hmul(ch a, ch b) {
  ch c;
  asm("v_pk_mul_f16 %0, %2, %4\n\tv_pk_mul_f16 %1, %2, %5\n\tv_pk_fma_f16 %0, %3, %5, %0 neg_lo:[1,0,0] neg_hi:[1,0,0]\n\tv_pk_fma_f16 %1, %3, %4, %1"
      : "=&v"(c.r), "=&v"(c.i) : "v"(a.r), "v"(a.i), "v"(b.r), "v"(b.i));
  return c;
}
__device__ __forceinline__ cf csq(cf a) { cf c; c.r = a.r * a.r - a.i * a.i; c.i = (a.r + a.r) * a.i; return c; }
__device__ __forceinline__ ch toh(cf a) { ch c; c.r = __builtin_convertvector(a.r, h2); c.i = __builtin_convertvector(a.i, h2); return c; }
__device__ __forceinline__ ch tohc(cf a) { ch c; c.r = __builtin_convertvector(a.r, h2); c.i = __builtin_convertvector(-a.i, h2); return c; }
__device__ __forceinline__ h2 ash(u32 v) { return __builtin_bit_cast(h2, v); }
__device__ __forceinline__ cf cexp_p(f2 a2, f2 brev, float p) {
  f2 e = a2 * p, t = brev * p;
  f2 mag = {__builtin_amdgcn_exp2f(e[0]), __builtin_amdgcn_exp2f(e[1])};
  float r0 = __builtin_amdgcn_fractf(t[0]), r1 = __builtin_amdgcn_fractf(t[1]);
  cf c;
  c.r = mag * f2{__builtin_amdgcn_cosf(r0), __builtin_amdgcn_cosf(r1)};
  c.i = mag * f2{__builtin_amdgcn_sinf(r0), __builtin_amdgcn_sinf(r1)};
  return c;
}
__device__ __forceinline__ u32 med3u(u32 a, u32 b, u32 c) { u32 r; asm("v_med3_u32 %0, %1, %2, %3" : "=v"(r) : "v"(a), "v"(b), "v"(c)); return r; }
__device__ __forceinline__ f2 unbf(u32 v) { return f2{bflo(v), bfhi(v)}; }
__device__ __forceinline__ float sigm(float v) { return __builtin_amdgcn_rcpf(1.f + __builtin_amdgcn_exp2f(-1.44269504088896341f * v)); }

#define TS2(A, B, C, D) "v_xor_b32 %5, " A ", %9\n\tv_xor_b32 %6, " B ", %9\n\tv_xor_b32 %7, " C ", %9\n\tv_xor_b32 %8, " D ", %9\n\t" \
    "v_fma_f32 %1, %5, 1.0, %1\n\tv_fma_f32 %2, %6, 1.0, %2\n\tv_or3_b32 %0, %0, %5, %6\n\tv_fma_f32 %3, %7, 1.0, %3\n\tv_fma_f32 %4, %8, 1.0, %4\n\tv_or3_b32 %0, %0, %7, %8\n\t"
#define TSR TS2("%10", "%11", "%12", "%13") TS2("%14", "%15", "%16", "%17") TS2("%18", "%19", "%20", "%21")
#define TSI "=&v"(t0), "=&v"(t1), "=&v"(t2), "=&v"(t3) : "v"(t10), "v"(p[0]), "v"(p[1]), "v"(p[2]), "v"(p[3]), "v"(q[0]), "v"(q[1]), "v"(q[2]), "v"(q[3]), \
    "v"(r[0]), "v"(r[1]), "v"(r[2]), "v"(r[3])
__device__ __forceinline__ f4 tsum4(u32& bad, u32 t10, u4v s, u4v p, u4v q, u4v r) {
  float s0, s1, s2, s3; u32 t0, t1, t2, t3;
  asm volatile("v_xor_b32 %1, %22, %9\n\tv_xor_b32 %2, %23, %9\n\tv_xor_b32 %3, %24, %9\n\tv_xor_b32 %4, %25, %9\n\tv_or3_b32 %0, %0, %1, %2\n\tv_or3_b32 %0, %0, %3, %4\n\t" TSR
               : "+v"(bad), "=&v"(s0), "=&v"(s1), "=&v"(s2), "=&v"(s3), TSI, "v"(s[0]), "v"(s[1]), "v"(s[2]), "v"(s[3]));
  return f4{s0, s1, s2, s3};
}
__device__ __forceinline__ f4 tsum3(u32& bad, u32 t10, f4 s, u4v p, u4v q, u4v r) {
  float s0 = s[0], s1 = s[1], s2 = s[2], s3 = s[3]; u32 t0, t1, t2, t3;
  asm volatile(TSR : "+v"(bad), "+v"(s0), "+v"(s1), "+v"(s2), "+v"(s3), TSI);
  return f4{s0, s1, s2, s3};
}
__device__ __forceinline__ void silu4(f4& g, float nl2e) {
  float g0 = g[0], g1 = g[1], g2 = g[2], g3 = g[3], t0, t1, t2, t3;
  asm volatile("v_fma_f32 %4, %0, %8, 0\n\tv_fma_f32 %5, %1, %8, 0\n\tv_fma_f32 %6, %2, %8, 0\n\tv_fma_f32 %7, %3, %8, 0\n\t"
               "v_exp_f32 %4, %4\n\tv_exp_f32 %5, %5\n\tv_exp_f32 %6, %6\n\tv_exp_f32 %7, %7\n\t"
               "v_fma_f32 %4, %4, 1.0, 1.0\n\tv_fma_f32 %5, %5, 1.0, 1.0\n\tv_fma_f32 %6, %6, 1.0, 1.0\n\tv_fma_f32 %7, %7, 1.0, 1.0\n\t"
               "v_rcp_f32 %4, %4\n\tv_rcp_f32 %5, %5\n\tv_rcp_f32 %6, %6\n\tv_rcp_f32 %7, %7\n\t"
               "v_fma_f32 %0, %0, %4, 0\n\tv_fma_f32 %1, %1, %5, 0\n\tv_fma_f32 %2, %2, %6, 0\n\tv_fma_f32 %3, %3, %7, 0"
               : "+v"(g0), "+v"(g1), "+v"(g2), "+v"(g3), "=&v"(t0), "=&v"(t1), "=&v"(t2), "=&v"(t3) : "v"(nl2e));
  g = f4{g0, g1, g2, g3};
}

constexpr int KT_PITCH = 52;
constexpr int kt_off(int p) {
  int i = 20, j = 0;
  if (p < 256) { i = p & 7; j = p >> 3; }
  else if (p < 512) { int q = (p - 256) >> 1; i = 8 + (q & 3); j = q >> 2; }
  else if (p < 1024) { int q = (p - 512) >> 2; i = 12 + (q & 1) + 2 * (q >> 6); j = (q >> 1) & 31; }
  else if (p < 2048) { int q = (p - 1024) >> 3; i = 16 + (q >> 5); j = q & 31; }
  return j * KT_PITCH + i * 2;
}
constexpr int KT_ZERO = 32 * KT_PITCH + 16;
constexpr int rnd_lag(int m) {
  const int st = m < 256 ? 1 : m < 512 ? 2 : m < 1024 ? 4 : 8;
  int q = m / st;
  const int rem = m - q * st;
  if (2 * rem > st || (2 * rem == st && (q & 1))) ++q;
  return q * st;
}
constexpr int tap_entry(int e) {
  if (e < 544) {
    if (543 - e <= 525) return kt_off(rnd_lag(543 - e));
  } else if (e >= 560 && e < 752) {
    const int q = (e - 560) / 48, x = (e - 560) - q * 48;
    const int p = 512 + 16 * (31 - x) + 4 * (q + 1);
    if (x < 32 && p > 524) return kt_off(p);
  } else if (e >= 752 && e < 816) {
    return kt_off(1032 + 16 * (815 - e));
  } else if (e >= 832 && e < 896) {
    return kt_off(1040 + 16 * (895 - e));
  } else if (e == SEG_SP + 27 || e == SEG_SP + 28) {
    return kt_off(1024);
  }
  return KT_ZERO;
}
struct TapIdx { u16 v[NTAP]; };
constexpr TapIdx make_tapidx() {
  TapIdx t{};
  for (int e = 0; e < NTAP; ++e) t.v[e] = (u16)tap_entry(2 * ((e >> 4) + 64 * ((e >> 1) & 7)) + (e & 1));
  return t;
}
__device__ __attribute__((aligned(16))) const TapIdx g_tapidx = make_tapidx();

constexpr int TROW = 544;
constexpr int O_OUT = 0;
constexpr int OT = 576;
constexpr int O_U = O_OUT + 8 * OT;
constexpr int O_TAP = O_U + 4608;
constexpr int TAP_PITCH = 2 * NTAP + 64;
constexpr int CHB = O_TAP + 16 * TROW + 32;
static_assert(16 * TROW >= 4 * TAP_PITCH, "lds");
static_assert((CHB / 4) % 32 == 8, "banks");
#define DEV 3
#define DWIN 8
#define DSEL 0
constexpr int NSLOT = 5, WWIN = 4;
constexpr int O_GT = 4 * CHB;
constexpr int O_RING = O_GT + 8192;
constexpr int O_WD = O_RING + 2 * NSLOT * 2048;
constexpr int O_FAR = O_WD + 32768;
constexpr int O_XS = O_FAR + 16384;
constexpr int XS_PITCH = 544;
constexpr int O_HB = O_XS + 8 * XS_PITCH;
constexpr int O_PART = O_HB + 2 * 528;
constexpr int O_SEL = O_PART + 512;
constexpr int O_PAR = O_SEL + 32;
constexpr int O_CTL = O_PAR + 16;
constexpr int O_JUNK = O_CTL + 512;
constexpr int K_LDS = O_JUNK + 1024;
static_assert(DWIN == 8, "flags of the down tile");
static_assert(K_LDS <= 163840 && O_RING % 16 == 0, "lds");
enum { C_PART = 0, C_GP, C_PUB, C_SEL, C_XS, C_HRDY, C_WGU, C_TL, C_XL, C_WD = 10, C_TAPS = 12, C_Q = 16, C_FAR = 32, C_TILE = 48 };

#define CTLA(word) (lbase + O_CTL + (word) * 4)
#define ARRIVE(word) asm volatile("s_mov_b64 exec, 1\n\tds_add_u32 %0, %1\n\ts_mov_b64 exec, -1" :: "v"(CTLA(word)), "v"(1) : "memory")
#define WAITQ(word, target) do { u32 t_; int n_ = 1 << 20; asm volatile( \
    "1:\n\tds_read_b32 %0, %2\n\ts_sub_u32 %1, %1, 1\n\ts_cbranch_scc1 2f\n\ts_waitcnt lgkmcnt(0)\n\tv_cmp_gt_u32 vcc, %3, %0\n\ts_cbranch_vccnz 1b\n2:\n\ts_waitcnt lgkmcnt(0)" \
    : "=&v"(t_), "+s"(n_) : "v"(CTLA(word)), "n"(target) : "vcc", "scc", "memory"); } while (0)
#define WAITS(word, target) do { u32 t_; int n_ = 1 << 20; asm volatile( \
    "1:\n\tds_read_b32 %0, %2\n\ts_sub_u32 %1, %1, 1\n\ts_cbranch_scc1 2f\n\ts_waitcnt lgkmcnt(0)\n\tv_cmp_le_u32 vcc, %3, %0\n\ts_cbranch_vccnz 2f\n\ts_sleep 2\n\ts_branch 1b\n2:\n\ts_waitcnt lgkmcnt(0)" \
    : "=&v"(t_), "+s"(n_) : "v"(CTLA(word)), "n"(target) : "vcc", "scc", "memory"); } while (0)
#define BAR() asm volatile("s_waitcnt lgkmcnt(0)\n\ts_barrier" ::: "memory")
__device__ __forceinline__ u2v lds8(const char* p) { return __builtin_bit_cast(u2v, __hip_atomic_load((const unsigned long long*)p, __ATOMIC_RELAXED, __HIP_MEMORY_SCOPE_WORKGROUP)); }

constexpr int WS_EPOCH = 0;
constexpr int WS_HEAD = 1024;
constexpr int WS_REC = 4096;
constexpr int WS_FLG = 40960;
constexpr int WS_KALL = WS_REC + 8192;
constexpr int WS_KEY = 32768;
constexpr int WS_P1 = 65536;
constexpr int WS_P2 = WS_P1 + 56 * 16384;
constexpr int WS_XT = WS_P2 + 256 * 7168;
constexpr int XT_PITCH = 33280;
constexpr int WS_END = WS_XT + 128 * XT_PITCH;
constexpr int SPIN_MAX = 1 << 16;
constexpr u32 TMASK = 0x3ffu;

#define DT_HEAD 110
#define WDELAY 60
#define DT_KEY 240
#define DT_REC 270
#define DT_FLG 380
#define DT_VIS 40
#define DT_LATE 50
#define DE_P1 350
#define DE_P2 590
#define DE_FAST 575
#define DT_MIN 25
#define DT_P1 100
#define DT_P2 130
#define SLEEP_AG1 12
#define SLEEP_G 6
#define STSLP 2
#define STWIN 4
#define H2_AT 9
#define CODE_B 28400

#define ROLE_END() do { STAMP_END(); asm volatile("s_endpgm" ::: "memory"); } while (0)
__device__ __forceinline__ u32 now32() { return (u32)__builtin_amdgcn_s_memrealtime(); }
__device__ __forceinline__ void pace(u32 t) {
#pragma unroll 1
  for (int i = 0; i < 1024 && (int)(now32() - t) < 0; ++i) __builtin_amdgcn_s_sleep(1);
}
__device__ __forceinline__ void pace2(u32 n, u32 t, int dmax) {
  int d = (int)(t - n);
  d = d < DT_MIN ? DT_MIN : (d > dmax ? dmax : d);
  pace(n + (u32)d);
}

#define ALL_OTHER "vcc", "scc", "memory", \
    "s0","s1","s2","s5","s6","s7","s33","s34","s35","s44","s45","s46","s47","s48","s49","s50","s51","s52","s53","s54","s55","s56","s57","s58","s59", \
    "s60","s61","s62","s63","s64","s65","s66","s67","s68","s69","s70","s71","s72","s73","s74","s75","s76","s77","s78","s79","s80","s81","s82","s83","s84", \
    "s85","s86","s87","s88","s89","s90","s91","s92","s93","s94","s95","s96","s97","s98","s99", \
    "v0","v1","v2","v3","v4","v5","v6","v7","v8","v9","v10","v11","v12","v13","v14","v15","v16","v17","v18","v19","v20","v21","v22","v23","v24","v25", \
    "v26","v27","v28","v29","v30","v31","v32","v33","v34","v35","v36","v37","v38","v39","v40","v41","v42","v43","v44","v45","v46","v47","v48","v49","v50", \
    "v51","v52","v53","v54","v55","v56","v57","v58","v59","v60","v61","v62","v63","v64","v65","v66","v67","v68","v69","v70","v71","v72","v73","v74","v75", \
    "v76","v77","v78","v79","v80","v81","v82","v83","v84","v85","v86","v87","v88","v89","v90","v91","v92","v93","v94","v95","v96","v97","v98","v99", \
    "v100","v101","v102","v103","v104","v105","v106","v107","v108","v109","v110","v111","v112","v113","v114","v115","v116","v117","v118","v119","v120", \
    "v121","v122","v123","v124","v125","v126","v127","v128","v129","v130","v131","v132","v133","v134","v135","v136","v137","v138","v139","v140","v141", \
    "v142","v143","v144","v145","v146","v147","v148","v149","v150","v151","v152","v153","v154","v155","v156","v157","v158","v159","v160","v161","v162", \
    "v163","v164","v165","v166","v167"
typedef __attribute__((address_space(1))) const u16* gp16;
typedef __attribute__((address_space(1))) const u32* gp32;
typedef __attribute__((address_space(1))) const u4v* gp128;
typedef unsigned long long u64;
typedef u64 q2 __attribute__((ext_vector_type(2)));
typedef u64 q4 __attribute__((ext_vector_type(4)));
typedef u64 q8 __attribute__((ext_vector_type(8)));

__global__ void __launch_bounds__(768, 3) k_main(
    const u16* x_, const u16* w_r_, const u16* gate_w_, const u16* logA_, const u16* Aim_, const u16* Cre_, const u16* Cim_,
    const u16* log_dt_, const u16* Dskip_, const u16* w_gate_, const u16* w_up_, const u16* w_down_, char* wsp_, u16* out_) {
  __shared__ __attribute__((aligned(256))) char smem[K_LDS];
  const u32 lbase = (u32)(uintptr_t)(__attribute__((address_space(3))) char*)smem;
#define XDMA(OFF) "global_load_lds_dwordx4 v1, s[8:9] offset:" #OFF " nt\n\ts_addk_i32 m0, 896\n\t"
  asm volatile("s_load_dwordx16 s[8:23], %0, 0\n\tv_readfirstlane_b32 s3, %2\n\ts_load_dwordx8 s[24:31], %0, 64\n\ts_load_dwordx4 s[36:39], %0, 96\n\t"
               "s_memrealtime s[40:41]\n\t" STAMP_T0 "s_mov_b32 s4, %1\n\ts_lshr_b32 s3, s3, 6\n\t"
               "s_cmp_ge_u32 s3, 8\n\ts_cbranch_scc1 .Lrole_g\n\ts_cmp_eq_u32 s3, 7\n\ts_cbranch_scc1 .Lrole_d\n\t"
               "s_cmp_ge_u32 s3, 5\n\ts_cbranch_scc1 .Lrole_h\n\ts_cmp_eq_u32 s3, 4\n\ts_cbranch_scc1 .Lrole_c\n\t"
               "s_lshr_b32 s5, s4, 3\n\tv_lshlrev_b32 v3, 2, %2\n\ts_mov_b32 m0, %4\n\tv_lshl_or_b32 v3, s5, 10, v3\n\ts_getpc_b64 s[6:7]\n\t"
               "v_min_u32 v3, %3, v3\n\tv_lshrrev_b32 v1, 3, %2\n\tglobal_load_lds_dword v3, s[6:7]\n\t"
               "v_and_b32 v2, 6, v1\n\ts_and_b32 s34, s4, 7\n\tv_xor_b32 v2, v2, %2\n\ts_lshl_b32 s34, s34, 5\n\tv_and_b32 v2, 7, v2\n\ts_add_u32 s5, s5, s34\n\t"
               "v_and_b32 v1, 7, v1\n\ts_lshl_b32 s5, s5, 5\n\tv_lshlrev_b32 v1, 11, v1\n\ts_add_u32 s5, s5, s3\n\tv_lshl_or_b32 v1, v2, 4, v1\n\t"
               "s_mul_i32 s34, s3, %5\n\tv_lshl_add_u32 v1, s5, 9, v1\n\ts_add_u32 m0, s34, %6\n\tv_and_b32 v2, 31, %2\n\ts_waitcnt lgkmcnt(0)\n\t"
               XDMA(0) "s_lshl_b32 s5, s3, 5\n\t" XDMA(128) "v_add_lshl_u32 v2, s5, v2, 4\n\t" XDMA(256) "s_lshl_b32 s5, s3, 10\n\t"
               "global_load_lds_dwordx4 v1, s[8:9] offset:384 nt\n\ts_add_u32 m0, s5, %7\n\ts_mov_b32 exec_hi, 0\n\t"
               "global_load_lds_dwordx4 v2, s[10:11]\n\ts_addk_i32 m0, 512\n\ts_nop 0\n\tglobal_load_lds_dwordx4 v2, s[12:13]\n\ts_mov_b32 exec_hi, -1"
               :: "s"(__builtin_amdgcn_kernarg_segment_ptr()), "s"(blockIdx.x), "v"(threadIdx.x), "n"(CODE_B - 256), "n"(O_JUNK), "n"(CHB), "n"(O_U + 512), "n"(O_FAR)
               : "s3", "s4", "s5", "s6", "s7", "s8", "s9", "s10", "s11", "s12", "s13", "s14", "s15", "s16", "s17", "s18", "s19", "s20", "s21", "s22", "s23",
                 "s24", "s25", "s26", "s27", "s28", "s29", "s30", "s31", "s34", "s36", "s37", "s38", "s39", "s40", "s41", "s42", "s43", "v1", "v2", "v3", "scc", "memory");
  int lane, c16, g, w, b, xg, xi, cg, cc, c, d;
  char* ch_;
  q8 ka; q4 kb; q2 kc, kt;
  u32* ctl = (u32*)(smem + O_CTL);
  u32* par = (u32*)(smem + O_PAR);
#define IDS() do { int z_ = 0; OPQ(z_); lane = __builtin_amdgcn_mbcnt_hi(~0u, __builtin_amdgcn_mbcnt_lo(~0u, z_)); c16 = lane & 15; g = lane >> 4; } while (0)
#define ROLE(name) \
  asm volatile(".Lrole_" name ":\n\ts_waitcnt lgkmcnt(0)" : "={s[8:23]}"(ka), "={s[24:31]}"(kb), "={s[36:39]}"(kc), "={s[40:43]}"(kt), "={s3}"(w), "={s4}"(b) :: ALL_OTHER); \
  if (__builtin_expect(w > 11, 0)) asm volatile("s_endpgm" ::: "memory");        \
  IDS(); \
  xg = b & 7; xi = b >> 3;                          \
  cg = xg * 32 + xi;                                \
  cc = (xi >> 4) * 128 + xg * 16 + (xi & 15);       \
  c = w & 3; ch_ = smem + c * CHB; d = cc * 4 + c; \
  const u32 ts0 = (u32)kt[0]; \
  auto wrs = __builtin_amdgcn_make_buffer_rsrc((void*)kc[0], 0, WS_END, 0x00020000); \
  STAMP0()

  const char* tapb;
  const char* ub;
  char* erp;
  const char* eup;
  const char* egp;
  u4v At[17], band[4];
  float dm1;
  f4 acc[8];
  auto conv_setup = [&]() AI {
    tapb = ch_ + O_TAP + ((15 - c16) & 3) * TAP_PITCH + ((15 - c16) & ~3) * 2;
    ub = ch_ + O_U - 32 + c16 * 32 + g * 16;
    erp = ch_ + O_OUT + g * 144 + c16 * 8;
    eup = ch_ + O_U + 512 + c16 * 32 + g * 8;
    egp = smem + O_GT + lane * 16;
  };
  auto band_setup = [&]() AI {
#pragma unroll
    for (int k = 0; k < 4; ++k) {
      const int ctr = k == 0 ? 12 : 8, hw = k + 1;
      const u32 m = ((1u << (2 * hw + 1)) - 1u) << (ctr - hw);
      const u32 bits = (m << c16) >> (8 * g);
#pragma unroll
      for (int j = 0; j < 4; ++j)
        band[k][j] = (__builtin_amdgcn_sbfe(bits, 2 * j, 1) & 0x3f80u) | (__builtin_amdgcn_sbfe(bits, 2 * j + 1, 1) & 0x3f800000u);
      OPQ(band[k]);
    }
  };
  auto frag2 = [&](const char* pa, const char* pb) AI { const u2v lo = lds8(pa), hi = lds8(pb); return u4v{lo[0], lo[1], hi[0], hi[1]}; };
  auto atload = [&](int s0, int s1) AI {
#pragma unroll
    for (int s = s0; s < s1; ++s) At[s] = frag2(tapb + 64 * s + g * 16, tapb + 64 * s + g * 16 + 8);
  };
  auto bin = [&](int pair, int be) AI { const char* p = tapb + g * 8 + (SEG_I + 96 * pair - 16 * be) * 2; return frag2(p, p + 96); };
  auto bout = [&](int be) AI { const char* p = tapb + g * 8 + (SEG_O0 - 16 * be) * 2; return frag2(p, p + (SEG_O1 - SEG_O0) * 2); };
  auto asp = [&]() AI { return frag2(tapb + SEG_SP * 2 + g * 16, tapb + SEG_SP * 2 + g * 16 + 8); };
  auto mma = [&](u4v a, u4v bq, f4 cin) AI { return __builtin_amdgcn_mfma_f32_16x16x32_bf16(as_b8(a), as_b8(bq), cin, 0, 0, 0); };
  auto uld = [&](int sg) AI { return *(const u4v*)(ub + 512 + sg * 64); };
  auto uld24 = [&](int un) AI { return *(const u4v*)(ub + 512 + un * 512 - 16); };
  u2v euw[8], eo[8]; f4 egv[8], efv[8];
  float eu[8][4], ev[8][4], ea[8][4];
  u4v Fq[57], ru[6], wa_[6][3];
  f4 wq[6][6];
  u32 pk[12], one, qa, fa;
  char* tfp; char* fp_;
#define LOPQ(p_) do { u32 a_ = (u32)(uintptr_t)(__attribute__((address_space(3))) const char*)p_; OPQ(a_); p_ = (decltype(p_))(__attribute__((address_space(3))) char*)(uintptr_t)a_; } while (0)
  auto m_setup = [&]() AI {
    tfp = smem + (lane == 0 ? O_CTL + c * 4 : O_JUNK + lane * 4);
    fp_ = smem + O_FAR + c * 4096 + lane * 16;
    qa = CTLA(C_Q) + (lane & 3) * 4; fa = CTLA(C_FAR + c); one = 1u;
    OPQ(qa); OPQ(fa); OPQ(one);
    LOPQ(tfp); LOPQ(fp_); LOPQ(erp); LOPQ(eup); LOPQ(egp); LOPQ(ub);
  };
#define WAITA(addr) do { u32 t_; int n_ = 1 << 20; asm volatile( \
    "1:\n\tds_read_b32 %0, %2\n\ts_sub_u32 %1, %1, 1\n\ts_cbranch_scc1 2f\n\ts_waitcnt lgkmcnt(0)\n\tv_cmp_eq_u32 vcc, 0, %0\n\ts_cbranch_vccnz 1b\n2:\n\ts_waitcnt lgkmcnt(0)" \
    : "=&v"(t_), "+s"(n_) : "v"(addr) : "vcc", "scc", "memory"); } while (0)
#define MF(d_, a_, b_) asm volatile("v_mfma_f32_16x16x32_bf16 %0, %1, %2, %0" : "+v"(d_) : "v"(a_), "v"(b_)); SB()
#define MF0(d_, a_, b_) asm volatile("v_mfma_f32_16x16x32_bf16 %0, %1, %2, 0" : "=&v"(d_) : "v"(a_), "v"(b_)); SB()
#define NOP(n_) asm volatile("s_nop " #n_); SB()
#define FL(i_, sg_) Fq[i_] = uld(sg_); SB()
#define RL(un_) ru[un_] = uld24(un_); SB()
#define AL(s_) atload(s_, s_ + 1); SB()
#define ULD(t_) euw[t_] = *(const u2v*)(eup + t_ * 512); SB()
#define GLD(t_) egv[t_] = *(const f4*)(egp + t_ * 1024); SB()
#define FLD(t_) efv[t_] = *(const f4*)(fp_ + (t_ - 2) * 1024); SB()
#define FST(un_) *(f4*)(fp_ + un_ * 1024) = acc[un_ + 2]; SB()
#define EUN(t_, e_) eu[t_][e_] = (e_ & 1) ? bfhi(euw[t_][e_ >> 1]) : bflo(euw[t_][e_ >> 1]); OPQ(eu[t_][e_]); ev[t_][e_] = eu[t_][e_]; SB()
#define EA1(t_, e_) ea[t_][e_] = fmaf(eu[t_][e_], dm1, acc[t_][e_]); OPQ(ea[t_][e_]); SB()
#define EFA(t_, e_) ev[t_][e_] = fmaf(egv[t_][e_], efv[t_][e_], eu[t_][e_]); OPQ(ev[t_][e_]); SB()
#define EA2(t_, e_) ea[t_][e_] = fmaf(egv[t_][e_], ea[t_][e_], ev[t_][e_]); OPQ(ea[t_][e_]); SB()
#define EPK(t_, k_) { u32 v_ = pk_bf(ea[t_][2 * k_], ea[t_][2 * k_ + 1]); OPQ(v_); eo[t_][k_] = v_; } SB()
#define OST(t_) *(u2v*)(erp + t_ * OT) = eo[t_]; SB()
#define WPK(un_, p_, j_) { u32 v_ = pk_bf(wq[un_][2 * p_ + j_ / 2][2 * (j_ & 1)], wq[un_][2 * p_ + j_ / 2][2 * (j_ & 1) + 1]); OPQ(v_); wa_[un_][p_][j_] = v_; } SB()
#define FLAG(w_) asm volatile("" ::: "memory"); *(u32*)(tfp + (w_) * 4) = one; SB()
#define PEEK(i_, a_) pk[i_] = __hip_atomic_load((__attribute__((address_space(3))) u32*)(uintptr_t)(a_), __ATOMIC_RELAXED, __HIP_MEMORY_SCOPE_WORKGROUP); SB()
#define CHECK(i_, a_) if (__builtin_expect(__builtin_amdgcn_ballot_w64(pk[i_] == 0u) != 0ull, 0)) WAITA(a_); asm volatile("" ::: "memory"); SB()
#define MIX2(nm, nd, nv) _Pragma("unroll") for (int r_ = 0; r_ < (nm); ++r_) { __builtin_amdgcn_sched_group_barrier(0x008, 1, 0); \
    if (nd) __builtin_amdgcn_sched_group_barrier(0x100, (nd), 0); if (nv) __builtin_amdgcn_sched_group_barrier(0x002, (nv), 0); }

  {
    ROLE("x");
    const int xw = w;
    char* img = ch_ + O_U + 512;
    STAMP(11);
    __builtin_amdgcn_s_setprio(2);
    if (xi == 31 && w == 0) { const u32 th = ts0; if (lane == 0) __builtin_amdgcn_raw_buffer_store_b64(u2v{th, ~th}, wrs, WS_HEAD + xg * 8, 0, 16); }
    const char* wp; const char* fp; const char* tp0; const char* tp1; char* pp;
    {
      const int r = lane & 7, q = (lane >> 2) & 3;
      fp = img + r * 128 + ((((lane & 8) / 2 + g) ^ (r & 6)) * 16);
      wp = smem + O_FAR + xw * 1024 + (lane & 1) * 512 + (lane & 4) * 16 + g * 16;
      tp0 = img + q * 128 + (((2 * g + ((lane >> 1) & 1)) ^ (q & 2)) * 16) + (lane & 1) * 8;
      tp1 = img + 512 + q * 128 + (((2 * g + ((lane >> 1) & 1)) ^ ((q & 2) + 4)) * 16) + (lane & 1) * 8;
      pp = smem + ((lane >> 3) == 0 || (lane >> 3) == 3 ? O_PART + r * 64 + xw * 16 + g * 8 : O_JUNK + lane * 8);
    }
    const int so = WS_XT + (32 * xw + (lane >> 3)) * XT_PITCH + cg * 128 + (lane & 7) * 16, sstep = 8 * XT_PITCH;
    u32 lo_ = WS_XT + (cc >> 1) * XT_PITCH + (16 * xw + (lane >> 2)) * 128 + (cc & 1) * 64 + (lane & 3) * 16;
    OPQ(lo_);
    SB();
    BAR();
    STAMP(12);
    asm volatile("s_waitcnt vmcnt(0)" ::: "memory");
    STAMP(1);
    {
      u4v wa[4], fb[4]; u2v ta[4], tb[4];
#pragma unroll
      for (int s = 0; s < 4; ++s) { wa[s] = *(const u4v*)(wp + s * 128); fb[s] = *(const u4v*)(fp + s * 1024); }
      SB();
      f4 r = {0.f, 0.f, 0.f, 0.f};
#pragma unroll
      for (int s = 0; s < 4; ++s) r = mma(wa[s], fb[s], r);
      *(f2*)pp = f2{r[0], r[1]};
      ARRIVE(C_PART);
      SB();
#pragma unroll
      for (int s = 0; s < 4; ++s) { ta[s] = tr_read(tp0 + s * 1024); tb[s] = tr_read(tp1 + s * 1024); }
      SB();
      WAITQ(C_GP, 1);
#pragma unroll
      for (int s = 0; s < 4; ++s) __builtin_amdgcn_raw_buffer_store_b128(u4v{ta[s][0], ta[s][1], tb[s][0], tb[s][1]}, wrs, so + s * sstep, 0, 16);
    }
    STAMP(2);
    const u32 dskw = ((gp16)kb[0])[d];
    conv_setup();
    band_setup();
    const u32 tag = par[0];
    ARRIVE(C_PUB);
    asm volatile("s_waitcnt vmcnt(0)" ::: "memory");
    if (lane == 0) __builtin_amdgcn_raw_buffer_store_b32(tag, wrs, WS_FLG + xw * 1024 + cg * 4, 0, 16);
    STAMP(3);
    dm1 = bflo(dskw) - 1.f;
    WAITS(C_TL, 1);
    const u32 tl0 = (u32)__builtin_amdgcn_readfirstlane((int)par[1]);
    u3v fq[2];
    auto rec_ld = [&]() AI {
#pragma unroll
      for (int k = 0; k < 2; ++k) fq[k] = __builtin_amdgcn_raw_buffer_load_b96(wrs, WS_REC + xw * 2048 + k * 1024 + lane * 16, 0, 16);
    };
    pace(tl0 + DT_REC);
    rec_ld();
    SB();
    {
#pragma unroll 1
      for (int spin = 0; spin < SPIN_MAX && __builtin_expect(__any(fq[0][2] != tag || fq[1][2] != tag), 0); ++spin) {
        __builtin_amdgcn_s_sleep(SLEEP_G);
        rec_ld();
        asm volatile("" ::: "memory");
      }
      char* gp = smem + O_GT + xw * 2048 + (lane & 3) * 256 + (lane >> 2) * 16;
#pragma unroll
      for (int k = 0; k < 2; ++k) *(f4*)(gp + k * 1024) = f4{bflo(fq[k][0]), bfhi(fq[k][0]), bflo(fq[k][1]), bfhi(fq[k][1])};
    }
    pace(tl0 + DT_FLG);
    u4v xq[4];
    bool late[4];
    {
      const u32 fl = __builtin_amdgcn_raw_buffer_load_b32(wrs, WS_FLG + (cc >> 6) * 1024 + xw * 64 + g * 256 + c16 * 4, 0, 16);
      const u64 bad = __builtin_amdgcn_ballot_w64(fl != tag);
      asm volatile("" ::: "memory");
#pragma unroll
      for (int k = 0; k < 4; ++k) {
        late[k] = ((bad >> (16 * k)) & 0xffffull) != 0ull;
        xq[k] = __builtin_amdgcn_raw_buffer_load_b128(wrs, lo_ | (late[k] ? 1u << 30 : 0u), k * 8192, 16);
        SB();
      }
    }
    ARRIVE(C_XL);
    STAMP(4);
    WAITQ(C_TAPS + c, 1);
    u4v Bi[2][3], Bo[4], As;
#pragma unroll
    for (int be = 0; be < 3; ++be) { Bi[0][be] = bin(0, be); Bi[1][be] = bin(1, be); }
#pragma unroll
    for (int be = 0; be < 4; ++be) Bo[be] = bout(be);
    As = asp();
    WAITQ(C_PUB, 4);
    char* dp = smem + (lane & 3) * CHB + O_U + 512 + xw * 256 + (lane >> 2) * 16;
    m_setup();
    auto stage = [&](int k) AI {
      SB();
      if (__builtin_expect(late[k], 0)) {
#pragma unroll 1
        for (int spin = 0; spin < SPIN_MAX; ++spin) {
          STAMP(14);
          const u32 f1 = __builtin_amdgcn_raw_buffer_load_b32(wrs, WS_FLG + (cc >> 6) * 1024 + xw * 64 + k * 256 + c16 * 4, 0, 16);
          if (!__any(f1 != tag)) break;
          __builtin_amdgcn_s_sleep(SLEEP_G);
          asm volatile("" ::: "memory");
        }
        asm volatile("" ::: "memory");
        xq[k] = __builtin_amdgcn_raw_buffer_load_b128(wrs, lo_, k * 8192, 16);
        asm volatile("s_waitcnt vmcnt(0)" : "+v"(xq[k]) :: "memory");
      }
      *(u4v*)(dp + k * 1024) = xq[k];
      FLAG(C_Q + k * 4);
    };
    stage(0);
    stage(1);
    STAMP(5);
    __builtin_amdgcn_s_setprio(0);
    WAITA(qa);
    SB();
    FL(0, 0); RL(0); FL(8, 8); RL(1);
    MF0(wq[0][0], Fq[0], band[0]);
    MF0(wq[0][1], Fq[0], band[1]);
    MF0(wq[0][2], ru[0], band[0]);
    MF0(wq[0][3], ru[0], band[1]);
    MF0(wq[0][4], Fq[0], band[2]);
    MF0(wq[0][5], ru[0], band[3]);
    MF0(acc[4], As, Fq[0]); PEEK(6, qa + 16);
    MF0(wq[1][0], Fq[8], band[0]); WPK(0, 0, 0); WPK(0, 0, 1);
    MF0(wq[1][1], Fq[8], band[1]); WPK(0, 0, 2); WPK(0, 0, 3);
    MF0(wq[1][2], ru[1], band[0]); WPK(0, 1, 0); WPK(0, 1, 1);
    MF0(wq[1][3], ru[1], band[1]); WPK(0, 1, 2); WPK(0, 1, 3);
    MF0(wq[1][4], Fq[8], band[2]); CHECK(6, qa + 16);
    MF0(wq[1][5], ru[1], band[3]); WPK(0, 2, 0); WPK(0, 2, 1);
    MF0(acc[5], As, Fq[8]); WPK(0, 2, 2); WPK(0, 2, 3);
    MF0(acc[2], wa_[0][0], Bi[0][0]); FL(16, 16); RL(2);
    MF(acc[2], wa_[0][1], Bi[1][0]); WPK(1, 0, 0); WPK(1, 0, 1);
    MF0(acc[3], wa_[0][0], Bi[0][1]); WPK(1, 0, 2); WPK(1, 0, 3);
    MF(acc[3], wa_[0][1], Bi[1][1]); WPK(1, 1, 0); WPK(1, 1, 1);
    MF(acc[4], wa_[0][0], Bi[0][2]); FST(0); FLAG(C_FAR + 0 * 4);
    MF(acc[4], wa_[0][1], Bi[1][2]); WPK(1, 1, 2); WPK(1, 1, 3);
    MF(acc[4], wa_[0][2], Bo[0]); WPK(1, 2, 0); WPK(1, 2, 1);
    MF(acc[5], wa_[0][2], Bo[1]); WPK(1, 2, 2); WPK(1, 2, 3);
    MF0(acc[6], wa_[0][2], Bo[2]);
    MF0(acc[7], wa_[0][2], Bo[3]); stage(2);
    MF0(wq[2][0], Fq[16], band[0]);
    MF0(wq[2][1], Fq[16], band[1]); stage(3);
    MF0(wq[2][2], ru[2], band[0]);
    MF0(wq[2][3], ru[2], band[1]);
    MF0(wq[2][4], Fq[16], band[2]); FL(24, 24); RL(3);
    MF0(wq[2][5], ru[2], band[3]); WPK(2, 0, 0); WPK(2, 0, 1);
    MF(acc[6], As, Fq[16]); WPK(2, 0, 2); WPK(2, 0, 3);
    MF(acc[3], wa_[1][0], Bi[0][0]); WPK(2, 1, 0); WPK(2, 1, 1);
    MF(acc[3], wa_[1][1], Bi[1][0]); WPK(2, 1, 2); WPK(2, 1, 3);
    MF(acc[4], wa_[1][0], Bi[0][1]); WPK(2, 2, 0); WPK(2, 2, 1);
    MF(acc[4], wa_[1][1], Bi[1][1]); WPK(2, 2, 2); WPK(2, 2, 3);
    MF(acc[5], wa_[1][0], Bi[0][2]); FST(1); FLAG(C_FAR + 1 * 4);
    MF(acc[5], wa_[1][1], Bi[1][2]); PEEK(7, qa + 32);
    MF(acc[5], wa_[1][2], Bo[0]);
    MF(acc[6], wa_[1][2], Bo[1]);
    MF(acc[7], wa_[1][2], Bo[2]);
    MF0(wq[3][0], Fq[24], band[0]);
    MF0(wq[3][1], Fq[24], band[1]); CHECK(7, qa + 32);
    MF0(wq[3][2], ru[3], band[0]);
    MF0(wq[3][3], ru[3], band[1]);
    MF0(wq[3][4], Fq[24], band[2]); FL(32, 32); RL(4);
    MF0(wq[3][5], ru[3], band[3]); WPK(3, 0, 0); WPK(3, 0, 1);
    MF(acc[7], As, Fq[24]); WPK(3, 0, 2); WPK(3, 0, 3);
    MF(acc[4], wa_[2][0], Bi[0][0]); WPK(3, 1, 0); WPK(3, 1, 1);
    MF(acc[4], wa_[2][1], Bi[1][0]); WPK(3, 1, 2); WPK(3, 1, 3);
    MF(acc[5], wa_[2][0], Bi[0][1]); WPK(3, 2, 0); WPK(3, 2, 1);
    MF(acc[5], wa_[2][1], Bi[1][1]); FL(40, 40); RL(5);
    MF(acc[6], wa_[2][0], Bi[0][2]); FST(2); FLAG(C_FAR + 2 * 4);
    MF(acc[6], wa_[2][1], Bi[1][2]); WPK(3, 2, 2); WPK(3, 2, 3);
    MF(acc[6], wa_[2][2], Bo[0]); AL(0);
    MF(acc[7], wa_[2][2], Bo[1]); AL(1);
    MF0(wq[4][0], Fq[32], band[0]); AL(2);
    MF0(wq[4][1], Fq[32], band[1]); AL(3);
    MF0(wq[4][2], ru[4], band[0]); AL(4);
    MF0(wq[4][3], ru[4], band[1]); WPK(4, 0, 0); WPK(4, 0, 1);
    MF(acc[5], wa_[3][0], Bi[0][0]); WPK(4, 0, 2); WPK(4, 0, 3);
    MF(acc[5], wa_[3][1], Bi[1][0]); WPK(4, 1, 0); WPK(4, 1, 1);
    MF(acc[6], wa_[3][0], Bi[0][1]); WPK(4, 1, 2); WPK(4, 1, 3);
    MF(acc[6], wa_[3][1], Bi[1][1]); AL(5);
    MF(acc[7], wa_[3][0], Bi[0][2]); FST(3); FLAG(C_FAR + 3 * 4);
    MF(acc[7], wa_[3][1], Bi[1][2]); AL(6);
    MF(acc[7], wa_[3][2], Bo[0]); FL(33, 33); AL(7);
    MF0(wq[5][0], Fq[40], band[0]); FL(34, 34); AL(8);
    MF0(wq[5][1], Fq[40], band[1]); FL(35, 35); AL(9);
    MF0(wq[5][2], ru[5], band[0]); WPK(5, 0, 0); WPK(5, 0, 1);
    MF0(wq[5][3], ru[5], band[1]); AL(10);
    MF(acc[6], wa_[4][0], Bi[0][0]); PEEK(8, qa + 48); FL(36, 36);
    MF(acc[6], wa_[4][1], Bi[1][0]); FL(37, 37); WPK(5, 0, 2);
    MF(acc[7], wa_[4][0], Bi[0][1]); FL(38, 38); WPK(5, 0, 3);
    MF(acc[7], wa_[4][1], Bi[1][1]); FL(39, 39); WPK(5, 1, 0);
    MF(acc[6], At[0], Fq[32]); WPK(5, 1, 1); WPK(5, 1, 2);
    MF(acc[6], At[1], Fq[33]); CHECK(8, qa + 48);
    MF(acc[6], At[2], Fq[34]); WPK(5, 1, 3); FL(41, 41);
    MF(acc[6], At[3], Fq[35]); AL(11);
    MF(acc[7], wa_[5][0], Bi[0][0]); FL(42, 42); AL(12);
    MF(acc[7], wa_[5][1], Bi[1][0]); AL(13);
    MF(acc[6], At[4], Fq[36]); FL(43, 43); AL(14);
    MF(acc[6], At[5], Fq[37]); AL(15);
    MF(acc[6], At[6], Fq[38]); FL(44, 44); AL(16);
    MF(acc[6], At[7], Fq[39]);
    MF(acc[6], At[8], Fq[40]); FL(45, 45);
    MF(acc[7], At[0], Fq[40]);
    MF(acc[6], At[9], Fq[41]); FL(46, 46);
    MF(acc[7], At[1], Fq[41]);
    MF(acc[6], At[10], Fq[42]); FL(47, 47);
    MF(acc[7], At[2], Fq[42]);
    MF(acc[6], At[11], Fq[43]); FL(48, 48);
    MF(acc[7], At[3], Fq[43]);
    MF(acc[6], At[12], Fq[44]); FL(49, 49); ULD(6);
    MF(acc[7], At[4], Fq[44]); FL(50, 50); GLD(6);
    MF(acc[6], At[13], Fq[45]); FL(51, 51);
    MF(acc[7], At[5], Fq[45]); FL(52, 52);
    MF(acc[6], At[14], Fq[46]); FL(53, 53);
    MF(acc[7], At[6], Fq[46]); FL(54, 54); EUN(6, 0);
    MF(acc[6], At[15], Fq[47]); FL(55, 55); EUN(6, 1);
    MF(acc[7], At[7], Fq[47]); FL(56, 56); EUN(6, 2);
    MF(acc[6], At[16], Fq[48]); EUN(6, 3);
    MF(acc[7], At[8], Fq[48]); ULD(7); GLD(7);
    MF(acc[7], At[9], Fq[49]);
    MF(acc[7], At[10], Fq[50]);
    MF(acc[7], At[11], Fq[51]);
    MF(acc[7], At[12], Fq[52]); EA1(6, 0); EA1(6, 1); EA1(6, 2); EA1(6, 3);
    MF(acc[7], At[13], Fq[53]); EA2(6, 0); EA2(6, 1); EA2(6, 2); EA2(6, 3);
    MF(acc[7], At[14], Fq[54]); EPK(6, 0); EPK(6, 1);
    MF(acc[7], At[15], Fq[55]); OST(6); FLAG(C_TILE + 6 * 4);
    MF(acc[7], At[16], Fq[56]); EUN(7, 0); EUN(7, 1); EUN(7, 2); EUN(7, 3); NOP(3); EA1(7, 0); EA1(7, 1); EA1(7, 2); EA1(7, 3); EA2(7, 0); EA2(7, 1); EA2(7, 2); EA2(7, 3); EPK(7, 0); EPK(7, 1); OST(7); FLAG(C_TILE + 7 * 4);
    STAMP(9);
    ROLE_END();
  }
  {
    ROLE("h");
    const int hw = w - 5;
    const int j = xg * 28 + xi;
    const int s1 = j >> 2, kq = j & 3;
    const bool own = xi < 28;
    const u64 wbase = hw == 0 ? kb[1] : kb[2];
    const int pitch = F * 2;
    auto hrs = __builtin_amdgcn_make_buffer_rsrc((void*)wbase, 0, own ? D * F * 2 : 0, 0x00020000);
    const int r8 = lane >> 3;
    const int vo = (256 * kq * F + 64 * s1) * 2 + (8 * (r8 >> 2) + (r8 & 3)) * pitch + (((lane & 7) * 16) ^ (((r8 >> 1) & 3) * 32));
    auto dma = [&](int h, int sB, int pt, u32 dst) AI {
      const int m = h >> 1;
#pragma unroll
      for (int i = 0; i < 2; ++i)
        asm volatile("s_mov_b32 m0, %3\n\ts_nop 0\n\tbuffer_load_dwordx4 %0, %1, %2 offen nt lds"
                     :: "v"(vo), "s"(hrs), "s"((m & 1) * 32 * pt + (m >> 1) * sB + (16 * i + 4 * (h & 1)) * pt), "s"(dst + i * 1024) : "memory");
    };
    const int q = c16 >> 2, p = c16 & 3;
    int tro[4];
#pragma unroll
    for (int ct = 0; ct < 4; ++ct) tro[ct] = (4 * g + q) * 128 + ((ct ^ (((4 * g + q) >> 1) & 3)) * 32) + p * 8;
    const int tok = lane & 7;
    u4v wd[2][8];
    auto wd_ld = [&](int k, int cb) AI {
#pragma unroll
      for (int i = 0; i < 8; ++i) {
        const char* pp = smem + O_WD + cb * 8192 + (i >> 2) * 4096 + tro[i & 3];
        const u2v lo = tr_read(pp), hi = tr_read(pp + 2048);
        wd[k][i] = u4v{lo[0], lo[1], hi[0], hi[1]};
      }
    };
    u32 p2o, p2k[4], hlo, hhi;
    auto f2_setup = [&](int cb0) AI {
      p2o = WS_P2 + ((64 * kq + 16 * cb0 + (lane & 8) / 2 + g) * 56 + s1) * 128 + tok * 16;
      hlo = lbase + O_HB + ((lane & 8) ? 512 : g * 128 + tok * 16);
      hhi = lbase + O_HB + ((lane & 8) ? g * 128 + tok * 16 : 512);
#pragma unroll
      for (int i = 0; i < (cb0 == 2 ? 4 : 2); ++i) { p2k[i] = p2o + i * 8 * 7168; OPQ(p2k[i]); }
      OPQ(hlo); OPQ(hhi);
    };
    auto f2_job = [&](int nb, u32 t10) AI {
      u4v hb[4];
      {
        u32 cnt; int n_ = 1 << 20;
        asm volatile("1:\n\tds_read_b32 %4, %6\n\tds_read_b128 %0, %7\n\tds_read_b128 %1, %8\n\tds_read_b128 %2, %7 offset:528\n\tds_read_b128 %3, %8 offset:528\n\t"
                     "s_sub_u32 %5, %5, 1\n\ts_cbranch_scc1 2f\n\ts_waitcnt lgkmcnt(0)\n\tv_cmp_gt_u32 vcc, 2, %4\n\ts_cbranch_vccnz 1b\n2:\n\ts_waitcnt lgkmcnt(0)"
                     : "=&v"(hb[0]), "=&v"(hb[1]), "=&v"(hb[2]), "=&v"(hb[3]), "=&v"(cnt), "+s"(n_) : "v"(CTLA(C_HRDY)), "v"(hlo), "v"(hhi) : "vcc", "scc", "memory");
      }
      STAMP(11);
      f4 r[4];
#pragma unroll
      for (int i = 0; i < 2 * nb; ++i) {
        const u4v* wp = &wd[i >> 1][(i & 1) * 2];
        r[i] = mma(wp[0], hb[0], f4{0.f, 0.f, 0.f, 0.f});
        r[i] = mma(wp[1], hb[1], r[i]);
        r[i] = mma(wp[4], hb[2], r[i]);
        r[i] = mma(wp[5], hb[3], r[i]);
      }
      SB();
#pragma unroll
      for (int i = 0; i < 2 * nb; ++i) {
        u4v v = __builtin_bit_cast(u4v, r[i]);
#pragma unroll
        for (int e = 0; e < 4; ++e) v[e] = __builtin_amdgcn_bitop3_b32(v[e], t10, TMASK, 0xd8);
        __builtin_amdgcn_raw_buffer_store_b128(v, wrs, p2k[i], 0, 16);
      }
      STAMP(12);
    };
    const u32 rb = lbase + O_RING + hw * (NSLOT * 2048);
    pace(ts0 + WDELAY);
    STAMP(2);
#pragma unroll
    for (int h = 0; h < WWIN; ++h) dma(h, 64 * F * 2, F * 2, rb + h * 2048);
    __builtin_amdgcn_s_setprio(3);
    BAR();
    STAMP(3);
    if (__builtin_expect(own, 1)) {
      u4v wv[32];
      f4 a1[4] = {f4{0.f, 0.f, 0.f, 0.f}, f4{0.f, 0.f, 0.f, 0.f}, f4{0.f, 0.f, 0.f, 0.f}, f4{0.f, 0.f, 0.f, 0.f}};
      u4v xb[8];
      auto f1 = [&](int k0, int k1) AI {
#pragma unroll
        for (int k = k0; k < k1; ++k)
#pragma unroll
          for (int ct = 0; ct < 4; ++ct) a1[ct] = mma(wv[4 * k + ct], xb[k], a1[ct]);
      };
      u32 xo = lbase + O_XS + tok * XS_PITCH + g * 16;
      u32 p1o = WS_P1 + s1 * 16384 + kq * 4096 + hw * 2048 + (lane & 8) * 64 + g * 128 + tok * 16;
      u32 t10, tpub;
      OPQ(xo); OPQ(p1o);
      constexpr int HX = (16 - WWIN + 1) & ~1;
#pragma unroll
      for (int h = 0; h < 16; ++h) {
        const int left = (h + WWIN < 16 ? WWIN : 16 - h) - 1;
        asm volatile("s_waitcnt vmcnt(%0)" :: "n"(2 * left) : "memory");
        if (h + WWIN < 16) {
          asm volatile("s_waitcnt lgkmcnt(0)" ::: "memory");
          dma(h + WWIN, 64 * F * 2, F * 2, rb + ((h + WWIN) % NSLOT) * 2048);
        }
#pragma unroll
        for (int ct = 0; ct < 4; ++ct) {
          const u2v t = tr_read(smem + O_RING + hw * (NSLOT * 2048) + (h % NSLOT) * 2048 + tro[ct]);
          wv[4 * (h >> 1) + ct][2 * (h & 1)] = t[0]; wv[4 * (h >> 1) + ct][2 * (h & 1) + 1] = t[1];
        }
        if (h == H2_AT) ARRIVE(C_WGU);
        if (h == HX - 1) {
          STAMP(4);
          WAITQ(C_XS, 1);
          STAMP(1);
#pragma unroll
          for (int k = 0; k < 8; ++k) xb[k] = *(const __attribute__((address_space(3))) u4v*)(uintptr_t)(xo + k * 64);
          t10 = par[0]; tpub = par[2];
          SB();
          f1(0, 2);
          SB();
        }
        if (h >= HX) { f1(h - HX + 2, h - HX + 3); SB(); }
      }
      STAMP(5);
      f1(18 - HX, 8);
      t10 &= TMASK;
#pragma unroll
      for (int a = 0; a < 2; ++a) {
        u4v v;
#pragma unroll
        for (int e = 0; e < 4; ++e)
          v[e] = ((u32)__builtin_amdgcn_update_dpp(__float_as_int(a1[2 * a][e]), __float_as_int(a1[2 * a + 1][e]), 0xe4, 0xf, 0xc, false) & ~TMASK) | t10;
        __builtin_amdgcn_raw_buffer_store_b128(v, wrs, p1o + a * 1024, 0, 16);
      }
      STAMP(6);
      const u32 tn = now32();
      u32 voff = WS_P1 + s1 * 16384 + hw * 1024 + lane * 16;
      u32 ho = lbase + O_HB + hw * 528 + g * 128 + tok * 16 + (lane & 8);
      float nl2e = -1.44269504088896341f;
      u32 wda = CTLA(C_WD) + hw * 4;
      OPQ(voff); OPQ(ho); OPQ(nl2e); OPQ(wda);
      f2_setup(hw & 1);
      pace2(tn, (u32)__builtin_amdgcn_readfirstlane((int)tpub) + DE_P1, DT_P1);
      u4v pb[8];
      auto p1_ld = [&]() AI {
#pragma unroll
        for (int i = 0; i < 8; ++i) { pb[i] = __builtin_amdgcn_raw_buffer_load_b128(wrs, voff + (i >> 2) * 2048, (i & 3) * 4096, 16); SB(); }
      };
      p1_ld();
      SB();
      f4 G, U;
      bool early = false;
#pragma unroll 1
      for (int spin = 0; spin < SPIN_MAX; ++spin) {
        u32 bad = 0;
        OPQ(bad);
        asm volatile("" :: "v"(pb[0]));
        const u32 cw = __hip_atomic_load((__attribute__((address_space(3))) u32*)(uintptr_t)wda, __ATOMIC_RELAXED, __HIP_MEMORY_SCOPE_WORKGROUP);
        G = tsum4(bad, t10, pb[0], pb[1], pb[2], pb[3]);
        early = __builtin_amdgcn_ballot_w64(cw == 0u) == 0ull;
        asm volatile("" ::: "memory");
        if (__builtin_expect(early, 1)) wd_ld(0, hw);
        SB();
        silu4(G, nl2e);
        U = tsum4(bad, t10, pb[4], pb[5], pb[6], pb[7]);
        if (__builtin_expect(!__any((bad & TMASK) != 0), 1)) break;
        STAMP(14);
        p1_ld();
        asm volatile("" ::: "memory");
      }
      STAMP(7);
      {
        float hv[4];
#pragma unroll
        for (int e = 0; e < 4; ++e) { hv[e] = G[e] * U[e]; asm("" : "+v"(hv[e])); }
        *(__attribute__((address_space(3))) u2v*)(uintptr_t)ho = u2v{pk_bf(hv[0], hv[1]), pk_bf(hv[2], hv[3])};
      }
      ARRIVE(C_HRDY);
      STAMP(8);
      if (__builtin_expect(!early, 0)) { WAITA(wda); wd_ld(0, hw); }
      f2_job(1, t10);
    }
    if (hw == 0) {
      WAITS(C_SEL, 1);
      const u32 t10 = par[0] & TMASK;
      const u32 tpub = (u32)__builtin_amdgcn_readfirstlane((int)par[2]);
      const u32 mykey = ((const u32*)(smem + O_SEL))[lane & 7];
      if (own) pace2(now32(), tpub + DE_P2, DT_P2); else pace(tpub + DE_P2 - 40);
      u4v pa[7];
      const int voff = WS_P2 + cc * 7168 + lane * 16;
#pragma unroll
      for (int k = 0; k < 7; ++k) pa[k] = __builtin_amdgcn_raw_buffer_load_b128(wrs, voff + (k & 3) * 1024, (k >> 2) * 4096, 16);
      SB();
      const int row = (int)(~mykey & 2047u);
      const float rsig = rb1(sigm(__uint_as_float((mykey ^ (~(u32)((int)mykey >> 31) | 0x80000000u)) & 0xffff0000u)));
      WAITA(CTLA(C_TILE) + (lane & 31) * 4);
      float cf_ = rsig * (1.f - *(const float*)(smem + O_GT + (row >> 8) * 1024 + ((row >> 2) & 3) * 256 + ((row >> 4) & 15) * 16 + (row & 3) * 4));
      float base[4];
#pragma unroll
      for (int e = 0; e < 4; ++e) base[e] = bflo(*(const u16*)(smem + e * CHB + O_OUT + (row >> 8) * OT + ((row >> 2) & 3) * 144 + ((row >> 4) & 15) * 8 + (row & 3) * 2));
      u64 op = kc[1] + (row * D + cc * 4) * 2;
      { float cq = cf_; OPQ(cq); cf_ = cq; OPQ(op); }
#pragma unroll
      for (int e = 0; e < 4; ++e) OPQ(base[e]);
      STAMP(13);
      f4 y;
#pragma unroll 1
      for (int spin = 0; spin < SPIN_MAX; ++spin) {
        u32 bad = 0;
        OPQ(bad);
        y = tsum4(bad, t10, pa[0], pa[1], pa[2], pa[3]);
        y = tsum3(bad, t10, y, pa[4], pa[5], pa[6]);
        if (__builtin_expect(!__any((bad & TMASK) != 0), 1)) break;
        STAMP(15);
#pragma unroll
        for (int k = 0; k < 7; ++k) pa[k] = __builtin_amdgcn_raw_buffer_load_b128(wrs, voff + (k & 3) * 1024, (k >> 2) * 4096, 16);
        asm volatile("" ::: "memory");
      }
      STAMP(9);
      float o[4];
#pragma unroll
      for (int e = 0; e < 4; ++e) {
        float v = y[e] + dpp_f<0x128>(y[e]);
        auto s16 = __builtin_amdgcn_permlane16_swap(__float_as_uint(v), __float_as_uint(v), false, false);
        v = __uint_as_float(s16[0]) + __uint_as_float(s16[1]);
        auto s32 = __builtin_amdgcn_permlane32_swap(__float_as_uint(v), __float_as_uint(v), false, false);
        o[e] = fmaf(__uint_as_float(s32[0]) + __uint_as_float(s32[1]), cf_, base[e]);
      }
      if (lane < 8) *(__attribute__((address_space(1))) u2v*)op = u2v{pk_bf(o[0], o[1]), pk_bf(o[2], o[3])};
    }
    ROLE_END();
  }
  {
    ROLE("g");
    const u32 pLA = __builtin_nontemporal_load((gp32)ka[3] + d * (NM / 2) + lane);
    const u32 pAI = __builtin_nontemporal_load((gp32)ka[4] + d * (NM / 2) + lane);
    const u32 pCR = __builtin_nontemporal_load((gp32)ka[5] + d * (NM / 2) + lane);
    const u32 pCI = __builtin_nontemporal_load((gp32)ka[6] + d * (NM / 2) + lane);
    const u32 ldt = ((gp16)ka[7])[d], dskw = ((gp16)kb[0])[d];
    u4v ixa[2];
#pragma unroll
    for (int r = 0; r < 2; ++r) ixa[r] = *(const u4v*)(g_tapidx.v + lane * 16 + r * 8);
    BAR();
    asm volatile("s_mov_b32 m0, %0\n\ts_nop 0" :: "s"(lbase + c * CHB) : "memory");
#define PUT1(off, val) asm volatile("ds_write_addtid_b32 %0 offset:%1" :: "v"(val), "n"(off) : "memory")
#define PUTR(off, val) do { const ch v_ = (val); asm volatile("ds_write_addtid_b32 %0 offset:%2\n\tds_write_addtid_b32 %1 offset:%3" :: "v"(v_.r), "v"(v_.i), "n"(off), "n"((off) + 256) : "memory"); } while (0)
    const char* pf = ch_ + c16 * TROW + g * 16;
    PUT1(O_TAP, 0x3c003c00u); PUT1(O_TAP + 256, 0u);
    PUT1(O_U, 0u); PUT1(O_U + 256, 0u);
    SB();
    const float dt = rb1(exp_bf(bflo(ldt)));
    const f2 a2 = f2{-rb1(exp_bf(bflo(pLA))), -rb1(exp_bf(bfhi(pLA)))} * dt * 1.44269504088896341f;
    const f2 br = unbf(pAI) * dt * 0.15915494309189535f;
    STAMP(1);
    u4v Lf[2][8], Wf[8];
    f4 kk[2][2];
    float fk;
    {
      const cf z1 = cexp_p(a2, br, 1.f);
      const cf z2 = csq(z1), z4 = csq(z2), z8 = csq(z4), z16 = csq(z8), z32 = csq(z16), z64 = csq(z32);
      ch base[4], anc[4];
      base[1] = tohc(z8); base[2] = tohc(z16); base[3] = tohc(cmul(z16, z8));
      anc[1] = tohc(z32); anc[2] = tohc(z64); anc[3] = tohc(cmul(z64, z32));
      PUTR(O_OUT + 5 * TROW, tohc(csq(z64)));
#pragma unroll
      for (int j = 1; j < 16; ++j) {
        const int q = j >> 2, r = j & 3;
        PUTR(O_TAP + j * TROW, q == 0 ? base[r] : r == 0 ? anc[q] : hmul(anc[q], base[r]));
      }
#pragma unroll
      for (int s = 0; s < 8; ++s) Lf[0][s] = *(const u4v*)(pf + O_TAP + s * 64);
#pragma unroll
      for (int s = 0; s < 8; ++s) Lf[1][s] = *(const u4v*)(ch_ + O_OUT + 5 * TROW + g * 16 + s * 64);
      SB();
      STAMP(11);
      u32 mx = max(max(pCR << 17, pCR << 1), max(pCI << 17, pCI << 1));
      mx = wave_max_u32(mx);
      int ex = (int)(mx >> 24);
      ex = ex < 1 ? 1 : (ex > 253 ? 253 : ex);
      const float sdn = __uint_as_float((u32)(254 - ex) << 23);
      fk = 2.f * dt * __uint_as_float((u32)ex << 23);
      cf C; C.r = unbf(pCR) * sdn; C.i = unbf(pCI) * sdn;
      const cf E = cexp_p(a2, br, 256.f);
      const ch y1 = toh(z1), y2 = toh(z2), y4 = toh(z4), y6 = toh(cmul(z4, z2));
      {
        const ch R0 = toh(C), R2 = hmul(R0, y2), R4 = hmul(R0, y4), R6 = hmul(R0, y6);
        PUTR(O_TAP, R0); PUTR(O_TAP + 2 * TROW, R2); PUTR(O_TAP + 4 * TROW, R4); PUTR(O_TAP + 6 * TROW, R6);
        PUTR(O_TAP + 1 * TROW, hmul(R0, y1)); PUTR(O_TAP + 3 * TROW, hmul(R2, y1)); PUTR(O_TAP + 5 * TROW, hmul(R4, y1)); PUTR(O_TAP + 7 * TROW, hmul(R6, y1));
        C = cmul(C, E);
        const ch R8 = toh(C);
        PUTR(O_TAP + 8 * TROW, R8); PUTR(O_TAP + 9 * TROW, hmul(R8, y2)); PUTR(O_TAP + 10 * TROW, hmul(R8, y4)); PUTR(O_TAP + 11 * TROW, hmul(R8, y6));
        C = cmul(C, E);
        const ch R12 = toh(C);
        PUTR(O_TAP + 12 * TROW, R12); PUTR(O_TAP + 13 * TROW, hmul(R12, y4));
        C = cmul(C, E);
        const ch R14 = toh(C);
        PUTR(O_TAP + 14 * TROW, R14); PUTR(O_TAP + 15 * TROW, hmul(R14, y4));
      }
#pragma unroll
      for (int s = 0; s < 8; ++s) Wf[s] = *(const u4v*)(pf + O_TAP + s * 64);
      SB();
      STAMP(12);
#pragma unroll
      for (int i = 0; i < 5; ++i) { C = cmul(C, E); PUTR(O_OUT + i * TROW, toh(C)); }
      SB();
      auto mh = [&](u4v a, u4v bq, f4 cin) AI { return __builtin_amdgcn_mfma_f32_16x16x32_f16(as_h8(a), as_h8(bq), cin, 0, 0, 0); };
      kk[0][0] = f4{0.f, 0.f, 0.f, 0.f}; kk[0][1] = kk[0][0]; kk[1][0] = kk[0][0]; kk[1][1] = kk[0][0];
#pragma unroll
      for (int s = 0; s < 5; ++s) {
#pragma unroll
        for (int k = 0; k < 4; ++k) {
          if (k < 2) { if (s < 4) kk[0][0] = mh(Wf[s + 4 * k], Lf[0][s + 4 * k], kk[0][0]); }
          else if (s > 0) { const int u = s - 1 + 4 * (k - 2); kk[0][1] = mh(Wf[u], Lf[1][u], kk[0][1]); Wf[u] = *(const u4v*)(pf + O_OUT + u * 64); }
          SB();
          if (s < 4) {
            const ch o = hmul(ch{ash(Lf[0][s][k]), ash(Lf[0][s + 4][k])}, ch{ash(Lf[1][s][k]), ash(Lf[1][s + 4][k])});
            Lf[1][s][k] = __builtin_bit_cast(u32, o.r); Lf[1][s + 4][k] = __builtin_bit_cast(u32, o.i);
          }
          if (s == 3 && k == 3) asm volatile("s_nop 1");
          SB();
        }
      }
#pragma unroll
      for (int s = 0; s < 8; ++s) { kk[1][0] = mh(Wf[s], Lf[0][s], kk[1][0]); kk[1][1] = mh(Wf[s], Lf[1][s], kk[1][1]); }
    }
    {
      u32 ia[16];
      const u32 kb_ = lbase + c * CHB + O_OUT;
#pragma unroll
      for (int e = 0; e < 16; ++e) { const u32 iw = ixa[e >> 3][(e >> 1) & 3]; ia[e] = kb_ + ((e & 1) ? (iw >> 16) : (iw & 0xffffu)); }
      MIX2(16, 0, 1)
      SB();
      STAMP(2);
      char* ktp = ch_ + O_OUT + c16 * KT_PITCH + g * 8;
#pragma unroll
      for (int hf = 1; hf >= 0; --hf) {
#pragma unroll
        for (int ct = 0; ct < 2; ++ct) {
          const f4 v = kk[hf][ct] * fk;
          *(u32*)(ktp + ct * 16 * KT_PITCH + hf * 32) = pk_bf(v[0], v[1]);
          *(u32*)(ktp + ct * 16 * KT_PITCH + hf * 32 + 4) = pk_bf(v[2], v[3]);
        }
        asm volatile("" ::: "memory");
      }
      u32 zz = 0u; OPQ(zz);
      *(u32*)(ch_ + O_OUT + KT_ZERO + (lane & 7) * 4) = zz;
      asm volatile("" ::: "memory");
      u32 v[16], pe[9];
#pragma unroll
      for (int e = 15; e >= 0; --e) v[e] = *(const __attribute__((address_space(3))) u16*)(uintptr_t)ia[e];
      SB();
      pe[8] = zz;
#pragma unroll
      for (int i = 7; i >= 0; --i) {
        pe[i] = v[2 * i] | (v[2 * i + 1] << 16);
        const u32 nx = (u32)__builtin_amdgcn_update_dpp((int)dpp_u<0x134>(pe[i + 1]), (int)pe[i], 0x130, 0xf, 0xf, false);
        const u32 po = __builtin_amdgcn_alignbyte(nx, pe[i], 2);
        PUT1(O_TAP + i * 256, pe[i]); PUT1(O_TAP + 2 * TAP_PITCH - 4 + i * 256, pe[i]);
        PUT1(O_TAP + TAP_PITCH + i * 256, po); PUT1(O_TAP + 3 * TAP_PITCH - 4 + i * 256, po);
      }
      ARRIVE(C_TAPS + c);
    }
    STAMP(3);
    IDS();
    conv_setup();
    dm1 = bflo(dskw) - 1.f;
    atload(0, 17);
    m_setup();
    __builtin_amdgcn_s_setprio(1);
    SB();
    WAITA(qa);
    STAMP(4);
    SB();
    FL(0, -7); FL(1, -6); FL(2, -5); FL(3, -4); FL(4, -3); FL(5, -2); FL(6, -1); FL(7, 0); ULD(0); GLD(0);
    MF0(acc[0], At[9], Fq[0]); EUN(0, 0); EUN(0, 1);
    MF(acc[0], At[10], Fq[1]); EUN(0, 2); EUN(0, 3);
    MF(acc[0], At[11], Fq[2]);
    MF(acc[0], At[12], Fq[3]);
    MF(acc[0], At[13], Fq[4]);
    MF(acc[0], At[14], Fq[5]);
    MF(acc[0], At[15], Fq[6]); FL(8, 1);
    MF(acc[0], At[16], Fq[7]);
    MF0(acc[1], At[1], Fq[0]); FL(9, 2);
    MF(acc[1], At[2], Fq[1]);
    MF(acc[1], At[3], Fq[2]); FL(10, 3);
    MF(acc[1], At[4], Fq[3]);
    MF(acc[1], At[5], Fq[4]); FL(11, 4); EA1(0, 0);
    MF(acc[1], At[6], Fq[5]); EA1(0, 1); EA1(0, 2);
    MF(acc[1], At[7], Fq[6]); EA1(0, 3); PEEK(0, qa + 16);
    MF(acc[1], At[8], Fq[7]); FL(12, 5); EA2(0, 0);
    MF0(acc[2], At[0], Fq[7]); EA2(0, 1); EA2(0, 2);
    MF(acc[1], At[9], Fq[8]); EA2(0, 3); EPK(0, 0);
    MF(acc[2], At[1], Fq[8]); EPK(0, 1); FL(13, 6);
    MF(acc[1], At[10], Fq[9]); OST(0); FLAG(C_TILE + 0 * 4);
    MF(acc[2], At[2], Fq[9]); FL(14, 7); FL(15, 8);
    MF(acc[1], At[11], Fq[10]); CHECK(0, qa + 16);
    MF(acc[2], At[3], Fq[10]);
    MF(acc[1], At[12], Fq[11]); FL(16, 9);
    MF(acc[2], At[4], Fq[11]); ULD(1); GLD(1);
    MF(acc[1], At[13], Fq[12]); FL(17, 10);
    MF(acc[2], At[5], Fq[12]);
    MF(acc[1], At[14], Fq[13]); FL(18, 11);
    MF(acc[2], At[6], Fq[13]);
    MF(acc[1], At[15], Fq[14]); FL(19, 12); EUN(1, 0);
    MF(acc[2], At[7], Fq[14]); EUN(1, 1); EUN(1, 2);
    MF(acc[1], At[16], Fq[15]); FL(20, 13); EUN(1, 3);
    MF(acc[2], At[8], Fq[15]);
    MF0(acc[3], At[0], Fq[15]); FL(21, 14);
    MF(acc[2], At[9], Fq[16]);
    MF(acc[3], At[1], Fq[16]); FL(22, 15); EA1(1, 0);
    MF(acc[2], At[10], Fq[17]); EA1(1, 1); EA1(1, 2);
    MF(acc[3], At[2], Fq[17]); FL(23, 16); EA1(1, 3);
    MF(acc[2], At[11], Fq[18]); PEEK(2, fa + 0); EA2(1, 0);
    MF(acc[3], At[3], Fq[18]); EA2(1, 1); EA2(1, 2);
    MF(acc[2], At[12], Fq[19]); EA2(1, 3); EPK(1, 0);
    MF(acc[3], At[4], Fq[19]); EPK(1, 1); FL(24, 17);
    MF(acc[2], At[13], Fq[20]); OST(1); FLAG(C_TILE + 1 * 4);
    MF(acc[3], At[5], Fq[20]); FL(25, 18); CHECK(2, fa + 0);
    MF(acc[2], At[14], Fq[21]); FL(26, 19); ULD(2);
    MF(acc[3], At[6], Fq[21]); FLD(2); GLD(2);
    MF(acc[2], At[15], Fq[22]); FL(27, 20); PEEK(1, qa + 32);
    MF(acc[3], At[7], Fq[22]);
    MF(acc[2], At[16], Fq[23]); FL(28, 21);
    MF(acc[3], At[8], Fq[23]); EUN(2, 0); EUN(2, 1);
    MF0(acc[4], At[0], Fq[23]); FL(29, 22); EUN(2, 2);
    MF(acc[3], At[9], Fq[24]); EUN(2, 3); EA1(2, 0);
    MF(acc[4], At[1], Fq[24]); FL(30, 23); EA1(2, 1);
    MF(acc[3], At[10], Fq[25]); EA1(2, 2); EA1(2, 3);
    MF(acc[4], At[2], Fq[25]); FL(31, 24); EFA(2, 0);
    MF(acc[3], At[11], Fq[26]); EFA(2, 1); EFA(2, 2);
    MF(acc[4], At[3], Fq[26]); EFA(2, 3); PEEK(3, fa + 16);
    MF(acc[3], At[12], Fq[27]); EA2(2, 0); EA2(2, 1);
    MF(acc[4], At[4], Fq[27]); EA2(2, 2); EA2(2, 3);
    MF(acc[3], At[13], Fq[28]); CHECK(1, qa + 32); EPK(2, 0); EPK(2, 1);
    MF(acc[4], At[5], Fq[28]); FL(32, 25); OST(2);
    MF(acc[3], At[14], Fq[29]); FLAG(C_TILE + 2 * 4); FL(33, 26);
    MF(acc[4], At[6], Fq[29]); FL(34, 27); CHECK(3, fa + 16);
    MF(acc[3], At[15], Fq[30]); ULD(3); FL(35, 28);
    MF(acc[4], At[7], Fq[30]); FLD(3); GLD(3);
    MF(acc[3], At[16], Fq[31]); FL(36, 29);
    MF(acc[4], At[8], Fq[31]);
    MF0(acc[5], At[0], Fq[31]); FL(37, 30);
    MF(acc[4], At[9], Fq[32]); EUN(3, 0); EUN(3, 1);
    MF(acc[5], At[1], Fq[32]); PEEK(4, fa + 32); FL(38, 31);
    MF(acc[4], At[10], Fq[33]); EUN(3, 2); EUN(3, 3);
    MF(acc[5], At[2], Fq[33]); FL(39, 32); EA1(3, 0);
    MF(acc[4], At[11], Fq[34]); EA1(3, 1); EA1(3, 2);
    MF(acc[5], At[3], Fq[34]); EA1(3, 3); EFA(3, 0);
    MF(acc[4], At[12], Fq[35]); EFA(3, 1); EFA(3, 2); EFA(3, 3);
    MF(acc[5], At[4], Fq[35]); EA2(3, 0); EA2(3, 1); EA2(3, 2); EA2(3, 3);
    MF(acc[4], At[13], Fq[36]); FL(40, 33); EPK(3, 0); EPK(3, 1);
    MF(acc[5], At[5], Fq[36]); FL(41, 34); OST(3);
    MF(acc[4], At[14], Fq[37]); FL(42, 35); FLAG(C_TILE + 3 * 4); CHECK(4, fa + 32); ULD(4);
    MF(acc[5], At[6], Fq[37]); FL(43, 36); FLD(4); GLD(4); PEEK(5, fa + 48);
    MF(acc[4], At[15], Fq[38]); FL(44, 37); FL(45, 38);
    MF(acc[5], At[7], Fq[38]); FL(46, 39); FL(47, 40);
    MF(acc[4], At[16], Fq[39]);
    MF(acc[5], At[8], Fq[39]); EUN(4, 0); EUN(4, 1);
    MF(acc[5], At[9], Fq[40]); EUN(4, 2); EUN(4, 3);
    MF(acc[5], At[10], Fq[41]); EFA(4, 0); EA1(4, 0);
    MF(acc[5], At[11], Fq[42]); EA1(4, 1); EA1(4, 2);
    MF(acc[5], At[12], Fq[43]); EA1(4, 3); EFA(4, 1); EFA(4, 2); EFA(4, 3); CHECK(5, fa + 48); ULD(5);
    MF(acc[5], At[13], Fq[44]); EA2(4, 0); EA2(4, 1); EA2(4, 2); EA2(4, 3); FLD(5); GLD(5);
    MF(acc[5], At[14], Fq[45]); EPK(4, 0); EPK(4, 1);
    MF(acc[5], At[15], Fq[46]); OST(4); FLAG(C_TILE + 4 * 4);
    MF(acc[5], At[16], Fq[47]); EUN(5, 0); EUN(5, 1); EUN(5, 2); EUN(5, 3); EFA(5, 0); EFA(5, 1); EFA(5, 2); EFA(5, 3); EA1(5, 0); EA1(5, 1); EA1(5, 2); EA1(5, 3); EA2(5, 0); EA2(5, 1); EA2(5, 2); EA2(5, 3); EPK(5, 0); EPK(5, 1); OST(5); FLAG(C_TILE + 5 * 4);
    STAMP(8);
    ROLE_END();
  }
  {
    ROLE("c");
    __builtin_amdgcn_s_setprio(3);
    const u32 ep = *(gp32)(kc[0] + WS_EPOCH);
    {
      u32 zz = 0u; OPQ(zz);
      *(u2v*)(ctl + 2 * lane) = u2v{zz, zz};
      if (lane < 2) *(u4v*)(smem + O_HB + 512 + lane * 528) = u4v{zz, zz, zz, zz};
    }
    BAR();
    u32* selw = (u32*)(smem + O_SEL);
    u32 tag = (u32)__builtin_amdgcn_readfirstlane((int)ep) + 1u;
    tag += tag == 0u;
    if (lane == 0) par[0] = tag;
    {
      const u32 pa = lbase + O_PART + (lane & 7) * 64;
      const u32 ib = 2047u - (u32)(cg * 8 + (lane & 7));
      f4 p0, p1, p2, p3; u32 cnt; int n_ = 1 << 20; unsigned long long tk;
      asm volatile("1:\n\tds_read_b32 %4, %7\n\tds_read_b128 %0, %8\n\tds_read_b128 %1, %8 offset:16\n\tds_read_b128 %2, %8 offset:32\n\tds_read_b128 %3, %8 offset:48\n\t"
                   "s_memrealtime %6\n\ts_sub_u32 %5, %5, 1\n\ts_cbranch_scc1 2f\n\ts_waitcnt lgkmcnt(0)\n\tv_cmp_gt_u32 vcc, 4, %4\n\ts_cbranch_vccnz 1b\n2:\n\ts_waitcnt lgkmcnt(0)"
                   : "=&v"(p0), "=&v"(p1), "=&v"(p2), "=&v"(p3), "=&v"(cnt), "+s"(n_), "=&s"(tk) : "v"(CTLA(C_PART)), "v"(pa) : "vcc", "scc", "memory");
      const f4 s4_ = (p0 + p1) + (p2 + p3);
      const u32 rp = pk_bf(s4_[1] + s4_[3], s4_[0] + s4_[2]);
      const u32 key = ((rp ^ ((u32)((int)rp >> 31) | 0x80000000u)) & 0xffff0000u) | ib;
      const u32 kn = dpp_u<0xb1>(key);
      u32 m1 = max(key, kn), m2 = min(key, kn), m3;
      { const u32 o1 = dpp_u<0x4e>(m1), o2 = dpp_u<0x4e>(m2); const u32 lo = min(m1, o1), hb = max(m2, o2); m1 = max(m1, o1); m2 = max(lo, hb); m3 = min(lo, hb); }
      { const u32 o1 = dpp_u<0x141>(m1), o2 = dpp_u<0x141>(m2), o3 = dpp_u<0x141>(m3); const u32 lo = min(m1, o1), hb = max(m2, o2);
        m1 = max(m1, o1); m2 = max(lo, hb); m3 = max(min(lo, hb), max(m3, o3)); }
      const u32 wm = (((u32)tk + 8u) >> 2) & 0xffu;
      if (lane == 0) __builtin_amdgcn_raw_buffer_store_b128(u4v{m1, m2, (m3 & 0xffff0000u) | wm, tag}, wrs, WS_KEY + cg * 16, 0, 16);
      SB();
      STAMP(1);
      ARRIVE(C_GP);
      {
        const float gv = sigm(bflo(rp));
        const u32 g2 = pk_bf(gv, dpp_f<0xb1>(gv));
        u32 g3 = dpp_u<0x4e>(g2);
        OPQ(g3);
        if ((lane & 59) == 0) __builtin_amdgcn_raw_buffer_store_b128(u4v{g2, g3, tag, tag}, wrs, WS_REC + cg * 32 + lane * 4, 0, 16);
      }
      if ((lane & 57) == 0) __builtin_amdgcn_raw_buffer_store_b128(u4v{key, kn, 0u, tag}, wrs, WS_KALL + cg * 64 + lane * 8, 0, 16);
    }
    u32 l7 = (lane & 7) * 16, ko = WS_KEY + lane * 16;
    OPQ(l7); OPQ(ko);
    WAITQ(C_TL, 1);
    STAMP(5);
    const u32 tkey = (u32)__builtin_amdgcn_readfirstlane((int)par[1]) + DT_KEY;
    pace(tkey);
    u4v gr[4];
    u32 mykey;
    u4v xs[4];
    auto xrs = __builtin_amdgcn_make_buffer_rsrc((void*)(ka[0] + (xi & 3) * 512), 0, L * D * 2 - 1536, 0x00020000);
    asm volatile("" : "+s"(xrs));
    auto xs_ld = [&]() AI {
      const u32 vo = ((~mykey & 2047u) << 11) | l7;
#pragma unroll
      for (int k = 0; k < 4; ++k) xs[k] = __builtin_amdgcn_raw_buffer_load_b128(xrs, vo + k * 128, 0, 0);
    };
#pragma unroll
    for (int i = 0; i < 4; ++i) gr[i] = __builtin_amdgcn_raw_buffer_load_b128(wrs, ko + i * 1024, 0, 16);
#pragma unroll 1
    for (int spin = 0; spin < SPIN_MAX; ++spin) {
      const u32 bad = (gr[0][3] ^ tag) | (gr[1][3] ^ tag) | (gr[2][3] ^ tag) | (gr[3][3] ^ tag);
      if (__builtin_expect(!__any(bad != 0), 1)) break;
      STAMP(13);
      __builtin_amdgcn_s_sleep(SLEEP_AG1);
#pragma unroll
      for (int i = 0; i < 4; ++i)
        if (gr[i][3] != tag) gr[i] = __builtin_amdgcn_raw_buffer_load_b128(wrs, ko + i * 1024, 0, 16);
      asm volatile("" ::: "memory");
    }
    STAMP(2);
    {
      u32 m1 = gr[0][0], m2 = gr[0][1], m3 = 0, m4 = 0;
#pragma unroll
      for (int i = 2; i < 8; ++i) {
        const u32 k = gr[i >> 1][i & 1];
        if (i > 2) m4 = med3u(m3, m4, k);
        m3 = med3u(m2, m3, k);
        m2 = (i & 1) ? max(m2, k) : med3u(m1, m2, k);
        if (!(i & 1)) m1 = max(m1, k);
      }
      const u32 third = max(max(gr[0][2], gr[1][2]), max(gr[2][2], gr[3][2]));
      u32 W2;
      mykey = wave_top8(m1, m2, m3, m4, W2);
      xs_ld();
      SB();
      if (__builtin_expect(__any(m1 == 0u || (third | 0xffffu) > W2), 0)) {
        u4v ak[16];
#pragma unroll 1
        for (int spin = 0; spin < SPIN_MAX; ++spin) {
          u32 bad = 0;
#pragma unroll
          for (int i = 0; i < 16; ++i) {
            ak[i] = __builtin_amdgcn_raw_buffer_load_b128(wrs, WS_KALL + (i & 3) * 1024 + lane * 16, (i >> 2) * 4096, 16);
            bad |= ak[i][3] ^ tag;
          }
          if (!__any(bad != 0)) break;
          __builtin_amdgcn_s_sleep(8);
          asm volatile("" ::: "memory");
        }
        u32 prev = 0xffffffffu;
#pragma unroll 1
        for (int it = 0; it < 8; ++it) {
          u32 mm_ = 0;
#pragma unroll
          for (int i = 0; i < 32; ++i) {
            const u32 k = ak[i >> 1][i & 1];
            mm_ = max(mm_, k < prev ? k : 0u);
          }
          OPQ(mm_);
          prev = wave_max_u32(mm_);
          mykey = (lane >> 3) == it ? prev : mykey;
        }
        xs_ld();
      }
      OPQ(mykey);
    }
    SB();
    STAMP(3);
    if (l7 == 0) selw[lane >> 3] = mykey;
    {
      const u32 tn = now32();
      u32 yg = 0;
#pragma unroll
      for (int i = 0; i < 4; ++i) yg = max(yg, (gr[i][2] - (tn >> 2) - 1u) & 0xffu);
      const u32 tpub = tn - 4u * (255u - wave_max_u32(yg)) + DT_VIS;
      if (lane == 0) par[2] = (int)(tpub - tkey) > 0 ? tpub + DT_LATE : tkey;
    }
    ARRIVE(C_SEL);
    if (b == 255 && lane == 0) __builtin_amdgcn_raw_buffer_store_b32(tag, wrs, WS_EPOCH, 0, 16);
    SB();
    {
      char* xp = smem + O_XS + (lane >> 3) * XS_PITCH + l7;
#pragma unroll
      for (int k = 0; k < 4; ++k) *(u4v*)(xp + k * 128) = xs[k];
      ARRIVE(C_XS);
      STAMP(6);
    }
    __builtin_amdgcn_s_setprio(2);
    auto ors = __builtin_amdgcn_make_buffer_rsrc((void*)kc[1], 0, L * D * 2, 0x00020000);
    const u32 rl = (lane + (xi & 15) * 4) & 63, un = (g * 4 + (lane & 3) + xi) & 15;
    u32 skip = 0;
#pragma unroll
    for (int k = 0; k < 8; ++k) {
      const u32 r = ~(u32)__builtin_amdgcn_readlane((int)mykey, 8 * k) & 2047u;
      skip |= (r & 63u) == rl ? 1u << (r >> 6) : 0u;
    }
    const char* op = smem + ((lane >> 2) & 3) * CHB + O_OUT + (un & 3) * 144 + (un >> 2) * 8;
    const u32 ob = rl * 2048 + cc * 8;
    const u32 tfast = (u32)__builtin_amdgcn_readfirstlane((int)par[2]) + DE_FAST;
#pragma unroll 1
    for (int t = 0; t < 8; ++t) {
      WAITA(CTLA(C_TILE) + t * 16 + (lane & 3) * 4);
      u2v yv[4];
#pragma unroll
      for (int e = 0; e < 4; ++e) yv[e] = tr_read(op + t * OT + e * 32);
      const bool slow = (int)(now32() - tfast) < 0;
#pragma unroll
      for (int e = 0; e < 4; ++e) {
        __builtin_amdgcn_raw_buffer_store_b64(yv[e], ors, ob | (((skip >> e) & 1u) << 30), t * 524288 + e * 131072, 0);
        if (slow) {
          SB(); __builtin_amdgcn_s_sleep(STSLP); SB();
          asm volatile("s_waitcnt vmcnt(%0)" :: "n"(STWIN - 1) : "memory");
        }
      }
      skip >>= 4;
    }
    STAMP(9);
    ROLE_END();
  }
  {
    ROLE("d");
        const int j = xg * 28 + xi;
    const int s1 = j >> 2, kq = j & 3;
    const bool own = xi < 28;
    const u64 wbase = kb[3];
    const int pitch = D * 2;
    auto hrs = __builtin_amdgcn_make_buffer_rsrc((void*)wbase, 0, own ? D * F * 2 : 0, 0x00020000);
    const int r8 = lane >> 3;
    const int vo = (64 * s1 * D + 256 * kq) * 2 + (8 * (r8 >> 2) + (r8 & 3)) * pitch + (((lane & 7) * 16) ^ (((r8 >> 1) & 3) * 32));
    auto dma = [&](int h, int sB, int pt, u32 dst) AI {
      const int m = h >> 1;
#pragma unroll
      for (int i = 0; i < 2; ++i)
        asm volatile("s_mov_b32 m0, %3\n\ts_nop 0\n\tbuffer_load_dwordx4 %0, %1, %2 offen nt lds"
                     :: "v"(vo), "s"(hrs), "s"((m & 1) * 32 * pt + (m >> 1) * sB + (16 * i + 4 * (h & 1)) * pt), "s"(dst + i * 1024) : "memory");
    };
    const int q = c16 >> 2, p = c16 & 3;
    int tro[4];
#pragma unroll
    for (int ct = 0; ct < 4; ++ct) tro[ct] = (4 * g + q) * 128 + ((ct ^ (((4 * g + q) >> 1) & 3)) * 32) + p * 8;
    const int tok = lane & 7;
    u4v wd[2][8];
    auto wd_ld = [&](int k, int cb) AI {
#pragma unroll
      for (int i = 0; i < 8; ++i) {
        const char* pp = smem + O_WD + cb * 8192 + (i >> 2) * 4096 + tro[i & 3];
        const u2v lo = tr_read(pp), hi = tr_read(pp + 2048);
        wd[k][i] = u4v{lo[0], lo[1], hi[0], hi[1]};
      }
    };
    u32 p2o, p2k[4], hlo, hhi;
    auto f2_setup = [&](int cb0) AI {
      p2o = WS_P2 + ((64 * kq + 16 * cb0 + (lane & 8) / 2 + g) * 56 + s1) * 128 + tok * 16;
      hlo = lbase + O_HB + ((lane & 8) ? 512 : g * 128 + tok * 16);
      hhi = lbase + O_HB + ((lane & 8) ? g * 128 + tok * 16 : 512);
#pragma unroll
      for (int i = 0; i < (cb0 == 2 ? 4 : 2); ++i) { p2k[i] = p2o + i * 8 * 7168; OPQ(p2k[i]); }
      OPQ(hlo); OPQ(hhi);
    };
    u4v hb[4];
    f4 r[4];
    auto hb_ld = [&]() AI {
      u32 cnt; int n_ = 1 << 20;
      asm volatile("1:\n\tds_read_b32 %4, %6\n\tds_read_b128 %0, %7\n\tds_read_b128 %1, %8\n\tds_read_b128 %2, %7 offset:528\n\tds_read_b128 %3, %8 offset:528\n\t"
                   "s_sub_u32 %5, %5, 1\n\ts_cbranch_scc1 2f\n\ts_waitcnt lgkmcnt(0)\n\tv_cmp_gt_u32 vcc, 2, %4\n\ts_cbranch_vccnz 1b\n2:\n\ts_waitcnt lgkmcnt(0)"
                   : "=&v"(hb[0]), "=&v"(hb[1]), "=&v"(hb[2]), "=&v"(hb[3]), "=&v"(cnt), "+s"(n_) : "v"(CTLA(C_HRDY)), "v"(hlo), "v"(hhi) : "vcc", "scc", "memory");
    };
    auto f2_mm = [&](int k) AI {
      const u4v* wp = wd[k];
      r[2 * k] = mma(wp[0], hb[0], f4{0.f, 0.f, 0.f, 0.f}); r[2 * k + 1] = mma(wp[2], hb[0], f4{0.f, 0.f, 0.f, 0.f});
      r[2 * k] = mma(wp[1], hb[1], r[2 * k]); r[2 * k + 1] = mma(wp[3], hb[1], r[2 * k + 1]);
      r[2 * k] = mma(wp[4], hb[2], r[2 * k]); r[2 * k + 1] = mma(wp[6], hb[2], r[2 * k + 1]);
      r[2 * k] = mma(wp[5], hb[3], r[2 * k]); r[2 * k + 1] = mma(wp[7], hb[3], r[2 * k + 1]);
    };
    auto f2_st = [&](int k, u32 t10) AI {
#pragma unroll
      for (int i = 2 * k; i < 2 * k + 2; ++i) {
        u4v v = __builtin_bit_cast(u4v, r[i]);
#pragma unroll
        for (int e = 0; e < 4; ++e) v[e] = __builtin_amdgcn_bitop3_b32(v[e], t10, TMASK, 0xd8);
        __builtin_amdgcn_raw_buffer_store_b128(v, wrs, p2k[i], 0, 16);
      }
    };
    {
      __builtin_amdgcn_s_setprio(3);
      BAR();
      const u32 ts = ts0 - 500u;
      u32 tl = ~0u;
      pace(ts0 + DT_HEAD);
      STAMP(3);
#pragma unroll 1
      for (int i = 0; i < 8 && tl >= 1500u; ++i) {
        const u2v hd = __builtin_amdgcn_raw_buffer_load_b64(wrs, WS_HEAD + (lane & 7) * 8, 0, 16);
        tl = wave_max_u32(hd[0] == ~hd[1] ? hd[0] - ts : ~0u);
        STAMP(14);
        asm volatile("" ::: "memory");
      }
      tl = tl < 1500u ? tl + ts : ts0;
      if (lane == 0) par[1] = tl;
      ARRIVE(C_TL);
      STAMP(2);
      if (own) {
        WAITS(C_WGU, 1);
#pragma unroll
        for (int h = 0; h < 16; ++h) {
          if (h == DSEL) WAITS(C_SEL, 1);
          if (h == DEV) WAITS(C_XL, 4);
          if (h >= DWIN) asm volatile("s_waitcnt vmcnt(%0)" :: "n"(2 * (DWIN - 1)) : "memory");
          if (h == 3 + DWIN) ARRIVE(C_WD);
          if (h == 7 + DWIN) ARRIVE(C_WD + 1);
          dma(h, 128, D * 2, lbase + O_WD + h * 2048);
        }
        f2_setup(2);
        const u32 t10 = par[0] & TMASK;
        asm volatile("s_waitcnt vmcnt(8)" ::: "memory");
        wd_ld(0, 2);
        const u32 hc = __hip_atomic_load(ctl + C_HRDY, __ATOMIC_RELAXED, __HIP_MEMORY_SCOPE_WORKGROUP);
        const bool early = __builtin_amdgcn_ballot_w64(hc < 2u) == 0ull;
        if (early) { hb_ld(); f2_mm(0); }
        asm volatile("s_waitcnt vmcnt(0)" ::: "memory");
        STAMP(5);
        wd_ld(1, 3);
        if (!early) { WAITS(C_HRDY, 1); hb_ld(); STAMP(11); f2_mm(0); }
        SB();
        f2_mm(1);
        f2_st(0, t10);
        SB();
        f2_st(1, t10);
        STAMP(12);
      }
      ROLE_END();
    }
  }
}

extern "C" void kernel_launch(void* const* d_in, const int* in_sizes, int n_in,
                              void* d_out, int out_size, void* d_ws, size_t ws_size,
                              hipStream_t stream) {
  k_main<<<256, 768, 0, stream>>>((const u16*)d_in[0], (const u16*)d_in[8], (const u16*)d_in[12],
                                  (const u16*)d_in[1], (const u16*)d_in[2], (const u16*)d_in[3],
                                  (const u16*)d_in[4], (const u16*)d_in[5], (const u16*)d_in[6],
                                  (const u16*)d_in[9], (const u16*)d_in[10], (const u16*)d_in[11],
                                  (char*)d_ws, (u16*)d_out);
}
```

```cpp
#include <hip/hip_runtime.h>
#include <stdint.h>

typedef unsigned short u16;
typedef unsigned u32;
typedef _Float16 h2 __attribute__((ext_vector_type(2)));
typedef _Float16 h8 __attribute__((ext_vector_type(8)));
typedef __bf16 b2 __attribute__((ext_vector_type(2)));
typedef __bf16 b8 __attribute__((ext_vector_type(8)));
typedef short s4 __attribute__((ext_vector_type(4)));
typedef float f2 __attribute__((ext_vector_type(2)));
typedef float f4 __attribute__((ext_vector_type(4)));
typedef u32 u2v __attribute__((ext_vector_type(2)));
typedef u32 u3v __attribute__((ext_vector_type(3)));
typedef u32 u4v __attribute__((ext_vector_type(4)));
typedef __attribute__((address_space(3))) s4* lds_s4p;

constexpr int L = 2048;
constexpr int D = 1024;
constexpr int NM = 128;
constexpr int F = 3584;
constexpr int NTAP = 1024;

constexpr int SEG_I = 576, SEG_O0 = 800, SEG_O1 = 880, SEG_SP = 896;

#define AI __attribute__((always_inline))
#define SB() __builtin_amdgcn_sched_barrier(0)
#define OPQ(x) asm volatile("" : "+v"(x))
#define DIAG_K 11
#define DIAG_M 15
#define STAMP0() u32 dg_ = 0; const bool dgw_ = (DIAG_M >> w) & 1; if (DIAG_K == 0 && dgw_) dg_ = (u32)kt[0] | 1u
#define STAMP(i) do { if (i == DIAG_K && dgw_) { SB(); dg_ = (u32)__builtin_amdgcn_s_memrealtime() | 1u; SB(); } } while (0)
#define STAMP_END() do { if (DIAG_K == 99 && dgw_) { asm volatile("s_waitcnt vmcnt(0)" ::: "memory"); dg_ = (u32)__builtin_amdgcn_s_memrealtime() | 1u; } if (dg_) { while ((int)((u32)__builtin_amdgcn_s_memrealtime() - (dg_ + 4000u)) < 0) __builtin_amdgcn_s_sleep(2); } } while (0)

#define STAMP_T0

__device__ __forceinline__ float bflo(u32 v) { return __uint_as_float(v << 16); }
__device__ __forceinline__ float bfhi(u32 v) { return __uint_as_float(v & 0xffff0000u); }
__device__ __forceinline__ u32 pk_bf(float a, float b) { f2 v = {a, b}; b2 r = __builtin_convertvector(v, b2); return __builtin_bit_cast(u32, r); }
__device__ __forceinline__ float rb1(float v) { return __uint_as_float(pk_bf(0.f, v)); }
__device__ __forceinline__ h8 as_h8(u4v v) { return __builtin_bit_cast(h8, v); }
__device__ __forceinline__ b8 as_b8(u4v v) { return __builtin_bit_cast(b8, v); }
__device__ __forceinline__ u2v tr_read(const char* p) {
  s4 t = __builtin_amdgcn_ds_read_tr16_b64_v4i16((lds_s4p)p);
  return __builtin_bit_cast(u2v, t);
}
template <int CTRL> __device__ __forceinline__ u32 dpp_u(u32 v) { return (u32)__builtin_amdgcn_update_dpp(0, (int)v, CTRL, 0xf, 0xf, false); }
template <int CTRL> __device__ __forceinline__ float dpp_f(float v) { return __uint_as_float(dpp_u<CTRL>(__float_as_uint(v))); }
__device__ __forceinline__ u32 wave_max_u32(u32 v) {
  v = max(v, dpp_u<0x111>(v)); v = max(v, dpp_u<0x112>(v)); v = max(v, dpp_u<0x114>(v)); v = max(v, dpp_u<0x118>(v));
  v = max(v, dpp_u<0x142>(v)); v = max(v, dpp_u<0x143>(v));
  return (u32)__builtin_amdgcn_readlane((int)v, 63);
}
#define DPM " row_mask:0xf bank_mask:0xf bound_ctrl:1\n\t"
#define T2S(HO, HI, LI, C) "v_max_u32_dpp " HO ", " HI ", " HI " " C DPM "v_min_u32_dpp %8, " HI ", " HI " " C DPM "v_max_u32_dpp %7, " LI ", " LI " " C DPM "v_max_u32 %7, %7, %8\n\t"
#define T2R(RM, REST) T2S("%5", "%0", "%1", "row_shr:1") T2S("%6", "%5", "%7", "row_shr:2") T2S("%5", "%6", "%7", "row_shr:4") T2S("%6", "%5", "%7", "row_shr:8") \
    T2S("%5", "%6", "%7", "row_bcast:15") T2S("%6", "%5", "%7", "row_bcast:31") \
    "v_readlane_b32 %9, %6, 63\n\tv_readlane_b32 %10, %7, 63\n\ts_nop 0\n\tv_mov_b32 %5, %9\n\tv_cmp_le_u32 %11, %10, %0\n\tv_cmp_le_u32 %12, %10, %1\n\tv_mov_b32 %6, %10\n\t" \
    "v_cndmask_b32 %0, %0, %1, %11\n\tv_mov_b32_dpp %4, %5 quad_perm:[0,1,2,3] row_mask:" RM " bank_mask:0x3\n\tv_cndmask_b32 %0, %0, %2, %12\n\t" \
    "v_mov_b32_dpp %4, %6 quad_perm:[0,1,2,3] row_mask:" RM " bank_mask:0xc\n\t" REST
#define T2P "v_cndmask_b32 %1, %1, %2, %11\n\tv_cndmask_b32 %2, %2, %3, %11\n\tv_cndmask_b32 %1, %1, %3, %12\n\tv_cndmask_b32 %3, %3, 0, %11\n\tv_cndmask_b32 %2, %2, 0, %12\n\t"
__device__ __forceinline__ u32 wave_top8(u32& m1, u32 m2, u32 m3, u32 m4, u32& w2) {
  u32 key, ta, tb, lo, t, w1;
  unsigned long long c1, c2;
  asm("s_nop 1\n\t" T2R("0x1", T2P) T2R("0x2", T2P) T2R("0x4", T2P) T2R("0x8", "") "s_nop 0"
      : "+v"(m1), "+v"(m2), "+v"(m3), "+v"(m4), "=&v"(key), "=&v"(ta), "=&v"(tb), "=&v"(lo), "=&v"(t), "=&s"(w1), "=&s"(w2), "=&s"(c1), "=&s"(c2));
  return key;
}

__device__ __forceinline__ float exp_bf(float v) {
  const float e = __builtin_amdgcn_exp2f(v * 1.44268798828125f);
  return fmaf(e, v * (7.0526077e-06f * 0.69314718f), e);
}
struct cf { f2 r, i; };
struct ch { h2 r, i; };
__device__ __forceinline__ cf cmul(cf a, cf b) { cf c; c.r = a.r * b.r - a.i * b.i; c.i = a.r * b.i + a.i * b.r; return c; }
__device__ __forceinline__ ch hmul(ch a, ch b) {
  ch c;
  asm("v_pk_mul_f16 %0, %2, %4\n\tv_pk_mul_f16 %1, %2, %5\n\tv_pk_fma_f16 %0, %3, %5, %0 neg_lo:[1,0,0] neg_hi:[1,0,0]\n\tv_pk_fma_f16 %1, %3, %4, %1"
      : "=&v"(c.r), "=&v"(c.i) : "v"(a.r), "v"(a.i), "v"(b.r), "v"(b.i));
  return c;
}
__device__ __forceinline__ cf csq(cf a) { cf c; c.r = a.r * a.r - a.i * a.i; c.i = (a.r + a.r) * a.i; return c; }
__device__ __forceinline__ ch toh(cf a) { ch c; c.r = __builtin_convertvector(a.r, h2); c.i = __builtin_convertvector(a.i, h2); return c; }
__device__ __forceinline__ ch tohc(cf a) { ch c; c.r = __builtin_convertvector(a.r, h2); c.i = __builtin_convertvector(-a.i, h2); return c; }
__device__ __forceinline__ h2 ash(u32 v) { return __builtin_bit_cast(h2, v); }
__device__ __forceinline__ cf cexp_p(f2 a2, f2 brev, float p) {
  f2 e = a2 * p, t = brev * p;
  f2 mag = {__builtin_amdgcn_exp2f(e[0]), __builtin_amdgcn_exp2f(e[1])};
  float r0 = __builtin_amdgcn_fractf(t[0]), r1 = __builtin_amdgcn_fractf(t[1]);
  cf c;
  c.r = mag * f2{__builtin_amdgcn_cosf(r0), __builtin_amdgcn_cosf(r1)};
  c.i = mag * f2{__builtin_amdgcn_sinf(r0), __builtin_amdgcn_sinf(r1)};
  return c;
}
__device__ __forceinline__ u32 med3u(u32 a, u32 b, u32 c) { u32 r; asm("v_med3_u32 %0, %1, %2, %3" : "=v"(r) : "v"(a), "v"(b), "v"(c)); return r; }
__device__ __forceinline__ f2 unbf(u32 v) { return f2{bflo(v), bfhi(v)}; }
__device__ __forceinline__ float sigm(float v) { return __builtin_amdgcn_rcpf(1.f + __builtin_amdgcn_exp2f(-1.44269504088896341f * v)); }

#define TS2(A, B, C, D) "v_xor_b32 %5, " A ", %9\n\tv_xor_b32 %6, " B ", %9\n\tv_xor_b32 %7, " C ", %9\n\tv_xor_b32 %8, " D ", %9\n\t" \
    "v_fma_f32 %1, %5, 1.0, %1\n\tv_fma_f32 %2, %6, 1.0, %2\n\tv_or3_b32 %0, %0, %5, %6\n\tv_fma_f32 %3, %7, 1.0, %3\n\tv_fma_f32 %4, %8, 1.0, %4\n\tv_or3_b32 %0, %0, %7, %8\n\t"
#define TSR TS2("%10", "%11", "%12", "%13") TS2("%14", "%15", "%16", "%17") TS2("%18", "%19", "%20", "%21")
#define TSI "=&v"(t0), "=&v"(t1), "=&v"(t2), "=&v"(t3) : "v"(t10), "v"(p[0]), "v"(p[1]), "v"(p[2]), "v"(p[3]), "v"(q[0]), "v"(q[1]), "v"(q[2]), "v"(q[3]), \
    "v"(r[0]), "v"(r[1]), "v"(r[2]), "v"(r[3])
__device__ __forceinline__ f4 tsum4(u32& bad, u32 t10, u4v s, u4v p, u4v q, u4v r) {
  float s0, s1, s2, s3; u32 t0, t1, t2, t3;
  asm volatile("v_xor_b32 %1, %22, %9\n\tv_xor_b32 %2, %23, %9\n\tv_xor_b32 %3, %24, %9\n\tv_xor_b32 %4, %25, %9\n\tv_or3_b32 %0, %0, %1, %2\n\tv_or3_b32 %0, %0, %3, %4\n\t" TSR
               : "+v"(bad), "=&v"(s0), "=&v"(s1), "=&v"(s2), "=&v"(s3), TSI, "v"(s[0]), "v"(s[1]), "v"(s[2]), "v"(s[3]));
  return f4{s0, s1, s2, s3};
}
__device__ __forceinline__ f4 tsum3(u32& bad, u32 t10, f4 s, u4v p, u4v q, u4v r) {
  float s0 = s[0], s1 = s[1], s2 = s[2], s3 = s[3]; u32 t0, t1, t2, t3;
  asm volatile(TSR : "+v"(bad), "+v"(s0), "+v"(s1), "+v"(s2), "+v"(s3), TSI);
  return f4{s0, s1, s2, s3};
}
__device__ __forceinline__ void silu4(f4& g, float nl2e) {
  float g0 = g[0], g1 = g[1], g2 = g[2], g3 = g[3], t0, t1, t2, t3;
  asm volatile("v_fma_f32 %4, %0, %8, 0\n\tv_fma_f32 %5, %1, %8, 0\n\tv_fma_f32 %6, %2, %8, 0\n\tv_fma_f32 %7, %3, %8, 0\n\t"
               "v_exp_f32 %4, %4\n\tv_exp_f32 %5, %5\n\tv_exp_f32 %6, %6\n\tv_exp_f32 %7, %7\n\t"
               "v_fma_f32 %4, %4, 1.0, 1.0\n\tv_fma_f32 %5, %5, 1.0, 1.0\n\tv_fma_f32 %6, %6, 1.0, 1.0\n\tv_fma_f32 %7, %7, 1.0, 1.0\n\t"
               "v_rcp_f32 %4, %4\n\tv_rcp_f32 %5, %5\n\tv_rcp_f32 %6, %6\n\tv_rcp_f32 %7, %7\n\t"
               "v_fma_f32 %0, %0, %4, 0\n\tv_fma_f32 %1, %1, %5, 0\n\tv_fma_f32 %2, %2, %6, 0\n\tv_fma_f32 %3, %3, %7, 0"
               : "+v"(g0), "+v"(g1), "+v"(g2), "+v"(g3), "=&v"(t0), "=&v"(t1), "=&v"(t2), "=&v"(t3) : "v"(nl2e));
  g = f4{g0, g1, g2, g3};
}

constexpr int KT_PITCH = 52;
constexpr int kt_off(int p) {
  int i = 20, j = 0;
  if (p < 256) { i = p & 7; j = p >> 3; }
  else if (p < 512) { int q = (p - 256) >> 1; i = 8 + (q & 3); j = q >> 2; }
  else if (p < 1024) { int q = (p - 512) >> 2; i = 12 + (q & 1) + 2 * (q >> 6); j = (q >> 1) & 31; }
  else if (p < 2048) { int q = (p - 1024) >> 3; i = 16 + (q >> 5); j = q & 31; }
  return j * KT_PITCH + i * 2;
}
constexpr int KT_ZERO = 32 * KT_PITCH + 16;
constexpr int rnd_lag(int m) {
  const int st = m < 256 ? 1 : m < 512 ? 2 : m < 1024 ? 4 : 8;
  int q = m / st;
  const int rem = m - q * st;
  if (2 * rem > st || (2 * rem == st && (q & 1))) ++q;
  return q * st;
}
constexpr int tap_entry(int e) {
  if (e < 544) {
    if (543 - e <= 525) return kt_off(rnd_lag(543 - e));
  } else if (e >= 560 && e < 752) {
    const int q = (e - 560) / 48, x = (e - 560) - q * 48;
    const int p = 512 + 16 * (31 - x) + 4 * (q + 1);
    if (x < 32 && p > 524) return kt_off(p);
  } else if (e >= 752 && e < 816) {
    return kt_off(1032 + 16 * (815 - e));
  } else if (e >= 832 && e < 896) {
    return kt_off(1040 + 16 * (895 - e));
  } else if (e == SEG_SP + 27 || e == SEG_SP + 28) {
    return kt_off(1024);
  }
  return KT_ZERO;
}
struct TapIdx { u16 v[NTAP]; };
constexpr TapIdx make_tapidx() {
  TapIdx t{};
  for (int e = 0; e < NTAP; ++e) t.v[e] = (u16)tap_entry(2 * ((e >> 4) + 64 * ((e >> 1) & 7)) + (e & 1));
  return t;
}
__device__ __attribute__((aligned(16))) const TapIdx g_tapidx = make_tapidx();

constexpr int TROW = 544;
constexpr int O_OUT = 0;
constexpr int OT = 576;
constexpr int O_U = O_OUT + 8 * OT;
constexpr int O_TAP = O_U + 4608;
constexpr int TAP_PITCH = 2 * NTAP + 64;
constexpr int CHB = O_TAP + 16 * TROW + 32;
static_assert(16 * TROW >= 4 * TAP_PITCH, "lds");
static_assert((CHB / 4) % 32 == 8, "banks");
#define DEV 3
#define DWIN 8
#define DSEL 0
constexpr int NSLOT = 5, WWIN = 4;
constexpr int O_GT = 4 * CHB;
constexpr int O_RING = O_GT + 8192;
constexpr int O_WD = O_RING + 2 * NSLOT * 2048;
constexpr int O_FAR = O_WD + 32768;
constexpr int O_XS = O_FAR + 16384;
constexpr int XS_PITCH = 544;
constexpr int O_HB = O_XS + 8 * XS_PITCH;
constexpr int O_PART = O_HB + 2 * 528;
constexpr int O_SEL = O_PART + 512;
constexpr int O_PAR = O_SEL + 32;
constexpr int O_CTL = O_PAR + 16;
constexpr int O_JUNK = O_CTL + 512;
constexpr int K_LDS = O_JUNK + 1024;
static_assert(DWIN == 8, "flags of the down tile");
static_assert(K_LDS <= 163840 && O_RING % 16 == 0, "lds");
enum { C_PART = 0, C_GP, C_PUB, C_SEL, C_XS, C_HRDY, C_WGU, C_TL, C_XL, C_WD = 10, C_TAPS = 12, C_Q = 16, C_FAR = 32, C_TILE = 48 };

#define CTLA(word) (lbase + O_CTL + (word) * 4)
#define ARRIVE(word) asm volatile("s_mov_b64 exec, 1\n\tds_add_u32 %0, %1\n\ts_mov_b64 exec, -1" :: "v"(CTLA(word)), "v"(1) : "memory")
#define WAITQ(word, target) do { u32 t_; int n_ = 1 << 20; asm volatile( \
    "1:\n\tds_read_b32 %0, %2\n\ts_sub_u32 %1, %1, 1\n\ts_cbranch_scc1 2f\n\ts_waitcnt lgkmcnt(0)\n\tv_cmp_gt_u32 vcc, %3, %0\n\ts_cbranch_vccnz 1b\n2:\n\ts_waitcnt lgkmcnt(0)" \
    : "=&v"(t_), "+s"(n_) : "v"(CTLA(word)), "n"(target) : "vcc", "scc", "memory"); } while (0)
#define WAITS(word, target) do { u32 t_; int n_ = 1 << 20; asm volatile( \
    "1:\n\tds_read_b32 %0, %2\n\ts_sub_u32 %1, %1, 1\n\ts_cbranch_scc1 2f\n\ts_waitcnt lgkmcnt(0)\n\tv_cmp_le_u32 vcc, %3, %0\n\ts_cbranch_vccnz 2f\n\ts_sleep 2\n\ts_branch 1b\n2:\n\ts_waitcnt lgkmcnt(0)" \
    : "=&v"(t_), "+s"(n_) : "v"(CTLA(word)), "n"(target) : "vcc", "scc", "memory"); } while (0)
#define BAR() asm volatile("s_waitcnt lgkmcnt(0)\n\ts_barrier" ::: "memory")
__device__ __forceinline__ u2v lds8(const char* p) { return __builtin_bit_cast(u2v, __hip_atomic_load((const unsigned long long*)p, __ATOMIC_RELAXED, __HIP_MEMORY_SCOPE_WORKGROUP)); }

constexpr int WS_EPOCH = 0;
constexpr int WS_HEAD = 1024;
constexpr int WS_REC = 4096;
constexpr int WS_FLG = 40960;
constexpr int WS_KALL = WS_REC + 8192;
constexpr int WS_KEY = 32768;
constexpr int WS_P1 = 65536;
constexpr int WS_P2 = WS_P1 + 56 * 16384;
constexpr int WS_XT = WS_P2 + 256 * 7168;
constexpr int XT_PITCH = 33280;
constexpr int WS_END = WS_XT + 128 * XT_PITCH;
constexpr int SPIN_MAX = 1 << 16;
constexpr u32 TMASK = 0x3ffu;

#define DT_HEAD 110
#define WDELAY 60
#define DT_KEY 240
#define DT_REC 270
#define DT_FLG 380
#define DT_VIS 40
#define DT_LATE 50
#define DE_P1 350
#define DE_P2 590
#define DE_FAST 575
#define DT_MIN 25
#define DT_P1 100
#define DT_P2 130
#define SLEEP_AG1 12
#define SLEEP_G 6
#define STSLP 2
#define STWIN 4
#define H2_AT 9
#define CODE_B 28400

#define ROLE_END() do { STAMP_END(); asm volatile("s_endpgm" ::: "memory"); } while (0)
__device__ __forceinline__ u32 now32() { return (u32)__builtin_amdgcn_s_memrealtime(); }
__device__ __forceinline__ void pace(u32 t) {
#pragma unroll 1
  for (int i = 0; i < 1024 && (int)(now32() - t) < 0; ++i) __builtin_amdgcn_s_sleep(1);
}
__device__ __forceinline__ void pace2(u32 n, u32 t, int dmax) {
  int d = (int)(t - n);
  d = d < DT_MIN ? DT_MIN : (d > dmax ? dmax : d);
  pace(n + (u32)d);
}

#define ALL_OTHER "vcc", "scc", "memory", \
    "s0","s1","s2","s5","s6","s7","s33","s34","s35","s44","s45","s46","s47","s48","s49","s50","s51","s52","s53","s54","s55","s56","s57","s58","s59", \
    "s60","s61","s62","s63","s64","s65","s66","s67","s68","s69","s70","s71","s72","s73","s74","s75","s76","s77","s78","s79","s80","s81","s82","s83","s84", \
    "s85","s86","s87","s88","s89","s90","s91","s92","s93","s94","s95","s96","s97","s98","s99", \
    "v0","v1","v2","v3","v4","v5","v6","v7","v8","v9","v10","v11","v12","v13","v14","v15","v16","v17","v18","v19","v20","v21","v22","v23","v24","v25", \
    "v26","v27","v28","v29","v30","v31","v32","v33","v34","v35","v36","v37","v38","v39","v40","v41","v42","v43","v44","v45","v46","v47","v48","v49","v50", \
    "v51","v52","v53","v54","v55","v56","v57","v58","v59","v60","v61","v62","v63","v64","v65","v66","v67","v68","v69","v70","v71","v72","v73","v74","v75", \
    "v76","v77","v78","v79","v80","v81","v82","v83","v84","v85","v86","v87","v88","v89","v90","v91","v92","v93","v94","v95","v96","v97","v98","v99", \
    "v100","v101","v102","v103","v104","v105","v106","v107","v108","v109","v110","v111","v112","v113","v114","v115","v116","v117","v118","v119","v120", \
    "v121","v122","v123","v124","v125","v126","v127","v128","v129","v130","v131","v132","v133","v134","v135","v136","v137","v138","v139","v140","v141", \
    "v142","v143","v144","v145","v146","v147","v148","v149","v150","v151","v152","v153","v154","v155","v156","v157","v158","v159","v160","v161","v162", \
    "v163","v164","v165","v166","v167"
typedef __attribute__((address_space(1))) const u16* gp16;
typedef __attribute__((address_space(1))) const u32* gp32;
typedef __attribute__((address_space(1))) const u4v* gp128;
typedef unsigned long long u64;
typedef u64 q2 __attribute__((ext_vector_type(2)));
typedef u64 q4 __attribute__((ext_vector_type(4)));
typedef u64 q8 __attribute__((ext_vector_type(8)));

__global__ void __launch_bounds__(768, 3) k_main(
    const u16* x_, const u16* w_r_, const u16* gate_w_, const u16* logA_, const u16* Aim_, const u16* Cre_, const u16* Cim_,
    const u16* log_dt_, const u16* Dskip_, const u16* w_gate_, const u16* w_up_, const u16* w_down_, char* wsp_, u16* out_) {
  __shared__ __attribute__((aligned(256))) char smem[K_LDS];
  const u32 lbase = (u32)(uintptr_t)(__attribute__((address_space(3))) char*)smem;
#define XDMA(OFF) "global_load_lds_dwordx4 v1, s[8:9] offset:" #OFF " nt\n\ts_addk_i32 m0, 896\n\t"
  asm volatile("s_load_dwordx16 s[8:23], %0, 0\n\tv_readfirstlane_b32 s3, %2\n\ts_load_dwordx8 s[24:31], %0, 64\n\ts_load_dwordx4 s[36:39], %0, 96\n\t"
               "s_memrealtime s[40:41]\n\t" STAMP_T0 "s_mov_b32 s4, %1\n\ts_lshr_b32 s3, s3, 6\n\t"
               "s_cmp_ge_u32 s3, 8\n\ts_cbranch_scc1 .Lrole_g\n\ts_cmp_eq_u32 s3, 7\n\ts_cbranch_scc1 .Lrole_d\n\t"
               "s_cmp_ge_u32 s3, 5\n\ts_cbranch_scc1 .Lrole_h\n\ts_cmp_eq_u32 s3, 4\n\ts_cbranch_scc1 .Lrole_c\n\t"
               "s_lshr_b32 s5, s4, 3\n\tv_lshlrev_b32 v3, 2, %2\n\ts_mov_b32 m0, %4\n\tv_lshl_or_b32 v3, s5, 10, v3\n\ts_getpc_b64 s[6:7]\n\t"
               "v_min_u32 v3, %3, v3\n\tv_lshrrev_b32 v1, 3, %2\n\tglobal_load_lds_dword v3, s[6:7]\n\t"
               "v_and_b32 v2, 6, v1\n\ts_and_b32 s34, s4, 7\n\tv_xor_b32 v2, v2, %2\n\ts_lshl_b32 s34, s34, 5\n\tv_and_b32 v2, 7, v2\n\ts_add_u32 s5, s5, s34\n\t"
               "v_and_b32 v1, 7, v1\n\ts_lshl_b32 s5, s5, 5\n\tv_lshlrev_b32 v1, 11, v1\n\ts_add_u32 s5, s5, s3\n\tv_lshl_or_b32 v1, v2, 4, v1\n\t"
               "s_mul_i32 s34, s3, %5\n\tv_lshl_add_u32 v1, s5, 9, v1\n\ts_add_u32 m0, s34, %6\n\tv_and_b32 v2, 31, %2\n\ts_waitcnt lgkmcnt(0)\n\t"
               XDMA(0) "s_lshl_b32 s5, s3, 5\n\t" XDMA(128) "v_add_lshl_u32 v2, s5, v2, 4\n\t" XDMA(256) "s_lshl_b32 s5, s3, 10\n\t"
               "global_load_lds_dwordx4 v1, s[8:9] offset:384 nt\n\ts_add_u32 m0, s5, %7\n\ts_mov_b32 exec_hi, 0\n\t"
               "global_load_lds_dwordx4 v2, s[10:11]\n\ts_addk_i32 m0, 512\n\ts_nop 0\n\tglobal_load_lds_dwordx4 v2, s[12:13]\n\ts_mov_b32 exec_hi, -1"
               :: "s"(__builtin_amdgcn_kernarg_segment_ptr()), "s"(blockIdx.x), "v"(threadIdx.x), "n"(CODE_B - 256), "n"(O_JUNK), "n"(CHB), "n"(O_U + 512), "n"(O_FAR)
               : "s3", "s4", "s5", "s6", "s7", "s8", "s9", "s10", "s11", "s12", "s13", "s14", "s15", "s16", "s17", "s18", "s19", "s20", "s21", "s22", "s23",
                 "s24", "s25", "s26", "s27", "s28", "s29", "s30", "s31", "s34", "s36", "s37", "s38", "s39", "s40", "s41", "s42", "s43", "v1", "v2", "v3", "scc", "memory");
  int lane, c16, g, w, b, xg, xi, cg, cc, c, d;
  char* ch_;
  q8 ka; q4 kb; q2 kc, kt;
  u32* ctl = (u32*)(smem + O_CTL);
  u32* par = (u32*)(smem + O_PAR);
#define IDS() do { int z_ = 0; OPQ(z_); lane = __builtin_amdgcn_mbcnt_hi(~0u, __builtin_amdgcn_mbcnt_lo(~0u, z_)); c16 = lane & 15; g = lane >> 4; } while (0)
#define ROLE(name) \
  asm volatile(".Lrole_" name ":\n\ts_waitcnt lgkmcnt(0)" : "={s[8:23]}"(ka), "={s[24:31]}"(kb), "={s[36:39]}"(kc), "={s[40:43]}"(kt), "={s3}"(w), "={s4}"(b) :: ALL_OTHER); \
  if (__builtin_expect(w > 11, 0)) asm volatile("s_endpgm" ::: "memory");        \
  IDS(); \
  xg = b & 7; xi = b >> 3;                          \
  cg = xg * 32 + xi;                                \
  cc = (xi >> 4) * 128 + xg * 16 + (xi & 15);       \
  c = w & 3; ch_ = smem + c * CHB; d = cc * 4 + c; \
  const u32 ts0 = (u32)kt[0]; \
  auto wrs = __builtin_amdgcn_make_buffer_rsrc((void*)kc[0], 0, WS_END, 0x00020000); \
  STAMP0()

  const char* tapb;
  const char* ub;
  char* erp;
  const char* eup;
  const char* egp;
  u4v At[17], band[4];
  float dm1;
  f4 acc[8];
  auto conv_setup = [&]() AI {
    tapb = ch_ + O_TAP + ((15 - c16) & 3) * TAP_PITCH + ((15 - c16) & ~3) * 2;
    ub = ch_ + O_U - 32 + c16 * 32 + g * 16;
    erp = ch_ + O_OUT + g * 144 + c16 * 8;
    eup = ch_ + O_U + 512 + c16 * 32 + g * 8;
    egp = smem + O_GT + lane * 16;
  };
  auto band_setup = [&]() AI {
#pragma unroll
    for (int k = 0; k < 4; ++k) {
      const int ctr = k == 0 ? 12 : 8, hw = k + 1;
      const u32 m = ((1u << (2 * hw + 1)) - 1u) << (ctr - hw);
      const u32 bits = (m << c16) >> (8 * g);
#pragma unroll
      for (int j = 0; j < 4; ++j)
        band[k][j] = (__builtin_amdgcn_sbfe(bits, 2 * j, 1) & 0x3f80u) | (__builtin_amdgcn_sbfe(bits, 2 * j + 1, 1) & 0x3f800000u);
      OPQ(band[k]);
    }
  };
  auto frag2 = [&](const char* pa, const char* pb) AI { const u2v lo = lds8(pa), hi = lds8(pb); return u4v{lo[0], lo[1], hi[0], hi[1]}; };
  auto atload = [&](int s0, int s1) AI {
#pragma unroll
    for (int s = s0; s < s1; ++s) At[s] = frag2(tapb + 64 * s + g * 16, tapb + 64 * s + g * 16 + 8);
  };
  auto bin = [&](int pair, int be) AI { const char* p = tapb + g * 8 + (SEG_I + 96 * pair - 16 * be) * 2; return frag2(p, p + 96); };
  auto bout = [&](int be) AI { const char* p = tapb + g * 8 + (SEG_O0 - 16 * be) * 2; return frag2(p, p + (SEG_O1 - SEG_O0) * 2); };
  auto asp = [&]() AI { return frag2(tapb + SEG_SP * 2 + g * 16, tapb + SEG_SP * 2 + g * 16 + 8); };
  auto mma = [&](u4v a, u4v bq, f4 cin) AI { return __builtin_amdgcn_mfma_f32_16x16x32_bf16(as_b8(a), as_b8(bq), cin, 0, 0, 0); };
  auto uld = [&](int sg) AI { return *(const u4v*)(ub + 512 + sg * 64); };
  auto uld24 = [&](int un) AI { return *(const u4v*)(ub + 512 + un * 512 - 16); };
  u2v euw[8], eo[8]; f4 egv[8], efv[8];
  float eu[8][4], ev[8][4], ea[8][4];
  u4v Fq[57], ru[6], wa_[6][3];
  f4 wq[6][6];
  u32 pk[12], one, qa, fa;
  char* tfp; char* fp_;
#define LOPQ(p_) do { u32 a_ = (u32)(uintptr_t)(__attribute__((address_space(3))) const char*)p_; OPQ(a_); p_ = (decltype(p_))(__attribute__((address_space(3))) char*)(uintptr_t)a_; } while (0)
  auto m_setup = [&]() AI {
    tfp = smem + (lane == 0 ? O_CTL + c * 4 : O_JUNK + lane * 4);
    fp_ = smem + O_FAR + c * 4096 + lane * 16;
    qa = CTLA(C_Q) + (lane & 3) * 4; fa = CTLA(C_FAR + c); one = 1u;
    OPQ(qa); OPQ(fa); OPQ(one);
    LOPQ(tfp); LOPQ(fp_); LOPQ(erp); LOPQ(eup); LOPQ(egp); LOPQ(ub);
  };
#define WAITA(addr) do { u32 t_; int n_ = 1 << 20; asm volatile( \
    "1:\n\tds_read_b32 %0, %2\n\ts_sub_u32 %1, %1, 1\n\ts_cbranch_scc1 2f\n\ts_waitcnt lgkmcnt(0)\n\tv_cmp_eq_u32 vcc, 0, %0\n\ts_cbranch_vccnz 1b\n2:\n\ts_waitcnt lgkmcnt(0)" \
    : "=&v"(t_), "+s"(n_) : "v"(addr) : "vcc", "scc", "memory"); } while (0)
#define MF(d_, a_, b_) asm volatile("v_mfma_f32_16x16x32_bf16 %0, %1, %2, %0" : "+v"(d_) : "v"(a_), "v"(b_)); SB()
#define MF0(d_, a_, b_) asm volatile("v_mfma_f32_16x16x32_bf16 %0, %1, %2, 0" : "=&v"(d_) : "v"(a_), "v"(b_)); SB()
#define NOP(n_) asm volatile("s_nop " #n_); SB()
#define FL(i_, sg_) Fq[i_] = uld(sg_); SB()
#define RL(un_) ru[un_] = uld24(un_); SB()
#define AL(s_) atload(s_, s_ + 1); SB()
#define ULD(t_) euw[t_] = *(const u2v*)(eup + t_ * 512); SB()
#define GLD(t_) egv[t_] = *(const f4*)(egp + t_ * 1024); SB()
#define FLD(t_) efv[t_] = *(const f4*)(fp_ + (t_ - 2) * 1024); SB()
#define FST(un_) *(f4*)(fp_ + un_ * 1024) = acc[un_ + 2]; SB()
#define EUN(t_, e_) eu[t_][e_] = (e_ & 1) ? bfhi(euw[t_][e_ >> 1]) : bflo(euw[t_][e_ >> 1]); OPQ(eu[t_][e_]); ev[t_][e_] = eu[t_][e_]; SB()
#define EA1(t_, e_) ea[t_][e_] = fmaf(eu[t_][e_], dm1, acc[t_][e_]); OPQ(ea[t_][e_]); SB()
#define EFA(t_, e_) ev[t_][e_] = fmaf(egv[t_][e_], efv[t_][e_], eu[t_][e_]); OPQ(ev[t_][e_]); SB()
#define EA2(t_, e_) ea[t_][e_] = fmaf(egv[t_][e_], ea[t_][e_], ev[t_][e_]); OPQ(ea[t_][e_]); SB()
#define EPK(t_, k_) { u32 v_ = pk_bf(ea[t_][2 * k_], ea[t_][2 * k_ + 1]); OPQ(v_); eo[t_][k_] = v_; } SB()
#define OST(t_) *(u2v*)(erp + t_ * OT) = eo[t_]; SB()
#define WPK(un_, p_, j_) { u32 v_ = pk_bf(wq[un_][2 * p_ + j_ / 2][2 * (j_ & 1)], wq[un_][2 * p_ + j_ / 2][2 * (j_ & 1) + 1]); OPQ(v_); wa_[un_][p_][j_] = v_; } SB()
#define FLAG(w_) asm volatile("" ::: "memory"); *(u32*)(tfp + (w_) * 4) = one; SB()
#define PEEK(i_, a_) pk[i_] = __hip_atomic_load((__attribute__((address_space(3))) u32*)(uintptr_t)(a_), __ATOMIC_RELAXED, __HIP_MEMORY_SCOPE_WORKGROUP); SB()
#define CHECK(i_, a_) if (__builtin_expect(__builtin_amdgcn_ballot_w64(pk[i_] == 0u) != 0ull, 0)) WAITA(a_); asm volatile("" ::: "memory"); SB()
#define MIX2(nm, nd, nv) _Pragma("unroll") for (int r_ = 0; r_ < (nm); ++r_) { __builtin_amdgcn_sched_group_barrier(0x008, 1, 0); \
    if (nd) __builtin_amdgcn_sched_group_barrier(0x100, (nd), 0); if (nv) __builtin_amdgcn_sched_group_barrier(0x002, (nv), 0); }

  {
    ROLE("x");
    const int xw = w;
    char* img = ch_ + O_U + 512;
    STAMP(11);
    __builtin_amdgcn_s_setprio(2);
    if (xi == 31 && w == 0) { const u32 th = ts0; if (lane == 0) __builtin_amdgcn_raw_buffer_store_b64(u2v{th, ~th}, wrs, WS_HEAD + xg * 8, 0, 16); }
    const char* wp; const char* fp; const char* tp0; const char* tp1; char* pp;
    {
      const int r = lane & 7, q = (lane >> 2) & 3;
      fp = img + r * 128 + ((((lane & 8) / 2 + g) ^ (r & 6)) * 16);
      wp = smem + O_FAR + xw * 1024 + (lane & 1) * 512 + (lane & 4) * 16 + g * 16;
      tp0 = img + q * 128 + (((2 * g + ((lane >> 1) & 1)) ^ (q & 2)) * 16) + (lane & 1) * 8;
      tp1 = img + 512 + q * 128 + (((2 * g + ((lane >> 1) & 1)) ^ ((q & 2) + 4)) * 16) + (lane & 1) * 8;
      pp = smem + ((lane >> 3) == 0 || (lane >> 3) == 3 ? O_PART + r * 64 + xw * 16 + g * 8 : O_JUNK + lane * 8);
    }
    const int so = WS_XT + (32 * xw + (lane >> 3)) * XT_PITCH + cg * 128 + (lane & 7) * 16, sstep = 8 * XT_PITCH;
    u32 lo_ = WS_XT + (cc >> 1) * XT_PITCH + (16 * xw + (lane >> 2)) * 128 + (cc & 1) * 64 + (lane & 3) * 16;
    OPQ(lo_);
    SB();
    BAR();
    STAMP(12);
    asm volatile("s_waitcnt vmcnt(0)" ::: "memory");
    STAMP(1);
    {
      u4v wa[4], fb[4]; u2v ta[4], tb[4];
#pragma unroll
      for (int s = 0; s < 4; ++s) { wa[s] = *(const u4v*)(wp + s * 128); fb[s] = *(const u4v*)(fp + s * 1024); }
      SB();
      f4 r = {0.f, 0.f, 0.f, 0.f};
#pragma unroll
      for (int s = 0; s < 4; ++s) r = mma(wa[s], fb[s], r);
      *(f2*)pp = f2{r[0], r[1]};
      ARRIVE(C_PART);
      SB();
#pragma unroll
      for (int s = 0; s < 4; ++s) { ta[s] = tr_read(tp0 + s * 1024); tb[s] = tr_read(tp1 + s * 1024); }
      SB();
      WAITQ(C_GP, 1);
#pragma unroll
      for (int s = 0; s < 4; ++s) __builtin_amdgcn_raw_buffer_store_b128(u4v{ta[s][0], ta[s][1], tb[s][0], tb[s][1]}, wrs, so + s * sstep, 0, 16);
    }
    STAMP(2);
    const u32 dskw = ((gp16)kb[0])[d];
    conv_setup();
    band_setup();
    const u32 tag = par[0];
    ARRIVE(C_PUB);
    asm volatile("s_waitcnt vmcnt(0)" ::: "memory");
    if (lane == 0) __builtin_amdgcn_raw_buffer_store_b32(tag, wrs, WS_FLG + xw * 1024 + cg * 4, 0, 16);
    STAMP(3);
    dm1 = bflo(dskw) - 1.f;
    WAITS(C_TL, 1);
    const u32 tl0 = (u32)__builtin_amdgcn_readfirstlane((int)par[1]);
    u3v fq[2];
    auto rec_ld = [&]() AI {
#pragma unroll
      for (int k = 0; k < 2; ++k) fq[k] = __builtin_amdgcn_raw_buffer_load_b96(wrs, WS_REC + xw * 2048 + k * 1024 + lane * 16, 0, 16);
    };
    pace(tl0 + DT_REC);
    rec_ld();
    SB();
    {
#pragma unroll 1
      for (int spin = 0; spin < SPIN_MAX && __builtin_expect(__any(fq[0][2] != tag || fq[1][2] != tag), 0); ++spin) {
        __builtin_amdgcn_s_sleep(SLEEP_G);
        rec_ld();
        asm volatile("" ::: "memory");
      }
      char* gp = smem + O_GT + xw * 2048 + (lane & 3) * 256 + (lane >> 2) * 16;
#pragma unroll
      for (int k = 0; k < 2; ++k) *(f4*)(gp + k * 1024) = f4{bflo(fq[k][0]), bfhi(fq[k][0]), bflo(fq[k][1]), bfhi(fq[k][1])};
    }
    pace(tl0 + DT_FLG);
    u4v xq[4];
    bool late[4];
    {
      const u32 fl = __builtin_amdgcn_raw_buffer_load_b32(wrs, WS_FLG + (cc >> 6) * 1024 + xw * 64 + g * 256 + c16 * 4, 0, 16);
      const u64 bad = __builtin_amdgcn_ballot_w64(fl != tag);
      asm volatile("" ::: "memory");
#pragma unroll
      for (int k = 0; k < 4; ++k) {
        late[k] = ((bad >> (16 * k)) & 0xffffull) != 0ull;
        xq[k] = __builtin_amdgcn_raw_buffer_load_b128(wrs, lo_ | (late[k] ? 1u << 30 : 0u), k * 8192, 16);
        SB();
      }
    }
    ARRIVE(C_XL);
    STAMP(4);
    WAITQ(C_TAPS + c, 1);
    u4v Bi[2][3], Bo[4], As;
#pragma unroll
    for (int be = 0; be < 3; ++be) { Bi[0][be] = bin(0, be); Bi[1][be] = bin(1, be); }
#pragma unroll
    for (int be = 0; be < 4; ++be) Bo[be] = bout(be);
    As = asp();
    WAITQ(C_PUB, 4);
    char* dp = smem + (lane & 3) * CHB + O_U + 512 + xw * 256 + (lane >> 2) * 16;
    m_setup();
    auto stage = [&](int k) AI {
      SB();
      if (__builtin_expect(late[k], 0)) {
#pragma unroll 1
        for (int spin = 0; spin < SPIN_MAX; ++spin) {
          STAMP(14);
          const u32 f1 = __builtin_amdgcn_raw_buffer_load_b32(wrs, WS_FLG + (cc >> 6) * 1024 + xw * 64 + k * 256 + c16 * 4, 0, 16);
          if (!__any(f1 != tag)) break;
          __builtin_amdgcn_s_sleep(SLEEP_G);
          asm volatile("" ::: "memory");
        }
        asm volatile("" ::: "memory");
        xq[k] = __builtin_amdgcn_raw_buffer_load_b128(wrs, lo_, k * 8192, 16);
        asm volatile("s_waitcnt vmcnt(0)" : "+v"(xq[k]) :: "memory");
      }
      *(u4v*)(dp + k * 1024) = xq[k];
      FLAG(C_Q + k * 4);
    };
    stage(0);
    stage(1);
    STAMP(5);
    __builtin_amdgcn_s_setprio(0);
    WAITA(qa);
    SB();
    FL(0, 0); RL(0); FL(8, 8); RL(1);
    MF0(wq[0][0], Fq[0], band[0]);
    MF0(wq[0][1], Fq[0], band[1]);
    MF0(wq[0][2], ru[0], band[0]);
    MF0(wq[0][3], ru[0], band[1]);
    MF0(wq[0][4], Fq[0], band[2]);
    MF0(wq[0][5], ru[0], band[3]);
    MF0(acc[4], As, Fq[0]); PEEK(6, qa + 16);
    MF0(wq[1][0], Fq[8], band[0]); WPK(0, 0, 0); WPK(0, 0, 1);
    MF0(wq[1][1], Fq[8], band[1]); WPK(0, 0, 2); WPK(0, 0, 3);
    MF0(wq[1][2], ru[1], band[0]); WPK(0, 1, 0); WPK(0, 1, 1);
    MF0(wq[1][3], ru[1], band[1]); WPK(0, 1, 2); WPK(0, 1, 3);
    MF0(wq[1][4], Fq[8], band[2]); CHECK(6, qa + 16);
    MF0(wq[1][5], ru[1], band[3]); WPK(0, 2, 0); WPK(0, 2, 1);
    MF0(acc[5], As, Fq[8]); WPK(0, 2, 2); WPK(0, 2, 3);
    MF0(acc[2], wa_[0][0], Bi[0][0]); FL(16, 16); RL(2);
    MF(acc[2], wa_[0][1], Bi[1][0]); WPK(1, 0, 0); WPK(1, 0, 1);
    MF0(acc[3], wa_[0][0], Bi[0][1]); WPK(1, 0, 2); WPK(1, 0, 3);
    MF(acc[3], wa_[0][1], Bi[1][1]); WPK(1, 1, 0); WPK(1, 1, 1);
    MF(acc[4], wa_[0][0], Bi[0][2]); FST(0); FLAG(C_FAR + 0 * 4);
    MF(acc[4], wa_[0][1], Bi[1][2]); WPK(1, 1, 2); WPK(1, 1, 3);
    MF(acc[4], wa_[0][2], Bo[0]); WPK(1, 2, 0); WPK(1, 2, 1);
    MF(acc[5], wa_[0][2], Bo[1]); WPK(1, 2, 2); WPK(1, 2, 3);
    MF0(acc[6], wa_[0][2], Bo[2]);
    MF0(acc[7], wa_[0][2], Bo[3]); stage(2);
    MF0(wq[2][0], Fq[16], band[0]);
    MF0(wq[2][1], Fq[16], band[1]); stage(3);
    MF0(wq[2][2], ru[2], band[0]);
    MF0(wq[2][3], ru[2], band[1]);
    MF0(wq[2][4], Fq[16], band[2]); FL(24, 24); RL(3);
    MF0(wq[2][5], ru[2], band[3]); WPK(2, 0, 0); WPK(2, 0, 1);
    MF(acc[6], As, Fq[16]); WPK(2, 0, 2); WPK(2, 0, 3);
    MF(acc[3], wa_[1][0], Bi[0][0]); WPK(2, 1, 0); WPK(2, 1, 1);
    MF(acc[3], wa_[1][1], Bi[1][0]); WPK(2, 1, 2); WPK(2, 1, 3);
    MF(acc[4], wa_[1][0], Bi[0][1]); WPK(2, 2, 0); WPK(2, 2, 1);
    MF(acc[4], wa_[1][1], Bi[1][1]); WPK(2, 2, 2); WPK(2, 2, 3);
    MF(acc[5], wa_[1][0], Bi[0][2]); FST(1); FLAG(C_FAR + 1 * 4);
    MF(acc[5], wa_[1][1], Bi[1][2]); PEEK(7, qa + 32);
    MF(acc[5], wa_[1][2], Bo[0]);
    MF(acc[6], wa_[1][2], Bo[1]);
    MF(acc[7], wa_[1][2], Bo[2]);
    MF0(wq[3][0], Fq[24], band[0]);
    MF0(wq[3][1], Fq[24], band[1]); CHECK(7, qa + 32);
    MF0(wq[3][2], ru[3], band[0]);
    MF0(wq[3][3], ru[3], band[1]);
    MF0(wq[3][4], Fq[24], band[2]); FL(32, 32); RL(4);
    MF0(wq[3][5], ru[3], band[3]); WPK(3, 0, 0); WPK(3, 0, 1);
    MF(acc[7], As, Fq[24]); WPK(3, 0, 2); WPK(3, 0, 3);
    MF(acc[4], wa_[2][0], Bi[0][0]); WPK(3, 1, 0); WPK(3, 1, 1);
    MF(acc[4], wa_[2][1], Bi[1][0]); WPK(3, 1, 2); WPK(3, 1, 3);
    MF(acc[5], wa_[2][0], Bi[0][1]); WPK(3, 2, 0); WPK(3, 2, 1);
    MF(acc[5], wa_[2][1], Bi[1][1]); FL(40, 40); RL(5);
    MF(acc[6], wa_[2][0], Bi[0][2]); FST(2); FLAG(C_FAR + 2 * 4);
    MF(acc[6], wa_[2][1], Bi[1][2]); WPK(3, 2, 2); WPK(3, 2, 3);
    MF(acc[6], wa_[2][2], Bo[0]); AL(0);
    MF(acc[7], wa_[2][2], Bo[1]); AL(1);
    MF0(wq[4][0], Fq[32], band[0]); AL(2);
    MF0(wq[4][1], Fq[32], band[1]); AL(3);
    MF0(wq[4][2], ru[4], band[0]); AL(4);
    MF0(wq[4][3], ru[4], band[1]); WPK(4, 0, 0); WPK(4, 0, 1);
    MF(acc[5], wa_[3][0], Bi[0][0]); WPK(4, 0, 2); WPK(4, 0, 3);
    MF(acc[5], wa_[3][1], Bi[1][0]); WPK(4, 1, 0); WPK(4, 1, 1);
    MF(acc[6], wa_[3][0], Bi[0][1]); WPK(4, 1, 2); WPK(4, 1, 3);
    MF(acc[6], wa_[3][1], Bi[1][1]); AL(5);
    MF(acc[7], wa_[3][0], Bi[0][2]); FST(3); FLAG(C_FAR + 3 * 4);
    MF(acc[7], wa_[3][1], Bi[1][2]); AL(6);
    MF(acc[7], wa_[3][2], Bo[0]); FL(33, 33); AL(7);
    MF0(wq[5][0], Fq[40], band[0]); FL(34, 34); AL(8);
    MF0(wq[5][1], Fq[40], band[1]); FL(35, 35); AL(9);
    MF0(wq[5][2], ru[5], band[0]); WPK(5, 0, 0); WPK(5, 0, 1);
    MF0(wq[5][3], ru[5], band[1]); AL(10);
    MF(acc[6], wa_[4][0], Bi[0][0]); PEEK(8, qa + 48); FL(36, 36);
    MF(acc[6], wa_[4][1], Bi[1][0]); FL(37, 37); WPK(5, 0, 2);
    MF(acc[7], wa_[4][0], Bi[0][1]); FL(38, 38); WPK(5, 0, 3);
    MF(acc[7], wa_[4][1], Bi[1][1]); FL(39, 39); WPK(5, 1, 0);
    MF(acc[6], At[0], Fq[32]); WPK(5, 1, 1); WPK(5, 1, 2);
    MF(acc[6], At[1], Fq[33]); CHECK(8, qa + 48);
    MF(acc[6], At[2], Fq[34]); WPK(5, 1, 3); FL(41, 41);
    MF(acc[6], At[3], Fq[35]); AL(11);
    MF(acc[7], wa_[5][0], Bi[0][0]); FL(42, 42); AL(12);
    MF(acc[7], wa_[5][1], Bi[1][0]); AL(13);
    MF(acc[6], At[4], Fq[36]); FL(43, 43); AL(14);
    MF(acc[6], At[5], Fq[37]); AL(15);
    MF(acc[6], At[6], Fq[38]); FL(44, 44); AL(16);
    MF(acc[6], At[7], Fq[39]);
    MF(acc[6], At[8], Fq[40]); FL(45, 45);
    MF(acc[7], At[0], Fq[40]);
    MF(acc[6], At[9], Fq[41]); FL(46, 46);
    MF(acc[7], At[1], Fq[41]);
    MF(acc[6], At[10], Fq[42]); FL(47, 47);
    MF(acc[7], At[2], Fq[42]);
    MF(acc[6], At[11], Fq[43]); FL(48, 48);
    MF(acc[7], At[3], Fq[43]);
    MF(acc[6], At[12], Fq[44]); FL(49, 49); ULD(6);
    MF(acc[7], At[4], Fq[44]); FL(50, 50); GLD(6);
    MF(acc[6], At[13], Fq[45]); FL(51, 51);
    MF(acc[7], At[5], Fq[45]); FL(52, 52);
    MF(acc[6], At[14], Fq[46]); FL(53, 53);
    MF(acc[7], At[6], Fq[46]); FL(54, 54); EUN(6, 0);
    MF(acc[6], At[15], Fq[47]); FL(55, 55); EUN(6, 1);
    MF(acc[7], At[7], Fq[47]); FL(56, 56); EUN(6, 2);
    MF(acc[6], At[16], Fq[48]); EUN(6, 3);
    MF(acc[7], At[8], Fq[48]); ULD(7); GLD(7);
    MF(acc[7], At[9], Fq[49]);
    MF(acc[7], At[10], Fq[50]);
    MF(acc[7], At[11], Fq[51]);
    MF(acc[7], At[12], Fq[52]); EA1(6, 0); EA1(6, 1); EA1(6, 2); EA1(6, 3);
    MF(acc[7], At[13], Fq[53]); EA2(6, 0); EA2(6, 1); EA2(6, 2); EA2(6, 3);
    MF(acc[7], At[14], Fq[54]); EPK(6, 0); EPK(6, 1);
    MF(acc[7], At[15], Fq[55]); OST(6); FLAG(C_TILE + 6 * 4);
    MF(acc[7], At[16], Fq[56]); EUN(7, 0); EUN(7, 1); EUN(7, 2); EUN(7, 3); NOP(3); EA1(7, 0); EA1(7, 1); EA1(7, 2); EA1(7, 3); EA2(7, 0); EA2(7, 1); EA2(7, 2); EA2(7, 3); EPK(7, 0); EPK(7, 1); OST(7); FLAG(C_TILE + 7 * 4);
    STAMP(9);
    ROLE_END();
  }
  {
    ROLE("h");
    const int hw = w - 5;
    const int j = xg * 28 + xi;
    const int s1 = j >> 2, kq = j & 3;
    const bool own = xi < 28;
    const u64 wbase = hw == 0 ? kb[1] : kb[2];
    const int pitch = F * 2;
    auto hrs = __builtin_amdgcn_make_buffer_rsrc((void*)wbase, 0, own ? D * F * 2 : 0, 0x00020000);
    const int r8 = lane >> 3;
    const int vo = (256 * kq * F + 64 * s1) * 2 + (8 * (r8 >> 2) + (r8 & 3)) * pitch + (((lane & 7) * 16) ^ (((r8 >> 1) & 3) * 32));
    auto dma = [&](int h, int sB, int pt, u32 dst) AI {
      const int m = h >> 1;
#pragma unroll
      for (int i = 0; i < 2; ++i)
        asm volatile("s_mov_b32 m0, %3\n\ts_nop 0\n\tbuffer_load_dwordx4 %0, %1, %2 offen nt lds"
                     :: "v"(vo), "s"(hrs), "s"((m & 1) * 32 * pt + (m >> 1) * sB + (16 * i + 4 * (h & 1)) * pt), "s"(dst + i * 1024) : "memory");
    };
    const int q = c16 >> 2, p = c16 & 3;
    int tro[4];
#pragma unroll
    for (int ct = 0; ct < 4; ++ct) tro[ct] = (4 * g + q) * 128 + ((ct ^ (((4 * g + q) >> 1) & 3)) * 32) + p * 8;
    const int tok = lane & 7;
    u4v wd[2][8];
    auto wd_ld = [&](int k, int cb) AI {
#pragma unroll
      for (int i = 0; i < 8; ++i) {
        const char* pp = smem + O_WD + cb * 8192 + (i >> 2) * 4096 + tro[i & 3];
        const u2v lo = tr_read(pp), hi = tr_read(pp + 2048);
        wd[k][i] = u4v{lo[0], lo[1], hi[0], hi[1]};
      }
    };
    u32 p2o, p2k[4], hlo, hhi;
    auto f2_setup = [&](int cb0) AI {
      p2o = WS_P2 + ((64 * kq + 16 * cb0 + (lane & 8) / 2 + g) * 56 + s1) * 128 + tok * 16;
      hlo = lbase + O_HB + ((lane & 8) ? 512 : g * 128 + tok * 16);
      hhi = lbase + O_HB + ((lane & 8) ? g * 128 + tok * 16 : 512);
#pragma unroll
      for (int i = 0; i < (cb0 == 2 ? 4 : 2); ++i) { p2k[i] = p2o + i * 8 * 7168; OPQ(p2k[i]); }
      OPQ(hlo); OPQ(hhi);
    };
    auto f2_job = [&](int nb, u32 t10) AI {
      u4v hb[4];
      {
        u32 cnt; int n_ = 1 << 20;
        asm volatile("1:\n\tds_read_b32 %4, %6\n\tds_read_b128 %0, %7\n\tds_read_b128 %1, %8\n\tds_read_b128 %2, %7 offset:528\n\tds_read_b128 %3, %8 offset:528\n\t"
                     "s_sub_u32 %5, %5, 1\n\ts_cbranch_scc1 2f\n\ts_waitcnt lgkmcnt(0)\n\tv_cmp_gt_u32 vcc, 2, %4\n\ts_cbranch_vccnz 1b\n2:\n\ts_waitcnt lgkmcnt(0)"
                     : "=&v"(hb[0]), "=&v"(hb[1]), "=&v"(hb[2]), "=&v"(hb[3]), "=&v"(cnt), "+s"(n_) : "v"(CTLA(C_HRDY)), "v"(hlo), "v"(hhi) : "vcc", "scc", "memory");
      }
      STAMP(11);
      f4 r[4];
#pragma unroll
      for (int i = 0; i < 2 * nb; ++i) {
        const u4v* wp = &wd[i >> 1][(i & 1) * 2];
        r[i] = mma(wp[0], hb[0], f4{0.f, 0.f, 0.f, 0.f});
        r[i] = mma(wp[1], hb[1], r[i]);
        r[i] = mma(wp[4], hb[2], r[i]);
        r[i] = mma(wp[5], hb[3], r[i]);
      }
      SB();
#pragma unroll
      for (int i = 0; i < 2 * nb; ++i) {
        u4v v = __builtin_bit_cast(u4v, r[i]);
#pragma unroll
        for (int e = 0; e < 4; ++e) v[e] = __builtin_amdgcn_bitop3_b32(v[e], t10, TMASK, 0xd8);
        __builtin_amdgcn_raw_buffer_store_b128(v, wrs, p2k[i], 0, 16);
      }
      STAMP(12);
    };
    const u32 rb = lbase + O_RING + hw * (NSLOT * 2048);
    pace(ts0 + WDELAY);
    STAMP(2);
#pragma unroll
    for (int h = 0; h < WWIN; ++h) dma(h, 64 * F * 2, F * 2, rb + h * 2048);
    __builtin_amdgcn_s_setprio(3);
    BAR();
    STAMP(3);
    if (__builtin_expect(own, 1)) {
      u4v wv[32];
      f4 a1[4] = {f4{0.f, 0.f, 0.f, 0.f}, f4{0.f, 0.f, 0.f, 0.f}, f4{0.f, 0.f, 0.f, 0.f}, f4{0.f, 0.f, 0.f, 0.f}};
      u4v xb[8];
      auto f1 = [&](int k0, int k1) AI {
#pragma unroll
        for (int k = k0; k < k1; ++k)
#pragma unroll
          for (int ct = 0; ct < 4; ++ct) a1[ct] = mma(wv[4 * k + ct], xb[k], a1[ct]);
      };
      u32 xo = lbase + O_XS + tok * XS_PITCH + g * 16;
      u32 p1o = WS_P1 + s1 * 16384 + kq * 4096 + hw * 2048 + (lane & 8) * 64 + g * 128 + tok * 16;
      u32 t10, tpub;
      OPQ(xo); OPQ(p1o);
      constexpr int HX = (16 - WWIN + 1) & ~1;
#pragma unroll
      for (int h = 0; h < 16; ++h) {
        const int left = (h + WWIN < 16 ? WWIN : 16 - h) - 1;
        asm volatile("s_waitcnt vmcnt(%0)" :: "n"(2 * left) : "memory");
        if (h + WWIN < 16) {
          asm volatile("s_waitcnt lgkmcnt(0)" ::: "memory");
          dma(h + WWIN, 64 * F * 2, F * 2, rb + ((h + WWIN) % NSLOT) * 2048);
        }
#pragma unroll
        for (int ct = 0; ct < 4; ++ct) {
          const u2v t = tr_read(smem + O_RING + hw * (NSLOT * 2048) + (h % NSLOT) * 2048 + tro[ct]);
          wv[4 * (h >> 1) + ct][2 * (h & 1)] = t[0]; wv[4 * (h >> 1) + ct][2 * (h & 1) + 1] = t[1];
        }
        if (h == H2_AT) ARRIVE(C_WGU);
        if (h == HX - 1) {
          STAMP(4);
          WAITQ(C_XS, 1);
          STAMP(1);
#pragma unroll
          for (int k = 0; k < 8; ++k) xb[k] = *(const __attribute__((address_space(3))) u4v*)(uintptr_t)(xo + k * 64);
          t10 = par[0]; tpub = par[2];
          SB();
          f1(0, 2);
          SB();
        }
        if (h >= HX) { f1(h - HX + 2, h - HX + 3); SB(); }
      }
      STAMP(5);
      f1(18 - HX, 8);
      t10 &= TMASK;
#pragma unroll
      for (int a = 0; a < 2; ++a) {
        u4v v;
#pragma unroll
        for (int e = 0; e < 4; ++e)
          v[e] = ((u32)__builtin_amdgcn_update_dpp(__float_as_int(a1[2 * a][e]), __float_as_int(a1[2 * a + 1][e]), 0xe4, 0xf, 0xc, false) & ~TMASK) | t10;
        __builtin_amdgcn_raw_buffer_store_b128(v, wrs, p1o + a * 1024, 0, 16);
      }
      STAMP(6);
      const u32 tn = now32();
      u32 voff = WS_P1 + s1 * 16384 + hw * 1024 + lane * 16;
      u32 ho = lbase + O_HB + hw * 528 + g * 128 + tok * 16 + (lane & 8);
      float nl2e = -1.44269504088896341f;
      u32 wda = CTLA(C_WD) + hw * 4;
      OPQ(voff); OPQ(ho); OPQ(nl2e); OPQ(wda);
      f2_setup(hw & 1);
      pace2(tn, (u32)__builtin_amdgcn_readfirstlane((int)tpub) + DE_P1, DT_P1);
      u4v pb[8];
      auto p1_ld = [&]() AI {
#pragma unroll
        for (int i = 0; i < 8; ++i) { pb[i] = __builtin_amdgcn_raw_buffer_load_b128(wrs, voff + (i >> 2) * 2048, (i & 3) * 4096, 16); SB(); }
      };
      p1_ld();
      SB();
      f4 G, U;
      bool early = false;
#pragma unroll 1
      for (int spin = 0; spin < SPIN_MAX; ++spin) {
        u32 bad = 0;
        OPQ(bad);
        asm volatile("" :: "v"(pb[0]));
        const u32 cw = __hip_atomic_load((__attribute__((address_space(3))) u32*)(uintptr_t)wda, __ATOMIC_RELAXED, __HIP_MEMORY_SCOPE_WORKGROUP);
        G = tsum4(bad, t10, pb[0], pb[1], pb[2], pb[3]);
        early = __builtin_amdgcn_ballot_w64(cw == 0u) == 0ull;
        asm volatile("" ::: "memory");
        if (__builtin_expect(early, 1)) wd_ld(0, hw);
        SB();
        silu4(G, nl2e);
        U = tsum4(bad, t10, pb[4], pb[5], pb[6], pb[7]);
        if (__builtin_expect(!__any((bad & TMASK) != 0), 1)) break;
        STAMP(14);
        p1_ld();
        asm volatile("" ::: "memory");
      }
      STAMP(7);
      {
        float hv[4];
#pragma unroll
        for (int e = 0; e < 4; ++e) { hv[e] = G[e] * U[e]; asm("" : "+v"(hv[e])); }
        *(__attribute__((address_space(3))) u2v*)(uintptr_t)ho = u2v{pk_bf(hv[0], hv[1]), pk_bf(hv[2], hv[3])};
      }
      ARRIVE(C_HRDY);
      STAMP(8);
      if (__builtin_expect(!early, 0)) { WAITA(wda); wd_ld(0, hw); }
      f2_job(1, t10);
    }
    if (hw == 0) {
      WAITS(C_SEL, 1);
      const u32 t10 = par[0] & TMASK;
      const u32 tpub = (u32)__builtin_amdgcn_readfirstlane((int)par[2]);
      const u32 mykey = ((const u32*)(smem + O_SEL))[lane & 7];
      if (own) pace2(now32(), tpub + DE_P2, DT_P2); else pace(tpub + DE_P2 - 40);
      u4v pa[7];
      const int voff = WS_P2 + cc * 7168 + lane * 16;
#pragma unroll
      for (int k = 0; k < 7; ++k) pa[k] = __builtin_amdgcn_raw_buffer_load_b128(wrs, voff + (k & 3) * 1024, (k >> 2) * 4096, 16);
      SB();
      const int row = (int)(~mykey & 2047u);
      const float rsig = rb1(sigm(__uint_as_float((mykey ^ (~(u32)((int)mykey >> 31) | 0x80000000u)) & 0xffff0000u)));
      WAITA(CTLA(C_TILE) + (lane & 31) * 4);
      float cf_ = rsig * (1.f - *(const float*)(smem + O_GT + (row >> 8) * 1024 + ((row >> 2) & 3) * 256 + ((row >> 4) & 15) * 16 + (row & 3) * 4));
      float base[4];
#pragma unroll
      for (int e = 0; e < 4; ++e) base[e] = bflo(*(const u16*)(smem + e * CHB + O_OUT + (row >> 8) * OT + ((row >> 2) & 3) * 144 + ((row >> 4) & 15) * 8 + (row & 3) * 2));
      u64 op = kc[1] + (row * D + cc * 4) * 2;
      { float cq = cf_; OPQ(cq); cf_ = cq; OPQ(op); }
#pragma unroll
      for (int e = 0; e < 4; ++e) OPQ(base[e]);
      STAMP(13);
      f4 y;
#pragma unroll 1
      for (int spin = 0; spin < SPIN_MAX; ++spin) {
        u32 bad = 0;
        OPQ(bad);
        y = tsum4(bad, t10, pa[0], pa[1], pa[2], pa[3]);
        y = tsum3(bad, t10, y, pa[4], pa[5], pa[6]);
        if (__builtin_expect(!__any((bad & TMASK) != 0), 1)) break;
        STAMP(15);
#pragma unroll
        for (int k = 0; k < 7; ++k) pa[k] = __builtin_amdgcn_raw_buffer_load_b128(wrs, voff + (k & 3) * 1024, (k >> 2) * 4096, 16);
        asm volatile("" ::: "memory");
      }
      STAMP(9);
      float o[4];
#pragma unroll
      for (int e = 0; e < 4; ++e) {
        float v = y[e] + dpp_f<0x128>(y[e]);
        auto s16 = __builtin_amdgcn_permlane16_swap(__float_as_uint(v), __float_as_uint(v), false, false);
        v = __uint_as_float(s16[0]) + __uint_as_float(s16[1]);
        auto s32 = __builtin_amdgcn_permlane32_swap(__float_as_uint(v), __float_as_uint(v), false, false);
        o[e] = fmaf(__uint_as_float(s32[0]) + __uint_as_float(s32[1]), cf_, base[e]);
      }
      if (lane < 8) *(__attribute__((address_space(1))) u2v*)op = u2v{pk_bf(o[0], o[1]), pk_bf(o[2], o[3])};
    }
    ROLE_END();
  }
  {
    ROLE("g");
    const u32 pLA = __builtin_nontemporal_load((gp32)ka[3] + d * (NM / 2) + lane);
    const u32 pAI = __builtin_nontemporal_load((gp32)ka[4] + d * (NM / 2) + lane);
    const u32 pCR = __builtin_nontemporal_load((gp32)ka[5] + d * (NM / 2) + lane);
    const u32 pCI = __builtin_nontemporal_load((gp32)ka[6] + d * (NM / 2) + lane);
    const u32 ldt = ((gp16)ka[7])[d], dskw = ((gp16)kb[0])[d];
    u4v ixa[2];
#pragma unroll
    for (int r = 0; r < 2; ++r) ixa[r] = *(const u4v*)(g_tapidx.v + lane * 16 + r * 8);
    BAR();
    asm volatile("s_mov_b32 m0, %0\n\ts_nop 0" :: "s"(lbase + c * CHB) : "memory");
#define PUT1(off, val) asm volatile("ds_write_addtid_b32 %0 offset:%1" :: "v"(val), "n"(off) : "memory")
#define PUTR(off, val) do { const ch v_ = (val); asm volatile("ds_write_addtid_b32 %0 offset:%2\n\tds_write_addtid_b32 %1 offset:%3" :: "v"(v_.r), "v"(v_.i), "n"(off), "n"((off) + 256) : "memory"); } while (0)
    const char* pf = ch_ + c16 * TROW + g * 16;
    PUT1(O_TAP, 0x3c003c00u); PUT1(O_TAP + 256, 0u);
    PUT1(O_U, 0u); PUT1(O_U + 256, 0u);
    SB();
    const float dt = rb1(exp_bf(bflo(ldt)));
    const f2 a2 = f2{-rb1(exp_bf(bflo(pLA))), -rb1(exp_bf(bfhi(pLA)))} * dt * 1.44269504088896341f;
    const f2 br = unbf(pAI) * dt * 0.15915494309189535f;
    STAMP(1);
    u4v Lf[2][8], Wf[8];
    f4 kk[2][2];
    float fk;
    {
      const cf z1 = cexp_p(a2, br, 1.f);
      const cf z2 = csq(z1), z4 = csq(z2), z8 = csq(z4), z16 = csq(z8), z32 = csq(z16), z64 = csq(z32);
      ch base[4], anc[4];
      base[1] = tohc(z8); base[2] = tohc(z16); base[3] = tohc(cmul(z16, z8));
      anc[1] = tohc(z32); anc[2] = tohc(z64); anc[3] = tohc(cmul(z64, z32));
      PUTR(O_OUT + 5 * TROW, tohc(csq(z64)));
#pragma unroll
      for (int j = 1; j < 16; ++j) {
        const int q = j >> 2, r = j & 3;
        PUTR(O_TAP + j * TROW, q == 0 ? base[r] : r == 0 ? anc[q] : hmul(anc[q], base[r]));
      }
#pragma unroll
      for (int s = 0; s < 8; ++s) Lf[0][s] = *(const u4v*)(pf + O_TAP + s * 64);
#pragma unroll
      for (int s = 0; s < 8; ++s) Lf[1][s] = *(const u4v*)(ch_ + O_OUT + 5 * TROW + g * 16 + s * 64);
      SB();
      STAMP(11);
      u32 mx = max(max(pCR << 17, pCR << 1), max(pCI << 17, pCI << 1));
      mx = wave_max_u32(mx);
      int ex = (int)(mx >> 24);
      ex = ex < 1 ? 1 : (ex > 253 ? 253 : ex);
      const float sdn = __uint_as_float((u32)(254 - ex) << 23);
      fk = 2.f * dt * __uint_as_float((u32)ex << 23);
      cf C; C.r = unbf(pCR) * sdn; C.i = unbf(pCI) * sdn;
      const cf E = cexp_p(a2, br, 256.f);
      const ch y1 = toh(z1), y2 = toh(z2), y4 = toh(z4), y6 = toh(cmul(z4, z2));
      {
        const ch R0 = toh(C), R2 = hmul(R0, y2), R4 = hmul(R0, y4), R6 = hmul(R0, y6);
        PUTR(O_TAP, R0); PUTR(O_TAP + 2 * TROW, R2); PUTR(O_TAP + 4 * TROW, R4); PUTR(O_TAP + 6 * TROW, R6);
        PUTR(O_TAP + 1 * TROW, hmul(R0, y1)); PUTR(O_TAP + 3 * TROW, hmul(R2, y1)); PUTR(O_TAP + 5 * TROW, hmul(R4, y1)); PUTR(O_TAP + 7 * TROW, hmul(R6, y1));
        C = cmul(C, E);
        const ch R8 = toh(C);
        PUTR(O_TAP + 8 * TROW, R8); PUTR(O_TAP + 9 * TROW, hmul(R8, y2)); PUTR(O_TAP + 10 * TROW, hmul(R8, y4)); PUTR(O_TAP + 11 * TROW, hmul(R8, y6));
        C = cmul(C, E);
        const ch R12 = toh(C);
        PUTR(O_TAP + 12 * TROW, R12); PUTR(O_TAP + 13 * TROW, hmul(R12, y4));
        C = cmul(C, E);
        const ch R14 = toh(C);
        PUTR(O_TAP + 14 * TROW, R14); PUTR(O_TAP + 15 * TROW, hmul(R14, y4));
      }
#pragma unroll
      for (int s = 0; s < 8; ++s) Wf[s] = *(const u4v*)(pf + O_TAP + s * 64);
      SB();
      STAMP(12);
#pragma unroll
      for (int i = 0; i < 5; ++i) { C = cmul(C, E); PUTR(O_OUT + i * TROW, toh(C)); }
      SB();
      auto mh = [&](u4v a, u4v bq, f4 cin) AI { return __builtin_amdgcn_mfma_f32_16x16x32_f16(as_h8(a), as_h8(bq), cin, 0, 0, 0); };
      kk[0][0] = f4{0.f, 0.f, 0.f, 0.f}; kk[0][1] = kk[0][0]; kk[1][0] = kk[0][0]; kk[1][1] = kk[0][0];
#pragma unroll
      for (int s = 0; s < 5; ++s) {
#pragma unroll
        for (int k = 0; k < 4; ++k) {
          if (k < 2) { if (s < 4) kk[0][0] = mh(Wf[s + 4 * k], Lf[0][s + 4 * k], kk[0][0]); }
          else if (s > 0) { const int u = s - 1 + 4 * (k - 2); kk[0][1] = mh(Wf[u], Lf[1][u], kk[0][1]); Wf[u] = *(const u4v*)(pf + O_OUT + u * 64); }
          SB();
          if (s < 4) {
            const ch o = hmul(ch{ash(Lf[0][s][k]), ash(Lf[0][s + 4][k])}, ch{ash(Lf[1][s][k]), ash(Lf[1][s + 4][k])});
            Lf[1][s][k] = __builtin_bit_cast(u32, o.r); Lf[1][s + 4][k] = __builtin_bit_cast(u32, o.i);
          }
          if (s == 3 && k == 3) asm volatile("s_nop 1");
          SB();
        }
      }
#pragma unroll
      for (int s = 0; s < 8; ++s) { kk[1][0] = mh(Wf[s], Lf[0][s], kk[1][0]); kk[1][1] = mh(Wf[s], Lf[1][s], kk[1][1]); }
    }
    {
      u32 ia[16];
      const u32 kb_ = lbase + c * CHB + O_OUT;
#pragma unroll
      for (int e = 0; e < 16; ++e) { const u32 iw = ixa[e >> 3][(e >> 1) & 3]; ia[e] = kb_ + ((e & 1) ? (iw >> 16) : (iw & 0xffffu)); }
      MIX2(16, 0, 1)
      SB();
      STAMP(2);
      char* ktp = ch_ + O_OUT + c16 * KT_PITCH + g * 8;
#pragma unroll
      for (int hf = 1; hf >= 0; --hf) {
#pragma unroll
        for (int ct = 0; ct < 2; ++ct) {
          const f4 v = kk[hf][ct] * fk;
          *(u32*)(ktp + ct * 16 * KT_PITCH + hf * 32) = pk_bf(v[0], v[1]);
          *(u32*)(ktp + ct * 16 * KT_PITCH + hf * 32 + 4) = pk_bf(v[2], v[3]);
        }
        asm volatile("" ::: "memory");
      }
      u32 zz = 0u; OPQ(zz);
      *(u32*)(ch_ + O_OUT + KT_ZERO + (lane & 7) * 4) = zz;
      asm volatile("" ::: "memory");
      u32 v[16], pe[9];
#pragma unroll
      for (int e = 15; e >= 0; --e) v[e] = *(const __attribute__((address_space(3))) u16*)(uintptr_t)ia[e];
      SB();
      pe[8] = zz;
#pragma unroll
      for (int i = 7; i >= 0; --i) {
        pe[i] = v[2 * i] | (v[2 * i + 1] << 16);
        const u32 nx = (u32)__builtin_amdgcn_update_dpp((int)dpp_u<0x134>(pe[i + 1]), (int)pe[i], 0x130, 0xf, 0xf, false);
        const u32 po = __builtin_amdgcn_alignbyte(nx, pe[i], 2);
        PUT1(O_TAP + i * 256, pe[i]); PUT1(O_TAP + 2 * TAP_PITCH - 4 + i * 256, pe[i]);
        PUT1(O_TAP + TAP_PITCH + i * 256, po); PUT1(O_TAP + 3 * TAP_PITCH - 4 + i * 256, po);
      }
      ARRIVE(C_TAPS + c);
    }
    STAMP(3);
    IDS();
    conv_setup();
    dm1 = bflo(dskw) - 1.f;
    atload(0, 17);
    m_setup();
    __builtin_amdgcn_s_setprio(1);
    SB();
    WAITA(qa);
    STAMP(4);
    SB();
    FL(0, -7); FL(1, -6); FL(2, -5); FL(3, -4); FL(4, -3); FL(5, -2); FL(6, -1); FL(7, 0); ULD(0); GLD(0);
    MF0(acc[0], At[9], Fq[0]); EUN(0, 0); EUN(0, 1);
    MF(acc[0], At[10], Fq[1]); EUN(0, 2); EUN(0, 3);
    MF(acc[0], At[11], Fq[2]);
    MF(acc[0], At[12], Fq[3]);
    MF(acc[0], At[13], Fq[4]);
    MF(acc[0], At[14], Fq[5]);
    MF(acc[0], At[15], Fq[6]); FL(8, 1);
    MF(acc[0], At[16], Fq[7]);
    MF0(acc[1], At[1], Fq[0]); FL(9, 2);
    MF(acc[1], At[2], Fq[1]);
    MF(acc[1], At[3], Fq[2]); FL(10, 3);
    MF(acc[1], At[4], Fq[3]);
    MF(acc[1], At[5], Fq[4]); FL(11, 4); EA1(0, 0);
    MF(acc[1], At[6], Fq[5]); EA1(0, 1); EA1(0, 2);
    MF(acc[1], At[7], Fq[6]); EA1(0, 3); PEEK(0, qa + 16);
    MF(acc[1], At[8], Fq[7]); FL(12, 5); EA2(0, 0);
    MF0(acc[2], At[0], Fq[7]); EA2(0, 1); EA2(0, 2);
    MF(acc[1], At[9], Fq[8]); EA2(0, 3); EPK(0, 0);
    MF(acc[2], At[1], Fq[8]); EPK(0, 1); FL(13, 6);
    MF(acc[1], At[10], Fq[9]); OST(0); FLAG(C_TILE + 0 * 4);
    MF(acc[2], At[2], Fq[9]); FL(14, 7); FL(15, 8);
    MF(acc[1], At[11], Fq[10]); CHECK(0, qa + 16);
    MF(acc[2], At[3], Fq[10]);
    MF(acc[1], At[12], Fq[11]); FL(16, 9);
    MF(acc[2], At[4], Fq[11]); ULD(1); GLD(1);
    MF(acc[1], At[13], Fq[12]); FL(17, 10);
    MF(acc[2], At[5], Fq[12]);
    MF(acc[1], At[14], Fq[13]); FL(18, 11);
    MF(acc[2], At[6], Fq[13]);
    MF(acc[1], At[15], Fq[14]); FL(19, 12); EUN(1, 0);
    MF(acc[2], At[7], Fq[14]); EUN(1, 1); EUN(1, 2);
    MF(acc[1], At[16], Fq[15]); FL(20, 13); EUN(1, 3);
    MF(acc[2], At[8], Fq[15]);
    MF0(acc[3], At[0], Fq[15]); FL(21, 14);
    MF(acc[2], At[9], Fq[16]);
    MF(acc[3], At[1], Fq[16]); FL(22, 15); EA1(1, 0);
    MF(acc[2], At[10], Fq[17]); EA1(1, 1); EA1(1, 2);
    MF(acc[3], At[2], Fq[17]); FL(23, 16); EA1(1, 3);
    MF(acc[2], At[11], Fq[18]); PEEK(2, fa + 0); EA2(1, 0);
    MF(acc[3], At[3], Fq[18]); EA2(1, 1); EA2(1, 2);
    MF(acc[2], At[12], Fq[19]); EA2(1, 3); EPK(1, 0);
    MF(acc[3], At[4], Fq[19]); EPK(1, 1); FL(24, 17);
    MF(acc[2], At[13], Fq[20]); OST(1); FLAG(C_TILE + 1 * 4);
    MF(acc[3], At[5], Fq[20]); FL(25, 18); CHECK(2, fa + 0);
    MF(acc[2], At[14], Fq[21]); FL(26, 19); ULD(2);
    MF(acc[3], At[6], Fq[21]); FLD(2); GLD(2);
    MF(acc[2], At[15], Fq[22]); FL(27, 20); PEEK(1, qa + 32);
    MF(acc[3], At[7], Fq[22]);
    MF(acc[2], At[16], Fq[23]); FL(28, 21);
    MF(acc[3], At[8], Fq[23]); EUN(2, 0); EUN(2, 1);
    MF0(acc[4], At[0], Fq[23]); FL(29, 22); EUN(2, 2);
    MF(acc[3], At[9], Fq[24]); EUN(2, 3); EA1(2, 0);
    MF(acc[4], At[1], Fq[24]); FL(30, 23); EA1(2, 1);
    MF(acc[3], At[10], Fq[25]); EA1(2, 2); EA1(2, 3);
    MF(acc[4], At[2], Fq[25]); FL(31, 24); EFA(2, 0);
    MF(acc[3], At[11], Fq[26]); EFA(2, 1); EFA(2, 2);
    MF(acc[4], At[3], Fq[26]); EFA(2, 3); PEEK(3, fa + 16);
    MF(acc[3], At[12], Fq[27]); EA2(2, 0); EA2(2, 1);
    MF(acc[4], At[4], Fq[27]); EA2(2, 2); EA2(2, 3);
    MF(acc[3], At[13], Fq[28]); CHECK(1, qa + 32); EPK(2, 0); EPK(2, 1);
    MF(acc[4], At[5], Fq[28]); FL(32, 25); OST(2);
    MF(acc[3], At[14], Fq[29]); FLAG(C_TILE + 2 * 4); FL(33, 26);
    MF(acc[4], At[6], Fq[29]); FL(34, 27); CHECK(3, fa + 16);
    MF(acc[3], At[15], Fq[30]); ULD(3); FL(35, 28);
    MF(acc[4], At[7], Fq[30]); FLD(3); GLD(3);
    MF(acc[3], At[16], Fq[31]); FL(36, 29);
    MF(acc[4], At[8], Fq[31]);
    MF0(acc[5], At[0], Fq[31]); FL(37, 30);
    MF(acc[4], At[9], Fq[32]); EUN(3, 0); EUN(3, 1);
    MF(acc[5], At[1], Fq[32]); PEEK(4, fa + 32); FL(38, 31);
    MF(acc[4], At[10], Fq[33]); EUN(3, 2); EUN(3, 3);
    MF(acc[5], At[2], Fq[33]); FL(39, 32); EA1(3, 0);
    MF(acc[4], At[11], Fq[34]); EA1(3, 1); EA1(3, 2);
    MF(acc[5], At[3], Fq[34]); EA1(3, 3); EFA(3, 0);
    MF(acc[4], At[12], Fq[35]); EFA(3, 1); EFA(3, 2); EFA(3, 3);
    MF(acc[5], At[4], Fq[35]); EA2(3, 0); EA2(3, 1); EA2(3, 2); EA2(3, 3);
    MF(acc[4], At[13], Fq[36]); FL(40, 33); EPK(3, 0); EPK(3, 1);
    MF(acc[5], At[5], Fq[36]); FL(41, 34); OST(3);
    MF(acc[4], At[14], Fq[37]); FL(42, 35); FLAG(C_TILE + 3 * 4); CHECK(4, fa + 32); ULD(4);
    MF(acc[5], At[6], Fq[37]); FL(43, 36); FLD(4); GLD(4); PEEK(5, fa + 48);
    MF(acc[4], At[15], Fq[38]); FL(44, 37); FL(45, 38);
    MF(acc[5], At[7], Fq[38]); FL(46, 39); FL(47, 40);
    MF(acc[4], At[16], Fq[39]);
    MF(acc[5], At[8], Fq[39]); EUN(4, 0); EUN(4, 1);
    MF(acc[5], At[9], Fq[40]); EUN(4, 2); EUN(4, 3);
    MF(acc[5], At[10], Fq[41]); EFA(4, 0); EA1(4, 0);
    MF(acc[5], At[11], Fq[42]); EA1(4, 1); EA1(4, 2);
    MF(acc[5], At[12], Fq[43]); EA1(4, 3); EFA(4, 1); EFA(4, 2); EFA(4, 3); CHECK(5, fa + 48); ULD(5);
    MF(acc[5], At[13], Fq[44]); EA2(4, 0); EA2(4, 1); EA2(4, 2); EA2(4, 3); FLD(5); GLD(5);
    MF(acc[5], At[14], Fq[45]); EPK(4, 0); EPK(4, 1);
    MF(acc[5], At[15], Fq[46]); OST(4); FLAG(C_TILE + 4 * 4);
    MF(acc[5], At[16], Fq[47]); EUN(5, 0); EUN(5, 1); EUN(5, 2); EUN(5, 3); EFA(5, 0); EFA(5, 1); EFA(5, 2); EFA(5, 3); EA1(5, 0); EA1(5, 1); EA1(5, 2); EA1(5, 3); EA2(5, 0); EA2(5, 1); EA2(5, 2); EA2(5, 3); EPK(5, 0); EPK(5, 1); OST(5); FLAG(C_TILE + 5 * 4);
    STAMP(8);
    ROLE_END();
  }
  {
    ROLE("c");
    __builtin_amdgcn_s_setprio(3);
    const u32 ep = *(gp32)(kc[0] + WS_EPOCH);
    {
      u32 zz = 0u; OPQ(zz);
      *(u2v*)(ctl + 2 * lane) = u2v{zz, zz};
      if (lane < 2) *(u4v*)(smem + O_HB + 512 + lane * 528) = u4v{zz, zz, zz, zz};
    }
    BAR();
    u32* selw = (u32*)(smem + O_SEL);
    u32 tag = (u32)__builtin_amdgcn_readfirstlane((int)ep) + 1u;
    tag += tag == 0u;
    if (lane == 0) par[0] = tag;
    STAMP(21);
    {
      const u32 pa = lbase + O_PART + (lane & 7) * 64;
      const u32 ib = 2047u - (u32)(cg * 8 + (lane & 7));
      f4 p0, p1, p2, p3; u32 cnt; int n_ = 1 << 20; unsigned long long tk;
      asm volatile("1:\n\tds_read_b32 %4, %7\n\tds_read_b128 %0, %8\n\tds_read_b128 %1, %8 offset:16\n\tds_read_b128 %2, %8 offset:32\n\tds_read_b128 %3, %8 offset:48\n\t"
                   "s_memrealtime %6\n\ts_sub_u32 %5, %5, 1\n\ts_cbranch_scc1 2f\n\ts_waitcnt lgkmcnt(0)\n\tv_cmp_gt_u32 vcc, 4, %4\n\ts_cbranch_vccnz 1b\n2:\n\ts_waitcnt lgkmcnt(0)"
                   : "=&v"(p0), "=&v"(p1), "=&v"(p2), "=&v"(p3), "=&v"(cnt), "+s"(n_), "=&s"(tk) : "v"(CTLA(C_PART)), "v"(pa) : "vcc", "scc", "memory");
      const f4 s4_ = (p0 + p1) + (p2 + p3);
      const u32 rp = pk_bf(s4_[1] + s4_[3], s4_[0] + s4_[2]);
      const u32 key = ((rp ^ ((u32)((int)rp >> 31) | 0x80000000u)) & 0xffff0000u) | ib;
      const u32 kn = dpp_u<0xb1>(key);
      u32 m1 = max(key, kn), m2 = min(key, kn), m3;
      { const u32 o1 = dpp_u<0x4e>(m1), o2 = dpp_u<0x4e>(m2); const u32 lo = min(m1, o1), hb = max(m2, o2); m1 = max(m1, o1); m2 = max(lo, hb); m3 = min(lo, hb); }
      { const u32 o1 = dpp_u<0x141>(m1), o2 = dpp_u<0x141>(m2), o3 = dpp_u<0x141>(m3); const u32 lo = min(m1, o1), hb = max(m2, o2);
        m1 = max(m1, o1); m2 = max(lo, hb); m3 = max(min(lo, hb), max(m3, o3)); }
      const u32 wm = (((u32)tk + 8u) >> 2) & 0xffu;
      if (lane == 0) __builtin_amdgcn_raw_buffer_store_b128(u4v{m1, m2, (m3 & 0xffff0000u) | wm, tag}, wrs, WS_KEY + cg * 16, 0, 16);
      SB();
      STAMP(1);
      ARRIVE(C_GP);
      {
        const float gv = sigm(bflo(rp));
        const u32 g2 = pk_bf(gv, dpp_f<0xb1>(gv));
        u32 g3 = dpp_u<0x4e>(g2);
        OPQ(g3);
        if ((lane & 59) == 0) __builtin_amdgcn_raw_buffer_store_b128(u4v{g2, g3, tag, tag}, wrs, WS_REC + cg * 32 + lane * 4, 0, 16);
      }
      if ((lane & 57) == 0) __builtin_amdgcn_raw_buffer_store_b128(u4v{key, kn, 0u, tag}, wrs, WS_KALL + cg * 64 + lane * 8, 0, 16);
    }
    u32 l7 = (lane & 7) * 16, ko = WS_KEY + lane * 16;
    OPQ(l7); OPQ(ko);
    WAITQ(C_TL, 1);
    STAMP(5);
    const u32 tkey = (u32)__builtin_amdgcn_readfirstlane((int)par[1]) + DT_KEY;
    pace(tkey);
    u4v gr[4];
    u32 mykey;
    u4v xs[4];
    auto xrs = __builtin_amdgcn_make_buffer_rsrc((void*)(ka[0] + (xi & 3) * 512), 0, L * D * 2 - 1536, 0x00020000);
    asm volatile("" : "+s"(xrs));
    auto xs_ld = [&]() AI {
      const u32 vo = ((~mykey & 2047u) << 11) | l7;
#pragma unroll
      for (int k = 0; k < 4; ++k) xs[k] = __builtin_amdgcn_raw_buffer_load_b128(xrs, vo + k * 128, 0, 0);
    };
#pragma unroll
    for (int i = 0; i < 4; ++i) gr[i] = __builtin_amdgcn_raw_buffer_load_b128(wrs, ko + i * 1024, 0, 16);
#pragma unroll 1
    for (int spin = 0; spin < SPIN_MAX; ++spin) {
      const u32 bad = (gr[0][3] ^ tag) | (gr[1][3] ^ tag) | (gr[2][3] ^ tag) | (gr[3][3] ^ tag);
      if (__builtin_expect(!__any(bad != 0), 1)) break;
      STAMP(13);
      __builtin_amdgcn_s_sleep(SLEEP_AG1);
#pragma unroll
      for (int i = 0; i < 4; ++i)
        if (gr[i][3] != tag) gr[i] = __builtin_amdgcn_raw_buffer_load_b128(wrs, ko + i * 1024, 0, 16);
      asm volatile("" ::: "memory");
    }
    STAMP(2);
    {
      u32 m1 = gr[0][0], m2 = gr[0][1], m3 = 0, m4 = 0;
#pragma unroll
      for (int i = 2; i < 8; ++i) {
        const u32 k = gr[i >> 1][i & 1];
        if (i > 2) m4 = med3u(m3, m4, k);
        m3 = med3u(m2, m3, k);
        m2 = (i & 1) ? max(m2, k) : med3u(m1, m2, k);
        if (!(i & 1)) m1 = max(m1, k);
      }
      const u32 third = max(max(gr[0][2], gr[1][2]), max(gr[2][2], gr[3][2]));
      u32 W2;
      mykey = wave_top8(m1, m2, m3, m4, W2);
      xs_ld();
      SB();
      if (__builtin_expect(__any(m1 == 0u || (third | 0xffffu) > W2), 0)) {
        u4v ak[16];
#pragma unroll 1
        for (int spin = 0; spin < SPIN_MAX; ++spin) {
          u32 bad = 0;
#pragma unroll
          for (int i = 0; i < 16; ++i) {
            ak[i] = __builtin_amdgcn_raw_buffer_load_b128(wrs, WS_KALL + (i & 3) * 1024 + lane * 16, (i >> 2) * 4096, 16);
            bad |= ak[i][3] ^ tag;
          }
          if (!__any(bad != 0)) break;
          __builtin_amdgcn_s_sleep(8);
          asm volatile("" ::: "memory");
        }
        u32 prev = 0xffffffffu;
#pragma unroll 1
        for (int it = 0; it < 8; ++it) {
          u32 mm_ = 0;
#pragma unroll
          for (int i = 0; i < 32; ++i) {
            const u32 k = ak[i >> 1][i & 1];
            mm_ = max(mm_, k < prev ? k : 0u);
          }
          OPQ(mm_);
          prev = wave_max_u32(mm_);
          mykey = (lane >> 3) == it ? prev : mykey;
        }
        xs_ld();
      }
      OPQ(mykey);
    }
    SB();
    STAMP(3);
    if (l7 == 0) selw[lane >> 3] = mykey;
    {
      const u32 tn = now32();
      u32 yg = 0;
#pragma unroll
      for (int i = 0; i < 4; ++i) yg = max(yg, (gr[i][2] - (tn >> 2) - 1u) & 0xffu);
      const u32 tpub = tn - 4u * (255u - wave_max_u32(yg)) + DT_VIS;
      if (lane == 0) par[2] = (int)(tpub - tkey) > 0 ? tpub + DT_LATE : tkey;
      if (__any((int)(tpub - tkey) > 0)) { STAMP(20); }
    }
    ARRIVE(C_SEL);
    if (b == 255 && lane == 0) __builtin_amdgcn_raw_buffer_store_b32(tag, wrs, WS_EPOCH, 0, 16);
    SB();
    {
      char* xp = smem + O_XS + (lane >> 3) * XS_PITCH + l7;
#pragma unroll
      for (int k = 0; k < 4; ++k) *(u4v*)(xp + k * 128) = xs[k];
      ARRIVE(C_XS);
      STAMP(6);
    }
    __builtin_amdgcn_s_setprio(2);
    auto ors = __builtin_amdgcn_make_buffer_rsrc((void*)kc[1], 0, L * D * 2, 0x00020000);
    const u32 rl = (lane + (xi & 15) * 4) & 63, un = (g * 4 + (lane & 3) + xi) & 15;
    u32 skip = 0;
#pragma unroll
    for (int k = 0; k < 8; ++k) {
      const u32 r = ~(u32)__builtin_amdgcn_readlane((int)mykey, 8 * k) & 2047u;
      skip |= (r & 63u) == rl ? 1u << (r >> 6) : 0u;
    }
    const char* op = smem + ((lane >> 2) & 3) * CHB + O_OUT + (un & 3) * 144 + (un >> 2) * 8;
    const u32 ob = rl * 2048 + cc * 8;
    const u32 tfast = (u32)__builtin_amdgcn_readfirstlane((int)par[2]) + DE_FAST;
#pragma unroll 1
    for (int t = 0; t < 8; ++t) {
      WAITA(CTLA(C_TILE) + t * 16 + (lane & 3) * 4);
      u2v yv[4];
#pragma unroll
      for (int e = 0; e < 4; ++e) yv[e] = tr_read(op + t * OT + e * 32);
      const bool slow = (int)(now32() - tfast) < 0;
#pragma unroll
      for (int e = 0; e < 4; ++e) {
        __builtin_amdgcn_raw_buffer_store_b64(yv[e], ors, ob | (((skip >> e) & 1u) << 30), t * 524288 + e * 131072, 0);
        if (slow) {
          SB(); __builtin_amdgcn_s_sleep(STSLP); SB();
          asm volatile("s_waitcnt vmcnt(%0)" :: "n"(STWIN - 1) : "memory");
        }
      }
      skip >>= 4;
    }
    STAMP(9);
    ROLE_END();
  }
  {
    ROLE("d");
        const int j = xg * 28 + xi;
    const int s1 = j >> 2, kq = j & 3;
    const bool own = xi < 28;
    const u64 wbase = kb[3];
    const int pitch = D * 2;
    auto hrs = __builtin_amdgcn_make_buffer_rsrc((void*)wbase, 0, own ? D * F * 2 : 0, 0x00020000);
    const int r8 = lane >> 3;
    const int vo = (64 * s1 * D + 256 * kq) * 2 + (8 * (r8 >> 2) + (r8 & 3)) * pitch + (((lane & 7) * 16) ^ (((r8 >> 1) & 3) * 32));
    auto dma = [&](int h, int sB, int pt, u32 dst) AI {
      const int m = h >> 1;
#pragma unroll
      for (int i = 0; i < 2; ++i)
        asm volatile("s_mov_b32 m0, %3\n\ts_nop 0\n\tbuffer_load_dwordx4 %0, %1, %2 offen nt lds"
                     :: "v"(vo), "s"(hrs), "s"((m & 1) * 32 * pt + (m >> 1) * sB + (16 * i + 4 * (h & 1)) * pt), "s"(dst + i * 1024) : "memory");
    };
    const int q = c16 >> 2, p = c16 & 3;
    int tro[4];
#pragma unroll
    for (int ct = 0; ct < 4; ++ct) tro[ct] = (4 * g + q) * 128 + ((ct ^ (((4 * g + q) >> 1) & 3)) * 32) + p * 8;
    const int tok = lane & 7;
    u4v wd[2][8];
    auto wd_ld = [&](int k, int cb) AI {
#pragma unroll
      for (int i = 0; i < 8; ++i) {
        const char* pp = smem + O_WD + cb * 8192 + (i >> 2) * 4096 + tro[i & 3];
        const u2v lo = tr_read(pp), hi = tr_read(pp + 2048);
        wd[k][i] = u4v{lo[0], lo[1], hi[0], hi[1]};
      }
    };
    u32 p2o, p2k[4], hlo, hhi;
    auto f2_setup = [&](int cb0) AI {
      p2o = WS_P2 + ((64 * kq + 16 * cb0 + (lane & 8) / 2 + g) * 56 + s1) * 128 + tok * 16;
      hlo = lbase + O_HB + ((lane & 8) ? 512 : g * 128 + tok * 16);
      hhi = lbase + O_HB + ((lane & 8) ? g * 128 + tok * 16 : 512);
#pragma unroll
      for (int i = 0; i < (cb0 == 2 ? 4 : 2); ++i) { p2k[i] = p2o + i * 8 * 7168; OPQ(p2k[i]); }
      OPQ(hlo); OPQ(hhi);
    };
    u4v hb[4];
    f4 r[4];
    auto hb_ld = [&]() AI {
      u32 cnt; int n_ = 1 << 20;
      asm volatile("1:\n\tds_read_b32 %4, %6\n\tds_read_b128 %0, %7\n\tds_read_b128 %1, %8\n\tds_read_b128 %2, %7 offset:528\n\tds_read_b128 %3, %8 offset:528\n\t"
                   "s_sub_u32 %5, %5, 1\n\ts_cbranch_scc1 2f\n\ts_waitcnt lgkmcnt(0)\n\tv_cmp_gt_u32 vcc, 2, %4\n\ts_cbranch_vccnz 1b\n2:\n\ts_waitcnt lgkmcnt(0)"
                   : "=&v"(hb[0]), "=&v"(hb[1]), "=&v"(hb[2]), "=&v"(hb[3]), "=&v"(cnt), "+s"(n_) : "v"(CTLA(C_HRDY)), "v"(hlo), "v"(hhi) : "vcc", "scc", "memory");
    };
    auto f2_mm = [&](int k) AI {
      const u4v* wp = wd[k];
      r[2 * k] = mma(wp[0], hb[0], f4{0.f, 0.f, 0.f, 0.f}); r[2 * k + 1] = mma(wp[2], hb[0], f4{0.f, 0.f, 0.f, 0.f});
      r[2 * k] = mma(wp[1], hb[1], r[2 * k]); r[2 * k + 1] = mma(wp[3], hb[1], r[2 * k + 1]);
      r[2 * k] = mma(wp[4], hb[2], r[2 * k]); r[2 * k + 1] = mma(wp[6], hb[2], r[2 * k + 1]);
      r[2 * k] = mma(wp[5], hb[3], r[2 * k]); r[2 * k + 1] = mma(wp[7], hb[3], r[2 * k + 1]);
    };
    auto f2_st = [&](int k, u32 t10) AI {
#pragma unroll
      for (int i = 2 * k; i < 2 * k + 2; ++i) {
        u4v v = __builtin_bit_cast(u4v, r[i]);
#pragma unroll
        for (int e = 0; e < 4; ++e) v[e] = __builtin_amdgcn_bitop3_b32(v[e], t10, TMASK, 0xd8);
        __builtin_amdgcn_raw_buffer_store_b128(v, wrs, p2k[i], 0, 16);
      }
    };
    {
      __builtin_amdgcn_s_setprio(3);
      BAR();
      const u32 ts = ts0 - 500u;
      u32 tl = ~0u;
      pace(ts0 + DT_HEAD);
      STAMP(3);
#pragma unroll 1
      for (int i = 0; i < 8 && tl >= 1500u; ++i) {
        const u2v hd = __builtin_amdgcn_raw_buffer_load_b64(wrs, WS_HEAD + (lane & 7) * 8, 0, 16);
        tl = wave_max_u32(hd[0] == ~hd[1] ? hd[0] - ts : ~0u);
        STAMP(14);
        asm volatile("" ::: "memory");
      }
      tl = tl < 1500u ? tl + ts : ts0;
      if (lane == 0) par[1] = tl;
      ARRIVE(C_TL);
      STAMP(2);
      if (own) {
        WAITS(C_WGU, 1);
#pragma unroll
        for (int h = 0; h < 16; ++h) {
          if (h == DSEL) WAITS(C_SEL, 1);
          if (h == DEV) WAITS(C_XL, 4);
          if (h >= DWIN) asm volatile("s_waitcnt vmcnt(%0)" :: "n"(2 * (DWIN - 1)) : "memory");
          if (h == 3 + DWIN) ARRIVE(C_WD);
          if (h == 7 + DWIN) ARRIVE(C_WD + 1);
          dma(h, 128, D * 2, lbase + O_WD + h * 2048);
        }
        f2_setup(2);
        const u32 t10 = par[0] & TMASK;
        asm volatile("s_waitcnt vmcnt(8)" ::: "memory");
        wd_ld(0, 2);
        const u32 hc = __hip_atomic_load(ctl + C_HRDY, __ATOMIC_RELAXED, __HIP_MEMORY_SCOPE_WORKGROUP);
        const bool early = __builtin_amdgcn_ballot_w64(hc < 2u) == 0ull;
        if (early) { hb_ld(); f2_mm(0); }
        asm volatile("s_waitcnt vmcnt(0)" ::: "memory");
        STAMP(5);
        wd_ld(1, 3);
        if (!early) { WAITS(C_HRDY, 1); hb_ld(); STAMP(11); f2_mm(0); }
        SB();
        f2_mm(1);
        f2_st(0, t10);
        SB();
        f2_st(1, t10);
        STAMP(12);
      }
      ROLE_END();
    }
  }
}

extern "C" void kernel_launch(void* const* d_in, const int* in_sizes, int n_in,
                              void* d_out, int out_size, void* d_ws, size_t ws_size,
                              hipStream_t stream) {
  k_main<<<256, 768, 0, stream>>>((const u16*)d_in[0], (const u16*)d_in[8], (const u16*)d_in[12],
                                  (const u16*)d_in[1], (const u16*)d_in[2], (const u16*)d_in[3],
                                  (const u16*)d_in[4], (const u16*)d_in[5], (const u16*)d_in[6],
                                  (const u16*)d_in[9], (const u16*)d_in[10], (const u16*)d_in[11],
                                  (char*)d_ws, (u16*)d_out);
}
```

```cpp
#include <hip/hip_runtime.h>
#include <stdint.h>

typedef unsigned short u16;
typedef unsigned u32;
typedef _Float16 h2 __attribute__((ext_vector_type(2)));
typedef _Float16 h8 __attribute__((ext_vector_type(8)));
typedef __bf16 b2 __attribute__((ext_vector_type(2)));
typedef __bf16 b8 __attribute__((ext_vector_type(8)));
typedef short s4 __attribute__((ext_vector_type(4)));
typedef float f2 __attribute__((ext_vector_type(2)));
typedef float f4 __attribute__((ext_vector_type(4)));
typedef u32 u2v __attribute__((ext_vector_type(2)));
typedef u32 u3v __attribute__((ext_vector_type(3)));
typedef u32 u4v __attribute__((ext_vector_type(4)));
typedef __attribute__((address_space(3))) s4* lds_s4p;

constexpr int L = 2048;
constexpr int D = 1024;
constexpr int NM = 128;
constexpr int F = 3584;
constexpr int NTAP = 1024;

constexpr int SEG_I = 576, SEG_O0 = 800, SEG_O1 = 880, SEG_SP = 896;

#define AI __attribute__((always_inline))
#define SB() __builtin_amdgcn_sched_barrier(0)
#define OPQ(x) asm volatile("" : "+v"(x))
#define DIAG_K 20
#define DIAG_M 16
#define STAMP0() u32 dg_ = 0; const bool dgw_ = (DIAG_M >> w) & 1; if (DIAG_K == 0 && dgw_) dg_ = (u32)kt[0] | 1u
#define STAMP(i) do { if (i == DIAG_K && dgw_) { SB(); dg_ = (u32)__builtin_amdgcn_s_memrealtime() | 1u; SB(); } } while (0)
#define STAMP_END() do { if (DIAG_K == 99 && dgw_) { asm volatile("s_waitcnt vmcnt(0)" ::: "memory"); dg_ = (u32)__builtin_amdgcn_s_memrealtime() | 1u; } if (dg_) { while ((int)((u32)__builtin_amdgcn_s_memrealtime() - (dg_ + 4000u)) < 0) __builtin_amdgcn_s_sleep(2); } } while (0)

#define STAMP_T0

__device__ __forceinline__ float bflo(u32 v) { return __uint_as_float(v << 16); }
__device__ __forceinline__ float bfhi(u32 v) { return __uint_as_float(v & 0xffff0000u); }
__device__ __forceinline__ u32 pk_bf(float a, float b) { f2 v = {a, b}; b2 r = __builtin_convertvector(v, b2); return __builtin_bit_cast(u32, r); }
__device__ __forceinline__ float rb1(float v) { return __uint_as_float(pk_bf(0.f, v)); }
__device__ __forceinline__ h8 as_h8(u4v v) { return __builtin_bit_cast(h8, v); }
__device__ __forceinline__ b8 as_b8(u4v v) { return __builtin_bit_cast(b8, v); }
__device__ __forceinline__ u2v tr_read(const char* p) {
  s4 t = __builtin_amdgcn_ds_read_tr16_b64_v4i16((lds_s4p)p);
  return __builtin_bit_cast(u2v, t);
}
template <int CTRL> __device__ __forceinline__ u32 dpp_u(u32 v) { return (u32)__builtin_amdgcn_update_dpp(0, (int)v, CTRL, 0xf, 0xf, false); }
template <int CTRL> __device__ __forceinline__ float dpp_f(float v) { return __uint_as_float(dpp_u<CTRL>(__float_as_uint(v))); }
__device__ __forceinline__ u32 wave_max_u32(u32 v) {
  v = max(v, dpp_u<0x111>(v)); v = max(v, dpp_u<0x112>(v)); v = max(v, dpp_u<0x114>(v)); v = max(v, dpp_u<0x118>(v));
  v = max(v, dpp_u<0x142>(v)); v = max(v, dpp_u<0x143>(v));
  return (u32)__builtin_amdgcn_readlane((int)v, 63);
}
#define DPM " row_mask:0xf bank_mask:0xf bound_ctrl:1\n\t"
#define T2S(HO, HI, LI, C) "v_max_u32_dpp " HO ", " HI ", " HI " " C DPM "v_min_u32_dpp %8, " HI ", " HI " " C DPM "v_max_u32_dpp %7, " LI ", " LI " " C DPM "v_max_u32 %7, %7, %8\n\t"
#define T2R(RM, REST) T2S("%5", "%0", "%1", "row_shr:1") T2S("%6", "%5", "%7", "row_shr:2") T2S("%5", "%6", "%7", "row_shr:4") T2S("%6", "%5", "%7", "row_shr:8") \
    T2S("%5", "%6", "%7", "row_bcast:15") T2S("%6", "%5", "%7", "row_bcast:31") \
    "v_readlane_b32 %9, %6, 63\n\tv_readlane_b32 %10, %7, 63\n\ts_nop 0\n\tv_mov_b32 %5, %9\n\tv_cmp_le_u32 %11, %10, %0\n\tv_cmp_le_u32 %12, %10, %1\n\tv_mov_b32 %6, %10\n\t" \
    "v_cndmask_b32 %0, %0, %1, %11\n\tv_mov_b32_dpp %4, %5 quad_perm:[0,1,2,3] row_mask:" RM " bank_mask:0x3\n\tv_cndmask_b32 %0, %0, %2, %12\n\t" \
    "v_mov_b32_dpp %4, %6 quad_perm:[0,1,2,3] row_mask:" RM " bank_mask:0xc\n\t" REST
#define T2P "v_cndmask_b32 %1, %1, %2, %11\n\tv_cndmask_b32 %2, %2, %3, %11\n\tv_cndmask_b32 %1, %1, %3, %12\n\tv_cndmask_b32 %3, %3, 0, %11\n\tv_cndmask_b32 %2, %2, 0, %12\n\t"
__device__ __forceinline__ u32 wave_top8(u32& m1, u32 m2, u32 m3, u32 m4, u32& w2) {
  u32 key, ta, tb, lo, t, w1;
  unsigned long long c1, c2;
  asm("s_nop 1\n\t" T2R("0x1", T2P) T2R("0x2", T2P) T2R("0x4", T2P) T2R("0x8", "") "s_nop 0"
      : "+v"(m1), "+v"(m2), "+v"(m3), "+v"(m4), "=&v"(key), "=&v"(ta), "=&v"(tb), "=&v"(lo), "=&v"(t), "=&s"(w1), "=&s"(w2), "=&s"(c1), "=&s"(c2));
  return key;
}

__device__ __forceinline__ float exp_bf(float v) {
  const float e = __builtin_amdgcn_exp2f(v * 1.44268798828125f);
  return fmaf(e, v * (7.0526077e-06f * 0.69314718f), e);
}
struct cf { f2 r, i; };
struct ch { h2 r, i; };
__device__ __forceinline__ cf cmul(cf a, cf b) { cf c; c.r = a.r * b.r - a.i * b.i; c.i = a.r * b.i + a.i * b.r; return c; }
__device__ __forceinline__ ch hmul(ch a, ch b) {
  ch c;
  asm("v_pk_mul_f16 %0, %2, %4\n\tv_pk_mul_f16 %1, %2, %5\n\tv_pk_fma_f16 %0, %3, %5, %0 neg_lo:[1,0,0] neg_hi:[1,0,0]\n\tv_pk_fma_f16 %1, %3, %4, %1"
      : "=&v"(c.r), "=&v"(c.i) : "v"(a.r), "v"(a.i), "v"(b.r), "v"(b.i));
  return c;
}
__device__ __forceinline__ cf csq(cf a) { cf c; c.r = a.r * a.r - a.i * a.i; c.i = (a.r + a.r) * a.i; return c; }
__device__ __forceinline__ ch toh(cf a) { ch c; c.r = __builtin_convertvector(a.r, h2); c.i = __builtin_convertvector(a.i, h2); return c; }
__device__ __forceinline__ ch tohc(cf a) { ch c; c.r = __builtin_convertvector(a.r, h2); c.i = __builtin_convertvector(-a.i, h2); return c; }
__device__ __forceinline__ h2 ash(u32 v) { return __builtin_bit_cast(h2, v); }
__device__ __forceinline__ cf cexp_p(f2 a2, f2 brev, float p) {
  f2 e = a2 * p, t = brev * p;
  f2 mag = {__builtin_amdgcn_exp2f(e[0]), __builtin_amdgcn_exp2f(e[1])};
  float r0 = __builtin_amdgcn_fractf(t[0]), r1 = __builtin_amdgcn_fractf(t[1]);
  cf c;
  c.r = mag * f2{__builtin_amdgcn_cosf(r0), __builtin_amdgcn_cosf(r1)};
  c.i = mag * f2{__builtin_amdgcn_sinf(r0), __builtin_amdgcn_sinf(r1)};
  return c;
}
__device__ __forceinline__ u32 med3u(u32 a, u32 b, u32 c) { u32 r; asm("v_med3_u32 %0, %1, %2, %3" : "=v"(r) : "v"(a), "v"(b), "v"(c)); return r; }
__device__ __forceinline__ f2 unbf(u32 v) { return f2{bflo(v), bfhi(v)}; }
__device__ __forceinline__ float sigm(float v) { return __builtin_amdgcn_rcpf(1.f + __builtin_amdgcn_exp2f(-1.44269504088896341f * v)); }

#define TS2(A, B, C, D) "v_xor_b32 %5, " A ", %9\n\tv_xor_b32 %6, " B ", %9\n\tv_xor_b32 %7, " C ", %9\n\tv_xor_b32 %8, " D ", %9\n\t" \
    "v_fma_f32 %1, %5, 1.0, %1\n\tv_fma_f32 %2, %6, 1.0, %2\n\tv_or3_b32 %0, %0, %5, %6\n\tv_fma_f32 %3, %7, 1.0, %3\n\tv_fma_f32 %4, %8, 1.0, %4\n\tv_or3_b32 %0, %0, %7, %8\n\t"
#define TSR TS2("%10", "%11", "%12", "%13") TS2("%14", "%15", "%16", "%17") TS2("%18", "%19", "%20", "%21")
#define TSI "=&v"(t0), "=&v"(t1), "=&v"(t2), "=&v"(t3) : "v"(t10), "v"(p[0]), "v"(p[1]), "v"(p[2]), "v"(p[3]), "v"(q[0]), "v"(q[1]), "v"(q[2]), "v"(q[3]), \
    "v"(r[0]), "v"(r[1]), "v"(r[2]), "v"(r[3])
__device__ __forceinline__ f4 tsum4(u32& bad, u32 t10, u4v s, u4v p, u4v q, u4v r) {
  float s0, s1, s2, s3; u32 t0, t1, t2, t3;
  asm volatile("v_xor_b32 %1, %22, %9\n\tv_xor_b32 %2, %23, %9\n\tv_xor_b32 %3, %24, %9\n\tv_xor_b32 %4, %25, %9\n\tv_or3_b32 %0, %0, %1, %2\n\tv_or3_b32 %0, %0, %3, %4\n\t" TSR
               : "+v"(bad), "=&v"(s0), "=&v"(s1), "=&v"(s2), "=&v"(s3), TSI, "v"(s[0]), "v"(s[1]), "v"(s[2]), "v"(s[3]));
  return f4{s0, s1, s2, s3};
}
__device__ __forceinline__ f4 tsum3(u32& bad, u32 t10, f4 s, u4v p, u4v q, u4v r) {
  float s0 = s[0], s1 = s[1], s2 = s[2], s3 = s[3]; u32 t0, t1, t2, t3;
  asm volatile(TSR : "+v"(bad), "+v"(s0), "+v"(s1), "+v"(s2), "+v"(s3), TSI);
  return f4{s0, s1, s2, s3};
}
__device__ __forceinline__ void silu4(f4& g, float nl2e) {
  float g0 = g[0], g1 = g[1], g2 = g[2], g3 = g[3], t0, t1, t2, t3;
  asm volatile("v_fma_f32 %4, %0, %8, 0\n\tv_fma_f32 %5, %1, %8, 0\n\tv_fma_f32 %6, %2, %8, 0\n\tv_fma_f32 %7, %3, %8, 0\n\t"
               "v_exp_f32 %4, %4\n\tv_exp_f32 %5, %5\n\tv_exp_f32 %6, %6\n\tv_exp_f32 %7, %7\n\t"
               "v_fma_f32 %4, %4, 1.0, 1.0\n\tv_fma_f32 %5, %5, 1.0, 1.0\n\tv_fma_f32 %6, %6, 1.0, 1.0\n\tv_fma_f32 %7, %7, 1.0, 1.0\n\t"
               "v_rcp_f32 %4, %4\n\tv_rcp_f32 %5, %5\n\tv_rcp_f32 %6, %6\n\tv_rcp_f32 %7, %7\n\t"
               "v_fma_f32 %0, %0, %4, 0\n\tv_fma_f32 %1, %1, %5, 0\n\tv_fma_f32 %2, %2, %6, 0\n\tv_fma_f32 %3, %3, %7, 0"
               : "+v"(g0), "+v"(g1), "+v"(g2), "+v"(g3), "=&v"(t0), "=&v"(t1), "=&v"(t2), "=&v"(t3) : "v"(nl2e));
  g = f4{g0, g1, g2, g3};
}

constexpr int KT_PITCH = 52;
constexpr int kt_off(int p) {
  int i = 20, j = 0;
  if (p < 256) { i = p & 7; j = p >> 3; }
  else if (p < 512) { int q = (p - 256) >> 1; i = 8 + (q & 3); j = q >> 2; }
  else if (p < 1024) { int q = (p - 512) >> 2; i = 12 + (q & 1) + 2 * (q >> 6); j = (q >> 1) & 31; }
  else if (p < 2048) { int q = (p - 1024) >> 3; i = 16 + (q >> 5); j = q & 31; }
  return j * KT_PITCH + i * 2;
}
constexpr int KT_ZERO = 32 * KT_PITCH + 16;
constexpr int rnd_lag(int m) {
  const int st = m < 256 ? 1 : m < 512 ? 2 : m < 1024 ? 4 : 8;
  int q = m / st;
  const int rem = m - q * st;
  if (2 * rem > st || (2 * rem == st && (q & 1))) ++q;
  return q * st;
}
constexpr int tap_entry(int e) {
  if (e < 544) {
    if (543 - e <= 525) return kt_off(rnd_lag(543 - e));
  } else if (e >= 560 && e < 752) {
    const int q = (e - 560) / 48, x = (e - 560) - q * 48;
    const int p = 512 + 16 * (31 - x) + 4 * (q + 1);
    if (x < 32 && p > 524) return kt_off(p);
  } else if (e >= 752 && e < 816) {
    return kt_off(1032 + 16 * (815 - e));
  } else if (e >= 832 && e < 896) {
    return kt_off(1040 + 16 * (895 - e));
  } else if (e == SEG_SP + 27 || e == SEG_SP + 28) {
    return kt_off(1024);
  }
  return KT_ZERO;
}
struct TapIdx { u16 v[NTAP]; };
constexpr TapIdx make_tapidx() {
  TapIdx t{};
  for (int e = 0; e < NTAP; ++e) t.v[e] = (u16)tap_entry(2 * ((e >> 4) + 64 * ((e >> 1) & 7)) + (e & 1));
  return t;
}
__device__ __attribute__((aligned(16))) const TapIdx g_tapidx = make_tapidx();

constexpr int TROW = 544;
constexpr int O_OUT = 0;
constexpr int OT = 576;
constexpr int O_U = O_OUT + 8 * OT;
constexpr int O_TAP = O_U + 4608;
constexpr int TAP_PITCH = 2 * NTAP + 64;
constexpr int CHB = O_TAP + 16 * TROW + 32;
static_assert(16 * TROW >= 4 * TAP_PITCH, "lds");
static_assert((CHB / 4) % 32 == 8, "banks");
#define DEV 3
#define DWIN 8
#define DSEL 0
constexpr int NSLOT = 5, WWIN = 4;
constexpr int O_GT = 4 * CHB;
constexpr int O_RING = O_GT + 8192;
constexpr int O_WD = O_RING + 2 * NSLOT * 2048;
constexpr int O_FAR = O_WD + 32768;
constexpr int O_XS = O_FAR + 16384;
constexpr int XS_PITCH = 544;
constexpr int O_HB = O_XS + 8 * XS_PITCH;
constexpr int O_PART = O_HB + 2 * 528;
constexpr int O_SEL = O_PART + 512;
constexpr int O_PAR = O_SEL + 32;
constexpr int O_CTL = O_PAR + 16;
constexpr int O_JUNK = O_CTL + 512;
constexpr int K_LDS = O_JUNK + 1024;
static_assert(DWIN == 8, "flags of the down tile");
static_assert(K_LDS <= 163840 && O_RING % 16 == 0, "lds");
enum { C_PART = 0, C_GP, C_PUB, C_SEL, C_XS, C_HRDY, C_WGU, C_TL, C_XL, C_WD = 10, C_TAPS = 12, C_Q = 16, C_FAR = 32, C_TILE = 48 };

#define CTLA(word) (lbase + O_CTL + (word) * 4)
#define ARRIVE(word) asm volatile("s_mov_b64 exec, 1\n\tds_add_u32 %0, %1\n\ts_mov_b64 exec, -1" :: "v"(CTLA(word)), "v"(1) : "memory")
#define WAITQ(word, target) do { u32 t_; int n_ = 1 << 20; asm volatile( \
    "1:\n\tds_read_b32 %0, %2\n\ts_sub_u32 %1, %1, 1\n\ts_cbranch_scc1 2f\n\ts_waitcnt lgkmcnt(0)\n\tv_cmp_gt_u32 vcc, %3, %0\n\ts_cbranch_vccnz 1b\n2:\n\ts_waitcnt lgkmcnt(0)" \
    : "=&v"(t_), "+s"(n_) : "v"(CTLA(word)), "n"(target) : "vcc", "scc", "memory"); } while (0)
#define WAITS(word, target) do { u32 t_; int n_ = 1 << 20; asm volatile( \
    "1:\n\tds_read_b32 %0, %2\n\ts_sub_u32 %1, %1, 1\n\ts_cbranch_scc1 2f\n\ts_waitcnt lgkmcnt(0)\n\tv_cmp_le_u32 vcc, %3, %0\n\ts_cbranch_vccnz 2f\n\ts_sleep 2\n\ts_branch 1b\n2:\n\ts_waitcnt lgkmcnt(0)" \
    : "=&v"(t_), "+s"(n_) : "v"(CTLA(word)), "n"(target) : "vcc", "scc", "memory"); } while (0)
#define BAR() asm volatile("s_waitcnt lgkmcnt(0)\n\ts_barrier" ::: "memory")
__device__ __forceinline__ u2v lds8(const char* p) { return __builtin_bit_cast(u2v, __hip_atomic_load((const unsigned long long*)p, __ATOMIC_RELAXED, __HIP_MEMORY_SCOPE_WORKGROUP)); }

constexpr int WS_EPOCH = 0;
constexpr int WS_HEAD = 1024;
constexpr int WS_REC = 4096;
constexpr int WS_FLG = 40960;
constexpr int WS_KALL = WS_REC + 8192;
constexpr int WS_KEY = 32768;
constexpr int WS_P1 = 65536;
constexpr int WS_P2 = WS_P1 + 56 * 16384;
constexpr int WS_XT = WS_P2 + 256 * 7168;
constexpr int XT_PITCH = 33280;
constexpr int WS_END = WS_XT + 128 * XT_PITCH;
constexpr int SPIN_MAX = 1 << 16;
constexpr u32 TMASK = 0x3ffu;

#define DT_HEAD 110
#define WDELAY 60
#define DT_KEY 240
#define DT_REC 270
#define DT_FLG 380
#define DT_VIS 40
#define DT_LATE 50
#define DE_P1 350
#define DE_P2 590
#define DE_FAST 575
#define DT_MIN 25
#define DT_P1 100
#define DT_P2 130
#define SLEEP_AG1 12
#define SLEEP_G 6
#define STSLP 2
#define STWIN 4
#define H2_AT 9
#define CODE_B 28400

#define ROLE_END() do { STAMP_END(); asm volatile("s_endpgm" ::: "memory"); } while (0)
__device__ __forceinline__ u32 now32() { return (u32)__builtin_amdgcn_s_memrealtime(); }
__device__ __forceinline__ void pace(u32 t) {
#pragma unroll 1
  for (int i = 0; i < 1024 && (int)(now32() - t) < 0; ++i) __builtin_amdgcn_s_sleep(1);
}
__device__ __forceinline__ void pace2(u32 n, u32 t, int dmax) {
  int d = (int)(t - n);
  d = d < DT_MIN ? DT_MIN : (d > dmax ? dmax : d);
  pace(n + (u32)d);
}

#define ALL_OTHER "vcc", "scc", "memory", \
    "s0","s1","s2","s5","s6","s7","s33","s34","s35","s44","s45","s46","s47","s48","s49","s50","s51","s52","s53","s54","s55","s56","s57","s58","s59", \
    "s60","s61","s62","s63","s64","s65","s66","s67","s68","s69","s70","s71","s72","s73","s74","s75","s76","s77","s78","s79","s80","s81","s82","s83","s84", \
    "s85","s86","s87","s88","s89","s90","s91","s92","s93","s94","s95","s96","s97","s98","s99", \
    "v0","v1","v2","v3","v4","v5","v6","v7","v8","v9","v10","v11","v12","v13","v14","v15","v16","v17","v18","v19","v20","v21","v22","v23","v24","v25", \
    "v26","v27","v28","v29","v30","v31","v32","v33","v34","v35","v36","v37","v38","v39","v40","v41","v42","v43","v44","v45","v46","v47","v48","v49","v50", \
    "v51","v52","v53","v54","v55","v56","v57","v58","v59","v60","v61","v62","v63","v64","v65","v66","v67","v68","v69","v70","v71","v72","v73","v74","v75", \
    "v76","v77","v78","v79","v80","v81","v82","v83","v84","v85","v86","v87","v88","v89","v90","v91","v92","v93","v94","v95","v96","v97","v98","v99", \
    "v100","v101","v102","v103","v104","v105","v106","v107","v108","v109","v110","v111","v112","v113","v114","v115","v116","v117","v118","v119","v120", \
    "v121","v122","v123","v124","v125","v126","v127","v128","v129","v130","v131","v132","v133","v134","v135","v136","v137","v138","v139","v140","v141", \
    "v142","v143","v144","v145","v146","v147","v148","v149","v150","v151","v152","v153","v154","v155","v156","v157","v158","v159","v160","v161","v162", \
    "v163","v164","v165","v166","v167"
typedef __attribute__((address_space(1))) const u16* gp16;
typedef __attribute__((address_space(1))) const u32* gp32;
typedef __attribute__((address_space(1))) const u4v* gp128;
typedef unsigned long long u64;
typedef u64 q2 __attribute__((ext_vector_type(2)));
typedef u64 q4 __attribute__((ext_vector_type(4)));
typedef u64 q8 __attribute__((ext_vector_type(8)));

__global__ void __launch_bounds__(768, 3) k_main(
    const u16* x_, const u16* w_r_, const u16* gate_w_, const u16* logA_, const u16* Aim_, const u16* Cre_, const u16* Cim_,
    const u16* log_dt_, const u16* Dskip_, const u16* w_gate_, const u16* w_up_, const u16* w_down_, char* wsp_, u16* out_) {
  __shared__ __attribute__((aligned(256))) char smem[K_LDS];
  const u32 lbase = (u32)(uintptr_t)(__attribute__((address_space(3))) char*)smem;
#define XDMA(OFF) "global_load_lds_dwordx4 v1, s[8:9] offset:" #OFF " nt\n\ts_addk_i32 m0, 896\n\t"
  asm volatile("s_load_dwordx16 s[8:23], %0, 0\n\tv_readfirstlane_b32 s3, %2\n\ts_load_dwordx8 s[24:31], %0, 64\n\ts_load_dwordx4 s[36:39], %0, 96\n\t"
               "s_memrealtime s[40:41]\n\t" STAMP_T0 "s_mov_b32 s4, %1\n\ts_lshr_b32 s3, s3, 6\n\t"
               "s_cmp_lt_u32 s3, 4\n\ts_cbranch_scc1 .Lrows\n\ts_barrier\n\t"
               "s_cmp_ge_u32 s3, 8\n\ts_cbranch_scc1 .Lrole_g\n\ts_cmp_eq_u32 s3, 7\n\ts_cbranch_scc1 .Lrole_d\n\t"
               "s_cmp_ge_u32 s3, 5\n\ts_cbranch_scc1 .Lrole_h\n\ts_branch .Lrole_c\n.Lrows:\n\t"
               "s_lshr_b32 s5, s4, 3\n\tv_lshlrev_b32 v3, 2, %2\n\ts_mov_b32 m0, %4\n\tv_lshl_or_b32 v3, s5, 10, v3\n\ts_getpc_b64 s[6:7]\n\t"
               "v_min_u32 v3, %3, v3\n\tv_lshrrev_b32 v1, 3, %2\n\tglobal_load_lds_dword v3, s[6:7]\n\t"
               "v_and_b32 v2, 6, v1\n\ts_and_b32 s34, s4, 7\n\tv_xor_b32 v2, v2, %2\n\ts_lshl_b32 s34, s34, 5\n\tv_and_b32 v2, 7, v2\n\ts_add_u32 s5, s5, s34\n\t"
               "v_and_b32 v1, 7, v1\n\ts_lshl_b32 s5, s5, 5\n\tv_lshlrev_b32 v1, 11, v1\n\ts_add_u32 s5, s5, s3\n\tv_lshl_or_b32 v1, v2, 4, v1\n\t"
               "s_mul_i32 s34, s3, %5\n\tv_lshl_add_u32 v1, s5, 9, v1\n\ts_add_u32 m0, s34, %6\n\tv_and_b32 v2, 31, %2\n\ts_waitcnt lgkmcnt(0)\n\t"
               XDMA(0) "s_lshl_b32 s5, s3, 5\n\t" XDMA(128) "v_add_lshl_u32 v2, s5, v2, 4\n\t" XDMA(256) "s_lshl_b32 s5, s3, 10\n\t"
               "global_load_lds_dwordx4 v1, s[8:9] offset:384 nt\n\ts_add_u32 m0, s5, %7\n\ts_mov_b32 exec_hi, 0\n\t"
               "global_load_lds_dwordx4 v2, s[10:11]\n\ts_addk_i32 m0, 512\n\ts_nop 0\n\tglobal_load_lds_dwordx4 v2, s[12:13]\n\t"
               "s_mov_b32 m0, %8\n\tv_mov_b32 v3, 0\n\tglobal_load_lds_dword v3, s[36:37]\n\ts_mov_b32 exec_hi, -1"
               :: "s"(__builtin_amdgcn_kernarg_segment_ptr()), "s"(blockIdx.x), "v"(threadIdx.x), "n"(CODE_B - 256), "n"(O_JUNK), "n"(CHB), "n"(O_U + 512), "n"(O_FAR), "n"(O_XS)
               : "s3", "s4", "s5", "s6", "s7", "s8", "s9", "s10", "s11", "s12", "s13", "s14", "s15", "s16", "s17", "s18", "s19", "s20", "s21", "s22", "s23",
                 "s24", "s25", "s26", "s27", "s28", "s29", "s30", "s31", "s34", "s36", "s37", "s38", "s39", "s40", "s41", "s42", "s43", "v1", "v2", "v3", "scc", "memory");
  int lane, c16, g, w, b, xg, xi, cg, cc, c, d;
  char* ch_;
  q8 ka; q4 kb; q2 kc, kt;
  u32* ctl = (u32*)(smem + O_CTL);
  u32* par = (u32*)(smem + O_PAR);
#define IDS() do { int z_ = 0; OPQ(z_); lane = __builtin_amdgcn_mbcnt_hi(~0u, __builtin_amdgcn_mbcnt_lo(~0u, z_)); c16 = lane & 15; g = lane >> 4; } while (0)
#define ROLE(name) \
  asm volatile(".Lrole_" name ":\n\ts_waitcnt lgkmcnt(0)" : "={s[8:23]}"(ka), "={s[24:31]}"(kb), "={s[36:39]}"(kc), "={s[40:43]}"(kt), "={s3}"(w), "={s4}"(b) :: ALL_OTHER); \
  if (__builtin_expect(w > 11, 0)) asm volatile("s_endpgm" ::: "memory");        \
  IDS(); \
  xg = b & 7; xi = b >> 3;                          \
  cg = xg * 32 + xi;                                \
  cc = (xi >> 4) * 128 + xg * 16 + (xi & 15);       \
  c = w & 3; ch_ = smem + c * CHB; d = cc * 4 + c; \
  const u32 ts0 = (u32)kt[0]; \
  auto wrs = __builtin_amdgcn_make_buffer_rsrc((void*)kc[0], 0, WS_END, 0x00020000); \
  STAMP0()

  const char* tapb;
  const char* ub;
  char* erp;
  const char* eup;
  const char* egp;
  u4v At[17], band[4];
  float dm1;
  f4 acc[8];
  auto conv_setup = [&]() AI {
    tapb = ch_ + O_TAP + ((15 - c16) & 3) * TAP_PITCH + ((15 - c16) & ~3) * 2;
    ub = ch_ + O_U - 32 + c16 * 32 + g * 16;
    erp = ch_ + O_OUT + g * 144 + c16 * 8;
    eup = ch_ + O_U + 512 + c16 * 32 + g * 8;
    egp = smem + O_GT + lane * 16;
  };
  auto band_setup = [&]() AI {
#pragma unroll
    for (int k = 0; k < 4; ++k) {
      const int ctr = k == 0 ? 12 : 8, hw = k + 1;
      const u32 m = ((1u << (2 * hw + 1)) - 1u) << (ctr - hw);
      const u32 bits = (m << c16) >> (8 * g);
#pragma unroll
      for (int j = 0; j < 4; ++j)
        band[k][j] = (__builtin_amdgcn_sbfe(bits, 2 * j, 1) & 0x3f80u) | (__builtin_amdgcn_sbfe(bits, 2 * j + 1, 1) & 0x3f800000u);
      OPQ(band[k]);
    }
  };
  auto frag2 = [&](const char* pa, const char* pb) AI { const u2v lo = lds8(pa), hi = lds8(pb); return u4v{lo[0], lo[1], hi[0], hi[1]}; };
  auto atload = [&](int s0, int s1) AI {
#pragma unroll
    for (int s = s0; s < s1; ++s) At[s] = frag2(tapb + 64 * s + g * 16, tapb + 64 * s + g * 16 + 8);
  };
  auto bin = [&](int pair, int be) AI { const char* p = tapb + g * 8 + (SEG_I + 96 * pair - 16 * be) * 2; return frag2(p, p + 96); };
  auto bout = [&](int be) AI { const char* p = tapb + g * 8 + (SEG_O0 - 16 * be) * 2; return frag2(p, p + (SEG_O1 - SEG_O0) * 2); };
  auto asp = [&]() AI { return frag2(tapb + SEG_SP * 2 + g * 16, tapb + SEG_SP * 2 + g * 16 + 8); };
  auto mma = [&](u4v a, u4v bq, f4 cin) AI { return __builtin_amdgcn_mfma_f32_16x16x32_bf16(as_b8(a), as_b8(bq), cin, 0, 0, 0); };
  auto uld = [&](int sg) AI { return *(const u4v*)(ub + 512 + sg * 64); };
  auto uld24 = [&](int un) AI { return *(const u4v*)(ub + 512 + un * 512 - 16); };
  u2v euw[8], eo[8]; f4 egv[8], efv[8];
  float eu[8][4], ev[8][4], ea[8][4];
  u4v Fq[57], ru[6], wa_[6][3];
  f4 wq[6][6];
  u32 pk[12], one, qa, fa;
  char* tfp; char* fp_;
#define LOPQ(p_) do { u32 a_ = (u32)(uintptr_t)(__attribute__((address_space(3))) const char*)p_; OPQ(a_); p_ = (decltype(p_))(__attribute__((address_space(3))) char*)(uintptr_t)a_; } while (0)
  auto m_setup = [&]() AI {
    tfp = smem + (lane == 0 ? O_CTL + c * 4 : O_JUNK + lane * 4);
    fp_ = smem + O_FAR + c * 4096 + lane * 16;
    qa = CTLA(C_Q) + (lane & 3) * 4; fa = CTLA(C_FAR + c); one = 1u;
    OPQ(qa); OPQ(fa); OPQ(one);
    LOPQ(tfp); LOPQ(fp_); LOPQ(erp); LOPQ(eup); LOPQ(egp); LOPQ(ub);
  };
#define WAITA(addr) do { u32 t_; int n_ = 1 << 20; asm volatile( \
    "1:\n\tds_read_b32 %0, %2\n\ts_sub_u32 %1, %1, 1\n\ts_cbranch_scc1 2f\n\ts_waitcnt lgkmcnt(0)\n\tv_cmp_eq_u32 vcc, 0, %0\n\ts_cbranch_vccnz 1b\n2:\n\ts_waitcnt lgkmcnt(0)" \
    : "=&v"(t_), "+s"(n_) : "v"(addr) : "vcc", "scc", "memory"); } while (0)
#define MF(d_, a_, b_) asm volatile("v_mfma_f32_16x16x32_bf16 %0, %1, %2, %0" : "+v"(d_) : "v"(a_), "v"(b_)); SB()
#define MF0(d_, a_, b_) asm volatile("v_mfma_f32_16x16x32_bf16 %0, %1, %2, 0" : "=&v"(d_) : "v"(a_), "v"(b_)); SB()
#define NOP(n_) asm volatile("s_nop " #n_); SB()
#define FL(i_, sg_) Fq[i_] = uld(sg_); SB()
#define RL(un_) ru[un_] = uld24(un_); SB()
#define AL(s_) atload(s_, s_ + 1); SB()
#define ULD(t_) euw[t_] = *(const u2v*)(eup + t_ * 512); SB()
#define GLD(t_) egv[t_] = *(const f4*)(egp + t_ * 1024); SB()
#define FLD(t_) efv[t_] = *(const f4*)(fp_ + (t_ - 2) * 1024); SB()
#define FST(un_) *(f4*)(fp_ + un_ * 1024) = acc[un_ + 2]; SB()
#define EUN(t_, e_) eu[t_][e_] = (e_ & 1) ? bfhi(euw[t_][e_ >> 1]) : bflo(euw[t_][e_ >> 1]); OPQ(eu[t_][e_]); ev[t_][e_] = eu[t_][e_]; SB()
#define EA1(t_, e_) ea[t_][e_] = fmaf(eu[t_][e_], dm1, acc[t_][e_]); OPQ(ea[t_][e_]); SB()
#define EFA(t_, e_) ev[t_][e_] = fmaf(egv[t_][e_], efv[t_][e_], eu[t_][e_]); OPQ(ev[t_][e_]); SB()
#define EA2(t_, e_) ea[t_][e_] = fmaf(egv[t_][e_], ea[t_][e_], ev[t_][e_]); OPQ(ea[t_][e_]); SB()
#define EPK(t_, k_) { u32 v_ = pk_bf(ea[t_][2 * k_], ea[t_][2 * k_ + 1]); OPQ(v_); eo[t_][k_] = v_; } SB()
#define OST(t_) *(u2v*)(erp + t_ * OT) = eo[t_]; SB()
#define WPK(un_, p_, j_) { u32 v_ = pk_bf(wq[un_][2 * p_ + j_ / 2][2 * (j_ & 1)], wq[un_][2 * p_ + j_ / 2][2 * (j_ & 1) + 1]); OPQ(v_); wa_[un_][p_][j_] = v_; } SB()
#define FLAG(w_) asm volatile("" ::: "memory"); *(u32*)(tfp + (w_) * 4) = one; SB()
#define PEEK(i_, a_) pk[i_] = __hip_atomic_load((__attribute__((address_space(3))) u32*)(uintptr_t)(a_), __ATOMIC_RELAXED, __HIP_MEMORY_SCOPE_WORKGROUP); SB()
#define CHECK(i_, a_) if (__builtin_expect(__builtin_amdgcn_ballot_w64(pk[i_] == 0u) != 0ull, 0)) WAITA(a_); asm volatile("" ::: "memory"); SB()
#define MIX2(nm, nd, nv) _Pragma("unroll") for (int r_ = 0; r_ < (nm); ++r_) { __builtin_amdgcn_sched_group_barrier(0x008, 1, 0); \
    if (nd) __builtin_amdgcn_sched_group_barrier(0x100, (nd), 0); if (nv) __builtin_amdgcn_sched_group_barrier(0x002, (nv), 0); }

  {
    ROLE("x");
    const int xw = w;
    char* img = ch_ + O_U + 512;
    STAMP(11);
    __builtin_amdgcn_s_setprio(2);
    { u32 zz = 0u; OPQ(zz); if (lane < 32) ctl[32 * xw + lane] = zz; }
    if (xi == 31 && w == 0) { const u32 th = ts0; if (lane == 0) __builtin_amdgcn_raw_buffer_store_b64(u2v{th, ~th}, wrs, WS_HEAD + xg * 8, 0, 16); }
    BAR();
    STAMP(12);
    const char* wp; const char* fp; const char* tp0; const char* tp1; char* pp;
    {
      const int r = lane & 7, q = (lane >> 2) & 3;
      fp = img + r * 128 + ((((lane & 8) / 2 + g) ^ (r & 6)) * 16);
      wp = smem + O_FAR + xw * 1024 + (lane & 1) * 512 + (lane & 4) * 16 + g * 16;
      tp0 = img + q * 128 + (((2 * g + ((lane >> 1) & 1)) ^ (q & 2)) * 16) + (lane & 1) * 8;
      tp1 = img + 512 + q * 128 + (((2 * g + ((lane >> 1) & 1)) ^ ((q & 2) + 4)) * 16) + (lane & 1) * 8;
      pp = smem + ((lane >> 3) == 0 || (lane >> 3) == 3 ? O_PART + r * 64 + xw * 16 + g * 8 : O_JUNK + lane * 8);
    }
    const int so = WS_XT + (32 * xw + (lane >> 3)) * XT_PITCH + cg * 128 + (lane & 7) * 16, sstep = 8 * XT_PITCH;
    u32 lo_ = WS_XT + (cc >> 1) * XT_PITCH + (16 * xw + (lane >> 2)) * 128 + (cc & 1) * 64 + (lane & 3) * 16;
    OPQ(lo_);
    SB();
    asm volatile("s_waitcnt vmcnt(0)" ::: "memory");
    STAMP(1);
    {
      u4v wa[4], fb[4]; u2v ta[4], tb[4];
#pragma unroll
      for (int s = 0; s < 4; ++s) { wa[s] = *(const u4v*)(wp + s * 128); fb[s] = *(const u4v*)(fp + s * 1024); }
      SB();
      f4 r = {0.f, 0.f, 0.f, 0.f};
#pragma unroll
      for (int s = 0; s < 4; ++s) r = mma(wa[s], fb[s], r);
      *(f2*)pp = f2{r[0], r[1]};
      ARRIVE(C_PART);
      SB();
#pragma unroll
      for (int s = 0; s < 4; ++s) { ta[s] = tr_read(tp0 + s * 1024); tb[s] = tr_read(tp1 + s * 1024); }
      SB();
      WAITQ(C_GP, 1);
#pragma unroll
      for (int s = 0; s < 4; ++s) __builtin_amdgcn_raw_buffer_store_b128(u4v{ta[s][0], ta[s][1], tb[s][0], tb[s][1]}, wrs, so + s * sstep, 0, 16);
    }
    STAMP(2);
    const u32 dskw = ((gp16)kb[0])[d];
    conv_setup();
    band_setup();
    const u32 tag = par[0];
    ARRIVE(C_PUB);
    asm volatile("s_waitcnt vmcnt(0)" ::: "memory");
    if (lane == 0) __builtin_amdgcn_raw_buffer_store_b32(tag, wrs, WS_FLG + xw * 1024 + cg * 4, 0, 16);
    STAMP(3);
    dm1 = bflo(dskw) - 1.f;
    WAITS(C_TL, 1);
    const u32 tl0 = (u32)__builtin_amdgcn_readfirstlane((int)par[1]);
    u3v fq[2];
    auto rec_ld = [&]() AI {
#pragma unroll
      for (int k = 0; k < 2; ++k) fq[k] = __builtin_amdgcn_raw_buffer_load_b96(wrs, WS_REC + xw * 2048 + k * 1024 + lane * 16, 0, 16);
    };
    pace(tl0 + DT_REC);
    rec_ld();
    SB();
    {
#pragma unroll 1
      for (int spin = 0; spin < SPIN_MAX && __builtin_expect(__any(fq[0][2] != tag || fq[1][2] != tag), 0); ++spin) {
        __builtin_amdgcn_s_sleep(SLEEP_G);
        rec_ld();
        asm volatile("" ::: "memory");
      }
      char* gp = smem + O_GT + xw * 2048 + (lane & 3) * 256 + (lane >> 2) * 16;
#pragma unroll
      for (int k = 0; k < 2; ++k) *(f4*)(gp + k * 1024) = f4{bflo(fq[k][0]), bfhi(fq[k][0]), bflo(fq[k][1]), bfhi(fq[k][1])};
    }
    pace(tl0 + DT_FLG);
    u4v xq[4];
    bool late[4];
    {
      const u32 fl = __builtin_amdgcn_raw_buffer_load_b32(wrs, WS_FLG + (cc >> 6) * 1024 + xw * 64 + g * 256 + c16 * 4, 0, 16);
      const u64 bad = __builtin_amdgcn_ballot_w64(fl != tag);
      asm volatile("" ::: "memory");
#pragma unroll
      for (int k = 0; k < 4; ++k) {
        late[k] = ((bad >> (16 * k)) & 0xffffull) != 0ull;
        xq[k] = __builtin_amdgcn_raw_buffer_load_b128(wrs, lo_ | (late[k] ? 1u << 30 : 0u), k * 8192, 16);
        SB();
      }
    }
    ARRIVE(C_XL);
    STAMP(4);
    WAITQ(C_TAPS + c, 1);
    u4v Bi[2][3], Bo[4], As;
#pragma unroll
    for (int be = 0; be < 3; ++be) { Bi[0][be] = bin(0, be); Bi[1][be] = bin(1, be); }
#pragma unroll
    for (int be = 0; be < 4; ++be) Bo[be] = bout(be);
    As = asp();
    WAITQ(C_PUB, 4);
    char* dp = smem + (lane & 3) * CHB + O_U + 512 + xw * 256 + (lane >> 2) * 16;
    m_setup();
    auto stage = [&](int k) AI {
      SB();
      if (__builtin_expect(late[k], 0)) {
#pragma unroll 1
        for (int spin = 0; spin < SPIN_MAX; ++spin) {
          STAMP(14);
          const u32 f1 = __builtin_amdgcn_raw_buffer_load_b32(wrs, WS_FLG + (cc >> 6) * 1024 + xw * 64 + k * 256 + c16 * 4, 0, 16);
          if (!__any(f1 != tag)) break;
          __builtin_amdgcn_s_sleep(SLEEP_G);
          asm volatile("" ::: "memory");
        }
        asm volatile("" ::: "memory");
        xq[k] = __builtin_amdgcn_raw_buffer_load_b128(wrs, lo_, k * 8192, 16);
        asm volatile("s_waitcnt vmcnt(0)" : "+v"(xq[k]) :: "memory");
      }
      *(u4v*)(dp + k * 1024) = xq[k];
      FLAG(C_Q + k * 4);
    };
    stage(0);
    stage(1);
    STAMP(5);
    __builtin_amdgcn_s_setprio(0);
    WAITA(qa);
    SB();
    FL(0, 0); RL(0); FL(8, 8); RL(1);
    MF0(wq[0][0], Fq[0], band[0]);
    MF0(wq[0][1], Fq[0], band[1]);
    MF0(wq[0][2], ru[0], band[0]);
    MF0(wq[0][3], ru[0], band[1]);
    MF0(wq[0][4], Fq[0], band[2]);
    MF0(wq[0][5], ru[0], band[3]);
    MF0(acc[4], As, Fq[0]); PEEK(6, qa + 16);
    MF0(wq[1][0], Fq[8], band[0]); WPK(0, 0, 0); WPK(0, 0, 1);
    MF0(wq[1][1], Fq[8], band[1]); WPK(0, 0, 2); WPK(0, 0, 3);
    MF0(wq[1][2], ru[1], band[0]); WPK(0, 1, 0); WPK(0, 1, 1);
    MF0(wq[1][3], ru[1], band[1]); WPK(0, 1, 2); WPK(0, 1, 3);
    MF0(wq[1][4], Fq[8], band[2]); CHECK(6, qa + 16);
    MF0(wq[1][5], ru[1], band[3]); WPK(0, 2, 0); WPK(0, 2, 1);
    MF0(acc[5], As, Fq[8]); WPK(0, 2, 2); WPK(0, 2, 3);
    MF0(acc[2], wa_[0][0], Bi[0][0]); FL(16, 16); RL(2);
    MF(acc[2], wa_[0][1], Bi[1][0]); WPK(1, 0, 0); WPK(1, 0, 1);
    MF0(acc[3], wa_[0][0], Bi[0][1]); WPK(1, 0, 2); WPK(1, 0, 3);
    MF(acc[3], wa_[0][1], Bi[1][1]); WPK(1, 1, 0); WPK(1, 1, 1);
    MF(acc[4], wa_[0][0], Bi[0][2]); FST(0); FLAG(C_FAR + 0 * 4);
    MF(acc[4], wa_[0][1], Bi[1][2]); WPK(1, 1, 2); WPK(1, 1, 3);
    MF(acc[4], wa_[0][2], Bo[0]); WPK(1, 2, 0); WPK(1, 2, 1);
    MF(acc[5], wa_[0][2], Bo[1]); WPK(1, 2, 2); WPK(1, 2, 3);
    MF0(acc[6], wa_[0][2], Bo[2]);
    MF0(acc[7], wa_[0][2], Bo[3]); stage(2);
    MF0(wq[2][0], Fq[16], band[0]);
    MF0(wq[2][1], Fq[16], band[1]); stage(3);
    MF0(wq[2][2], ru[2], band[0]);
    MF0(wq[2][3], ru[2], band[1]);
    MF0(wq[2][4], Fq[16], band[2]); FL(24, 24); RL(3);
    MF0(wq[2][5], ru[2], band[3]); WPK(2, 0, 0); WPK(2, 0, 1);
    MF(acc[6], As, Fq[16]); WPK(2, 0, 2); WPK(2, 0, 3);
    MF(acc[3], wa_[1][0], Bi[0][0]); WPK(2, 1, 0); WPK(2, 1, 1);
    MF(acc[3], wa_[1][1], Bi[1][0]); WPK(2, 1, 2); WPK(2, 1, 3);
    MF(acc[4], wa_[1][0], Bi[0][1]); WPK(2, 2, 0); WPK(2, 2, 1);
    MF(acc[4], wa_[1][1], Bi[1][1]); WPK(2, 2, 2); WPK(2, 2, 3);
    MF(acc[5], wa_[1][0], Bi[0][2]); FST(1); FLAG(C_FAR + 1 * 4);
    MF(acc[5], wa_[1][1], Bi[1][2]); PEEK(7, qa + 32);
    MF(acc[5], wa_[1][2], Bo[0]);
    MF(acc[6], wa_[1][2], Bo[1]);
    MF(acc[7], wa_[1][2], Bo[2]);
    MF0(wq[3][0], Fq[24], band[0]);
    MF0(wq[3][1], Fq[24], band[1]); CHECK(7, qa + 32);
    MF0(wq[3][2], ru[3], band[0]);
    MF0(wq[3][3], ru[3], band[1]);
    MF0(wq[3][4], Fq[24], band[2]); FL(32, 32); RL(4);
    MF0(wq[3][5], ru[3], band[3]); WPK(3, 0, 0); WPK(3, 0, 1);
    MF(acc[7], As, Fq[24]); WPK(3, 0, 2); WPK(3, 0, 3);
    MF(acc[4], wa_[2][0], Bi[0][0]); WPK(3, 1, 0); WPK(3, 1, 1);
    MF(acc[4], wa_[2][1], Bi[1][0]); WPK(3, 1, 2); WPK(3, 1, 3);
    MF(acc[5], wa_[2][0], Bi[0][1]); WPK(3, 2, 0); WPK(3, 2, 1);
    MF(acc[5], wa_[2][1], Bi[1][1]); FL(40, 40); RL(5);
    MF(acc[6], wa_[2][0], Bi[0][2]); FST(2); FLAG(C_FAR + 2 * 4);
    MF(acc[6], wa_[2][1], Bi[1][2]); WPK(3, 2, 2); WPK(3, 2, 3);
    MF(acc[6], wa_[2][2], Bo[0]); AL(0);
    MF(acc[7], wa_[2][2], Bo[1]); AL(1);
    MF0(wq[4][0], Fq[32], band[0]); AL(2);
    MF0(wq[4][1], Fq[32], band[1]); AL(3);
    MF0(wq[4][2], ru[4], band[0]); AL(4);
    MF0(wq[4][3], ru[4], band[1]); WPK(4, 0, 0); WPK(4, 0, 1);
    MF(acc[5], wa_[3][0], Bi[0][0]); WPK(4, 0, 2); WPK(4, 0, 3);
    MF(acc[5], wa_[3][1], Bi[1][0]); WPK(4, 1, 0); WPK(4, 1, 1);
    MF(acc[6], wa_[3][0], Bi[0][1]); WPK(4, 1, 2); WPK(4, 1, 3);
    MF(acc[6], wa_[3][1], Bi[1][1]); AL(5);
    MF(acc[7], wa_[3][0], Bi[0][2]); FST(3); FLAG(C_FAR + 3 * 4);
    MF(acc[7], wa_[3][1], Bi[1][2]); AL(6);
    MF(acc[7], wa_[3][2], Bo[0]); FL(33, 33); AL(7);
    MF0(wq[5][0], Fq[40], band[0]); FL(34, 34); AL(8);
    MF0(wq[5][1], Fq[40], band[1]); FL(35, 35); AL(9);
    MF0(wq[5][2], ru[5], band[0]); WPK(5, 0, 0); WPK(5, 0, 1);
    MF0(wq[5][3], ru[5], band[1]); AL(10);
    MF(acc[6], wa_[4][0], Bi[0][0]); PEEK(8, qa + 48); FL(36, 36);
    MF(acc[6], wa_[4][1], Bi[1][0]); FL(37, 37); WPK(5, 0, 2);
    MF(acc[7], wa_[4][0], Bi[0][1]); FL(38, 38); WPK(5, 0, 3);
    MF(acc[7], wa_[4][1], Bi[1][1]); FL(39, 39); WPK(5, 1, 0);
    MF(acc[6], At[0], Fq[32]); WPK(5, 1, 1); WPK(5, 1, 2);
    MF(acc[6], At[1], Fq[33]); CHECK(8, qa + 48);
    MF(acc[6], At[2], Fq[34]); WPK(5, 1, 3); FL(41, 41);
    MF(acc[6], At[3], Fq[35]); AL(11);
    MF(acc[7], wa_[5][0], Bi[0][0]); FL(42, 42); AL(12);
    MF(acc[7], wa_[5][1], Bi[1][0]); AL(13);
    MF(acc[6], At[4], Fq[36]); FL(43, 43); AL(14);
    MF(acc[6], At[5], Fq[37]); AL(15);
    MF(acc[6], At[6], Fq[38]); FL(44, 44); AL(16);
    MF(acc[6], At[7], Fq[39]);
    MF(acc[6], At[8], Fq[40]); FL(45, 45);
    MF(acc[7], At[0], Fq[40]);
    MF(acc[6], At[9], Fq[41]); FL(46, 46);
    MF(acc[7], At[1], Fq[41]);
    MF(acc[6], At[10], Fq[42]); FL(47, 47);
    MF(acc[7], At[2], Fq[42]);
    MF(acc[6], At[11], Fq[43]); FL(48, 48);
    MF(acc[7], At[3], Fq[43]);
    MF(acc[6], At[12], Fq[44]); FL(49, 49); ULD(6);
    MF(acc[7], At[4], Fq[44]); FL(50, 50); GLD(6);
    MF(acc[6], At[13], Fq[45]); FL(51, 51);
    MF(acc[7], At[5], Fq[45]); FL(52, 52);
    MF(acc[6], At[14], Fq[46]); FL(53, 53);
    MF(acc[7], At[6], Fq[46]); FL(54, 54); EUN(6, 0);
    MF(acc[6], At[15], Fq[47]); FL(55, 55); EUN(6, 1);
    MF(acc[7], At[7], Fq[47]); FL(56, 56); EUN(6, 2);
    MF(acc[6], At[16], Fq[48]); EUN(6, 3);
    MF(acc[7], At[8], Fq[48]); ULD(7); GLD(7);
    MF(acc[7], At[9], Fq[49]);
    MF(acc[7], At[10], Fq[50]);
    MF(acc[7], At[11], Fq[51]);
    MF(acc[7], At[12], Fq[52]); EA1(6, 0); EA1(6, 1); EA1(6, 2); EA1(6, 3);
    MF(acc[7], At[13], Fq[53]); EA2(6, 0); EA2(6, 1); EA2(6, 2); EA2(6, 3);
    MF(acc[7], At[14], Fq[54]); EPK(6, 0); EPK(6, 1);
    MF(acc[7], At[15], Fq[55]); OST(6); FLAG(C_TILE + 6 * 4);
    MF(acc[7], At[16], Fq[56]); EUN(7, 0); EUN(7, 1); EUN(7, 2); EUN(7, 3); NOP(3); EA1(7, 0); EA1(7, 1); EA1(7, 2); EA1(7, 3); EA2(7, 0); EA2(7, 1); EA2(7, 2); EA2(7, 3); EPK(7, 0); EPK(7, 1); OST(7); FLAG(C_TILE + 7 * 4);
    STAMP(9);
    ROLE_END();
  }
  {
    ROLE("h");
    const int hw = w - 5;
    const int j = xg * 28 + xi;
    const int s1 = j >> 2, kq = j & 3;
    const bool own = xi < 28;
    const u64 wbase = hw == 0 ? kb[1] : kb[2];
    const int pitch = F * 2;
    auto hrs = __builtin_amdgcn_make_buffer_rsrc((void*)wbase, 0, own ? D * F * 2 : 0, 0x00020000);
    const int r8 = lane >> 3;
    const int vo = (256 * kq * F + 64 * s1) * 2 + (8 * (r8 >> 2) + (r8 & 3)) * pitch + (((lane & 7) * 16) ^ (((r8 >> 1) & 3) * 32));
    auto dma = [&](int h, int sB, int pt, u32 dst) AI {
      const int m = h >> 1;
#pragma unroll
      for (int i = 0; i < 2; ++i)
        asm volatile("s_mov_b32 m0, %3\n\ts_nop 0\n\tbuffer_load_dwordx4 %0, %1, %2 offen nt lds"
                     :: "v"(vo), "s"(hrs), "s"((m & 1) * 32 * pt + (m >> 1) * sB + (16 * i + 4 * (h & 1)) * pt), "s"(dst + i * 1024) : "memory");
    };
    const int q = c16 >> 2, p = c16 & 3;
    int tro[4];
#pragma unroll
    for (int ct = 0; ct < 4; ++ct) tro[ct] = (4 * g + q) * 128 + ((ct ^ (((4 * g + q) >> 1) & 3)) * 32) + p * 8;
    const int tok = lane & 7;
    u4v wd[2][8];
    auto wd_ld = [&](int k, int cb) AI {
#pragma unroll
      for (int i = 0; i < 8; ++i) {
        const char* pp = smem + O_WD + cb * 8192 + (i >> 2) * 4096 + tro[i & 3];
        const u2v lo = tr_read(pp), hi = tr_read(pp + 2048);
        wd[k][i] = u4v{lo[0], lo[1], hi[0], hi[1]};
      }
    };
    u32 p2o, p2k[4], hlo, hhi;
    auto f2_setup = [&](int cb0) AI {
      p2o = WS_P2 + ((64 * kq + 16 * cb0 + (lane & 8) / 2 + g) * 56 + s1) * 128 + tok * 16;
      hlo = lbase + O_HB + ((lane & 8) ? 512 : g * 128 + tok * 16);
      hhi = lbase + O_HB + ((lane & 8) ? g * 128 + tok * 16 : 512);
#pragma unroll
      for (int i = 0; i < (cb0 == 2 ? 4 : 2); ++i) { p2k[i] = p2o + i * 8 * 7168; OPQ(p2k[i]); }
      OPQ(hlo); OPQ(hhi);
    };
    auto f2_job = [&](int nb, u32 t10) AI {
      u4v hb[4];
      {
        u32 cnt; int n_ = 1 << 20;
        asm volatile("1:\n\tds_read_b32 %4, %6\n\tds_read_b128 %0, %7\n\tds_read_b128 %1, %8\n\tds_read_b128 %2, %7 offset:528\n\tds_read_b128 %3, %8 offset:528\n\t"
                     "s_sub_u32 %5, %5, 1\n\ts_cbranch_scc1 2f\n\ts_waitcnt lgkmcnt(0)\n\tv_cmp_gt_u32 vcc, 2, %4\n\ts_cbranch_vccnz 1b\n2:\n\ts_waitcnt lgkmcnt(0)"
                     : "=&v"(hb[0]), "=&v"(hb[1]), "=&v"(hb[2]), "=&v"(hb[3]), "=&v"(cnt), "+s"(n_) : "v"(CTLA(C_HRDY)), "v"(hlo), "v"(hhi) : "vcc", "scc", "memory");
      }
      STAMP(11);
      f4 r[4];
#pragma unroll
      for (int i = 0; i < 2 * nb; ++i) {
        const u4v* wp = &wd[i >> 1][(i & 1) * 2];
        r[i] = mma(wp[0], hb[0], f4{0.f, 0.f, 0.f, 0.f});
        r[i] = mma(wp[1], hb[1], r[i]);
        r[i] = mma(wp[4], hb[2], r[i]);
        r[i] = mma(wp[5], hb[3], r[i]);
      }
      SB();
#pragma unroll
      for (int i = 0; i < 2 * nb; ++i) {
        u4v v = __builtin_bit_cast(u4v, r[i]);
#pragma unroll
        for (int e = 0; e < 4; ++e) v[e] = __builtin_amdgcn_bitop3_b32(v[e], t10, TMASK, 0xd8);
        __builtin_amdgcn_raw_buffer_store_b128(v, wrs, p2k[i], 0, 16);
      }
      STAMP(12);
    };
    const u32 rb = lbase + O_RING + hw * (NSLOT * 2048);
    pace(ts0 + WDELAY);
    STAMP(2);
#pragma unroll
    for (int h = 0; h < WWIN; ++h) dma(h, 64 * F * 2, F * 2, rb + h * 2048);
    __builtin_amdgcn_s_setprio(3);
    STAMP(3);
    if (__builtin_expect(own, 1)) {
      u4v wv[32];
      f4 a1[4] = {f4{0.f, 0.f, 0.f, 0.f}, f4{0.f, 0.f, 0.f, 0.f}, f4{0.f, 0.f, 0.f, 0.f}, f4{0.f, 0.f, 0.f, 0.f}};
      u4v xb[8];
      auto f1 = [&](int k0, int k1) AI {
#pragma unroll
        for (int k = k0; k < k1; ++k)
#pragma unroll
          for (int ct = 0; ct < 4; ++ct) a1[ct] = mma(wv[4 * k + ct], xb[k], a1[ct]);
      };
      u32 xo = lbase + O_XS + tok * XS_PITCH + g * 16;
      u32 p1o = WS_P1 + s1 * 16384 + kq * 4096 + hw * 2048 + (lane & 8) * 64 + g * 128 + tok * 16;
      u32 t10, tpub;
      OPQ(xo); OPQ(p1o);
      constexpr int HX = (16 - WWIN + 1) & ~1;
#pragma unroll
      for (int h = 0; h < 16; ++h) {
        const int left = (h + WWIN < 16 ? WWIN : 16 - h) - 1;
        asm volatile("s_waitcnt vmcnt(%0)" :: "n"(2 * left) : "memory");
        if (h + WWIN < 16) {
          asm volatile("s_waitcnt lgkmcnt(0)" ::: "memory");
          dma(h + WWIN, 64 * F * 2, F * 2, rb + ((h + WWIN) % NSLOT) * 2048);
        }
#pragma unroll
        for (int ct = 0; ct < 4; ++ct) {
          const u2v t = tr_read(smem + O_RING + hw * (NSLOT * 2048) + (h % NSLOT) * 2048 + tro[ct]);
          wv[4 * (h >> 1) + ct][2 * (h & 1)] = t[0]; wv[4 * (h >> 1) + ct][2 * (h & 1) + 1] = t[1];
        }
        if (h == H2_AT) ARRIVE(C_WGU);
        if (h == HX - 1) {
          STAMP(4);
          WAITQ(C_XS, 1);
          STAMP(1);
#pragma unroll
          for (int k = 0; k < 8; ++k) xb[k] = *(const __attribute__((address_space(3))) u4v*)(uintptr_t)(xo + k * 64);
          t10 = par[0]; tpub = par[2];
          SB();
          f1(0, 2);
          SB();
        }
        if (h >= HX) { f1(h - HX + 2, h - HX + 3); SB(); }
      }
      STAMP(5);
      f1(18 - HX, 8);
      t10 &= TMASK;
#pragma unroll
      for (int a = 0; a < 2; ++a) {
        u4v v;
#pragma unroll
        for (int e = 0; e < 4; ++e)
          v[e] = ((u32)__builtin_amdgcn_update_dpp(__float_as_int(a1[2 * a][e]), __float_as_int(a1[2 * a + 1][e]), 0xe4, 0xf, 0xc, false) & ~TMASK) | t10;
        __builtin_amdgcn_raw_buffer_store_b128(v, wrs, p1o + a * 1024, 0, 16);
      }
      STAMP(6);
      const u32 tn = now32();
      u32 voff = WS_P1 + s1 * 16384 + hw * 1024 + lane * 16;
      u32 ho = lbase + O_HB + hw * 528 + g * 128 + tok * 16 + (lane & 8);
      float nl2e = -1.44269504088896341f;
      u32 wda = CTLA(C_WD) + hw * 4;
      OPQ(voff); OPQ(ho); OPQ(nl2e); OPQ(wda);
      f2_setup(hw & 1);
      pace2(tn, (u32)__builtin_amdgcn_readfirstlane((int)tpub) + DE_P1, DT_P1);
      u4v pb[8];
      auto p1_ld = [&]() AI {
#pragma unroll
        for (int i = 0; i < 8; ++i) { pb[i] = __builtin_amdgcn_raw_buffer_load_b128(wrs, voff + (i >> 2) * 2048, (i & 3) * 4096, 16); SB(); }
      };
      p1_ld();
      SB();
      f4 G, U;
      bool early = false;
#pragma unroll 1
      for (int spin = 0; spin < SPIN_MAX; ++spin) {
        u32 bad = 0;
        OPQ(bad);
        asm volatile("" :: "v"(pb[0]));
        const u32 cw = __hip_atomic_load((__attribute__((address_space(3))) u32*)(uintptr_t)wda, __ATOMIC_RELAXED, __HIP_MEMORY_SCOPE_WORKGROUP);
        G = tsum4(bad, t10, pb[0], pb[1], pb[2], pb[3]);
        early = __builtin_amdgcn_ballot_w64(cw == 0u) == 0ull;
        asm volatile("" ::: "memory");
        if (__builtin_expect(early, 1)) wd_ld(0, hw);
        SB();
        silu4(G, nl2e);
        U = tsum4(bad, t10, pb[4], pb[5], pb[6], pb[7]);
        if (__builtin_expect(!__any((bad & TMASK) != 0), 1)) break;
        STAMP(14);
        p1_ld();
        asm volatile("" ::: "memory");
      }
      STAMP(7);
      {
        float hv[4];
#pragma unroll
        for (int e = 0; e < 4; ++e) { hv[e] = G[e] * U[e]; asm("" : "+v"(hv[e])); }
        *(__attribute__((address_space(3))) u2v*)(uintptr_t)ho = u2v{pk_bf(hv[0], hv[1]), pk_bf(hv[2], hv[3])};
      }
      ARRIVE(C_HRDY);
      STAMP(8);
      if (__builtin_expect(!early, 0)) { WAITA(wda); wd_ld(0, hw); }
      f2_job(1, t10);
    }
    if (hw == 0) {
      WAITS(C_SEL, 1);
      const u32 t10 = par[0] & TMASK;
      const u32 tpub = (u32)__builtin_amdgcn_readfirstlane((int)par[2]);
      const u32 mykey = ((const u32*)(smem + O_SEL))[lane & 7];
      if (own) pace2(now32(), tpub + DE_P2, DT_P2); else pace(tpub + DE_P2 - 40);
      u4v pa[7];
      const int voff = WS_P2 + cc * 7168 + lane * 16;
#pragma unroll
      for (int k = 0; k < 7; ++k) pa[k] = __builtin_amdgcn_raw_buffer_load_b128(wrs, voff + (k & 3) * 1024, (k >> 2) * 4096, 16);
      SB();
      const int row = (int)(~mykey & 2047u);
      const float rsig = rb1(sigm(__uint_as_float((mykey ^ (~(u32)((int)mykey >> 31) | 0x80000000u)) & 0xffff0000u)));
      WAITA(CTLA(C_TILE) + (lane & 31) * 4);
      float cf_ = rsig * (1.f - *(const float*)(smem + O_GT + (row >> 8) * 1024 + ((row >> 2) & 3) * 256 + ((row >> 4) & 15) * 16 + (row & 3) * 4));
      float base[4];
#pragma unroll
      for (int e = 0; e < 4; ++e) base[e] = bflo(*(const u16*)(smem + e * CHB + O_OUT + (row >> 8) * OT + ((row >> 2) & 3) * 144 + ((row >> 4) & 15) * 8 + (row & 3) * 2));
      u64 op = kc[1] + (row * D + cc * 4) * 2;
      { float cq = cf_; OPQ(cq); cf_ = cq; OPQ(op); }
#pragma unroll
      for (int e = 0; e < 4; ++e) OPQ(base[e]);
      STAMP(13);
      f4 y;
#pragma unroll 1
      for (int spin = 0; spin < SPIN_MAX; ++spin) {
        u32 bad = 0;
        OPQ(bad);
        y = tsum4(bad, t10, pa[0], pa[1], pa[2], pa[3]);
        y = tsum3(bad, t10, y, pa[4], pa[5], pa[6]);
        if (__builtin_expect(!__any((bad & TMASK) != 0), 1)) break;
        STAMP(15);
#pragma unroll
        for (int k = 0; k < 7; ++k) pa[k] = __builtin_amdgcn_raw_buffer_load_b128(wrs, voff + (k & 3) * 1024, (k >> 2) * 4096, 16);
        asm volatile("" ::: "memory");
      }
      STAMP(9);
      float o[4];
#pragma unroll
      for (int e = 0; e < 4; ++e) {
        float v = y[e] + dpp_f<0x128>(y[e]);
        auto s16 = __builtin_amdgcn_permlane16_swap(__float_as_uint(v), __float_as_uint(v), false, false);
        v = __uint_as_float(s16[0]) + __uint_as_float(s16[1]);
        auto s32 = __builtin_amdgcn_permlane32_swap(__float_as_uint(v), __float_as_uint(v), false, false);
        o[e] = fmaf(__uint_as_float(s32[0]) + __uint_as_float(s32[1]), cf_, base[e]);
      }
      if (lane < 8) *(__attribute__((address_space(1))) u2v*)op = u2v{pk_bf(o[0], o[1]), pk_bf(o[2], o[3])};
    }
    ROLE_END();
  }
  {
    ROLE("g");
    const u32 pLA = __builtin_nontemporal_load((gp32)ka[3] + d * (NM / 2) + lane);
    const u32 pAI = __builtin_nontemporal_load((gp32)ka[4] + d * (NM / 2) + lane);
    const u32 pCR = __builtin_nontemporal_load((gp32)ka[5] + d * (NM / 2) + lane);
    const u32 pCI = __builtin_nontemporal_load((gp32)ka[6] + d * (NM / 2) + lane);
    const u32 ldt = ((gp16)ka[7])[d], dskw = ((gp16)kb[0])[d];
    u4v ixa[2];
#pragma unroll
    for (int r = 0; r < 2; ++r) ixa[r] = *(const u4v*)(g_tapidx.v + lane * 16 + r * 8);
    SB();
    asm volatile("s_mov_b32 m0, %0\n\ts_nop 0" :: "s"(lbase + c * CHB) : "memory");
#define PUT1(off, val) asm volatile("ds_write_addtid_b32 %0 offset:%1" :: "v"(val), "n"(off) : "memory")
#define PUTR(off, val) do { const ch v_ = (val); asm volatile("ds_write_addtid_b32 %0 offset:%2\n\tds_write_addtid_b32 %1 offset:%3" :: "v"(v_.r), "v"(v_.i), "n"(off), "n"((off) + 256) : "memory"); } while (0)
    const char* pf = ch_ + c16 * TROW + g * 16;
    PUT1(O_TAP, 0x3c003c00u); PUT1(O_TAP + 256, 0u);
    PUT1(O_U, 0u); PUT1(O_U + 256, 0u);
    SB();
    const float dt = rb1(exp_bf(bflo(ldt)));
    const f2 a2 = f2{-rb1(exp_bf(bflo(pLA))), -rb1(exp_bf(bfhi(pLA)))} * dt * 1.44269504088896341f;
    const f2 br = unbf(pAI) * dt * 0.15915494309189535f;
    STAMP(1);
    u4v Lf[2][8], Wf[8];
    f4 kk[2][2];
    float fk;
    {
      const cf z1 = cexp_p(a2, br, 1.f);
      const cf z2 = csq(z1), z4 = csq(z2), z8 = csq(z4), z16 = csq(z8), z32 = csq(z16), z64 = csq(z32);
      ch base[4], anc[4];
      base[1] = tohc(z8); base[2] = tohc(z16); base[3] = tohc(cmul(z16, z8));
      anc[1] = tohc(z32); anc[2] = tohc(z64); anc[3] = tohc(cmul(z64, z32));
      PUTR(O_OUT + 5 * TROW, tohc(csq(z64)));
#pragma unroll
      for (int j = 1; j < 16; ++j) {
        const int q = j >> 2, r = j & 3;
        PUTR(O_TAP + j * TROW, q == 0 ? base[r] : r == 0 ? anc[q] : hmul(anc[q], base[r]));
      }
#pragma unroll
      for (int s = 0; s < 8; ++s) Lf[0][s] = *(const u4v*)(pf + O_TAP + s * 64);
#pragma unroll
      for (int s = 0; s < 8; ++s) Lf[1][s] = *(const u4v*)(ch_ + O_OUT + 5 * TROW + g * 16 + s * 64);
      SB();
      STAMP(11);
      u32 mx = max(max(pCR << 17, pCR << 1), max(pCI << 17, pCI << 1));
      mx = wave_max_u32(mx);
      int ex = (int)(mx >> 24);
      ex = ex < 1 ? 1 : (ex > 253 ? 253 : ex);
      const float sdn = __uint_as_float((u32)(254 - ex) << 23);
      fk = 2.f * dt * __uint_as_float((u32)ex << 23);
      cf C; C.r = unbf(pCR) * sdn; C.i = unbf(pCI) * sdn;
      const cf E = cexp_p(a2, br, 256.f);
      const ch y1 = toh(z1), y2 = toh(z2), y4 = toh(z4), y6 = toh(cmul(z4, z2));
      {
        const ch R0 = toh(C), R2 = hmul(R0, y2), R4 = hmul(R0, y4), R6 = hmul(R0, y6);
        PUTR(O_TAP, R0); PUTR(O_TAP + 2 * TROW, R2); PUTR(O_TAP + 4 * TROW, R4); PUTR(O_TAP + 6 * TROW, R6);
        PUTR(O_TAP + 1 * TROW, hmul(R0, y1)); PUTR(O_TAP + 3 * TROW, hmul(R2, y1)); PUTR(O_TAP + 5 * TROW, hmul(R4, y1)); PUTR(O_TAP + 7 * TROW, hmul(R6, y1));
        C = cmul(C, E);
        const ch R8 = toh(C);
        PUTR(O_TAP + 8 * TROW, R8); PUTR(O_TAP + 9 * TROW, hmul(R8, y2)); PUTR(O_TAP + 10 * TROW, hmul(R8, y4)); PUTR(O_TAP + 11 * TROW, hmul(R8, y6));
        C = cmul(C, E);
        const ch R12 = toh(C);
        PUTR(O_TAP + 12 * TROW, R12); PUTR(O_TAP + 13 * TROW, hmul(R12, y4));
        C = cmul(C, E);
        const ch R14 = toh(C);
        PUTR(O_TAP + 14 * TROW, R14); PUTR(O_TAP + 15 * TROW, hmul(R14, y4));
      }
#pragma unroll
      for (int s = 0; s < 8; ++s) Wf[s] = *(const u4v*)(pf + O_TAP + s * 64);
      SB();
      STAMP(12);
#pragma unroll
      for (int i = 0; i < 5; ++i) { C = cmul(C, E); PUTR(O_OUT + i * TROW, toh(C)); }
      SB();
      auto mh = [&](u4v a, u4v bq, f4 cin) AI { return __builtin_amdgcn_mfma_f32_16x16x32_f16(as_h8(a), as_h8(bq), cin, 0, 0, 0); };
      kk[0][0] = f4{0.f, 0.f, 0.f, 0.f}; kk[0][1] = kk[0][0]; kk[1][0] = kk[0][0]; kk[1][1] = kk[0][0];
#pragma unroll
      for (int s = 0; s < 5; ++s) {
#pragma unroll
        for (int k = 0; k < 4; ++k) {
          if (k < 2) { if (s < 4) kk[0][0] = mh(Wf[s + 4 * k], Lf[0][s + 4 * k], kk[0][0]); }
          else if (s > 0) { const int u = s - 1 + 4 * (k - 2); kk[0][1] = mh(Wf[u], Lf[1][u], kk[0][1]); Wf[u] = *(const u4v*)(pf + O_OUT + u * 64); }
          SB();
          if (s < 4) {
            const ch o = hmul(ch{ash(Lf[0][s][k]), ash(Lf[0][s + 4][k])}, ch{ash(Lf[1][s][k]), ash(Lf[1][s + 4][k])});
            Lf[1][s][k] = __builtin_bit_cast(u32, o.r); Lf[1][s + 4][k] = __builtin_bit_cast(u32, o.i);
          }
          if (s == 3 && k == 3) asm volatile("s_nop 1");
          SB();
        }
      }
#pragma unroll
      for (int s = 0; s < 8; ++s) { kk[1][0] = mh(Wf[s], Lf[0][s], kk[1][0]); kk[1][1] = mh(Wf[s], Lf[1][s], kk[1][1]); }
    }
    {
      u32 ia[16];
      const u32 kb_ = lbase + c * CHB + O_OUT;
#pragma unroll
      for (int e = 0; e < 16; ++e) { const u32 iw = ixa[e >> 3][(e >> 1) & 3]; ia[e] = kb_ + ((e & 1) ? (iw >> 16) : (iw & 0xffffu)); }
      MIX2(16, 0, 1)
      SB();
      STAMP(2);
      char* ktp = ch_ + O_OUT + c16 * KT_PITCH + g * 8;
#pragma unroll
      for (int hf = 1; hf >= 0; --hf) {
#pragma unroll
        for (int ct = 0; ct < 2; ++ct) {
          const f4 v = kk[hf][ct] * fk;
          *(u32*)(ktp + ct * 16 * KT_PITCH + hf * 32) = pk_bf(v[0], v[1]);
          *(u32*)(ktp + ct * 16 * KT_PITCH + hf * 32 + 4) = pk_bf(v[2], v[3]);
        }
        asm volatile("" ::: "memory");
      }
      u32 zz = 0u; OPQ(zz);
      *(u32*)(ch_ + O_OUT + KT_ZERO + (lane & 7) * 4) = zz;
      asm volatile("" ::: "memory");
      u32 v[16], pe[9];
#pragma unroll
      for (int e = 15; e >= 0; --e) v[e] = *(const __attribute__((address_space(3))) u16*)(uintptr_t)ia[e];
      SB();
      pe[8] = zz;
#pragma unroll
      for (int i = 7; i >= 0; --i) {
        pe[i] = v[2 * i] | (v[2 * i + 1] << 16);
        const u32 nx = (u32)__builtin_amdgcn_update_dpp((int)dpp_u<0x134>(pe[i + 1]), (int)pe[i], 0x130, 0xf, 0xf, false);
        const u32 po = __builtin_amdgcn_alignbyte(nx, pe[i], 2);
        PUT1(O_TAP + i * 256, pe[i]); PUT1(O_TAP + 2 * TAP_PITCH - 4 + i * 256, pe[i]);
        PUT1(O_TAP + TAP_PITCH + i * 256, po); PUT1(O_TAP + 3 * TAP_PITCH - 4 + i * 256, po);
      }
      ARRIVE(C_TAPS + c);
    }
    STAMP(3);
    IDS();
    conv_setup();
    dm1 = bflo(dskw) - 1.f;
    atload(0, 17);
    m_setup();
    __builtin_amdgcn_s_setprio(1);
    SB();
    WAITA(qa);
    STAMP(4);
    SB();
    FL(0, -7); FL(1, -6); FL(2, -5); FL(3, -4); FL(4, -3); FL(5, -2); FL(6, -1); FL(7, 0); ULD(0); GLD(0);
    MF0(acc[0], At[9], Fq[0]); EUN(0, 0); EUN(0, 1);
    MF(acc[0], At[10], Fq[1]); EUN(0, 2); EUN(0, 3);
    MF(acc[0], At[11], Fq[2]);
    MF(acc[0], At[12], Fq[3]);
    MF(acc[0], At[13], Fq[4]);
    MF(acc[0], At[14], Fq[5]);
    MF(acc[0], At[15], Fq[6]); FL(8, 1);
    MF(acc[0], At[16], Fq[7]);
    MF0(acc[1], At[1], Fq[0]); FL(9, 2);
    MF(acc[1], At[2], Fq[1]);
    MF(acc[1], At[3], Fq[2]); FL(10, 3);
    MF(acc[1], At[4], Fq[3]);
    MF(acc[1], At[5], Fq[4]); FL(11, 4); EA1(0, 0);
    MF(acc[1], At[6], Fq[5]); EA1(0, 1); EA1(0, 2);
    MF(acc[1], At[7], Fq[6]); EA1(0, 3); PEEK(0, qa + 16);
    MF(acc[1], At[8], Fq[7]); FL(12, 5); EA2(0, 0);
    MF0(acc[2], At[0], Fq[7]); EA2(0, 1); EA2(0, 2);
    MF(acc[1], At[9], Fq[8]); EA2(0, 3); EPK(0, 0);
    MF(acc[2], At[1], Fq[8]); EPK(0, 1); FL(13, 6);
    MF(acc[1], At[10], Fq[9]); OST(0); FLAG(C_TILE + 0 * 4);
    MF(acc[2], At[2], Fq[9]); FL(14, 7); FL(15, 8);
    MF(acc[1], At[11], Fq[10]); CHECK(0, qa + 16);
    MF(acc[2], At[3], Fq[10]);
    MF(acc[1], At[12], Fq[11]); FL(16, 9);
    MF(acc[2], At[4], Fq[11]); ULD(1); GLD(1);
    MF(acc[1], At[13], Fq[12]); FL(17, 10);
    MF(acc[2], At[5], Fq[12]);
    MF(acc[1], At[14], Fq[13]); FL(18, 11);
    MF(acc[2], At[6], Fq[13]);
    MF(acc[1], At[15], Fq[14]); FL(19, 12); EUN(1, 0);
    MF(acc[2], At[7], Fq[14]); EUN(1, 1); EUN(1, 2);
    MF(acc[1], At[16], Fq[15]); FL(20, 13); EUN(1, 3);
    MF(acc[2], At[8], Fq[15]);
    MF0(acc[3], At[0], Fq[15]); FL(21, 14);
    MF(acc[2], At[9], Fq[16]);
    MF(acc[3], At[1], Fq[16]); FL(22, 15); EA1(1, 0);
    MF(acc[2], At[10], Fq[17]); EA1(1, 1); EA1(1, 2);
    MF(acc[3], At[2], Fq[17]); FL(23, 16); EA1(1, 3);
    MF(acc[2], At[11], Fq[18]); PEEK(2, fa + 0); EA2(1, 0);
    MF(acc[3], At[3], Fq[18]); EA2(1, 1); EA2(1, 2);
    MF(acc[2], At[12], Fq[19]); EA2(1, 3); EPK(1, 0);
    MF(acc[3], At[4], Fq[19]); EPK(1, 1); FL(24, 17);
    MF(acc[2], At[13], Fq[20]); OST(1); FLAG(C_TILE + 1 * 4);
    MF(acc[3], At[5], Fq[20]); FL(25, 18); CHECK(2, fa + 0);
    MF(acc[2], At[14], Fq[21]); FL(26, 19); ULD(2);
    MF(acc[3], At[6], Fq[21]); FLD(2); GLD(2);
    MF(acc[2], At[15], Fq[22]); FL(27, 20); PEEK(1, qa + 32);
    MF(acc[3], At[7], Fq[22]);
    MF(acc[2], At[16], Fq[23]); FL(28, 21);
    MF(acc[3], At[8], Fq[23]); EUN(2, 0); EUN(2, 1);
    MF0(acc[4], At[0], Fq[23]); FL(29, 22); EUN(2, 2);
    MF(acc[3], At[9], Fq[24]); EUN(2, 3); EA1(2, 0);
    MF(acc[4], At[1], Fq[24]); FL(30, 23); EA1(2, 1);
    MF(acc[3], At[10], Fq[25]); EA1(2, 2); EA1(2, 3);
    MF(acc[4], At[2], Fq[25]); FL(31, 24); EFA(2, 0);
    MF(acc[3], At[11], Fq[26]); EFA(2, 1); EFA(2, 2);
    MF(acc[4], At[3], Fq[26]); EFA(2, 3); PEEK(3, fa + 16);
    MF(acc[3], At[12], Fq[27]); EA2(2, 0); EA2(2, 1);
    MF(acc[4], At[4], Fq[27]); EA2(2, 2); EA2(2, 3);
    MF(acc[3], At[13], Fq[28]); CHECK(1, qa + 32); EPK(2, 0); EPK(2, 1);
    MF(acc[4], At[5], Fq[28]); FL(32, 25); OST(2);
    MF(acc[3], At[14], Fq[29]); FLAG(C_TILE + 2 * 4); FL(33, 26);
    MF(acc[4], At[6], Fq[29]); FL(34, 27); CHECK(3, fa + 16);
    MF(acc[3], At[15], Fq[30]); ULD(3); FL(35, 28);
    MF(acc[4], At[7], Fq[30]); FLD(3); GLD(3);
    MF(acc[3], At[16], Fq[31]); FL(36, 29);
    MF(acc[4], At[8], Fq[31]);
    MF0(acc[5], At[0], Fq[31]); FL(37, 30);
    MF(acc[4], At[9], Fq[32]); EUN(3, 0); EUN(3, 1);
    MF(acc[5], At[1], Fq[32]); PEEK(4, fa + 32); FL(38, 31);
    MF(acc[4], At[10], Fq[33]); EUN(3, 2); EUN(3, 3);
    MF(acc[5], At[2], Fq[33]); FL(39, 32); EA1(3, 0);
    MF(acc[4], At[11], Fq[34]); EA1(3, 1); EA1(3, 2);
    MF(acc[5], At[3], Fq[34]); EA1(3, 3); EFA(3, 0);
    MF(acc[4], At[12], Fq[35]); EFA(3, 1); EFA(3, 2); EFA(3, 3);
    MF(acc[5], At[4], Fq[35]); EA2(3, 0); EA2(3, 1); EA2(3, 2); EA2(3, 3);
    MF(acc[4], At[13], Fq[36]); FL(40, 33); EPK(3, 0); EPK(3, 1);
    MF(acc[5], At[5], Fq[36]); FL(41, 34); OST(3);
    MF(acc[4], At[14], Fq[37]); FL(42, 35); FLAG(C_TILE + 3 * 4); CHECK(4, fa + 32); ULD(4);
    MF(acc[5], At[6], Fq[37]); FL(43, 36); FLD(4); GLD(4); PEEK(5, fa + 48);
    MF(acc[4], At[15], Fq[38]); FL(44, 37); FL(45, 38);
    MF(acc[5], At[7], Fq[38]); FL(46, 39); FL(47, 40);
    MF(acc[4], At[16], Fq[39]);
    MF(acc[5], At[8], Fq[39]); EUN(4, 0); EUN(4, 1);
    MF(acc[5], At[9], Fq[40]); EUN(4, 2); EUN(4, 3);
    MF(acc[5], At[10], Fq[41]); EFA(4, 0); EA1(4, 0);
    MF(acc[5], At[11], Fq[42]); EA1(4, 1); EA1(4, 2);
    MF(acc[5], At[12], Fq[43]); EA1(4, 3); EFA(4, 1); EFA(4, 2); EFA(4, 3); CHECK(5, fa + 48); ULD(5);
    MF(acc[5], At[13], Fq[44]); EA2(4, 0); EA2(4, 1); EA2(4, 2); EA2(4, 3); FLD(5); GLD(5);
    MF(acc[5], At[14], Fq[45]); EPK(4, 0); EPK(4, 1);
    MF(acc[5], At[15], Fq[46]); OST(4); FLAG(C_TILE + 4 * 4);
    MF(acc[5], At[16], Fq[47]); EUN(5, 0); EUN(5, 1); EUN(5, 2); EUN(5, 3); EFA(5, 0); EFA(5, 1); EFA(5, 2); EFA(5, 3); EA1(5, 0); EA1(5, 1); EA1(5, 2); EA1(5, 3); EA2(5, 0); EA2(5, 1); EA2(5, 2); EA2(5, 3); EPK(5, 0); EPK(5, 1); OST(5); FLAG(C_TILE + 5 * 4);
    STAMP(8);
    ROLE_END();
  }
  {
    ROLE("c");
    __builtin_amdgcn_s_setprio(3);
    {
      u32 zz = 0u; OPQ(zz);
      if (lane < 2) *(u4v*)(smem + O_HB + 512 + lane * 528) = u4v{zz, zz, zz, zz};
    }
    u32* selw = (u32*)(smem + O_SEL);
    u32 tag;
    {
      const u32 pa = lbase + O_PART + (lane & 7) * 64;
      const u32 ib = 2047u - (u32)(cg * 8 + (lane & 7));
      f4 p0, p1, p2, p3; u32 cnt, ep; int n_ = 1 << 20; unsigned long long tk;
      asm volatile("1:\n\tds_read_b32 %4, %8\n\tds_read_b128 %0, %9\n\tds_read_b128 %1, %9 offset:16\n\tds_read_b128 %2, %9 offset:32\n\tds_read_b128 %3, %9 offset:48\n\t"
                   "ds_read_b32 %7, %10\n\ts_memrealtime %6\n\ts_sub_u32 %5, %5, 1\n\ts_cbranch_scc1 2f\n\ts_waitcnt lgkmcnt(0)\n\tv_cmp_gt_u32 vcc, 4, %4\n\ts_cbranch_vccnz 1b\n2:\n\ts_waitcnt lgkmcnt(0)"
                   : "=&v"(p0), "=&v"(p1), "=&v"(p2), "=&v"(p3), "=&v"(cnt), "+s"(n_), "=&s"(tk), "=&v"(ep) : "v"(CTLA(C_PART)), "v"(pa), "v"(lbase + O_XS) : "vcc", "scc", "memory");
      tag = (u32)__builtin_amdgcn_readfirstlane((int)ep) + 1u;
      tag += tag == 0u;
      const f4 s4_ = (p0 + p1) + (p2 + p3);
      const u32 rp = pk_bf(s4_[1] + s4_[3], s4_[0] + s4_[2]);
      const u32 key = ((rp ^ ((u32)((int)rp >> 31) | 0x80000000u)) & 0xffff0000u) | ib;
      const u32 kn = dpp_u<0xb1>(key);
      u32 m1 = max(key, kn), m2 = min(key, kn), m3;
      { const u32 o1 = dpp_u<0x4e>(m1), o2 = dpp_u<0x4e>(m2); const u32 lo = min(m1, o1), hb = max(m2, o2); m1 = max(m1, o1); m2 = max(lo, hb); m3 = min(lo, hb); }
      { const u32 o1 = dpp_u<0x141>(m1), o2 = dpp_u<0x141>(m2), o3 = dpp_u<0x141>(m3); const u32 lo = min(m1, o1), hb = max(m2, o2);
        m1 = max(m1, o1); m2 = max(lo, hb); m3 = max(min(lo, hb), max(m3, o3)); }
      const u32 wm = (((u32)tk + 8u) >> 2) & 0xffu;
      if (lane == 0) __builtin_amdgcn_raw_buffer_store_b128(u4v{m1, m2, (m3 & 0xffff0000u) | wm, tag}, wrs, WS_KEY + cg * 16, 0, 16);
      SB();
      STAMP(1);
      if (lane == 0) par[0] = tag;
      ARRIVE(C_GP);
      {
        const float gv = sigm(bflo(rp));
        const u32 g2 = pk_bf(gv, dpp_f<0xb1>(gv));
        u32 g3 = dpp_u<0x4e>(g2);
        OPQ(g3);
        if ((lane & 59) == 0) __builtin_amdgcn_raw_buffer_store_b128(u4v{g2, g3, tag, tag}, wrs, WS_REC + cg * 32 + lane * 4, 0, 16);
      }
      if ((lane & 57) == 0) __builtin_amdgcn_raw_buffer_store_b128(u4v{key, kn, 0u, tag}, wrs, WS_KALL + cg * 64 + lane * 8, 0, 16);
    }
    u32 l7 = (lane & 7) * 16, ko = WS_KEY + lane * 16;
    OPQ(l7); OPQ(ko);
    WAITQ(C_TL, 1);
    STAMP(5);
    const u32 tkey = (u32)__builtin_amdgcn_readfirstlane((int)par[1]) + DT_KEY;
    pace(tkey);
    u4v gr[4];
    u32 mykey;
    u4v xs[4];
    auto xrs = __builtin_amdgcn_make_buffer_rsrc((void*)(ka[0] + (xi & 3) * 512), 0, L * D * 2 - 1536, 0x00020000);
    asm volatile("" : "+s"(xrs));
    auto xs_ld = [&]() AI {
      const u32 vo = ((~mykey & 2047u) << 11) | l7;
#pragma unroll
      for (int k = 0; k < 4; ++k) xs[k] = __builtin_amdgcn_raw_buffer_load_b128(xrs, vo + k * 128, 0, 0);
    };
#pragma unroll
    for (int i = 0; i < 4; ++i) gr[i] = __builtin_amdgcn_raw_buffer_load_b128(wrs, ko + i * 1024, 0, 16);
#pragma unroll 1
    for (int spin = 0; spin < SPIN_MAX; ++spin) {
      const u32 bad = (gr[0][3] ^ tag) | (gr[1][3] ^ tag) | (gr[2][3] ^ tag) | (gr[3][3] ^ tag);
      if (__builtin_expect(!__any(bad != 0), 1)) break;
      STAMP(13);
      __builtin_amdgcn_s_sleep(SLEEP_AG1);
#pragma unroll
      for (int i = 0; i < 4; ++i)
        if (gr[i][3] != tag) gr[i] = __builtin_amdgcn_raw_buffer_load_b128(wrs, ko + i * 1024, 0, 16);
      asm volatile("" ::: "memory");
    }
    STAMP(2);
    {
      u32 m1 = gr[0][0], m2 = gr[0][1], m3 = 0, m4 = 0;
#pragma unroll
      for (int i = 2; i < 8; ++i) {
        const u32 k = gr[i >> 1][i & 1];
        if (i > 2) m4 = med3u(m3, m4, k);
        m3 = med3u(m2, m3, k);
        m2 = (i & 1) ? max(m2, k) : med3u(m1, m2, k);
        if (!(i & 1)) m1 = max(m1, k);
      }
      const u32 third = max(max(gr[0][2], gr[1][2]), max(gr[2][2], gr[3][2]));
      u32 W2;
      mykey = wave_top8(m1, m2, m3, m4, W2);
      xs_ld();
      SB();
      if (__builtin_expect(__any(m1 == 0u || (third | 0xffffu) > W2), 0)) {
        u4v ak[16];
#pragma unroll 1
        for (int spin = 0; spin < SPIN_MAX; ++spin) {
          u32 bad = 0;
#pragma unroll
          for (int i = 0; i < 16; ++i) {
            ak[i] = __builtin_amdgcn_raw_buffer_load_b128(wrs, WS_KALL + (i & 3) * 1024 + lane * 16, (i >> 2) * 4096, 16);
            bad |= ak[i][3] ^ tag;
          }
          if (!__any(bad != 0)) break;
          __builtin_amdgcn_s_sleep(8);
          asm volatile("" ::: "memory");
        }
        u32 prev = 0xffffffffu;
#pragma unroll 1
        for (int it = 0; it < 8; ++it) {
          u32 mm_ = 0;
#pragma unroll
          for (int i = 0; i < 32; ++i) {
            const u32 k = ak[i >> 1][i & 1];
            mm_ = max(mm_, k < prev ? k : 0u);
          }
          OPQ(mm_);
          prev = wave_max_u32(mm_);
          mykey = (lane >> 3) == it ? prev : mykey;
        }
        xs_ld();
      }
      OPQ(mykey);
    }
    SB();
    STAMP(3);
    if (l7 == 0) selw[lane >> 3] = mykey;
    {
      const u32 tn = now32();
      u32 yg = 0;
#pragma unroll
      for (int i = 0; i < 4; ++i) yg = max(yg, (gr[i][2] - (tn >> 2) - 1u) & 0xffu);
      const u32 tpub = tn - 4u * (255u - wave_max_u32(yg)) + DT_VIS;
      if (lane == 0) par[2] = (int)(tpub - tkey) > 0 ? tpub + DT_LATE : tkey;
      if (__any((int)(tpub - tkey) > 0)) { STAMP(20); }
    }
    ARRIVE(C_SEL);
    if (b == 255 && lane == 0) __builtin_amdgcn_raw_buffer_store_b32(tag, wrs, WS_EPOCH, 0, 16);
    SB();
    {
      char* xp = smem + O_XS + (lane >> 3) * XS_PITCH + l7;
#pragma unroll
      for (int k = 0; k < 4; ++k) *(u4v*)(xp + k * 128) = xs[k];
      ARRIVE(C_XS);
      STAMP(6);
    }
    __builtin_amdgcn_s_setprio(2);
    auto ors = __builtin_amdgcn_make_buffer_rsrc((void*)kc[1], 0, L * D * 2, 0x00020000);
    const u32 rl = (lane + (xi & 15) * 4) & 63, un = (g * 4 + (lane & 3) + xi) & 15;
    u32 skip = 0;
#pragma unroll
    for (int k = 0; k < 8; ++k) {
      const u32 r = ~(u32)__builtin_amdgcn_readlane((int)mykey, 8 * k) & 2047u;
      skip |= (r & 63u) == rl ? 1u << (r >> 6) : 0u;
    }
    const char* op = smem + ((lane >> 2) & 3) * CHB + O_OUT + (un & 3) * 144 + (un >> 2) * 8;
    const u32 ob = rl * 2048 + cc * 8;
    const u32 tfast = (u32)__builtin_amdgcn_readfirstlane((int)par[2]) + DE_FAST;
#pragma unroll 1
    for (int t = 0; t < 8; ++t) {
      WAITA(CTLA(C_TILE) + t * 16 + (lane & 3) * 4);
      u2v yv[4];
#pragma unroll
      for (int e = 0; e < 4; ++e) yv[e] = tr_read(op + t * OT + e * 32);
      const bool slow = (int)(now32() - tfast) < 0;
#pragma unroll
      for (int e = 0; e < 4; ++e) {
        __builtin_amdgcn_raw_buffer_store_b64(yv[e], ors, ob | (((skip >> e) & 1u) << 30), t * 524288 + e * 131072, 0);
        if (slow) {
          SB(); __builtin_amdgcn_s_sleep(STSLP); SB();
          asm volatile("s_waitcnt vmcnt(%0)" :: "n"(STWIN - 1) : "memory");
        }
      }
      skip >>= 4;
    }
    STAMP(9);
    ROLE_END();
  }
  {
    ROLE("d");
        const int j = xg * 28 + xi;
    const int s1 = j >> 2, kq = j & 3;
    const bool own = xi < 28;
    const u64 wbase = kb[3];
    const int pitch = D * 2;
    auto hrs = __builtin_amdgcn_make_buffer_rsrc((void*)wbase, 0, own ? D * F * 2 : 0, 0x00020000);
    const int r8 = lane >> 3;
    const int vo = (64 * s1 * D + 256 * kq) * 2 + (8 * (r8 >> 2) + (r8 & 3)) * pitch + (((lane & 7) * 16) ^ (((r8 >> 1) & 3) * 32));
    auto dma = [&](int h, int sB, int pt, u32 dst) AI {
      const int m = h >> 1;
#pragma unroll
      for (int i = 0; i < 2; ++i)
        asm volatile("s_mov_b32 m0, %3\n\ts_nop 0\n\tbuffer_load_dwordx4 %0, %1, %2 offen nt lds"
                     :: "v"(vo), "s"(hrs), "s"((m & 1) * 32 * pt + (m >> 1) * sB + (16 * i + 4 * (h & 1)) * pt), "s"(dst + i * 1024) : "memory");
    };
    const int q = c16 >> 2, p = c16 & 3;
    int tro[4];
#pragma unroll
    for (int ct = 0; ct < 4; ++ct) tro[ct] = (4 * g + q) * 128 + ((ct ^ (((4 * g + q) >> 1) & 3)) * 32) + p * 8;
    const int tok = lane & 7;
    u4v wd[2][8];
    auto wd_ld = [&](int k, int cb) AI {
#pragma unroll
      for (int i = 0; i < 8; ++i) {
        const char* pp = smem + O_WD + cb * 8192 + (i >> 2) * 4096 + tro[i & 3];
        const u2v lo = tr_read(pp), hi = tr_read(pp + 2048);
        wd[k][i] = u4v{lo[0], lo[1], hi[0], hi[1]};
      }
    };
    u32 p2o, p2k[4], hlo, hhi;
    auto f2_setup = [&](int cb0) AI {
      p2o = WS_P2 + ((64 * kq + 16 * cb0 + (lane & 8) / 2 + g) * 56 + s1) * 128 + tok * 16;
      hlo = lbase + O_HB + ((lane & 8) ? 512 : g * 128 + tok * 16);
      hhi = lbase + O_HB + ((lane & 8) ? g * 128 + tok * 16 : 512);
#pragma unroll
      for (int i = 0; i < (cb0 == 2 ? 4 : 2); ++i) { p2k[i] = p2o + i * 8 * 7168; OPQ(p2k[i]); }
      OPQ(hlo); OPQ(hhi);
    };
    u4v hb[4];
    f4 r[4];
    auto hb_ld = [&]() AI {
      u32 cnt; int n_ = 1 << 20;
      asm volatile("1:\n\tds_read_b32 %4, %6\n\tds_read_b128 %0, %7\n\tds_read_b128 %1, %8\n\tds_read_b128 %2, %7 offset:528\n\tds_read_b128 %3, %8 offset:528\n\t"
                   "s_sub_u32 %5, %5, 1\n\ts_cbranch_scc1 2f\n\ts_waitcnt lgkmcnt(0)\n\tv_cmp_gt_u32 vcc, 2, %4\n\ts_cbranch_vccnz 1b\n2:\n\ts_waitcnt lgkmcnt(0)"
                   : "=&v"(hb[0]), "=&v"(hb[1]), "=&v"(hb[2]), "=&v"(hb[3]), "=&v"(cnt), "+s"(n_) : "v"(CTLA(C_HRDY)), "v"(hlo), "v"(hhi) : "vcc", "scc", "memory");
    };
    auto f2_mm = [&](int k) AI {
      const u4v* wp = wd[k];
      r[2 * k] = mma(wp[0], hb[0], f4{0.f, 0.f, 0.f, 0.f}); r[2 * k + 1] = mma(wp[2], hb[0], f4{0.f, 0.f, 0.f, 0.f});
      r[2 * k] = mma(wp[1], hb[1], r[2 * k]); r[2 * k + 1] = mma(wp[3], hb[1], r[2 * k + 1]);
      r[2 * k] = mma(wp[4], hb[2], r[2 * k]); r[2 * k + 1] = mma(wp[6], hb[2], r[2 * k + 1]);
      r[2 * k] = mma(wp[5], hb[3], r[2 * k]); r[2 * k + 1] = mma(wp[7], hb[3], r[2 * k + 1]);
    };
    auto f2_st = [&](int k, u32 t10) AI {
#pragma unroll
      for (int i = 2 * k; i < 2 * k + 2; ++i) {
        u4v v = __builtin_bit_cast(u4v, r[i]);
#pragma unroll
        for (int e = 0; e < 4; ++e) v[e] = __builtin_amdgcn_bitop3_b32(v[e], t10, TMASK, 0xd8);
        __builtin_amdgcn_raw_buffer_store_b128(v, wrs, p2k[i], 0, 16);
      }
    };
    {
      __builtin_amdgcn_s_setprio(3);
      const u32 ts = ts0 - 500u;
      u32 tl = ~0u;
      pace(ts0 + DT_HEAD);
      STAMP(3);
#pragma unroll 1
      for (int i = 0; i < 8 && tl >= 1500u; ++i) {
        const u2v hd = __builtin_amdgcn_raw_buffer_load_b64(wrs, WS_HEAD + (lane & 7) * 8, 0, 16);
        tl = wave_max_u32(hd[0] == ~hd[1] ? hd[0] - ts : ~0u);
        STAMP(14);
        asm volatile("" ::: "memory");
      }
      tl = tl < 1500u ? tl + ts : ts0;
      if (lane == 0) par[1] = tl;
      ARRIVE(C_TL);
      STAMP(2);
      if (own) {
        WAITS(C_WGU, 1);
#pragma unroll
        for (int h = 0; h < 16; ++h) {
          if (h == DSEL) WAITS(C_SEL, 1);
          if (h == DEV) WAITS(C_XL, 4);
          if (h >= DWIN) asm volatile("s_waitcnt vmcnt(%0)" :: "n"(2 * (DWIN - 1)) : "memory");
          if (h == 3 + DWIN) ARRIVE(C_WD);
          if (h == 7 + DWIN) ARRIVE(C_WD + 1);
          dma(h, 128, D * 2, lbase + O_WD + h * 2048);
        }
        f2_setup(2);
        const u32 t10 = par[0] & TMASK;
        asm volatile("s_waitcnt vmcnt(8)" ::: "memory");
        wd_ld(0, 2);
        const u32 hc = __hip_atomic_load(ctl + C_HRDY, __ATOMIC_RELAXED, __HIP_MEMORY_SCOPE_WORKGROUP);
        const bool early = __builtin_amdgcn_ballot_w64(hc < 2u) == 0ull;
        if (early) { hb_ld(); f2_mm(0); }
        asm volatile("s_waitcnt vmcnt(0)" ::: "memory");
        STAMP(5);
        wd_ld(1, 3);
        if (!early) { WAITS(C_HRDY, 1); hb_ld(); STAMP(11); f2_mm(0); }
        SB();
        f2_mm(1);
        f2_st(0, t10);
        SB();
        f2_st(1, t10);
        STAMP(12);
      }
      ROLE_END();
    }
  }
}

extern "C" void kernel_launch(void* const* d_in, const int* in_sizes, int n_in,
                              void* d_out, int out_size, void* d_ws, size_t ws_size,
                              hipStream_t stream) {
  k_main<<<256, 768, 0, stream>>>((const u16*)d_in[0], (const u16*)d_in[8], (const u16*)d_in[12],
                                  (const u16*)d_in[1], (const u16*)d_in[2], (const u16*)d_in[3],
                                  (const u16*)d_in[4], (const u16*)d_in[5], (const u16*)d_in[6],
                                  (const u16*)d_in[9], (const u16*)d_in[10], (const u16*)d_in[11],
                                  (char*)d_ws, (u16*)d_out);
}
```

```cpp
#include <hip/hip_runtime.h>
#include <stdint.h>

typedef unsigned short u16;
typedef unsigned u32;
typedef _Float16 h2 __attribute__((ext_vector_type(2)));
typedef _Float16 h8 __attribute__((ext_vector_type(8)));
typedef __bf16 b2 __attribute__((ext_vector_type(2)));
typedef __bf16 b8 __attribute__((ext_vector_type(8)));
typedef short s4 __attribute__((ext_vector_type(4)));
typedef float f2 __attribute__((ext_vector_type(2)));
typedef float f4 __attribute__((ext_vector_type(4)));
typedef u32 u2v __attribute__((ext_vector_type(2)));
typedef u32 u3v __attribute__((ext_vector_type(3)));
typedef u32 u4v __attribute__((ext_vector_type(4)));
typedef __attribute__((address_space(3))) s4* lds_s4p;

constexpr int L = 2048;
constexpr int D = 1024;
constexpr int NM = 128;
constexpr int F = 3584;
constexpr int NTAP = 1024;

constexpr int SEG_I = 576, SEG_O0 = 800, SEG_O1 = 880, SEG_SP = 896;

#define AI __attribute__((always_inline))
#define SB() __builtin_amdgcn_sched_barrier(0)
#define OPQ(x) asm volatile("" : "+v"(x))
#define STAMP0() do {} while (0)
#define STAMP(i)
#define STAMP_END()
#define STAMP_T0

__device__ __forceinline__ float bflo(u32 v) { return __uint_as_float(v << 16); }
__device__ __forceinline__ float bfhi(u32 v) { return __uint_as_float(v & 0xffff0000u); }
__device__ __forceinline__ u32 pk_bf(float a, float b) { f2 v = {a, b}; b2 r = __builtin_convertvector(v, b2); return __builtin_bit_cast(u32, r); }
__device__ __forceinline__ float rb1(float v) { return __uint_as_float(pk_bf(0.f, v)); }
__device__ __forceinline__ h8 as_h8(u4v v) { return __builtin_bit_cast(h8, v); }
__device__ __forceinline__ b8 as_b8(u4v v) { return __builtin_bit_cast(b8, v); }
__device__ __forceinline__ u2v tr_read(const char* p) {
  s4 t = __builtin_amdgcn_ds_read_tr16_b64_v4i16((lds_s4p)p);
  return __builtin_bit_cast(u2v, t);
}
template <int CTRL> __device__ __forceinline__ u32 dpp_u(u32 v) { return (u32)__builtin_amdgcn_update_dpp(0, (int)v, CTRL, 0xf, 0xf, false); }
template <int CTRL> __device__ __forceinline__ float dpp_f(float v) { return __uint_as_float(dpp_u<CTRL>(__float_as_uint(v))); }
__device__ __forceinline__ u32 wave_max_u32(u32 v) {
  v = max(v, dpp_u<0x111>(v)); v = max(v, dpp_u<0x112>(v)); v = max(v, dpp_u<0x114>(v)); v = max(v, dpp_u<0x118>(v));
  v = max(v, dpp_u<0x142>(v)); v = max(v, dpp_u<0x143>(v));
  return (u32)__builtin_amdgcn_readlane((int)v, 63);
}
#define DPM " row_mask:0xf bank_mask:0xf bound_ctrl:1\n\t"
#define T2S(HO, HI, LI, C) "v_max_u32_dpp " HO ", " HI ", " HI " " C DPM "v_min_u32_dpp %8, " HI ", " HI " " C DPM "v_max_u32_dpp %7, " LI ", " LI " " C DPM "v_max_u32 %7, %7, %8\n\t"
#define T2R(RM, REST) T2S("%5", "%0", "%1", "row_shr:1") T2S("%6", "%5", "%7", "row_shr:2") T2S("%5", "%6", "%7", "row_shr:4") T2S("%6", "%5", "%7", "row_shr:8") \
    T2S("%5", "%6", "%7", "row_bcast:15") T2S("%6", "%5", "%7", "row_bcast:31") \
    "v_readlane_b32 %9, %6, 63\n\tv_readlane_b32 %10, %7, 63\n\ts_nop 0\n\tv_mov_b32 %5, %9\n\tv_cmp_le_u32 %11, %10, %0\n\tv_cmp_le_u32 %12, %10, %1\n\tv_mov_b32 %6, %10\n\t" \
    "v_cndmask_b32 %0, %0, %1, %11\n\tv_mov_b32_dpp %4, %5 quad_perm:[0,1,2,3] row_mask:" RM " bank_mask:0x3\n\tv_cndmask_b32 %0, %0, %2, %12\n\t" \
    "v_mov_b32_dpp %4, %6 quad_perm:[0,1,2,3] row_mask:" RM " bank_mask:0xc\n\t" REST
#define T2P "v_cndmask_b32 %1, %1, %2, %11\n\tv_cndmask_b32 %2, %2, %3, %11\n\tv_cndmask_b32 %1, %1, %3, %12\n\tv_cndmask_b32 %3, %3, 0, %11\n\tv_cndmask_b32 %2, %2, 0, %12\n\t"
__device__ __forceinline__ u32 wave_top8(u32& m1, u32 m2, u32 m3, u32 m4, u32& w2) {
  u32 key, ta, tb, lo, t, w1;
  unsigned long long c1, c2;
  asm("s_nop 1\n\t" T2R("0x1", T2P) T2R("0x2", T2P) T2R("0x4", T2P) T2R("0x8", "") "s_nop 0"
      : "+v"(m1), "+v"(m2), "+v"(m3), "+v"(m4), "=&v"(key), "=&v"(ta), "=&v"(tb), "=&v"(lo), "=&v"(t), "=&s"(w1), "=&s"(w2), "=&s"(c1), "=&s"(c2));
  return key;
}

__device__ __forceinline__ float exp_bf(float v) {
  const float e = __builtin_amdgcn_exp2f(v * 1.44268798828125f);
  return fmaf(e, v * (7.0526077e-06f * 0.69314718f), e);
}
struct cf { f2 r, i; };
struct ch { h2 r, i; };
__device__ __forceinline__ cf cmul(cf a, cf b) { cf c; c.r = a.r * b.r - a.i * b.i; c.i = a.r * b.i + a.i * b.r; return c; }
__device__ __forceinline__ ch hmul(ch a, ch b) {
  ch c;
  asm("v_pk_mul_f16 %0, %2, %4\n\tv_pk_mul_f16 %1, %2, %5\n\tv_pk_fma_f16 %0, %3, %5, %0 neg_lo:[1,0,0] neg_hi:[1,0,0]\n\tv_pk_fma_f16 %1, %3, %4, %1"
      : "=&v"(c.r), "=&v"(c.i) : "v"(a.r), "v"(a.i), "v"(b.r), "v"(b.i));
  return c;
}
__device__ __forceinline__ cf csq(cf a) { cf c; c.r = a.r * a.r - a.i * a.i; c.i = (a.r + a.r) * a.i; return c; }
__device__ __forceinline__ ch toh(cf a) { ch c; c.r = __builtin_convertvector(a.r, h2); c.i = __builtin_convertvector(a.i, h2); return c; }
__device__ __forceinline__ ch tohc(cf a) { ch c; c.r = __builtin_convertvector(a.r, h2); c.i = __builtin_convertvector(-a.i, h2); return c; }
__device__ __forceinline__ h2 ash(u32 v) { return __builtin_bit_cast(h2, v); }
__device__ __forceinline__ cf cexp_p(f2 a2, f2 brev, float p) {
  f2 e = a2 * p, t = brev * p;
  f2 mag = {__builtin_amdgcn_exp2f(e[0]), __builtin_amdgcn_exp2f(e[1])};
  float r0 = __builtin_amdgcn_fractf(t[0]), r1 = __builtin_amdgcn_fractf(t[1]);
  cf c;
  c.r = mag * f2{__builtin_amdgcn_cosf(r0), __builtin_amdgcn_cosf(r1)};
  c.i = mag * f2{__builtin_amdgcn_sinf(r0), __builtin_amdgcn_sinf(r1)};
  return c;
}
__device__ __forceinline__ u32 med3u(u32 a, u32 b, u32 c) { u32 r; asm("v_med3_u32 %0, %1, %2, %3" : "=v"(r) : "v"(a), "v"(b), "v"(c)); return r; }
__device__ __forceinline__ f2 unbf(u32 v) { return f2{bflo(v), bfhi(v)}; }
__device__ __forceinline__ float sigm(float v) { return __builtin_amdgcn_rcpf(1.f + __builtin_amdgcn_exp2f(-1.44269504088896341f * v)); }

#define TS2(A, B, C, D) "v_xor_b32 %5, " A ", %9\n\tv_xor_b32 %6, " B ", %9\n\tv_xor_b32 %7, " C ", %9\n\tv_xor_b32 %8, " D ", %9\n\t" \
    "v_fma_f32 %1, %5, 1.0, %1\n\tv_fma_f32 %2, %6, 1.0, %2\n\tv_or3_b32 %0, %0, %5, %6\n\tv_fma_f32 %3, %7, 1.0, %3\n\tv_fma_f32 %4, %8, 1.0, %4\n\tv_or3_b32 %0, %0, %7, %8\n\t"
#define TSR TS2("%10", "%11", "%12", "%13") TS2("%14", "%15", "%16", "%17") TS2("%18", "%19", "%20", "%21")
#define TSI "=&v"(t0), "=&v"(t1), "=&v"(t2), "=&v"(t3) : "v"(t10), "v"(p[0]), "v"(p[1]), "v"(p[2]), "v"(p[3]), "v"(q[0]), "v"(q[1]), "v"(q[2]), "v"(q[3]), \
    "v"(r[0]), "v"(r[1]), "v"(r[2]), "v"(r[3])
__device__ __forceinline__ f4 tsum4(u32& bad, u32 t10, u4v s, u4v p, u4v q, u4v r) {
  float s0, s1, s2, s3; u32 t0, t1, t2, t3;
  asm volatile("v_xor_b32 %1, %22, %9\n\tv_xor_b32 %2, %23, %9\n\tv_xor_b32 %3, %24, %9\n\tv_xor_b32 %4, %25, %9\n\tv_or3_b32 %0, %0, %1, %2\n\tv_or3_b32 %0, %0, %3, %4\n\t" TSR
               : "+v"(bad), "=&v"(s0), "=&v"(s1), "=&v"(s2), "=&v"(s3), TSI, "v"(s[0]), "v"(s[1]), "v"(s[2]), "v"(s[3]));
  return f4{s0, s1, s2, s3};
}
__device__ __forceinline__ f4 tsum3(u32& bad, u32 t10, f4 s, u4v p, u4v q, u4v r) {
  float s0 = s[0], s1 = s[1], s2 = s[2], s3 = s[3]; u32 t0, t1, t2, t3;
  asm volatile(TSR : "+v"(bad), "+v"(s0), "+v"(s1), "+v"(s2), "+v"(s3), TSI);
  return f4{s0, s1, s2, s3};
}
__device__ __forceinline__ void silu4(f4& g, float nl2e) {
  float g0 = g[0], g1 = g[1], g2 = g[2], g3 = g[3], t0, t1, t2, t3;
  asm volatile("v_fma_f32 %4, %0, %8, 0\n\tv_fma_f32 %5, %1, %8, 0\n\tv_fma_f32 %6, %2, %8, 0\n\tv_fma_f32 %7, %3, %8, 0\n\t"
               "v_exp_f32 %4, %4\n\tv_exp_f32 %5, %5\n\tv_exp_f32 %6, %6\n\tv_exp_f32 %7, %7\n\t"
               "v_fma_f32 %4, %4, 1.0, 1.0\n\tv_fma_f32 %5, %5, 1.0, 1.0\n\tv_fma_f32 %6, %6, 1.0, 1.0\n\tv_fma_f32 %7, %7, 1.0, 1.0\n\t"
               "v_rcp_f32 %4, %4\n\tv_rcp_f32 %5, %5\n\tv_rcp_f32 %6, %6\n\tv_rcp_f32 %7, %7\n\t"
               "v_fma_f32 %0, %0, %4, 0\n\tv_fma_f32 %1, %1, %5, 0\n\tv_fma_f32 %2, %2, %6, 0\n\tv_fma_f32 %3, %3, %7, 0"
               : "+v"(g0), "+v"(g1), "+v"(g2), "+v"(g3), "=&v"(t0), "=&v"(t1), "=&v"(t2), "=&v"(t3) : "v"(nl2e));
  g = f4{g0, g1, g2, g3};
}

constexpr int KT_PITCH = 52;
constexpr int kt_off(int p) {
  int i = 20, j = 0;
  if (p < 256) { i = p & 7; j = p >> 3; }
  else if (p < 512) { int q = (p - 256) >> 1; i = 8 + (q & 3); j = q >> 2; }
  else if (p < 1024) { int q = (p - 512) >> 2; i = 12 + (q & 1) + 2 * (q >> 6); j = (q >> 1) & 31; }
  else if (p < 2048) { int q = (p - 1024) >> 3; i = 16 + (q >> 5); j = q & 31; }
  return j * KT_PITCH + i * 2;
}
constexpr int KT_ZERO = 32 * KT_PITCH + 16;
constexpr int rnd_lag(int m) {
  const int st = m < 256 ? 1 : m < 512 ? 2 : m < 1024 ? 4 : 8;
  int q = m / st;
  const int rem = m - q * st;
  if (2 * rem > st || (2 * rem == st && (q & 1))) ++q;
  return q * st;
}
constexpr int tap_entry(int e) {
  if (e < 544) {
    if (543 - e <= 525) return kt_off(rnd_lag(543 - e));
  } else if (e >= 560 && e < 752) {
    const int q = (e - 560) / 48, x = (e - 560) - q * 48;
    const int p = 512 + 16 * (31 - x) + 4 * (q + 1);
    if (x < 32 && p > 524) return kt_off(p);
  } else if (e >= 752 && e < 816) {
    return kt_off(1032 + 16 * (815 - e));
  } else if (e >= 832 && e < 896) {
    return kt_off(1040 + 16 * (895 - e));
  } else if (e == SEG_SP + 27 || e == SEG_SP + 28) {
    return kt_off(1024);
  }
  return KT_ZERO;
}
struct TapIdx { u16 v[NTAP]; };
constexpr TapIdx make_tapidx() {
  TapIdx t{};
  for (int e = 0; e < NTAP; ++e) t.v[e] = (u16)tap_entry(2 * ((e >> 4) + 64 * ((e >> 1) & 7)) + (e & 1));
  return t;
}
__device__ __attribute__((aligned(16))) const TapIdx g_tapidx = make_tapidx();

constexpr int TROW = 544;
constexpr int O_OUT = 0;
constexpr int OT = 576;
constexpr int O_U = O_OUT + 8 * OT;
constexpr int O_TAP = O_U + 4608;
constexpr int TAP_PITCH = 2 * NTAP + 64;
constexpr int CHB = O_TAP + 16 * TROW + 32;
static_assert(16 * TROW >= 4 * TAP_PITCH, "lds");
static_assert((CHB / 4) % 32 == 8, "banks");
#define DEV 3
#define DWIN 8
#define DSEL 0
constexpr int NSLOT = 5, WWIN = 4;
constexpr int O_GT = 4 * CHB;
constexpr int O_RING = O_GT + 8192;
constexpr int O_WD = O_RING + 2 * NSLOT * 2048;
constexpr int O_FAR = O_WD + 32768;
constexpr int O_XS = O_FAR + 16384;
constexpr int XS_PITCH = 544;
constexpr int O_HB = O_XS + 8 * XS_PITCH;
constexpr int O_PART = O_HB + 2 * 528;
constexpr int O_SEL = O_PART + 512;
constexpr int O_PAR = O_SEL + 32;
constexpr int O_CTL = O_PAR + 16;
constexpr int O_JUNK = O_CTL + 512;
constexpr int K_LDS = O_JUNK + 1024;
static_assert(DWIN == 8, "flags of the down tile");
static_assert(K_LDS <= 163840 && O_RING % 16 == 0, "lds");
enum { C_PART = 0, C_GP, C_PUB, C_SEL, C_XS, C_HRDY, C_WGU, C_TL, C_XL, C_WD = 10, C_TAPS = 12, C_Q = 16, C_FAR = 32, C_TILE = 48 };

#define CTLA(word) (lbase + O_CTL + (word) * 4)
#define ARRIVE(word) asm volatile("s_mov_b64 exec, 1\n\tds_add_u32 %0, %1\n\ts_mov_b64 exec, -1" :: "v"(CTLA(word)), "v"(1) : "memory")
#define WAITQ(word, target) do { u32 t_; int n_ = 1 << 20; asm volatile( \
    "1:\n\tds_read_b32 %0, %2\n\ts_sub_u32 %1, %1, 1\n\ts_cbranch_scc1 2f\n\ts_waitcnt lgkmcnt(0)\n\tv_cmp_gt_u32 vcc, %3, %0\n\ts_cbranch_vccnz 1b\n2:\n\ts_waitcnt lgkmcnt(0)" \
    : "=&v"(t_), "+s"(n_) : "v"(CTLA(word)), "n"(target) : "vcc", "scc", "memory"); } while (0)
#define WAITS(word, target) do { u32 t_; int n_ = 1 << 20; asm volatile( \
    "1:\n\tds_read_b32 %0, %2\n\ts_sub_u32 %1, %1, 1\n\ts_cbranch_scc1 2f\n\ts_waitcnt lgkmcnt(0)\n\tv_cmp_le_u32 vcc, %3, %0\n\ts_cbranch_vccnz 2f\n\ts_sleep 2\n\ts_branch 1b\n2:\n\ts_waitcnt lgkmcnt(0)" \
    : "=&v"(t_), "+s"(n_) : "v"(CTLA(word)), "n"(target) : "vcc", "scc", "memory"); } while (0)
#define BAR() asm volatile("s_waitcnt lgkmcnt(0)\n\ts_barrier" ::: "memory")
__device__ __forceinline__ u2v lds8(const char* p) { return __builtin_bit_cast(u2v, __hip_atomic_load((const unsigned long long*)p, __ATOMIC_RELAXED, __HIP_MEMORY_SCOPE_WORKGROUP)); }

constexpr int WS_EPOCH = 0;
constexpr int WS_HEAD = 1024;
constexpr int WS_REC = 4096;
constexpr int WS_FLG = 40960;
constexpr int WS_KALL = WS_REC + 8192;
constexpr int WS_KEY = 32768;
constexpr int WS_P1 = 65536;
constexpr int WS_P2 = WS_P1 + 56 * 16384;
constexpr int WS_XT = WS_P2 + 256 * 7168;
constexpr int XT_PITCH = 33280;
constexpr int WS_END = WS_XT + 128 * XT_PITCH;
constexpr int SPIN_MAX = 1 << 16;
constexpr u32 TMASK = 0x3ffu;

#define DT_HEAD 110
#define DT_ARG 45
#define WDELAY 60
#define DT_KEY 240
#define DT_REC 270
#define DT_FLG 380
#define DT_VIS 40
#define DE_P1 375
#define DE_P2 610
#define DE_FAST 595
#define DT_MIN 25
#define DT_P1 100
#define DT_P2 130
#define SLEEP_AG1 12
#define SLEEP_G 6
#define STSLP 2
#define STWIN 4
#define H2_AT 9
#define CODE_B 28400

#define ROLE_END() do { STAMP_END(); asm volatile("s_endpgm" ::: "memory"); } while (0)
__device__ __forceinline__ u32 now32() { return (u32)__builtin_amdgcn_s_memrealtime(); }
__device__ __forceinline__ u32 later(u32 a, u32 b) { return (int)(b - a) > 0 ? b : a; }
__device__ __forceinline__ void pace(u32 t) {
#pragma unroll 1
  for (int i = 0; i < 1024 && (int)(now32() - t) < 0; ++i) __builtin_amdgcn_s_sleep(1);
}
__device__ __forceinline__ void pace2(u32 n, u32 t, int dmax) {
  int d = (int)(t - n);
  d = d < DT_MIN ? DT_MIN : (d > dmax ? dmax : d);
  pace(n + (u32)d);
}

#define ALL_OTHER "vcc", "scc", "memory", \
    "s0","s1","s2","s5","s6","s7","s33","s34","s35","s44","s45","s46","s47","s48","s49","s50","s51","s52","s53","s54","s55","s56","s57","s58","s59", \
    "s60","s61","s62","s63","s64","s65","s66","s67","s68","s69","s70","s71","s72","s73","s74","s75","s76","s77","s78","s79","s80","s81","s82","s83","s84", \
    "s85","s86","s87","s88","s89","s90","s91","s92","s93","s94","s95","s96","s97","s98","s99", \
    "v0","v1","v2","v3","v4","v5","v6","v7","v8","v9","v10","v11","v12","v13","v14","v15","v16","v17","v18","v19","v20","v21","v22","v23","v24","v25", \
    "v26","v27","v28","v29","v30","v31","v32","v33","v34","v35","v36","v37","v38","v39","v40","v41","v42","v43","v44","v45","v46","v47","v48","v49","v50", \
    "v51","v52","v53","v54","v55","v56","v57","v58","v59","v60","v61","v62","v63","v64","v65","v66","v67","v68","v69","v70","v71","v72","v73","v74","v75", \
    "v76","v77","v78","v79","v80","v81","v82","v83","v84","v85","v86","v87","v88","v89","v90","v91","v92","v93","v94","v95","v96","v97","v98","v99", \
    "v100","v101","v102","v103","v104","v105","v106","v107","v108","v109","v110","v111","v112","v113","v114","v115","v116","v117","v118","v119","v120", \
    "v121","v122","v123","v124","v125","v126","v127","v128","v129","v130","v131","v132","v133","v134","v135","v136","v137","v138","v139","v140","v141", \
    "v142","v143","v144","v145","v146","v147","v148","v149","v150","v151","v152","v153","v154","v155","v156","v157","v158","v159","v160","v161","v162", \
    "v163","v164","v165","v166","v167"
typedef __attribute__((address_space(1))) const u16* gp16;
typedef __attribute__((address_space(1))) const u32* gp32;
typedef __attribute__((address_space(1))) const u4v* gp128;
typedef unsigned long long u64;
typedef u64 q2 __attribute__((ext_vector_type(2)));
typedef u64 q4 __attribute__((ext_vector_type(4)));
typedef u64 q8 __attribute__((ext_vector_type(8)));

__global__ void __launch_bounds__(768, 3) k_main(
    const u16* x_, const u16* w_r_, const u16* gate_w_, const u16* logA_, const u16* Aim_, const u16* Cre_, const u16* Cim_,
    const u16* log_dt_, const u16* Dskip_, const u16* w_gate_, const u16* w_up_, const u16* w_down_, char* wsp_, u16* out_) {
  __shared__ __attribute__((aligned(256))) char smem[K_LDS];
  const u32 lbase = (u32)(uintptr_t)(__attribute__((address_space(3))) char*)smem;
#define XDMA(OFF) "global_load_lds_dwordx4 v1, s[8:9] offset:" #OFF " nt\n\ts_addk_i32 m0, 896\n\t"
  asm volatile("s_load_dwordx16 s[8:23], %0, 0\n\tv_readfirstlane_b32 s3, %2\n\ts_load_dwordx8 s[24:31], %0, 64\n\ts_load_dwordx4 s[36:39], %0, 96\n\t"
               "s_memrealtime s[40:41]\n\t" STAMP_T0 "s_mov_b32 s4, %1\n\ts_lshr_b32 s3, s3, 6\n\t"
               "s_cmp_lt_u32 s3, 4\n\ts_cbranch_scc1 .Lrows\n\ts_barrier\n\t"
               "s_cmp_ge_u32 s3, 8\n\ts_cbranch_scc1 .Lrole_g\n\ts_cmp_eq_u32 s3, 7\n\ts_cbranch_scc1 .Lrole_d\n\t"
               "s_cmp_ge_u32 s3, 5\n\ts_cbranch_scc1 .Lrole_h\n\ts_branch .Lrole_c\n.Lrows:\n\t"
               "s_lshr_b32 s5, s4, 3\n\tv_lshlrev_b32 v3, 2, %2\n\ts_mov_b32 m0, %4\n\tv_lshl_or_b32 v3, s5, 10, v3\n\ts_getpc_b64 s[6:7]\n\t"
               "v_min_u32 v3, %3, v3\n\tv_lshrrev_b32 v1, 3, %2\n\tglobal_load_lds_dword v3, s[6:7]\n\t"
               "v_and_b32 v2, 6, v1\n\ts_and_b32 s34, s4, 7\n\tv_xor_b32 v2, v2, %2\n\ts_lshl_b32 s34, s34, 5\n\tv_and_b32 v2, 7, v2\n\ts_add_u32 s5, s5, s34\n\t"
               "v_and_b32 v1, 7, v1\n\ts_lshl_b32 s5, s5, 5\n\tv_lshlrev_b32 v1, 11, v1\n\ts_add_u32 s5, s5, s3\n\tv_lshl_or_b32 v1, v2, 4, v1\n\t"
               "s_mul_i32 s34, s3, %5\n\tv_lshl_add_u32 v1, s5, 9, v1\n\ts_add_u32 m0, s34, %6\n\tv_and_b32 v2, 31, %2\n\ts_waitcnt lgkmcnt(0)\n\t"
               XDMA(0) "s_lshl_b32 s5, s3, 5\n\t" XDMA(128) "v_add_lshl_u32 v2, s5, v2, 4\n\t" XDMA(256) "s_lshl_b32 s5, s3, 10\n\t"
               "global_load_lds_dwordx4 v1, s[8:9] offset:384 nt\n\ts_add_u32 m0, s5, %7\n\ts_mov_b32 exec_hi, 0\n\t"
               "global_load_lds_dwordx4 v2, s[10:11]\n\ts_addk_i32 m0, 512\n\ts_nop 0\n\tglobal_load_lds_dwordx4 v2, s[12:13]\n\t"
               "s_mov_b32 m0, %8\n\tv_mov_b32 v3, 0\n\tglobal_load_lds_dword v3, s[36:37]\n\ts_mov_b32 exec_hi, -1"
               :: "s"(__builtin_amdgcn_kernarg_segment_ptr()), "s"(blockIdx.x), "v"(threadIdx.x), "n"(CODE_B - 256), "n"(O_JUNK), "n"(CHB), "n"(O_U + 512), "n"(O_FAR), "n"(O_XS)
               : "s3", "s4", "s5", "s6", "s7", "s8", "s9", "s10", "s11", "s12", "s13", "s14", "s15", "s16", "s17", "s18", "s19", "s20", "s21", "s22", "s23",
                 "s24", "s25", "s26", "s27", "s28", "s29", "s30", "s31", "s34", "s36", "s37", "s38", "s39", "s40", "s41", "s42", "s43", "v1", "v2", "v3", "scc", "memory");
  int lane, c16, g, w, b, xg, xi, cg, cc, c, d;
  char* ch_;
  q8 ka; q4 kb; q2 kc, kt;
  u32* ctl = (u32*)(smem + O_CTL);
  u32* par = (u32*)(smem + O_PAR);
#define IDS() do { int z_ = 0; OPQ(z_); lane = __builtin_amdgcn_mbcnt_hi(~0u, __builtin_amdgcn_mbcnt_lo(~0u, z_)); c16 = lane & 15; g = lane >> 4; } while (0)
#define ROLE(name) \
  asm volatile(".Lrole_" name ":\n\ts_waitcnt lgkmcnt(0)" : "={s[8:23]}"(ka), "={s[24:31]}"(kb), "={s[36:39]}"(kc), "={s[40:43]}"(kt), "={s3}"(w), "={s4}"(b) :: ALL_OTHER); \
  if (__builtin_expect(w > 11, 0)) asm volatile("s_endpgm" ::: "memory");        \
  IDS(); \
  xg = b & 7; xi = b >> 3;                          \
  cg = xg * 32 + xi;                                \
  cc = (xi >> 4) * 128 + xg * 16 + (xi & 15);       \
  c = w & 3; ch_ = smem + c * CHB; d = cc * 4 + c; \
  const u32 ts0 = (u32)kt[0]; \
  auto wrs = __builtin_amdgcn_make_buffer_rsrc((void*)kc[0], 0, WS_END, 0x00020000); \
  STAMP0()

  const char* tapb;
  const char* ub;
  char* erp;
  const char* eup;
  const char* egp;
  u4v At[17], band[4];
  float dm1;
  f4 acc[8];
  auto conv_setup = [&]() AI {
    tapb = ch_ + O_TAP + ((15 - c16) & 3) * TAP_PITCH + ((15 - c16) & ~3) * 2;
    ub = ch_ + O_U - 32 + c16 * 32 + g * 16;
    erp = ch_ + O_OUT + g * 144 + c16 * 8;
    eup = ch_ + O_U + 512 + c16 * 32 + g * 8;
    egp = smem + O_GT + lane * 16;
  };
  auto band_setup = [&]() AI {
#pragma unroll
    for (int k = 0; k < 4; ++k) {
      const int ctr = k == 0 ? 12 : 8, hw = k + 1;
      const u32 m = ((1u << (2 * hw + 1)) - 1u) << (ctr - hw);
      const u32 bits = (m << c16) >> (8 * g);
#pragma unroll
      for (int j = 0; j < 4; ++j)
        band[k][j] = (__builtin_amdgcn_sbfe(bits, 2 * j, 1) & 0x3f80u) | (__builtin_amdgcn_sbfe(bits, 2 * j + 1, 1) & 0x3f800000u);
      OPQ(band[k]);
    }
  };
  auto frag2 = [&](const char* pa, const char* pb) AI { const u2v lo = lds8(pa), hi = lds8(pb); return u4v{lo[0], lo[1], hi[0], hi[1]}; };
  auto atload = [&](int s0, int s1) AI {
#pragma unroll
    for (int s = s0; s < s1; ++s) At[s] = frag2(tapb + 64 * s + g * 16, tapb + 64 * s + g * 16 + 8);
  };
  auto bin = [&](int pair, int be) AI { const char* p = tapb + g * 8 + (SEG_I + 96 * pair - 16 * be) * 2; return frag2(p, p + 96); };
  auto bout = [&](int be) AI { const char* p = tapb + g * 8 + (SEG_O0 - 16 * be) * 2; return frag2(p, p + (SEG_O1 - SEG_O0) * 2); };
  auto asp = [&]() AI { return frag2(tapb + SEG_SP * 2 + g * 16, tapb + SEG_SP * 2 + g * 16 + 8); };
  auto mma = [&](u4v a, u4v bq, f4 cin) AI { return __builtin_amdgcn_mfma_f32_16x16x32_bf16(as_b8(a), as_b8(bq), cin, 0, 0, 0); };
  auto uld = [&](int sg) AI { return *(const u4v*)(ub + 512 + sg * 64); };
  auto uld24 = [&](int un) AI { return *(const u4v*)(ub + 512 + un * 512 - 16); };
  u2v euw[8], eo[8]; f4 egv[8], efv[8];
  float eu[8][4], ev[8][4], ea[8][4];
  u4v Fq[57], ru[6], wa_[6][3];
  f4 wq[6][6];
  u32 pk[12], one, qa, fa;
  char* tfp; char* fp_;
#define LOPQ(p_) do { u32 a_ = (u32)(uintptr_t)(__attribute__((address_space(3))) const char*)p_; OPQ(a_); p_ = (decltype(p_))(__attribute__((address_space(3))) char*)(uintptr_t)a_; } while (0)
  auto m_setup = [&]() AI {
    tfp = smem + (lane == 0 ? O_CTL + c * 4 : O_JUNK + lane * 4);
    fp_ = smem + O_FAR + c * 4096 + lane * 16;
    qa = CTLA(C_Q) + (lane & 3) * 4; fa = CTLA(C_FAR + c); one = 1u;
    OPQ(qa); OPQ(fa); OPQ(one);
    LOPQ(tfp); LOPQ(fp_); LOPQ(erp); LOPQ(eup); LOPQ(egp); LOPQ(ub);
  };
#define WAITA(addr) do { u32 t_; int n_ = 1 << 20; asm volatile( \
    "1:\n\tds_read_b32 %0, %2\n\ts_sub_u32 %1, %1, 1\n\ts_cbranch_scc1 2f\n\ts_waitcnt lgkmcnt(0)\n\tv_cmp_eq_u32 vcc, 0, %0\n\ts_cbranch_vccnz 1b\n2:\n\ts_waitcnt lgkmcnt(0)" \
    : "=&v"(t_), "+s"(n_) : "v"(addr) : "vcc", "scc", "memory"); } while (0)
#define MF(d_, a_, b_) asm volatile("v_mfma_f32_16x16x32_bf16 %0, %1, %2, %0" : "+v"(d_) : "v"(a_), "v"(b_)); SB()
#define MF0(d_, a_, b_) asm volatile("v_mfma_f32_16x16x32_bf16 %0, %1, %2, 0" : "=&v"(d_) : "v"(a_), "v"(b_)); SB()
#define NOP(n_) asm volatile("s_nop " #n_); SB()
#define FL(i_, sg_) Fq[i_] = uld(sg_); SB()
#define RL(un_) ru[un_] = uld24(un_); SB()
#define AL(s_) atload(s_, s_ + 1); SB()
#define ULD(t_) euw[t_] = *(const u2v*)(eup + t_ * 512); SB()
#define GLD(t_) egv[t_] = *(const f4*)(egp + t_ * 1024); SB()
#define FLD(t_) efv[t_] = *(const f4*)(fp_ + (t_ - 2) * 1024); SB()
#define FST(un_) *(f4*)(fp_ + un_ * 1024) = acc[un_ + 2]; SB()
#define EUN(t_, e_) eu[t_][e_] = (e_ & 1) ? bfhi(euw[t_][e_ >> 1]) : bflo(euw[t_][e_ >> 1]); OPQ(eu[t_][e_]); ev[t_][e_] = eu[t_][e_]; SB()
#define EA1(t_, e_) ea[t_][e_] = fmaf(eu[t_][e_], dm1, acc[t_][e_]); OPQ(ea[t_][e_]); SB()
#define EFA(t_, e_) ev[t_][e_] = fmaf(egv[t_][e_], efv[t_][e_], eu[t_][e_]); OPQ(ev[t_][e_]); SB()
#define EA2(t_, e_) ea[t_][e_] = fmaf(egv[t_][e_], ea[t_][e_], ev[t_][e_]); OPQ(ea[t_][e_]); SB()
#define EPK(t_, k_) { u32 v_ = pk_bf(ea[t_][2 * k_], ea[t_][2 * k_ + 1]); OPQ(v_); eo[t_][k_] = v_; } SB()
#define OST(t_) *(u2v*)(erp + t_ * OT) = eo[t_]; SB()
#define WPK(un_, p_, j_) { u32 v_ = pk_bf(wq[un_][2 * p_ + j_ / 2][2 * (j_ & 1)], wq[un_][2 * p_ + j_ / 2][2 * (j_ & 1) + 1]); OPQ(v_); wa_[un_][p_][j_] = v_; } SB()
#define FLAG(w_) asm volatile("" ::: "memory"); *(u32*)(tfp + (w_) * 4) = one; SB()
#define PEEK(i_, a_) pk[i_] = __hip_atomic_load((__attribute__((address_space(3))) u32*)(uintptr_t)(a_), __ATOMIC_RELAXED, __HIP_MEMORY_SCOPE_WORKGROUP); SB()
#define CHECK(i_, a_) if (__builtin_expect(__builtin_amdgcn_ballot_w64(pk[i_] == 0u) != 0ull, 0)) WAITA(a_); asm volatile("" ::: "memory"); SB()
#define MIX2(nm, nd, nv) _Pragma("unroll") for (int r_ = 0; r_ < (nm); ++r_) { __builtin_amdgcn_sched_group_barrier(0x008, 1, 0); \
    if (nd) __builtin_amdgcn_sched_group_barrier(0x100, (nd), 0); if (nv) __builtin_amdgcn_sched_group_barrier(0x002, (nv), 0); }

  {
    ROLE("x");
    const int xw = w;
    char* img = ch_ + O_U + 512;
    STAMP(11);
    __builtin_amdgcn_s_setprio(2);
    { u32 zz = 0u; OPQ(zz); if (lane < 32) ctl[32 * xw + lane] = zz; }
    if (xi == 31 && w == 0) { const u32 th = later(ts0, now32() - DT_ARG); if (lane == 0) __builtin_amdgcn_raw_buffer_store_b64(u2v{th, ~th}, wrs, WS_HEAD + xg * 8, 0, 16); }
    BAR();
    STAMP(12);
    const char* wp; const char* fp; const char* tp0; const char* tp1; char* pp;
    {
      const int r = lane & 7, q = (lane >> 2) & 3;
      fp = img + r * 128 + ((((lane & 8) / 2 + g) ^ (r & 6)) * 16);
      wp = smem + O_FAR + xw * 1024 + (lane & 1) * 512 + (lane & 4) * 16 + g * 16;
      tp0 = img + q * 128 + (((2 * g + ((lane >> 1) & 1)) ^ (q & 2)) * 16) + (lane & 1) * 8;
      tp1 = img + 512 + q * 128 + (((2 * g + ((lane >> 1) & 1)) ^ ((q & 2) + 4)) * 16) + (lane & 1) * 8;
      pp = smem + ((lane >> 3) == 0 || (lane >> 3) == 3 ? O_PART + r * 64 + xw * 16 + g * 8 : O_JUNK + lane * 8);
    }
    const int so = WS_XT + (32 * xw + (lane >> 3)) * XT_PITCH + cg * 128 + (lane & 7) * 16, sstep = 8 * XT_PITCH;
    u32 lo_ = WS_XT + (cc >> 1) * XT_PITCH + (16 * xw + (lane >> 2)) * 128 + (cc & 1) * 64 + (lane & 3) * 16;
    OPQ(lo_);
    SB();
    asm volatile("s_waitcnt vmcnt(0)" ::: "memory");
    STAMP(1);
    {
      u4v wa[4], fb[4]; u2v ta[4], tb[4];
#pragma unroll
      for (int s = 0; s < 4; ++s) { wa[s] = *(const u4v*)(wp + s * 128); fb[s] = *(const u4v*)(fp + s * 1024); }
      SB();
      f4 r = {0.f, 0.f, 0.f, 0.f};
#pragma unroll
      for (int s = 0; s < 4; ++s) r = mma(wa[s], fb[s], r);
      *(f2*)pp = f2{r[0], r[1]};
      ARRIVE(C_PART);
      SB();
#pragma unroll
      for (int s = 0; s < 4; ++s) { ta[s] = tr_read(tp0 + s * 1024); tb[s] = tr_read(tp1 + s * 1024); }
      SB();
      WAITQ(C_GP, 1);
#pragma unroll
      for (int s = 0; s < 4; ++s) __builtin_amdgcn_raw_buffer_store_b128(u4v{ta[s][0], ta[s][1], tb[s][0], tb[s][1]}, wrs, so + s * sstep, 0, 16);
    }
    STAMP(2);
    const u32 dskw = ((gp16)kb[0])[d];
    conv_setup();
    band_setup();
    const u32 tag = par[0];
    ARRIVE(C_PUB);
    asm volatile("s_waitcnt vmcnt(0)" ::: "memory");
    if (lane == 0) __builtin_amdgcn_raw_buffer_store_b32(tag, wrs, WS_FLG + xw * 1024 + cg * 4, 0, 16);
    STAMP(3);
    dm1 = bflo(dskw) - 1.f;
    WAITS(C_TL, 1);
    const u32 tl0 = (u32)__builtin_amdgcn_readfirstlane((int)par[1]);
    u3v fq[2];
    auto rec_ld = [&]() AI {
#pragma unroll
      for (int k = 0; k < 2; ++k) fq[k] = __builtin_amdgcn_raw_buffer_load_b96(wrs, WS_REC + xw * 2048 + k * 1024 + lane * 16, 0, 16);
    };
    pace(tl0 + DT_REC);
    rec_ld();
    SB();
    {
#pragma unroll 1
      for (int spin = 0; spin < SPIN_MAX && __builtin_expect(__any(fq[0][2] != tag || fq[1][2] != tag), 0); ++spin) {
        __builtin_amdgcn_s_sleep(SLEEP_G);
        rec_ld();
        asm volatile("" ::: "memory");
      }
      char* gp = smem + O_GT + xw * 2048 + (lane & 3) * 256 + (lane >> 2) * 16;
#pragma unroll
      for (int k = 0; k < 2; ++k) *(f4*)(gp + k * 1024) = f4{bflo(fq[k][0]), bfhi(fq[k][0]), bflo(fq[k][1]), bfhi(fq[k][1])};
    }
    pace(tl0 + DT_FLG);
    u4v xq[4];
    bool late[4];
    {
      const u32 fl = __builtin_amdgcn_raw_buffer_load_b32(wrs, WS_FLG + (cc >> 6) * 1024 + xw * 64 + g * 256 + c16 * 4, 0, 16);
      const u64 bad = __builtin_amdgcn_ballot_w64(fl != tag);
      asm volatile("" ::: "memory");
#pragma unroll
      for (int k = 0; k < 4; ++k) {
        late[k] = ((bad >> (16 * k)) & 0xffffull) != 0ull;
        xq[k] = __builtin_amdgcn_raw_buffer_load_b128(wrs, lo_ | (late[k] ? 1u << 30 : 0u), k * 8192, 16);
        SB();
      }
    }
    ARRIVE(C_XL);
    STAMP(4);
    WAITQ(C_TAPS + c, 1);
    u4v Bi[2][3], Bo[4], As;
#pragma unroll
    for (int be = 0; be < 3; ++be) { Bi[0][be] = bin(0, be); Bi[1][be] = bin(1, be); }
#pragma unroll
    for (int be = 0; be < 4; ++be) Bo[be] = bout(be);
    As = asp();
    WAITQ(C_PUB, 4);
    char* dp = smem + (lane & 3) * CHB + O_U + 512 + xw * 256 + (lane >> 2) * 16;
    m_setup();
    auto stage = [&](int k) AI {
      SB();
      if (__builtin_expect(late[k], 0)) {
#pragma unroll 1
        for (int spin = 0; spin < SPIN_MAX; ++spin) {
          STAMP(14);
          const u32 f1 = __builtin_amdgcn_raw_buffer_load_b32(wrs, WS_FLG + (cc >> 6) * 1024 + xw * 64 + k * 256 + c16 * 4, 0, 16);
          if (!__any(f1 != tag)) break;
          __builtin_amdgcn_s_sleep(SLEEP_G);
          asm volatile("" ::: "memory");
        }
        asm volatile("" ::: "memory");
        xq[k] = __builtin_amdgcn_raw_buffer_load_b128(wrs, lo_, k * 8192, 16);
        asm volatile("s_waitcnt vmcnt(0)" : "+v"(xq[k]) :: "memory");
      }
      *(u4v*)(dp + k * 1024) = xq[k];
      FLAG(C_Q + k * 4);
    };
    stage(0);
    stage(1);
    STAMP(5);
    __builtin_amdgcn_s_setprio(0);
    WAITA(qa);
    SB();
    FL(0, 0); RL(0); FL(8, 8); RL(1);
    MF0(wq[0][0], Fq[0], band[0]);
    MF0(wq[0][1], Fq[0], band[1]);
    MF0(wq[0][2], ru[0], band[0]);
    MF0(wq[0][3], ru[0], band[1]);
    MF0(wq[0][4], Fq[0], band[2]);
    MF0(wq[0][5], ru[0], band[3]);
    MF0(acc[4], As, Fq[0]); PEEK(6, qa + 16);
    MF0(wq[1][0], Fq[8], band[0]); WPK(0, 0, 0); WPK(0, 0, 1);
    MF0(wq[1][1], Fq[8], band[1]); WPK(0, 0, 2); WPK(0, 0, 3);
    MF0(wq[1][2], ru[1], band[0]); WPK(0, 1, 0); WPK(0, 1, 1);
    MF0(wq[1][3], ru[1], band[1]); WPK(0, 1, 2); WPK(0, 1, 3);
    MF0(wq[1][4], Fq[8], band[2]); CHECK(6, qa + 16);
    MF0(wq[1][5], ru[1], band[3]); WPK(0, 2, 0); WPK(0, 2, 1);
    MF0(acc[5], As, Fq[8]); WPK(0, 2, 2); WPK(0, 2, 3);
    MF0(acc[2], wa_[0][0], Bi[0][0]); FL(16, 16); RL(2);
    MF(acc[2], wa_[0][1], Bi[1][0]); WPK(1, 0, 0); WPK(1, 0, 1);
    MF0(acc[3], wa_[0][0], Bi[0][1]); WPK(1, 0, 2); WPK(1, 0, 3);
    MF(acc[3], wa_[0][1], Bi[1][1]); WPK(1, 1, 0); WPK(1, 1, 1);
    MF(acc[4], wa_[0][0], Bi[0][2]); FST(0); FLAG(C_FAR + 0 * 4);
    MF(acc[4], wa_[0][1], Bi[1][2]); WPK(1, 1, 2); WPK(1, 1, 3);
    MF(acc[4], wa_[0][2], Bo[0]); WPK(1, 2, 0); WPK(1, 2, 1);
    MF(acc[5], wa_[0][2], Bo[1]); WPK(1, 2, 2); WPK(1, 2, 3);
    MF0(acc[6], wa_[0][2], Bo[2]);
    MF0(acc[7], wa_[0][2], Bo[3]); stage(2);
    MF0(wq[2][0], Fq[16], band[0]);
    MF0(wq[2][1], Fq[16], band[1]); stage(3);
    MF0(wq[2][2], ru[2], band[0]);
    MF0(wq[2][3], ru[2], band[1]);
    MF0(wq[2][4], Fq[16], band[2]); FL(24, 24); RL(3);
    MF0(wq[2][5], ru[2], band[3]); WPK(2, 0, 0); WPK(2, 0, 1);
    MF(acc[6], As, Fq[16]); WPK(2, 0, 2); WPK(2, 0, 3);
    MF(acc[3], wa_[1][0], Bi[0][0]); WPK(2, 1, 0); WPK(2, 1, 1);
    MF(acc[3], wa_[1][1], Bi[1][0]); WPK(2, 1, 2); WPK(2, 1, 3);
    MF(acc[4], wa_[1][0], Bi[0][1]); WPK(2, 2, 0); WPK(2, 2, 1);
    MF(acc[4], wa_[1][1], Bi[1][1]); WPK(2, 2, 2); WPK(2, 2, 3);
    MF(acc[5], wa_[1][0], Bi[0][2]); FST(1); FLAG(C_FAR + 1 * 4);
    MF(acc[5], wa_[1][1], Bi[1][2]); PEEK(7, qa + 32);
    MF(acc[5], wa_[1][2], Bo[0]);
    MF(acc[6], wa_[1][2], Bo[1]);
    MF(acc[7], wa_[1][2], Bo[2]);
    MF0(wq[3][0], Fq[24], band[0]);
    MF0(wq[3][1], Fq[24], band[1]); CHECK(7, qa + 32);
    MF0(wq[3][2], ru[3], band[0]);
    MF0(wq[3][3], ru[3], band[1]);
    MF0(wq[3][4], Fq[24], band[2]); FL(32, 32); RL(4);
    MF0(wq[3][5], ru[3], band[3]); WPK(3, 0, 0); WPK(3, 0, 1);
    MF(acc[7], As, Fq[24]); WPK(3, 0, 2); WPK(3, 0, 3);
    MF(acc[4], wa_[2][0], Bi[0][0]); WPK(3, 1, 0); WPK(3, 1, 1);
    MF(acc[4], wa_[2][1], Bi[1][0]); WPK(3, 1, 2); WPK(3, 1, 3);
    MF(acc[5], wa_[2][0], Bi[0][1]); WPK(3, 2, 0); WPK(3, 2, 1);
    MF(acc[5], wa_[2][1], Bi[1][1]); FL(40, 40); RL(5);
    MF(acc[6], wa_[2][0], Bi[0][2]); FST(2); FLAG(C_FAR + 2 * 4);
    MF(acc[6], wa_[2][1], Bi[1][2]); WPK(3, 2, 2); WPK(3, 2, 3);
    MF(acc[6], wa_[2][2], Bo[0]); AL(0);
    MF(acc[7], wa_[2][2], Bo[1]); AL(1);
    MF0(wq[4][0], Fq[32], band[0]); AL(2);
    MF0(wq[4][1], Fq[32], band[1]); AL(3);
    MF0(wq[4][2], ru[4], band[0]); AL(4);
    MF0(wq[4][3], ru[4], band[1]); WPK(4, 0, 0); WPK(4, 0, 1);
    MF(acc[5], wa_[3][0], Bi[0][0]); WPK(4, 0, 2); WPK(4, 0, 3);
    MF(acc[5], wa_[3][1], Bi[1][0]); WPK(4, 1, 0); WPK(4, 1, 1);
    MF(acc[6], wa_[3][0], Bi[0][1]); WPK(4, 1, 2); WPK(4, 1, 3);
    MF(acc[6], wa_[3][1], Bi[1][1]); AL(5);
    MF(acc[7], wa_[3][0], Bi[0][2]); FST(3); FLAG(C_FAR + 3 * 4);
    MF(acc[7], wa_[3][1], Bi[1][2]); AL(6);
    MF(acc[7], wa_[3][2], Bo[0]); FL(33, 33); AL(7);
    MF0(wq[5][0], Fq[40], band[0]); FL(34, 34); AL(8);
    MF0(wq[5][1], Fq[40], band[1]); FL(35, 35); AL(9);
    MF0(wq[5][2], ru[5], band[0]); WPK(5, 0, 0); WPK(5, 0, 1);
    MF0(wq[5][3], ru[5], band[1]); AL(10);
    MF(acc[6], wa_[4][0], Bi[0][0]); PEEK(8, qa + 48); FL(36, 36);
    MF(acc[6], wa_[4][1], Bi[1][0]); FL(37, 37); WPK(5, 0, 2);
    MF(acc[7], wa_[4][0], Bi[0][1]); FL(38, 38); WPK(5, 0, 3);
    MF(acc[7], wa_[4][1], Bi[1][1]); FL(39, 39); WPK(5, 1, 0);
    MF(acc[6], At[0], Fq[32]); WPK(5, 1, 1); WPK(5, 1, 2);
    MF(acc[6], At[1], Fq[33]); CHECK(8, qa + 48);
    MF(acc[6], At[2], Fq[34]); WPK(5, 1, 3); FL(41, 41);
    MF(acc[6], At[3], Fq[35]); AL(11);
    MF(acc[7], wa_[5][0], Bi[0][0]); FL(42, 42); AL(12);
    MF(acc[7], wa_[5][1], Bi[1][0]); AL(13);
    MF(acc[6], At[4], Fq[36]); FL(43, 43); AL(14);
    MF(acc[6], At[5], Fq[37]); AL(15);
    MF(acc[6], At[6], Fq[38]); FL(44, 44); AL(16);
    MF(acc[6], At[7], Fq[39]);
    MF(acc[6], At[8], Fq[40]); FL(45, 45);
    MF(acc[7], At[0], Fq[40]);
    MF(acc[6], At[9], Fq[41]); FL(46, 46);
    MF(acc[7], At[1], Fq[41]);
    MF(acc[6], At[10], Fq[42]); FL(47, 47);
    MF(acc[7], At[2], Fq[42]);
    MF(acc[6], At[11], Fq[43]); FL(48, 48);
    MF(acc[7], At[3], Fq[43]);
    MF(acc[6], At[12], Fq[44]); FL(49, 49); ULD(6);
    MF(acc[7], At[4], Fq[44]); FL(50, 50); GLD(6);
    MF(acc[6], At[13], Fq[45]); FL(51, 51);
    MF(acc[7], At[5], Fq[45]); FL(52, 52);
    MF(acc[6], At[14], Fq[46]); FL(53, 53);
    MF(acc[7], At[6], Fq[46]); FL(54, 54); EUN(6, 0);
    MF(acc[6], At[15], Fq[47]); FL(55, 55); EUN(6, 1);
    MF(acc[7], At[7], Fq[47]); FL(56, 56); EUN(6, 2);
    MF(acc[6], At[16], Fq[48]); EUN(6, 3);
    MF(acc[7], At[8], Fq[48]); ULD(7); GLD(7);
    MF(acc[7], At[9], Fq[49]);
    MF(acc[7], At[10], Fq[50]);
    MF(acc[7], At[11], Fq[51]);
    MF(acc[7], At[12], Fq[52]); EA1(6, 0); EA1(6, 1); EA1(6, 2); EA1(6, 3);
    MF(acc[7], At[13], Fq[53]); EA2(6, 0); EA2(6, 1); EA2(6, 2); EA2(6, 3);
    MF(acc[7], At[14], Fq[54]); EPK(6, 0); EPK(6, 1);
    MF(acc[7], At[15], Fq[55]); OST(6); FLAG(C_TILE + 6 * 4);
    MF(acc[7], At[16], Fq[56]); EUN(7, 0); EUN(7, 1); EUN(7, 2); EUN(7, 3); NOP(3); EA1(7, 0); EA1(7, 1); EA1(7, 2); EA1(7, 3); EA2(7, 0); EA2(7, 1); EA2(7, 2); EA2(7, 3); EPK(7, 0); EPK(7, 1); OST(7); FLAG(C_TILE + 7 * 4);
    STAMP(9);
    ROLE_END();
  }
  {
    ROLE("h");
    const int hw = w - 5;
    const int j = xg * 28 + xi;
    const int s1 = j >> 2, kq = j & 3;
    const bool own = xi < 28;
    const u64 wbase = hw == 0 ? kb[1] : kb[2];
    const int pitch = F * 2;
    auto hrs = __builtin_amdgcn_make_buffer_rsrc((void*)wbase, 0, own ? D * F * 2 : 0, 0x00020000);
    const int r8 = lane >> 3;
    const int vo = (256 * kq * F + 64 * s1) * 2 + (8 * (r8 >> 2) + (r8 & 3)) * pitch + (((lane & 7) * 16) ^ (((r8 >> 1) & 3) * 32));
    auto dma = [&](int h, int sB, int pt, u32 dst) AI {
      const int m = h >> 1;
#pragma unroll
      for (int i = 0; i < 2; ++i)
        asm volatile("s_mov_b32 m0, %3\n\ts_nop 0\n\tbuffer_load_dwordx4 %0, %1, %2 offen nt lds"
                     :: "v"(vo), "s"(hrs), "s"((m & 1) * 32 * pt + (m >> 1) * sB + (16 * i + 4 * (h & 1)) * pt), "s"(dst + i * 1024) : "memory");
    };
    const int q = c16 >> 2, p = c16 & 3;
    int tro[4];
#pragma unroll
    for (int ct = 0; ct < 4; ++ct) tro[ct] = (4 * g + q) * 128 + ((ct ^ (((4 * g + q) >> 1) & 3)) * 32) + p * 8;
    const int tok = lane & 7;
    u4v wd[2][8];
    auto wd_ld = [&](int k, int cb) AI {
#pragma unroll
      for (int i = 0; i < 8; ++i) {
        const char* pp = smem + O_WD + cb * 8192 + (i >> 2) * 4096 + tro[i & 3];
        const u2v lo = tr_read(pp), hi = tr_read(pp + 2048);
        wd[k][i] = u4v{lo[0], lo[1], hi[0], hi[1]};
      }
    };
    u32 p2o, p2k[4], hlo, hhi;
    auto f2_setup = [&](int cb0) AI {
      p2o = WS_P2 + ((64 * kq + 16 * cb0 + (lane & 8) / 2 + g) * 56 + s1) * 128 + tok * 16;
      hlo = lbase + O_HB + ((lane & 8) ? 512 : g * 128 + tok * 16);
      hhi = lbase + O_HB + ((lane & 8) ? g * 128 + tok * 16 : 512);
#pragma unroll
      for (int i = 0; i < (cb0 == 2 ? 4 : 2); ++i) { p2k[i] = p2o + i * 8 * 7168; OPQ(p2k[i]); }
      OPQ(hlo); OPQ(hhi);
    };
    auto f2_job = [&](int nb, u32 t10) AI {
      u4v hb[4];
      {
        u32 cnt; int n_ = 1 << 20;
        asm volatile("1:\n\tds_read_b32 %4, %6\n\tds_read_b128 %0, %7\n\tds_read_b128 %1, %8\n\tds_read_b128 %2, %7 offset:528\n\tds_read_b128 %3, %8 offset:528\n\t"
                     "s_sub_u32 %5, %5, 1\n\ts_cbranch_scc1 2f\n\ts_waitcnt lgkmcnt(0)\n\tv_cmp_gt_u32 vcc, 2, %4\n\ts_cbranch_vccnz 1b\n2:\n\ts_waitcnt lgkmcnt(0)"
                     : "=&v"(hb[0]), "=&v"(hb[1]), "=&v"(hb[2]), "=&v"(hb[3]), "=&v"(cnt), "+s"(n_) : "v"(CTLA(C_HRDY)), "v"(hlo), "v"(hhi) : "vcc", "scc", "memory");
      }
      STAMP(11);
      f4 r[4];
#pragma unroll
      for (int i = 0; i < 2 * nb; ++i) {
        const u4v* wp = &wd[i >> 1][(i & 1) * 2];
        r[i] = mma(wp[0], hb[0], f4{0.f, 0.f, 0.f, 0.f});
        r[i] = mma(wp[1], hb[1], r[i]);
        r[i] = mma(wp[4], hb[2], r[i]);
        r[i] = mma(wp[5], hb[3], r[i]);
      }
      SB();
#pragma unroll
      for (int i = 0; i < 2 * nb; ++i) {
        u4v v = __builtin_bit_cast(u4v, r[i]);
#pragma unroll
        for (int e = 0; e < 4; ++e) v[e] = __builtin_amdgcn_bitop3_b32(v[e], t10, TMASK, 0xd8);
        __builtin_amdgcn_raw_buffer_store_b128(v, wrs, p2k[i], 0, 16);
      }
      STAMP(12);
    };
    const u32 rb = lbase + O_RING + hw * (NSLOT * 2048);
    pace(ts0 + WDELAY);
    STAMP(2);
#pragma unroll
    for (int h = 0; h < WWIN; ++h) dma(h, 64 * F * 2, F * 2, rb + h * 2048);
    __builtin_amdgcn_s_setprio(3);
    STAMP(3);
    if (__builtin_expect(own, 1)) {
      u4v wv[32];
      f4 a1[4] = {f4{0.f, 0.f, 0.f, 0.f}, f4{0.f, 0.f, 0.f, 0.f}, f4{0.f, 0.f, 0.f, 0.f}, f4{0.f, 0.f, 0.f, 0.f}};
      u4v xb[8];
      auto f1 = [&](int k0, int k1) AI {
#pragma unroll
        for (int k = k0; k < k1; ++k)
#pragma unroll
          for (int ct = 0; ct < 4; ++ct) a1[ct] = mma(wv[4 * k + ct], xb[k], a1[ct]);
      };
      u32 xo = lbase + O_XS + tok * XS_PITCH + g * 16;
      u32 p1o = WS_P1 + s1 * 16384 + kq * 4096 + hw * 2048 + (lane & 8) * 64 + g * 128 + tok * 16;
      u32 t10, tpub;
      OPQ(xo); OPQ(p1o);
      constexpr int HX = (16 - WWIN + 1) & ~1;
#pragma unroll
      for (int h = 0; h < 16; ++h) {
        const int left = (h + WWIN < 16 ? WWIN : 16 - h) - 1;
        asm volatile("s_waitcnt vmcnt(%0)" :: "n"(2 * left) : "memory");
        if (h + WWIN < 16) {
          asm volatile("s_waitcnt lgkmcnt(0)" ::: "memory");
          dma(h + WWIN, 64 * F * 2, F * 2, rb + ((h + WWIN) % NSLOT) * 2048);
        }
#pragma unroll
        for (int ct = 0; ct < 4; ++ct) {
          const u2v t = tr_read(smem + O_RING + hw * (NSLOT * 2048) + (h % NSLOT) * 2048 + tro[ct]);
          wv[4 * (h >> 1) + ct][2 * (h & 1)] = t[0]; wv[4 * (h >> 1) + ct][2 * (h & 1) + 1] = t[1];
        }
        if (h == H2_AT) ARRIVE(C_WGU);
        if (h == HX - 1) {
          STAMP(4);
          WAITQ(C_XS, 1);
          STAMP(1);
#pragma unroll
          for (int k = 0; k < 8; ++k) xb[k] = *(const __attribute__((address_space(3))) u4v*)(uintptr_t)(xo + k * 64);
          t10 = par[0]; tpub = par[2];
          SB();
          f1(0, 2);
          SB();
        }
        if (h >= HX) { f1(h - HX + 2, h - HX + 3); SB(); }
      }
      STAMP(5);
      f1(18 - HX, 8);
      t10 &= TMASK;
#pragma unroll
      for (int a = 0; a < 2; ++a) {
        u4v v;
#pragma unroll
        for (int e = 0; e < 4; ++e)
          v[e] = ((u32)__builtin_amdgcn_update_dpp(__float_as_int(a1[2 * a][e]), __float_as_int(a1[2 * a + 1][e]), 0xe4, 0xf, 0xc, false) & ~TMASK) | t10;
        __builtin_amdgcn_raw_buffer_store_b128(v, wrs, p1o + a * 1024, 0, 16);
      }
      STAMP(6);
      const u32 tn = now32();
      u32 voff = WS_P1 + s1 * 16384 + hw * 1024 + lane * 16;
      u32 ho = lbase + O_HB + hw * 528 + g * 128 + tok * 16 + (lane & 8);
      float nl2e = -1.44269504088896341f;
      u32 wda = CTLA(C_WD) + hw * 4;
      OPQ(voff); OPQ(ho); OPQ(nl2e); OPQ(wda);
      f2_setup(hw & 1);
      pace2(tn, (u32)__builtin_amdgcn_readfirstlane((int)tpub) + DE_P1, DT_P1);
      u4v pb[8];
      auto p1_ld = [&]() AI {
#pragma unroll
        for (int i = 0; i < 8; ++i) { pb[i] = __builtin_amdgcn_raw_buffer_load_b128(wrs, voff + (i >> 2) * 2048, (i & 3) * 4096, 16); SB(); }
      };
      p1_ld();
      SB();
      f4 G, U;
      bool early = false;
#pragma unroll 1
      for (int spin = 0; spin < SPIN_MAX; ++spin) {
        u32 bad = 0;
        OPQ(bad);
        asm volatile("" :: "v"(pb[0]));
        const u32 cw = __hip_atomic_load((__attribute__((address_space(3))) u32*)(uintptr_t)wda, __ATOMIC_RELAXED, __HIP_MEMORY_SCOPE_WORKGROUP);
        G = tsum4(bad, t10, pb[0], pb[1], pb[2], pb[3]);
        early = __builtin_amdgcn_ballot_w64(cw == 0u) == 0ull;
        asm volatile("" ::: "memory");
        if (__builtin_expect(early, 1)) wd_ld(0, hw);
        SB();
        silu4(G, nl2e);
        U = tsum4(bad, t10, pb[4], pb[5], pb[6], pb[7]);
        if (__builtin_expect(!__any((bad & TMASK) != 0), 1)) break;
        STAMP(14);
        p1_ld();
        asm volatile("" ::: "memory");
      }
      STAMP(7);
      {
        float hv[4];
#pragma unroll
        for (int e = 0; e < 4; ++e) { hv[e] = G[e] * U[e]; asm("" : "+v"(hv[e])); }
        *(__attribute__((address_space(3))) u2v*)(uintptr_t)ho = u2v{pk_bf(hv[0], hv[1]), pk_bf(hv[2], hv[3])};
      }
      ARRIVE(C_HRDY);
      STAMP(8);
      if (__builtin_expect(!early, 0)) { WAITA(wda); wd_ld(0, hw); }
      f2_job(1, t10);
    }
    if (hw == 0) {
      WAITS(C_SEL, 1);
      const u32 t10 = par[0] & TMASK;
      const u32 tpub = (u32)__builtin_amdgcn_readfirstlane((int)par[2]);
      const u32 mykey = ((const u32*)(smem + O_SEL))[lane & 7];
      if (own) pace2(now32(), tpub + DE_P2, DT_P2); else pace(tpub + DE_P2 - 40);
      u4v pa[7];
      const int voff = WS_P2 + cc * 7168 + lane * 16;
#pragma unroll
      for (int k = 0; k < 7; ++k) pa[k] = __builtin_amdgcn_raw_buffer_load_b128(wrs, voff + (k & 3) * 1024, (k >> 2) * 4096, 16);
      SB();
      const int row = (int)(~mykey & 2047u);
      const float rsig = rb1(sigm(__uint_as_float((mykey ^ (~(u32)((int)mykey >> 31) | 0x80000000u)) & 0xffff0000u)));
      WAITA(CTLA(C_TILE) + (lane & 31) * 4);
      float cf_ = rsig * (1.f - *(const float*)(smem + O_GT + (row >> 8) * 1024 + ((row >> 2) & 3) * 256 + ((row >> 4) & 15) * 16 + (row & 3) * 4));
      float base[4];
#pragma unroll
      for (int e = 0; e < 4; ++e) base[e] = bflo(*(const u16*)(smem + e * CHB + O_OUT + (row >> 8) * OT + ((row >> 2) & 3) * 144 + ((row >> 4) & 15) * 8 + (row & 3) * 2));
      u64 op = kc[1] + (row * D + cc * 4) * 2;
      { float cq = cf_; OPQ(cq); cf_ = cq; OPQ(op); }
#pragma unroll
      for (int e = 0; e < 4; ++e) OPQ(base[e]);
      STAMP(13);
      f4 y;
#pragma unroll 1
      for (int spin = 0; spin < SPIN_MAX; ++spin) {
        u32 bad = 0;
        OPQ(bad);
        y = tsum4(bad, t10, pa[0], pa[1], pa[2], pa[3]);
        y = tsum3(bad, t10, y, pa[4], pa[5], pa[6]);
        if (__builtin_expect(!__any((bad & TMASK) != 0), 1)) break;
        STAMP(15);
#pragma unroll
        for (int k = 0; k < 7; ++k) pa[k] = __builtin_amdgcn_raw_buffer_load_b128(wrs, voff + (k & 3) * 1024, (k >> 2) * 4096, 16);
        asm volatile("" ::: "memory");
      }
      STAMP(9);
      float o[4];
#pragma unroll
      for (int e = 0; e < 4; ++e) {
        float v = y[e] + dpp_f<0x128>(y[e]);
        auto s16 = __builtin_amdgcn_permlane16_swap(__float_as_uint(v), __float_as_uint(v), false, false);
        v = __uint_as_float(s16[0]) + __uint_as_float(s16[1]);
        auto s32 = __builtin_amdgcn_permlane32_swap(__float_as_uint(v), __float_as_uint(v), false, false);
        o[e] = fmaf(__uint_as_float(s32[0]) + __uint_as_float(s32[1]), cf_, base[e]);
      }
      if (lane < 8) *(__attribute__((address_space(1))) u2v*)op = u2v{pk_bf(o[0], o[1]), pk_bf(o[2], o[3])};
    }
    ROLE_END();
  }
  {
    ROLE("g");
    const u32 pLA = __builtin_nontemporal_load((gp32)ka[3] + d * (NM / 2) + lane);
    const u32 pAI = __builtin_nontemporal_load((gp32)ka[4] + d * (NM / 2) + lane);
    const u32 pCR = __builtin_nontemporal_load((gp32)ka[5] + d * (NM / 2) + lane);
    const u32 pCI = __builtin_nontemporal_load((gp32)ka[6] + d * (NM / 2) + lane);
    const u32 ldt = ((gp16)ka[7])[d], dskw = ((gp16)kb[0])[d];
    u4v ixa[2];
#pragma unroll
    for (int r = 0; r < 2; ++r) ixa[r] = *(const u4v*)(g_tapidx.v + lane * 16 + r * 8);
    SB();
    asm volatile("s_mov_b32 m0, %0\n\ts_nop 0" :: "s"(lbase + c * CHB) : "memory");
#define PUT1(off, val) asm volatile("ds_write_addtid_b32 %0 offset:%1" :: "v"(val), "n"(off) : "memory")
#define PUTR(off, val) do { const ch v_ = (val); asm volatile("ds_write_addtid_b32 %0 offset:%2\n\tds_write_addtid_b32 %1 offset:%3" :: "v"(v_.r), "v"(v_.i), "n"(off), "n"((off) + 256) : "memory"); } while (0)
    const char* pf = ch_ + c16 * TROW + g * 16;
    PUT1(O_TAP, 0x3c003c00u); PUT1(O_TAP + 256, 0u);
    PUT1(O_U, 0u); PUT1(O_U + 256, 0u);
    SB();
    const float dt = rb1(exp_bf(bflo(ldt)));
    const f2 a2 = f2{-rb1(exp_bf(bflo(pLA))), -rb1(exp_bf(bfhi(pLA)))} * dt * 1.44269504088896341f;
    const f2 br = unbf(pAI) * dt * 0.15915494309189535f;
    STAMP(1);
    u4v Lf[2][8], Wf[8];
    f4 kk[2][2];
    float fk;
    {
      const cf z1 = cexp_p(a2, br, 1.f);
      const cf z2 = csq(z1), z4 = csq(z2), z8 = csq(z4), z16 = csq(z8), z32 = csq(z16), z64 = csq(z32);
      ch base[4], anc[4];
      base[1] = tohc(z8); base[2] = tohc(z16); base[3] = tohc(cmul(z16, z8));
      anc[1] = tohc(z32); anc[2] = tohc(z64); anc[3] = tohc(cmul(z64, z32));
      PUTR(O_OUT + 5 * TROW, tohc(csq(z64)));
#pragma unroll
      for (int j = 1; j < 16; ++j) {
        const int q = j >> 2, r = j & 3;
        PUTR(O_TAP + j * TROW, q == 0 ? base[r] : r == 0 ? anc[q] : hmul(anc[q], base[r]));
      }
#pragma unroll
      for (int s = 0; s < 8; ++s) Lf[0][s] = *(const u4v*)(pf + O_TAP + s * 64);
#pragma unroll
      for (int s = 0; s < 8; ++s) Lf[1][s] = *(const u4v*)(ch_ + O_OUT + 5 * TROW + g * 16 + s * 64);
      SB();
      STAMP(11);
      u32 mx = max(max(pCR << 17, pCR << 1), max(pCI << 17, pCI << 1));
      mx = wave_max_u32(mx);
      int ex = (int)(mx >> 24);
      ex = ex < 1 ? 1 : (ex > 253 ? 253 : ex);
      const float sdn = __uint_as_float((u32)(254 - ex) << 23);
      fk = 2.f * dt * __uint_as_float((u32)ex << 23);
      cf C; C.r = unbf(pCR) * sdn; C.i = unbf(pCI) * sdn;
      const cf E = cexp_p(a2, br, 256.f);
      const ch y1 = toh(z1), y2 = toh(z2), y4 = toh(z4), y6 = toh(cmul(z4, z2));
      {
        const ch R0 = toh(C), R2 = hmul(R0, y2), R4 = hmul(R0, y4), R6 = hmul(R0, y6);
        PUTR(O_TAP, R0); PUTR(O_TAP + 2 * TROW, R2); PUTR(O_TAP + 4 * TROW, R4); PUTR(O_TAP + 6 * TROW, R6);
        PUTR(O_TAP + 1 * TROW, hmul(R0, y1)); PUTR(O_TAP + 3 * TROW, hmul(R2, y1)); PUTR(O_TAP + 5 * TROW, hmul(R4, y1)); PUTR(O_TAP + 7 * TROW, hmul(R6, y1));
        C = cmul(C, E);
        const ch R8 = toh(C);
        PUTR(O_TAP + 8 * TROW, R8); PUTR(O_TAP + 9 * TROW, hmul(R8, y2)); PUTR(O_TAP + 10 * TROW, hmul(R8, y4)); PUTR(O_TAP + 11 * TROW, hmul(R8, y6));
        C = cmul(C, E);
        const ch R12 = toh(C);
        PUTR(O_TAP + 12 * TROW, R12); PUTR(O_TAP + 13 * TROW, hmul(R12, y4));
        C = cmul(C, E);
        const ch R14 = toh(C);
        PUTR(O_TAP + 14 * TROW, R14); PUTR(O_TAP + 15 * TROW, hmul(R14, y4));
      }
#pragma unroll
      for (int s = 0; s < 8; ++s) Wf[s] = *(const u4v*)(pf + O_TAP + s * 64);
      SB();
      STAMP(12);
#pragma unroll
      for (int i = 0; i < 5; ++i) { C = cmul(C, E); PUTR(O_OUT + i * TROW, toh(C)); }
      SB();
      auto mh = [&](u4v a, u4v bq, f4 cin) AI { return __builtin_amdgcn_mfma_f32_16x16x32_f16(as_h8(a), as_h8(bq), cin, 0, 0, 0); };
      kk[0][0] = f4{0.f, 0.f, 0.f, 0.f}; kk[0][1] = kk[0][0]; kk[1][0] = kk[0][0]; kk[1][1] = kk[0][0];
#pragma unroll
      for (int s = 0; s < 5; ++s) {
#pragma unroll
        for (int k = 0; k < 4; ++k) {
          if (k < 2) { if (s < 4) kk[0][0] = mh(Wf[s + 4 * k], Lf[0][s + 4 * k], kk[0][0]); }
          else if (s > 0) { const int u = s - 1 + 4 * (k - 2); kk[0][1] = mh(Wf[u], Lf[1][u], kk[0][1]); Wf[u] = *(const u4v*)(pf + O_OUT + u * 64); }
          SB();
          if (s < 4) {
            const ch o = hmul(ch{ash(Lf[0][s][k]), ash(Lf[0][s + 4][k])}, ch{ash(Lf[1][s][k]), ash(Lf[1][s + 4][k])});
            Lf[1][s][k] = __builtin_bit_cast(u32, o.r); Lf[1][s + 4][k] = __builtin_bit_cast(u32, o.i);
          }
          if (s == 3 && k == 3) asm volatile("s_nop 1");
          SB();
        }
      }
#pragma unroll
      for (int s = 0; s < 8; ++s) { kk[1][0] = mh(Wf[s], Lf[0][s], kk[1][0]); kk[1][1] = mh(Wf[s], Lf[1][s], kk[1][1]); }
    }
    {
      u32 ia[16];
      const u32 kb_ = lbase + c * CHB + O_OUT;
#pragma unroll
      for (int e = 0; e < 16; ++e) { const u32 iw = ixa[e >> 3][(e >> 1) & 3]; ia[e] = kb_ + ((e & 1) ? (iw >> 16) : (iw & 0xffffu)); }
      MIX2(16, 0, 1)
      SB();
      STAMP(2);
      char* ktp = ch_ + O_OUT + c16 * KT_PITCH + g * 8;
#pragma unroll
      for (int hf = 1; hf >= 0; --hf) {
#pragma unroll
        for (int ct = 0; ct < 2; ++ct) {
          const f4 v = kk[hf][ct] * fk;
          *(u32*)(ktp + ct * 16 * KT_PITCH + hf * 32) = pk_bf(v[0], v[1]);
          *(u32*)(ktp + ct * 16 * KT_PITCH + hf * 32 + 4) = pk_bf(v[2], v[3]);
        }
        asm volatile("" ::: "memory");
      }
      u32 zz = 0u; OPQ(zz);
      *(u32*)(ch_ + O_OUT + KT_ZERO + (lane & 7) * 4) = zz;
      asm volatile("" ::: "memory");
      u32 v[16], pe[9];
#pragma unroll
      for (int e = 15; e >= 0; --e) v[e] = *(const __attribute__((address_space(3))) u16*)(uintptr_t)ia[e];
      SB();
      pe[8] = zz;
#pragma unroll
      for (int i = 7; i >= 0; --i) {
        pe[i] = v[2 * i] | (v[2 * i + 1] << 16);
        const u32 nx = (u32)__builtin_amdgcn_update_dpp((int)dpp_u<0x134>(pe[i + 1]), (int)pe[i], 0x130, 0xf, 0xf, false);
        const u32 po = __builtin_amdgcn_alignbyte(nx, pe[i], 2);
        PUT1(O_TAP + i * 256, pe[i]); PUT1(O_TAP + 2 * TAP_PITCH - 4 + i * 256, pe[i]);
        PUT1(O_TAP + TAP_PITCH + i * 256, po); PUT1(O_TAP + 3 * TAP_PITCH - 4 + i * 256, po);
      }
      ARRIVE(C_TAPS + c);
    }
    STAMP(3);
    IDS();
    conv_setup();
    dm1 = bflo(dskw) - 1.f;
    atload(0, 17);
    m_setup();
    __builtin_amdgcn_s_setprio(1);
    SB();
    WAITA(qa);
    STAMP(4);
    SB();
    FL(0, -7); FL(1, -6); FL(2, -5); FL(3, -4); FL(4, -3); FL(5, -2); FL(6, -1); FL(7, 0); ULD(0); GLD(0);
    MF0(acc[0], At[9], Fq[0]); EUN(0, 0); EUN(0, 1);
    MF(acc[0], At[10], Fq[1]); EUN(0, 2); EUN(0, 3);
    MF(acc[0], At[11], Fq[2]);
    MF(acc[0], At[12], Fq[3]);
    MF(acc[0], At[13], Fq[4]);
    MF(acc[0], At[14], Fq[5]);
    MF(acc[0], At[15], Fq[6]); FL(8, 1);
    MF(acc[0], At[16], Fq[7]);
    MF0(acc[1], At[1], Fq[0]); FL(9, 2);
    MF(acc[1], At[2], Fq[1]);
    MF(acc[1], At[3], Fq[2]); FL(10, 3);
    MF(acc[1], At[4], Fq[3]);
    MF(acc[1], At[5], Fq[4]); FL(11, 4); EA1(0, 0);
    MF(acc[1], At[6], Fq[5]); EA1(0, 1); EA1(0, 2);
    MF(acc[1], At[7], Fq[6]); EA1(0, 3); PEEK(0, qa + 16);
    MF(acc[1], At[8], Fq[7]); FL(12, 5); EA2(0, 0);
    MF0(acc[2], At[0], Fq[7]); EA2(0, 1); EA2(0, 2);
    MF(acc[1], At[9], Fq[8]); EA2(0, 3); EPK(0, 0);
    MF(acc[2], At[1], Fq[8]); EPK(0, 1); FL(13, 6);
    MF(acc[1], At[10], Fq[9]); OST(0); FLAG(C_TILE + 0 * 4);
    MF(acc[2], At[2], Fq[9]); FL(14, 7); FL(15, 8);
    MF(acc[1], At[11], Fq[10]); CHECK(0, qa + 16);
    MF(acc[2], At[3], Fq[10]);
    MF(acc[1], At[12], Fq[11]); FL(16, 9);
    MF(acc[2], At[4], Fq[11]); ULD(1); GLD(1);
    MF(acc[1], At[13], Fq[12]); FL(17, 10);
    MF(acc[2], At[5], Fq[12]);
    MF(acc[1], At[14], Fq[13]); FL(18, 11);
    MF(acc[2], At[6], Fq[13]);
    MF(acc[1], At[15], Fq[14]); FL(19, 12); EUN(1, 0);
    MF(acc[2], At[7], Fq[14]); EUN(1, 1); EUN(1, 2);
    MF(acc[1], At[16], Fq[15]); FL(20, 13); EUN(1, 3);
    MF(acc[2], At[8], Fq[15]);
    MF0(acc[3], At[0], Fq[15]); FL(21, 14);
    MF(acc[2], At[9], Fq[16]);
    MF(acc[3], At[1], Fq[16]); FL(22, 15); EA1(1, 0);
    MF(acc[2], At[10], Fq[17]); EA1(1, 1); EA1(1, 2);
    MF(acc[3], At[2], Fq[17]); FL(23, 16); EA1(1, 3);
    MF(acc[2], At[11], Fq[18]); PEEK(2, fa + 0); EA2(1, 0);
    MF(acc[3], At[3], Fq[18]); EA2(1, 1); EA2(1, 2);
    MF(acc[2], At[12], Fq[19]); EA2(1, 3); EPK(1, 0);
    MF(acc[3], At[4], Fq[19]); EPK(1, 1); FL(24, 17);
    MF(acc[2], At[13], Fq[20]); OST(1); FLAG(C_TILE + 1 * 4);
    MF(acc[3], At[5], Fq[20]); FL(25, 18); CHECK(2, fa + 0);
    MF(acc[2], At[14], Fq[21]); FL(26, 19); ULD(2);
    MF(acc[3], At[6], Fq[21]); FLD(2); GLD(2);
    MF(acc[2], At[15], Fq[22]); FL(27, 20); PEEK(1, qa + 32);
    MF(acc[3], At[7], Fq[22]);
    MF(acc[2], At[16], Fq[23]); FL(28, 21);
    MF(acc[3], At[8], Fq[23]); EUN(2, 0); EUN(2, 1);
    MF0(acc[4], At[0], Fq[23]); FL(29, 22); EUN(2, 2);
    MF(acc[3], At[9], Fq[24]); EUN(2, 3); EA1(2, 0);
    MF(acc[4], At[1], Fq[24]); FL(30, 23); EA1(2, 1);
    MF(acc[3], At[10], Fq[25]); EA1(2, 2); EA1(2, 3);
    MF(acc[4], At[2], Fq[25]); FL(31, 24); EFA(2, 0);
    MF(acc[3], At[11], Fq[26]); EFA(2, 1); EFA(2, 2);
    MF(acc[4], At[3], Fq[26]); EFA(2, 3); PEEK(3, fa + 16);
    MF(acc[3], At[12], Fq[27]); EA2(2, 0); EA2(2, 1);
    MF(acc[4], At[4], Fq[27]); EA2(2, 2); EA2(2, 3);
    MF(acc[3], At[13], Fq[28]); CHECK(1, qa + 32); EPK(2, 0); EPK(2, 1);
    MF(acc[4], At[5], Fq[28]); FL(32, 25); OST(2);
    MF(acc[3], At[14], Fq[29]); FLAG(C_TILE + 2 * 4); FL(33, 26);
    MF(acc[4], At[6], Fq[29]); FL(34, 27); CHECK(3, fa + 16);
    MF(acc[3], At[15], Fq[30]); ULD(3); FL(35, 28);
    MF(acc[4], At[7], Fq[30]); FLD(3); GLD(3);
    MF(acc[3], At[16], Fq[31]); FL(36, 29);
    MF(acc[4], At[8], Fq[31]);
    MF0(acc[5], At[0], Fq[31]); FL(37, 30);
    MF(acc[4], At[9], Fq[32]); EUN(3, 0); EUN(3, 1);
    MF(acc[5], At[1], Fq[32]); PEEK(4, fa + 32); FL(38, 31);
    MF(acc[4], At[10], Fq[33]); EUN(3, 2); EUN(3, 3);
    MF(acc[5], At[2], Fq[33]); FL(39, 32); EA1(3, 0);
    MF(acc[4], At[11], Fq[34]); EA1(3, 1); EA1(3, 2);
    MF(acc[5], At[3], Fq[34]); EA1(3, 3); EFA(3, 0);
    MF(acc[4], At[12], Fq[35]); EFA(3, 1); EFA(3, 2); EFA(3, 3);
    MF(acc[5], At[4], Fq[35]); EA2(3, 0); EA2(3, 1); EA2(3, 2); EA2(3, 3);
    MF(acc[4], At[13], Fq[36]); FL(40, 33); EPK(3, 0); EPK(3, 1);
    MF(acc[5], At[5], Fq[36]); FL(41, 34); OST(3);
    MF(acc[4], At[14], Fq[37]); FL(42, 35); FLAG(C_TILE + 3 * 4); CHECK(4, fa + 32); ULD(4);
    MF(acc[5], At[6], Fq[37]); FL(43, 36); FLD(4); GLD(4); PEEK(5, fa + 48);
    MF(acc[4], At[15], Fq[38]); FL(44, 37); FL(45, 38);
    MF(acc[5], At[7], Fq[38]); FL(46, 39); FL(47, 40);
    MF(acc[4], At[16], Fq[39]);
    MF(acc[5], At[8], Fq[39]); EUN(4, 0); EUN(4, 1);
    MF(acc[5], At[9], Fq[40]); EUN(4, 2); EUN(4, 3);
    MF(acc[5], At[10], Fq[41]); EFA(4, 0); EA1(4, 0);
    MF(acc[5], At[11], Fq[42]); EA1(4, 1); EA1(4, 2);
    MF(acc[5], At[12], Fq[43]); EA1(4, 3); EFA(4, 1); EFA(4, 2); EFA(4, 3); CHECK(5, fa + 48); ULD(5);
    MF(acc[5], At[13], Fq[44]); EA2(4, 0); EA2(4, 1); EA2(4, 2); EA2(4, 3); FLD(5); GLD(5);
    MF(acc[5], At[14], Fq[45]); EPK(4, 0); EPK(4, 1);
    MF(acc[5], At[15], Fq[46]); OST(4); FLAG(C_TILE + 4 * 4);
    MF(acc[5], At[16], Fq[47]); EUN(5, 0); EUN(5, 1); EUN(5, 2); EUN(5, 3); EFA(5, 0); EFA(5, 1); EFA(5, 2); EFA(5, 3); EA1(5, 0); EA1(5, 1); EA1(5, 2); EA1(5, 3); EA2(5, 0); EA2(5, 1); EA2(5, 2); EA2(5, 3); EPK(5, 0); EPK(5, 1); OST(5); FLAG(C_TILE + 5 * 4);
    STAMP(8);
    ROLE_END();
  }
  {
    ROLE("c");
    __builtin_amdgcn_s_setprio(3);
    {
      u32 zz = 0u; OPQ(zz);
      if (lane < 2) *(u4v*)(smem + O_HB + 512 + lane * 528) = u4v{zz, zz, zz, zz};
    }
    u32* selw = (u32*)(smem + O_SEL);
    u32 tag;
    {
      const u32 pa = lbase + O_PART + (lane & 7) * 64;
      const u32 ib = 2047u - (u32)(cg * 8 + (lane & 7));
      f4 p0, p1, p2, p3; u32 cnt, ep; int n_ = 1 << 20; unsigned long long tk;
      asm volatile("1:\n\tds_read_b32 %4, %8\n\tds_read_b128 %0, %9\n\tds_read_b128 %1, %9 offset:16\n\tds_read_b128 %2, %9 offset:32\n\tds_read_b128 %3, %9 offset:48\n\t"
                   "ds_read_b32 %7, %10\n\ts_memrealtime %6\n\ts_sub_u32 %5, %5, 1\n\ts_cbranch_scc1 2f\n\ts_waitcnt lgkmcnt(0)\n\tv_cmp_gt_u32 vcc, 4, %4\n\ts_cbranch_vccnz 1b\n2:\n\ts_waitcnt lgkmcnt(0)"
                   : "=&v"(p0), "=&v"(p1), "=&v"(p2), "=&v"(p3), "=&v"(cnt), "+s"(n_), "=&s"(tk), "=&v"(ep) : "v"(CTLA(C_PART)), "v"(pa), "v"(lbase + O_XS) : "vcc", "scc", "memory");
      tag = (u32)__builtin_amdgcn_readfirstlane((int)ep) + 1u;
      tag += tag == 0u;
      const f4 s4_ = (p0 + p1) + (p2 + p3);
      const u32 rp = pk_bf(s4_[1] + s4_[3], s4_[0] + s4_[2]);
      const u32 key = ((rp ^ ((u32)((int)rp >> 31) | 0x80000000u)) & 0xffff0000u) | ib;
      const u32 kn = dpp_u<0xb1>(key);
      u32 m1 = max(key, kn), m2 = min(key, kn), m3;
      { const u32 o1 = dpp_u<0x4e>(m1), o2 = dpp_u<0x4e>(m2); const u32 lo = min(m1, o1), hb = max(m2, o2); m1 = max(m1, o1); m2 = max(lo, hb); m3 = min(lo, hb); }
      { const u32 o1 = dpp_u<0x141>(m1), o2 = dpp_u<0x141>(m2), o3 = dpp_u<0x141>(m3); const u32 lo = min(m1, o1), hb = max(m2, o2);
        m1 = max(m1, o1); m2 = max(lo, hb); m3 = max(min(lo, hb), max(m3, o3)); }
      const u32 wm = (((u32)tk + 8u) >> 2) & 0xffu;
      if (lane == 0) __builtin_amdgcn_raw_buffer_store_b128(u4v{m1, m2, (m3 & 0xffff0000u) | wm, tag}, wrs, WS_KEY + cg * 16, 0, 16);
      SB();
      STAMP(1);
      if (lane == 0) par[0] = tag;
      ARRIVE(C_GP);
      {
        const float gv = sigm(bflo(rp));
        const u32 g2 = pk_bf(gv, dpp_f<0xb1>(gv));
        u32 g3 = dpp_u<0x4e>(g2);
        OPQ(g3);
        if ((lane & 59) == 0) __builtin_amdgcn_raw_buffer_store_b128(u4v{g2, g3, tag, tag}, wrs, WS_REC + cg * 32 + lane * 4, 0, 16);
      }
      if ((lane & 57) == 0) __builtin_amdgcn_raw_buffer_store_b128(u4v{key, kn, 0u, tag}, wrs, WS_KALL + cg * 64 + lane * 8, 0, 16);
    }
    u32 l7 = (lane & 7) * 16, ko = WS_KEY + lane * 16;
    OPQ(l7); OPQ(ko);
    WAITQ(C_TL, 1);
    STAMP(5);
    const u32 tkey = (u32)__builtin_amdgcn_readfirstlane((int)par[1]) + DT_KEY;
    pace(tkey);
    u4v gr[4];
    u32 mykey;
    u4v xs[4];
    auto xrs = __builtin_amdgcn_make_buffer_rsrc((void*)(ka[0] + (xi & 3) * 512), 0, L * D * 2 - 1536, 0x00020000);
    asm volatile("" : "+s"(xrs));
    auto xs_ld = [&]() AI {
      const u32 vo = ((~mykey & 2047u) << 11) | l7;
#pragma unroll
      for (int k = 0; k < 4; ++k) xs[k] = __builtin_amdgcn_raw_buffer_load_b128(xrs, vo + k * 128, 0, 0);
    };
#pragma unroll
    for (int i = 0; i < 4; ++i) gr[i] = __builtin_amdgcn_raw_buffer_load_b128(wrs, ko + i * 1024, 0, 16);
#pragma unroll 1
    for (int spin = 0; spin < SPIN_MAX; ++spin) {
      const u32 bad = (gr[0][3] ^ tag) | (gr[1][3] ^ tag) | (gr[2][3] ^ tag) | (gr[3][3] ^ tag);
      if (__builtin_expect(!__any(bad != 0), 1)) break;
      STAMP(13);
      __builtin_amdgcn_s_sleep(SLEEP_AG1);
#pragma unroll
      for (int i = 0; i < 4; ++i)
        if (gr[i][3] != tag) gr[i] = __builtin_amdgcn_raw_buffer_load_b128(wrs, ko + i * 1024, 0, 16);
      asm volatile("" ::: "memory");
    }
    STAMP(2);
    {
      u32 m1 = gr[0][0], m2 = gr[0][1], m3 = 0, m4 = 0;
#pragma unroll
      for (int i = 2; i < 8; ++i) {
        const u32 k = gr[i >> 1][i & 1];
        if (i > 2) m4 = med3u(m3, m4, k);
        m3 = med3u(m2, m3, k);
        m2 = (i & 1) ? max(m2, k) : med3u(m1, m2, k);
        if (!(i & 1)) m1 = max(m1, k);
      }
      const u32 third = max(max(gr[0][2], gr[1][2]), max(gr[2][2], gr[3][2]));
      u32 W2;
      mykey = wave_top8(m1, m2, m3, m4, W2);
      xs_ld();
      SB();
      if (__builtin_expect(__any(m1 == 0u || (third | 0xffffu) > W2), 0)) {
        u4v ak[16];
#pragma unroll 1
        for (int spin = 0; spin < SPIN_MAX; ++spin) {
          u32 bad = 0;
#pragma unroll
          for (int i = 0; i < 16; ++i) {
            ak[i] = __builtin_amdgcn_raw_buffer_load_b128(wrs, WS_KALL + (i & 3) * 1024 + lane * 16, (i >> 2) * 4096, 16);
            bad |= ak[i][3] ^ tag;
          }
          if (!__any(bad != 0)) break;
          __builtin_amdgcn_s_sleep(8);
          asm volatile("" ::: "memory");
        }
        u32 prev = 0xffffffffu;
#pragma unroll 1
        for (int it = 0; it < 8; ++it) {
          u32 mm_ = 0;
#pragma unroll
          for (int i = 0; i < 32; ++i) {
            const u32 k = ak[i >> 1][i & 1];
            mm_ = max(mm_, k < prev ? k : 0u);
          }
          OPQ(mm_);
          prev = wave_max_u32(mm_);
          mykey = (lane >> 3) == it ? prev : mykey;
        }
        xs_ld();
      }
      OPQ(mykey);
    }
    SB();
    STAMP(3);
    if (l7 == 0) selw[lane >> 3] = mykey;
    {
      const u32 tn = now32();
      u32 yg = 0;
#pragma unroll
      for (int i = 0; i < 4; ++i) yg = max(yg, (gr[i][2] - (tn >> 2) - 1u) & 0xffu);
      const u32 tpub = tn - 4u * (255u - wave_max_u32(yg)) + DT_VIS;
      if (lane == 0) par[2] = later(tkey, tpub);
    }
    ARRIVE(C_SEL);
    if (b == 255 && lane == 0) __builtin_amdgcn_raw_buffer_store_b32(tag, wrs, WS_EPOCH, 0, 16);
    SB();
    {
      char* xp = smem + O_XS + (lane >> 3) * XS_PITCH + l7;
#pragma unroll
      for (int k = 0; k < 4; ++k) *(u4v*)(xp + k * 128) = xs[k];
      ARRIVE(C_XS);
      STAMP(6);
    }
    __builtin_amdgcn_s_setprio(2);
    auto ors = __builtin_amdgcn_make_buffer_rsrc((void*)kc[1], 0, L * D * 2, 0x00020000);
    const u32 rl = (lane + (xi & 15) * 4) & 63, un = (g * 4 + (lane & 3) + xi) & 15;
    u32 skip = 0;
#pragma unroll
    for (int k = 0; k < 8; ++k) {
      const u32 r = ~(u32)__builtin_amdgcn_readlane((int)mykey, 8 * k) & 2047u;
      skip |= (r & 63u) == rl ? 1u << (r >> 6) : 0u;
    }
    const char* op = smem + ((lane >> 2) & 3) * CHB + O_OUT + (un & 3) * 144 + (un >> 2) * 8;
    const u32 ob = rl * 2048 + cc * 8;
    const u32 tfast = (u32)__builtin_amdgcn_readfirstlane((int)par[2]) + DE_FAST;
#pragma unroll 1
    for (int t = 0; t < 8; ++t) {
      WAITA(CTLA(C_TILE) + t * 16 + (lane & 3) * 4);
      u2v yv[4];
#pragma unroll
      for (int e = 0; e < 4; ++e) yv[e] = tr_read(op + t * OT + e * 32);
      const bool slow = (int)(now32() - tfast) < 0;
#pragma unroll
      for (int e = 0; e < 4; ++e) {
        __builtin_amdgcn_raw_buffer_store_b64(yv[e], ors, ob | (((skip >> e) & 1u) << 30), t * 524288 + e * 131072, 0);
        if (slow) {
          SB(); __builtin_amdgcn_s_sleep(STSLP); SB();
          asm volatile("s_waitcnt vmcnt(%0)" :: "n"(STWIN - 1) : "memory");
        }
      }
      skip >>= 4;
    }
    STAMP(9);
    ROLE_END();
  }
  {
    ROLE("d");
        const int j = xg * 28 + xi;
    const int s1 = j >> 2, kq = j & 3;
    const bool own = xi < 28;
    const u64 wbase = kb[3];
    const int pitch = D * 2;
    auto hrs = __builtin_amdgcn_make_buffer_rsrc((void*)wbase, 0, own ? D * F * 2 : 0, 0x00020000);
    const int r8 = lane >> 3;
    const int vo = (64 * s1 * D + 256 * kq) * 2 + (8 * (r8 >> 2) + (r8 & 3)) * pitch + (((lane & 7) * 16) ^ (((r8 >> 1) & 3) * 32));
    auto dma = [&](int h, int sB, int pt, u32 dst) AI {
      const int m = h >> 1;
#pragma unroll
      for (int i = 0; i < 2; ++i)
        asm volatile("s_mov_b32 m0, %3\n\ts_nop 0\n\tbuffer_load_dwordx4 %0, %1, %2 offen nt lds"
                     :: "v"(vo), "s"(hrs), "s"((m & 1) * 32 * pt + (m >> 1) * sB + (16 * i + 4 * (h & 1)) * pt), "s"(dst + i * 1024) : "memory");
    };
    const int q = c16 >> 2, p = c16 & 3;
    int tro[4];
#pragma unroll
    for (int ct = 0; ct < 4; ++ct) tro[ct] = (4 * g + q) * 128 + ((ct ^ (((4 * g + q) >> 1) & 3)) * 32) + p * 8;
    const int tok = lane & 7;
    u4v wd[2][8];
    auto wd_ld = [&](int k, int cb) AI {
#pragma unroll
      for (int i = 0; i < 8; ++i) {
        const char* pp = smem + O_WD + cb * 8192 + (i >> 2) * 4096 + tro[i & 3];
        const u2v lo = tr_read(pp), hi = tr_read(pp + 2048);
        wd[k][i] = u4v{lo[0], lo[1], hi[0], hi[1]};
      }
    };
    u32 p2o, p2k[4], hlo, hhi;
    auto f2_setup = [&](int cb0) AI {
      p2o = WS_P2 + ((64 * kq + 16 * cb0 + (lane & 8) / 2 + g) * 56 + s1) * 128 + tok * 16;
      hlo = lbase + O_HB + ((lane & 8) ? 512 : g * 128 + tok * 16);
      hhi = lbase + O_HB + ((lane & 8) ? g * 128 + tok * 16 : 512);
#pragma unroll
      for (int i = 0; i < (cb0 == 2 ? 4 : 2); ++i) { p2k[i] = p2o + i * 8 * 7168; OPQ(p2k[i]); }
      OPQ(hlo); OPQ(hhi);
    };
    u4v hb[4];
    f4 r[4];
    auto hb_ld = [&]() AI {
      u32 cnt; int n_ = 1 << 20;
      asm volatile("1:\n\tds_read_b32 %4, %6\n\tds_read_b128 %0, %7\n\tds_read_b128 %1, %8\n\tds_read_b128 %2, %7 offset:528\n\tds_read_b128 %3, %8 offset:528\n\t"
                   "s_sub_u32 %5, %5, 1\n\ts_cbranch_scc1 2f\n\ts_waitcnt lgkmcnt(0)\n\tv_cmp_gt_u32 vcc, 2, %4\n\ts_cbranch_vccnz 1b\n2:\n\ts_waitcnt lgkmcnt(0)"
                   : "=&v"(hb[0]), "=&v"(hb[1]), "=&v"(hb[2]), "=&v"(hb[3]), "=&v"(cnt), "+s"(n_) : "v"(CTLA(C_HRDY)), "v"(hlo), "v"(hhi) : "vcc", "scc", "memory");
    };
    auto f2_mm = [&](int k) AI {
      const u4v* wp = wd[k];
      r[2 * k] = mma(wp[0], hb[0], f4{0.f, 0.f, 0.f, 0.f}); r[2 * k + 1] = mma(wp[2], hb[0], f4{0.f, 0.f, 0.f, 0.f});
      r[2 * k] = mma(wp[1], hb[1], r[2 * k]); r[2 * k + 1] = mma(wp[3], hb[1], r[2 * k + 1]);
      r[2 * k] = mma(wp[4], hb[2], r[2 * k]); r[2 * k + 1] = mma(wp[6], hb[2], r[2 * k + 1]);
      r[2 * k] = mma(wp[5], hb[3], r[2 * k]); r[2 * k + 1] = mma(wp[7], hb[3], r[2 * k + 1]);
    };
    auto f2_st = [&](int k, u32 t10) AI {
#pragma unroll
      for (int i = 2 * k; i < 2 * k + 2; ++i) {
        u4v v = __builtin_bit_cast(u4v, r[i]);
#pragma unroll
        for (int e = 0; e < 4; ++e) v[e] = __builtin_amdgcn_bitop3_b32(v[e], t10, TMASK, 0xd8);
        __builtin_amdgcn_raw_buffer_store_b128(v, wrs, p2k[i], 0, 16);
      }
    };
    {
      __builtin_amdgcn_s_setprio(3);
      const u32 ts = ts0 - 500u;
      u32 tl = ~0u;
      pace(later(ts0, now32() - DT_ARG - 15) + DT_HEAD);
      STAMP(3);
#pragma unroll 1
      for (int i = 0; i < 8 && tl >= 1500u; ++i) {
        const u2v hd = __builtin_amdgcn_raw_buffer_load_b64(wrs, WS_HEAD + (lane & 7) * 8, 0, 16);
        tl = wave_max_u32(hd[0] == ~hd[1] ? hd[0] - ts : ~0u);
        STAMP(14);
        asm volatile("" ::: "memory");
      }
      tl = tl < 1500u ? tl + ts : ts0;
      if (lane == 0) par[1] = tl;
      ARRIVE(C_TL);
      STAMP(2);
      if (own) {
        WAITS(C_WGU, 1);
#pragma unroll
        for (int h = 0; h < 16; ++h) {
          if (h == DSEL) WAITS(C_SEL, 1);
          if (h == DEV) WAITS(C_XL, 4);
          if (h >= DWIN) asm volatile("s_waitcnt vmcnt(%0)" :: "n"(2 * (DWIN - 1)) : "memory");
          if (h == 3 + DWIN) ARRIVE(C_WD);
          if (h == 7 + DWIN) ARRIVE(C_WD + 1);
          dma(h, 128, D * 2, lbase + O_WD + h * 2048);
        }
        f2_setup(2);
        const u32 t10 = par[0] & TMASK;
        asm volatile("s_waitcnt vmcnt(8)" ::: "memory");
        wd_ld(0, 2);
        const u32 hc = __hip_atomic_load(ctl + C_HRDY, __ATOMIC_RELAXED, __HIP_MEMORY_SCOPE_WORKGROUP);
        const bool early = __builtin_amdgcn_ballot_w64(hc < 2u) == 0ull;
        if (early) { hb_ld(); f2_mm(0); }
        asm volatile("s_waitcnt vmcnt(0)" ::: "memory");
        STAMP(5);
        wd_ld(1, 3);
        if (!early) { WAITS(C_HRDY, 1); hb_ld(); STAMP(11); f2_mm(0); }
        SB();
        f2_mm(1);
        f2_st(0, t10);
        SB();
        f2_st(1, t10);
        STAMP(12);
      }
      ROLE_END();
    }
  }
}

extern "C" void kernel_launch(void* const* d_in, const int* in_sizes, int n_in,
                              void* d_out, int out_size, void* d_ws, size_t ws_size,
                              hipStream_t stream) {
  k_main<<<256, 768, 0, stream>>>((const u16*)d_in[0], (const u16*)d_in[8], (const u16*)d_in[12],
                                  (const u16*)d_in[1], (const u16*)d_in[2], (const u16*)d_in[3],
                                  (const u16*)d_in[4], (const u16*)d_in[5], (const u16*)d_in[6],
                                  (const u16*)d_in[9], (const u16*)d_in[10], (const u16*)d_in[11],
                                  (char*)d_ws, (u16*)d_out);
}
```

```cpp
#include <hip/hip_runtime.h>
#include <stdint.h>

typedef unsigned short u16;
typedef unsigned u32;
typedef _Float16 h2 __attribute__((ext_vector_type(2)));
typedef _Float16 h8 __attribute__((ext_vector_type(8)));
typedef __bf16 b2 __attribute__((ext_vector_type(2)));
typedef __bf16 b8 __attribute__((ext_vector_type(8)));
typedef short s4 __attribute__((ext_vector_type(4)));
typedef float f2 __attribute__((ext_vector_type(2)));
typedef float f4 __attribute__((ext_vector_type(4)));
typedef u32 u2v __attribute__((ext_vector_type(2)));
typedef u32 u3v __attribute__((ext_vector_type(3)));
typedef u32 u4v __attribute__((ext_vector_type(4)));
typedef __attribute__((address_space(3))) s4* lds_s4p;

constexpr int L = 2048;
constexpr int D = 1024;
constexpr int NM = 128;
constexpr int F = 3584;
constexpr int NTAP = 1024;

constexpr int SEG_I = 576, SEG_O0 = 800, SEG_O1 = 880, SEG_SP = 896;

#define AI __attribute__((always_inline))
#define SB() __builtin_amdgcn_sched_barrier(0)
#define OPQ(x) asm volatile("" : "+v"(x))
#define DIAG_K 4
#define DIAG_M 96
#define STAMP0() u32 dg_ = 0; const bool dgw_ = (DIAG_M >> w) & 1; if (DIAG_K == 0 && dgw_) dg_ = (u32)kt[0] | 1u
#define STAMP(i) do { if (i == DIAG_K && dgw_) { SB(); dg_ = (u32)__builtin_amdgcn_s_memrealtime() | 1u; SB(); } } while (0)
#define STAMP_END() do { if (DIAG_K == 99 && dgw_) { asm volatile("s_waitcnt vmcnt(0)" ::: "memory"); dg_ = (u32)__builtin_amdgcn_s_memrealtime() | 1u; } if (dg_) { while ((int)((u32)__builtin_amdgcn_s_memrealtime() - (dg_ + 4000u)) < 0) __builtin_amdgcn_s_sleep(2); } } while (0)

#define STAMP_T0

__device__ __forceinline__ float bflo(u32 v) { return __uint_as_float(v << 16); }
__device__ __forceinline__ float bfhi(u32 v) { return __uint_as_float(v & 0xffff0000u); }
__device__ __forceinline__ u32 pk_bf(float a, float b) { f2 v = {a, b}; b2 r = __builtin_convertvector(v, b2); return __builtin_bit_cast(u32, r); }
__device__ __forceinline__ float rb1(float v) { return __uint_as_float(pk_bf(0.f, v)); }
__device__ __forceinline__ h8 as_h8(u4v v) { return __builtin_bit_cast(h8, v); }
__device__ __forceinline__ b8 as_b8(u4v v) { return __builtin_bit_cast(b8, v); }
__device__ __forceinline__ u2v tr_read(const char* p) {
  s4 t = __builtin_amdgcn_ds_read_tr16_b64_v4i16((lds_s4p)p);
  return __builtin_bit_cast(u2v, t);
}
template <int CTRL> __device__ __forceinline__ u32 dpp_u(u32 v) { return (u32)__builtin_amdgcn_update_dpp(0, (int)v, CTRL, 0xf, 0xf, false); }
template <int CTRL> __device__ __forceinline__ float dpp_f(float v) { return __uint_as_float(dpp_u<CTRL>(__float_as_uint(v))); }
__device__ __forceinline__ u32 wave_max_u32(u32 v) {
  v = max(v, dpp_u<0x111>(v)); v = max(v, dpp_u<0x112>(v)); v = max(v, dpp_u<0x114>(v)); v = max(v, dpp_u<0x118>(v));
  v = max(v, dpp_u<0x142>(v)); v = max(v, dpp_u<0x143>(v));
  return (u32)__builtin_amdgcn_readlane((int)v, 63);
}
#define DPM " row_mask:0xf bank_mask:0xf bound_ctrl:1\n\t"
#define T2S(HO, HI, LI, C) "v_max_u32_dpp " HO ", " HI ", " HI " " C DPM "v_min_u32_dpp %8, " HI ", " HI " " C DPM "v_max_u32_dpp %7, " LI ", " LI " " C DPM "v_max_u32 %7, %7, %8\n\t"
#define T2R(RM, REST) T2S("%5", "%0", "%1", "row_shr:1") T2S("%6", "%5", "%7", "row_shr:2") T2S("%5", "%6", "%7", "row_shr:4") T2S("%6", "%5", "%7", "row_shr:8") \
    T2S("%5", "%6", "%7", "row_bcast:15") T2S("%6", "%5", "%7", "row_bcast:31") \
    "v_readlane_b32 %9, %6, 63\n\tv_readlane_b32 %10, %7, 63\n\ts_nop 0\n\tv_mov_b32 %5, %9\n\tv_cmp_le_u32 %11, %10, %0\n\tv_cmp_le_u32 %12, %10, %1\n\tv_mov_b32 %6, %10\n\t" \
    "v_cndmask_b32 %0, %0, %1, %11\n\tv_mov_b32_dpp %4, %5 quad_perm:[0,1,2,3] row_mask:" RM " bank_mask:0x3\n\tv_cndmask_b32 %0, %0, %2, %12\n\t" \
    "v_mov_b32_dpp %4, %6 quad_perm:[0,1,2,3] row_mask:" RM " bank_mask:0xc\n\t" REST
#define T2P "v_cndmask_b32 %1, %1, %2, %11\n\tv_cndmask_b32 %2, %2, %3, %11\n\tv_cndmask_b32 %1, %1, %3, %12\n\tv_cndmask_b32 %3, %3, 0, %11\n\tv_cndmask_b32 %2, %2, 0, %12\n\t"
__device__ __forceinline__ u32 wave_top8(u32& m1, u32 m2, u32 m3, u32 m4, u32& w2) {
  u32 key, ta, tb, lo, t, w1;
  unsigned long long c1, c2;
  asm("s_nop 1\n\t" T2R("0x1", T2P) T2R("0x2", T2P) T2R("0x4", T2P) T2R("0x8", "") "s_nop 0"
      : "+v"(m1), "+v"(m2), "+v"(m3), "+v"(m4), "=&v"(key), "=&v"(ta), "=&v"(tb), "=&v"(lo), "=&v"(t), "=&s"(w1), "=&s"(w2), "=&s"(c1), "=&s"(c2));
  return key;
}

__device__ __forceinline__ float exp_bf(float v) {
  const float e = __builtin_amdgcn_exp2f(v * 1.44268798828125f);
  return fmaf(e, v * (7.0526077e-06f * 0.69314718f), e);
}
struct cf { f2 r, i; };
struct ch { h2 r, i; };
__device__ __forceinline__ cf cmul(cf a, cf b) { cf c; c.r = a.r * b.r - a.i * b.i; c.i = a.r * b.i + a.i * b.r; return c; }
__device__ __forceinline__ ch hmul(ch a, ch b) {
  ch c;
  asm("v_pk_mul_f16 %0, %2, %4\n\tv_pk_mul_f16 %1, %2, %5\n\tv_pk_fma_f16 %0, %3, %5, %0 neg_lo:[1,0,0] neg_hi:[1,0,0]\n\tv_pk_fma_f16 %1, %3, %4, %1"
      : "=&v"(c.r), "=&v"(c.i) : "v"(a.r), "v"(a.i), "v"(b.r), "v"(b.i));
  return c;
}
__device__ __forceinline__ cf csq(cf a) { cf c; c.r = a.r * a.r - a.i * a.i; c.i = (a.r + a.r) * a.i; return c; }
__device__ __forceinline__ ch toh(cf a) { ch c; c.r = __builtin_convertvector(a.r, h2); c.i = __builtin_convertvector(a.i, h2); return c; }
__device__ __forceinline__ ch tohc(cf a) { ch c; c.r = __builtin_convertvector(a.r, h2); c.i = __builtin_convertvector(-a.i, h2); return c; }
__device__ __forceinline__ h2 ash(u32 v) { return __builtin_bit_cast(h2, v); }
__device__ __forceinline__ cf cexp_p(f2 a2, f2 brev, float p) {
  f2 e = a2 * p, t = brev * p;
  f2 mag = {__builtin_amdgcn_exp2f(e[0]), __builtin_amdgcn_exp2f(e[1])};
  float r0 = __builtin_amdgcn_fractf(t[0]), r1 = __builtin_amdgcn_fractf(t[1]);
  cf c;
  c.r = mag * f2{__builtin_amdgcn_cosf(r0), __builtin_amdgcn_cosf(r1)};
  c.i = mag * f2{__builtin_amdgcn_sinf(r0), __builtin_amdgcn_sinf(r1)};
  return c;
}
__device__ __forceinline__ u32 med3u(u32 a, u32 b, u32 c) { u32 r; asm("v_med3_u32 %0, %1, %2, %3" : "=v"(r) : "v"(a), "v"(b), "v"(c)); return r; }
__device__ __forceinline__ f2 unbf(u32 v) { return f2{bflo(v), bfhi(v)}; }
__device__ __forceinline__ float sigm(float v) { return __builtin_amdgcn_rcpf(1.f + __builtin_amdgcn_exp2f(-1.44269504088896341f * v)); }

#define TS2(A, B, C, D) "v_xor_b32 %5, " A ", %9\n\tv_xor_b32 %6, " B ", %9\n\tv_xor_b32 %7, " C ", %9\n\tv_xor_b32 %8, " D ", %9\n\t" \
    "v_fma_f32 %1, %5, 1.0, %1\n\tv_fma_f32 %2, %6, 1.0, %2\n\tv_or3_b32 %0, %0, %5, %6\n\tv_fma_f32 %3, %7, 1.0, %3\n\tv_fma_f32 %4, %8, 1.0, %4\n\tv_or3_b32 %0, %0, %7, %8\n\t"
#define TSR TS2("%10", "%11", "%12", "%13") TS2("%14", "%15", "%16", "%17") TS2("%18", "%19", "%20", "%21")
#define TSI "=&v"(t0), "=&v"(t1), "=&v"(t2), "=&v"(t3) : "v"(t10), "v"(p[0]), "v"(p[1]), "v"(p[2]), "v"(p[3]), "v"(q[0]), "v"(q[1]), "v"(q[2]), "v"(q[3]), \
    "v"(r[0]), "v"(r[1]), "v"(r[2]), "v"(r[3])
__device__ __forceinline__ f4 tsum4(u32& bad, u32 t10, u4v s, u4v p, u4v q, u4v r) {
  float s0, s1, s2, s3; u32 t0, t1, t2, t3;
  asm volatile("v_xor_b32 %1, %22, %9\n\tv_xor_b32 %2, %23, %9\n\tv_xor_b32 %3, %24, %9\n\tv_xor_b32 %4, %25, %9\n\tv_or3_b32 %0, %0, %1, %2\n\tv_or3_b32 %0, %0, %3, %4\n\t" TSR
               : "+v"(bad), "=&v"(s0), "=&v"(s1), "=&v"(s2), "=&v"(s3), TSI, "v"(s[0]), "v"(s[1]), "v"(s[2]), "v"(s[3]));
  return f4{s0, s1, s2, s3};
}
__device__ __forceinline__ f4 tsum3(u32& bad, u32 t10, f4 s, u4v p, u4v q, u4v r) {
  float s0 = s[0], s1 = s[1], s2 = s[2], s3 = s[3]; u32 t0, t1, t2, t3;
  asm volatile(TSR : "+v"(bad), "+v"(s0), "+v"(s1), "+v"(s2), "+v"(s3), TSI);
  return f4{s0, s1, s2, s3};
}
__device__ __forceinline__ void silu4(f4& g, float nl2e) {
  float g0 = g[0], g1 = g[1], g2 = g[2], g3 = g[3], t0, t1, t2, t3;
  asm volatile("v_fma_f32 %4, %0, %8, 0\n\tv_fma_f32 %5, %1, %8, 0\n\tv_fma_f32 %6, %2, %8, 0\n\tv_fma_f32 %7, %3, %8, 0\n\t"
               "v_exp_f32 %4, %4\n\tv_exp_f32 %5, %5\n\tv_exp_f32 %6, %6\n\tv_exp_f32 %7, %7\n\t"
               "v_fma_f32 %4, %4, 1.0, 1.0\n\tv_fma_f32 %5, %5, 1.0, 1.0\n\tv_fma_f32 %6, %6, 1.0, 1.0\n\tv_fma_f32 %7, %7, 1.0, 1.0\n\t"
               "v_rcp_f32 %4, %4\n\tv_rcp_f32 %5, %5\n\tv_rcp_f32 %6, %6\n\tv_rcp_f32 %7, %7\n\t"
               "v_fma_f32 %0, %0, %4, 0\n\tv_fma_f32 %1, %1, %5, 0\n\tv_fma_f32 %2, %2, %6, 0\n\tv_fma_f32 %3, %3, %7, 0"
               : "+v"(g0), "+v"(g1), "+v"(g2), "+v"(g3), "=&v"(t0), "=&v"(t1), "=&v"(t2), "=&v"(t3) : "v"(nl2e));
  g = f4{g0, g1, g2, g3};
}

constexpr int KT_PITCH = 52;
constexpr int kt_off(int p) {
  int i = 20, j = 0;
  if (p < 256) { i = p & 7; j = p >> 3; }
  else if (p < 512) { int q = (p - 256) >> 1; i = 8 + (q & 3); j = q >> 2; }
  else if (p < 1024) { int q = (p - 512) >> 2; i = 12 + (q & 1) + 2 * (q >> 6); j = (q >> 1) & 31; }
  else if (p < 2048) { int q = (p - 1024) >> 3; i = 16 + (q >> 5); j = q & 31; }
  return j * KT_PITCH + i * 2;
}
constexpr int KT_ZERO = 32 * KT_PITCH + 16;
constexpr int rnd_lag(int m) {
  const int st = m < 256 ? 1 : m < 512 ? 2 : m < 1024 ? 4 : 8;
  int q = m / st;
  const int rem = m - q * st;
  if (2 * rem > st || (2 * rem == st && (q & 1))) ++q;
  return q * st;
}
constexpr int tap_entry(int e) {
  if (e < 544) {
    if (543 - e <= 525) return kt_off(rnd_lag(543 - e));
  } else if (e >= 560 && e < 752) {
    const int q = (e - 560) / 48, x = (e - 560) - q * 48;
    const int p = 512 + 16 * (31 - x) + 4 * (q + 1);
    if (x < 32 && p > 524) return kt_off(p);
  } else if (e >= 752 && e < 816) {
    return kt_off(1032 + 16 * (815 - e));
  } else if (e >= 832 && e < 896) {
    return kt_off(1040 + 16 * (895 - e));
  } else if (e == SEG_SP + 27 || e == SEG_SP + 28) {
    return kt_off(1024);
  }
  return KT_ZERO;
}
struct TapIdx { u16 v[NTAP]; };
constexpr TapIdx make_tapidx() {
  TapIdx t{};
  for (int e = 0; e < NTAP; ++e) t.v[e] = (u16)tap_entry(2 * ((e >> 4) + 64 * ((e >> 1) & 7)) + (e & 1));
  return t;
}
__device__ __attribute__((aligned(16))) const TapIdx g_tapidx = make_tapidx();

constexpr int TROW = 544;
constexpr int O_OUT = 0;
constexpr int OT = 576;
constexpr int O_U = O_OUT + 8 * OT;
constexpr int O_TAP = O_U + 4608;
constexpr int TAP_PITCH = 2 * NTAP + 64;
constexpr int CHB = O_TAP + 16 * TROW + 32;
static_assert(16 * TROW >= 4 * TAP_PITCH, "lds");
static_assert((CHB / 4) % 32 == 8, "banks");
#define DEV 3
#define DWIN 8
#define DSEL 0
constexpr int NSLOT = 5, WWIN = 4;
constexpr int O_GT = 4 * CHB;
constexpr int O_RING = O_GT + 8192;
constexpr int O_WD = O_RING + 2 * NSLOT * 2048;
constexpr int O_FAR = O_WD + 32768;
constexpr int O_XS = O_FAR + 16384;
constexpr int XS_PITCH = 544;
constexpr int O_HB = O_XS + 8 * XS_PITCH;
constexpr int O_PART = O_HB + 2 * 528;
constexpr int O_SEL = O_PART + 512;
constexpr int O_PAR = O_SEL + 32;
constexpr int O_CTL = O_PAR + 16;
constexpr int O_JUNK = O_CTL + 512;
constexpr int K_LDS = O_JUNK + 1024;
static_assert(DWIN == 8, "flags of the down tile");
static_assert(K_LDS <= 163840 && O_RING % 16 == 0, "lds");
enum { C_PART = 0, C_GP, C_PUB, C_SEL, C_XS, C_HRDY, C_WGU, C_TL, C_XL, C_WD = 10, C_TAPS = 12, C_Q = 16, C_FAR = 32, C_TILE = 48 };

#define CTLA(word) (lbase + O_CTL + (word) * 4)
#define ARRIVE(word) asm volatile("s_mov_b64 exec, 1\n\tds_add_u32 %0, %1\n\ts_mov_b64 exec, -1" :: "v"(CTLA(word)), "v"(1) : "memory")
#define WAITQ(word, target) do { u32 t_; int n_ = 1 << 20; asm volatile( \
    "1:\n\tds_read_b32 %0, %2\n\ts_sub_u32 %1, %1, 1\n\ts_cbranch_scc1 2f\n\ts_waitcnt lgkmcnt(0)\n\tv_cmp_gt_u32 vcc, %3, %0\n\ts_cbranch_vccnz 1b\n2:\n\ts_waitcnt lgkmcnt(0)" \
    : "=&v"(t_), "+s"(n_) : "v"(CTLA(word)), "n"(target) : "vcc", "scc", "memory"); } while (0)
#define WAITS(word, target) do { u32 t_; int n_ = 1 << 20; asm volatile( \
    "1:\n\tds_read_b32 %0, %2\n\ts_sub_u32 %1, %1, 1\n\ts_cbranch_scc1 2f\n\ts_waitcnt lgkmcnt(0)\n\tv_cmp_le_u32 vcc, %3, %0\n\ts_cbranch_vccnz 2f\n\ts_sleep 2\n\ts_branch 1b\n2:\n\ts_waitcnt lgkmcnt(0)" \
    : "=&v"(t_), "+s"(n_) : "v"(CTLA(word)), "n"(target) : "vcc", "scc", "memory"); } while (0)
#define BAR() asm volatile("s_waitcnt lgkmcnt(0)\n\ts_barrier" ::: "memory")
__device__ __forceinline__ u2v lds8(const char* p) { return __builtin_bit_cast(u2v, __hip_atomic_load((const unsigned long long*)p, __ATOMIC_RELAXED, __HIP_MEMORY_SCOPE_WORKGROUP)); }

constexpr int WS_EPOCH = 0;
constexpr int WS_HEAD = 1024;
constexpr int WS_REC = 4096;
constexpr int WS_FLG = 40960;
constexpr int WS_KALL = WS_REC + 8192;
constexpr int WS_KEY = 32768;
constexpr int WS_P1 = 65536;
constexpr int WS_P2 = WS_P1 + 56 * 16384;
constexpr int WS_XT = WS_P2 + 256 * 7168;
constexpr int XT_PITCH = 33280;
constexpr int WS_END = WS_XT + 128 * XT_PITCH;
constexpr int SPIN_MAX = 1 << 16;
constexpr u32 TMASK = 0x3ffu;

#define DT_HEAD 110
#define DT_ARG 45
#define WDELAY 60
#define DT_KEY 225
#define DT_REC 255
#define DT_FLG 365
#define DT_VIS 40
#define DE_P1 385
#define DE_P2 620
#define DE_FAST 605
#define DT_MIN 25
#define DT_P1 100
#define DT_P2 130
#define SLEEP_AG1 12
#define SLEEP_G 6
#define STSLP 2
#define STWIN 4
#define H2_AT 9
#define CODE_B 28400

#define ROLE_END() do { STAMP_END(); asm volatile("s_endpgm" ::: "memory"); } while (0)
__device__ __forceinline__ u32 now32() { return (u32)__builtin_amdgcn_s_memrealtime(); }
__device__ __forceinline__ u32 later(u32 a, u32 b) { return (int)(b - a) > 0 ? b : a; }
__device__ __forceinline__ void pace(u32 t) {
#pragma unroll 1
  for (int i = 0; i < 1024 && (int)(now32() - t) < 0; ++i) __builtin_amdgcn_s_sleep(1);
}
__device__ __forceinline__ void pace2(u32 n, u32 t, int dmax) {
  int d = (int)(t - n);
  d = d < DT_MIN ? DT_MIN : (d > dmax ? dmax : d);
  pace(n + (u32)d);
}

#define ALL_OTHER "vcc", "scc", "memory", \
    "s0","s1","s2","s5","s6","s7","s33","s34","s35","s44","s45","s46","s47","s48","s49","s50","s51","s52","s53","s54","s55","s56","s57","s58","s59", \
    "s60","s61","s62","s63","s64","s65","s66","s67","s68","s69","s70","s71","s72","s73","s74","s75","s76","s77","s78","s79","s80","s81","s82","s83","s84", \
    "s85","s86","s87","s88","s89","s90","s91","s92","s93","s94","s95","s96","s97","s98","s99", \
    "v0","v1","v2","v3","v4","v5","v6","v7","v8","v9","v10","v11","v12","v13","v14","v15","v16","v17","v18","v19","v20","v21","v22","v23","v24","v25", \
    "v26","v27","v28","v29","v30","v31","v32","v33","v34","v35","v36","v37","v38","v39","v40","v41","v42","v43","v44","v45","v46","v47","v48","v49","v50", \
    "v51","v52","v53","v54","v55","v56","v57","v58","v59","v60","v61","v62","v63","v64","v65","v66","v67","v68","v69","v70","v71","v72","v73","v74","v75", \
    "v76","v77","v78","v79","v80","v81","v82","v83","v84","v85","v86","v87","v88","v89","v90","v91","v92","v93","v94","v95","v96","v97","v98","v99", \
    "v100","v101","v102","v103","v104","v105","v106","v107","v108","v109","v110","v111","v112","v113","v114","v115","v116","v117","v118","v119","v120", \
    "v121","v122","v123","v124","v125","v126","v127","v128","v129","v130","v131","v132","v133","v134","v135","v136","v137","v138","v139","v140","v141", \
    "v142","v143","v144","v145","v146","v147","v148","v149","v150","v151","v152","v153","v154","v155","v156","v157","v158","v159","v160","v161","v162", \
    "v163","v164","v165","v166","v167"
typedef __attribute__((address_space(1))) const u16* gp16;
typedef __attribute__((address_space(1))) const u32* gp32;
typedef __attribute__((address_space(1))) const u4v* gp128;
typedef unsigned long long u64;
typedef u64 q2 __attribute__((ext_vector_type(2)));
typedef u64 q4 __attribute__((ext_vector_type(4)));
typedef u64 q8 __attribute__((ext_vector_type(8)));

__global__ void __launch_bounds__(768, 3) k_main(
    const u16* x_, const u16* w_r_, const u16* gate_w_, const u16* logA_, const u16* Aim_, const u16* Cre_, const u16* Cim_,
    const u16* log_dt_, const u16* Dskip_, const u16* w_gate_, const u16* w_up_, const u16* w_down_, char* wsp_, u16* out_) {
  __shared__ __attribute__((aligned(256))) char smem[K_LDS];
  const u32 lbase = (u32)(uintptr_t)(__attribute__((address_space(3))) char*)smem;
#define XDMA(OFF) "global_load_lds_dwordx4 v1, s[8:9] offset:" #OFF " nt\n\ts_addk_i32 m0, 896\n\t"
  asm volatile("s_load_dwordx16 s[8:23], %0, 0\n\tv_readfirstlane_b32 s3, %2\n\ts_load_dwordx8 s[24:31], %0, 64\n\ts_load_dwordx4 s[36:39], %0, 96\n\t"
               "s_memrealtime s[40:41]\n\t" STAMP_T0 "s_mov_b32 s4, %1\n\ts_lshr_b32 s3, s3, 6\n\t"
               "s_cmp_lt_u32 s3, 4\n\ts_cbranch_scc1 .Lrows\n\ts_barrier\n\t"
               "s_cmp_ge_u32 s3, 8\n\ts_cbranch_scc1 .Lrole_g\n\ts_cmp_eq_u32 s3, 7\n\ts_cbranch_scc1 .Lrole_d\n\t"
               "s_cmp_ge_u32 s3, 5\n\ts_cbranch_scc1 .Lrole_h\n\ts_branch .Lrole_c\n.Lrows:\n\t"
               "s_lshr_b32 s5, s4, 3\n\tv_lshlrev_b32 v3, 2, %2\n\ts_mov_b32 m0, %4\n\tv_lshl_or_b32 v3, s5, 10, v3\n\ts_getpc_b64 s[6:7]\n\t"
               "v_min_u32 v3, %3, v3\n\tv_lshrrev_b32 v1, 3, %2\n\tglobal_load_lds_dword v3, s[6:7]\n\t"
               "v_and_b32 v2, 6, v1\n\ts_and_b32 s34, s4, 7\n\tv_xor_b32 v2, v2, %2\n\ts_lshl_b32 s34, s34, 5\n\tv_and_b32 v2, 7, v2\n\ts_add_u32 s5, s5, s34\n\t"
               "v_and_b32 v1, 7, v1\n\ts_lshl_b32 s5, s5, 5\n\tv_lshlrev_b32 v1, 11, v1\n\ts_add_u32 s5, s5, s3\n\tv_lshl_or_b32 v1, v2, 4, v1\n\t"
               "s_mul_i32 s34, s3, %5\n\tv_lshl_add_u32 v1, s5, 9, v1\n\ts_add_u32 m0, s34, %6\n\tv_and_b32 v2, 31, %2\n\ts_waitcnt lgkmcnt(0)\n\t"
               XDMA(0) "s_lshl_b32 s5, s3, 5\n\t" XDMA(128) "v_add_lshl_u32 v2, s5, v2, 4\n\t" XDMA(256) "s_lshl_b32 s5, s3, 10\n\t"
               "global_load_lds_dwordx4 v1, s[8:9] offset:384 nt\n\ts_add_u32 m0, s5, %7\n\ts_mov_b32 exec_hi, 0\n\t"
               "global_load_lds_dwordx4 v2, s[10:11]\n\ts_addk_i32 m0, 512\n\ts_nop 0\n\tglobal_load_lds_dwordx4 v2, s[12:13]\n\t"
               "s_mov_b32 m0, %8\n\tv_mov_b32 v3, 0\n\tglobal_load_lds_dword v3, s[36:37]\n\ts_mov_b32 exec_hi, -1"
               :: "s"(__builtin_amdgcn_kernarg_segment_ptr()), "s"(blockIdx.x), "v"(threadIdx.x), "n"(CODE_B - 256), "n"(O_JUNK), "n"(CHB), "n"(O_U + 512), "n"(O_FAR), "n"(O_XS)
               : "s3", "s4", "s5", "s6", "s7", "s8", "s9", "s10", "s11", "s12", "s13", "s14", "s15", "s16", "s17", "s18", "s19", "s20", "s21", "s22", "s23",
                 "s24", "s25", "s26", "s27", "s28", "s29", "s30", "s31", "s34", "s36", "s37", "s38", "s39", "s40", "s41", "s42", "s43", "v1", "v2", "v3", "scc", "memory");
  int lane, c16, g, w, b, xg, xi, cg, cc, c, d;
  char* ch_;
  q8 ka; q4 kb; q2 kc, kt;
  u32* ctl = (u32*)(smem + O_CTL);
  u32* par = (u32*)(smem + O_PAR);
#define IDS() do { int z_ = 0; OPQ(z_); lane = __builtin_amdgcn_mbcnt_hi(~0u, __builtin_amdgcn_mbcnt_lo(~0u, z_)); c16 = lane & 15; g = lane >> 4; } while (0)
#define ROLE(name) \
  asm volatile(".Lrole_" name ":\n\ts_waitcnt lgkmcnt(0)" : "={s[8:23]}"(ka), "={s[24:31]}"(kb), "={s[36:39]}"(kc), "={s[40:43]}"(kt), "={s3}"(w), "={s4}"(b) :: ALL_OTHER); \
  if (__builtin_expect(w > 11, 0)) asm volatile("s_endpgm" ::: "memory");        \
  IDS(); \
  xg = b & 7; xi = b >> 3;                          \
  cg = xg * 32 + xi;                                \
  cc = (xi >> 4) * 128 + xg * 16 + (xi & 15);       \
  c = w & 3; ch_ = smem + c * CHB; d = cc * 4 + c; \
  const u32 ts0 = (u32)kt[0]; \
  auto wrs = __builtin_amdgcn_make_buffer_rsrc((void*)kc[0], 0, WS_END, 0x00020000); \
  STAMP0()

  const char* tapb;
  const char* ub;
  char* erp;
  const char* eup;
  const char* egp;
  u4v At[17], band[4];
  float dm1;
  f4 acc[8];
  auto conv_setup = [&]() AI {
    tapb = ch_ + O_TAP + ((15 - c16) & 3) * TAP_PITCH + ((15 - c16) & ~3) * 2;
    ub = ch_ + O_U - 32 + c16 * 32 + g * 16;
    erp = ch_ + O_OUT + g * 144 + c16 * 8;
    eup = ch_ + O_U + 512 + c16 * 32 + g * 8;
    egp = smem + O_GT + lane * 16;
  };
  auto band_setup = [&]() AI {
#pragma unroll
    for (int k = 0; k < 4; ++k) {
      const int ctr = k == 0 ? 12 : 8, hw = k + 1;
      const u32 m = ((1u << (2 * hw + 1)) - 1u) << (ctr - hw);
      const u32 bits = (m << c16) >> (8 * g);
#pragma unroll
      for (int j = 0; j < 4; ++j)
        band[k][j] = (__builtin_amdgcn_sbfe(bits, 2 * j, 1) & 0x3f80u) | (__builtin_amdgcn_sbfe(bits, 2 * j + 1, 1) & 0x3f800000u);
      OPQ(band[k]);
    }
  };
  auto frag2 = [&](const char* pa, const char* pb) AI { const u2v lo = lds8(pa), hi = lds8(pb); return u4v{lo[0], lo[1], hi[0], hi[1]}; };
  auto atload = [&](int s0, int s1) AI {
#pragma unroll
    for (int s = s0; s < s1; ++s) At[s] = frag2(tapb + 64 * s + g * 16, tapb + 64 * s + g * 16 + 8);
  };
  auto bin = [&](int pair, int be) AI { const char* p = tapb + g * 8 + (SEG_I + 96 * pair - 16 * be) * 2; return frag2(p, p + 96); };
  auto bout = [&](int be) AI { const char* p = tapb + g * 8 + (SEG_O0 - 16 * be) * 2; return frag2(p, p + (SEG_O1 - SEG_O0) * 2); };
  auto asp = [&]() AI { return frag2(tapb + SEG_SP * 2 + g * 16, tapb + SEG_SP * 2 + g * 16 + 8); };
  auto mma = [&](u4v a, u4v bq, f4 cin) AI { return __builtin_amdgcn_mfma_f32_16x16x32_bf16(as_b8(a), as_b8(bq), cin, 0, 0, 0); };
  auto uld = [&](int sg) AI { return *(const u4v*)(ub + 512 + sg * 64); };
  auto uld24 = [&](int un) AI { return *(const u4v*)(ub + 512 + un * 512 - 16); };
  u2v euw[8], eo[8]; f4 egv[8], efv[8];
  float eu[8][4], ev[8][4], ea[8][4];
  u4v Fq[57], ru[6], wa_[6][3];
  f4 wq[6][6];
  u32 pk[12], one, qa, fa;
  char* tfp; char* fp_;
#define LOPQ(p_) do { u32 a_ = (u32)(uintptr_t)(__attribute__((address_space(3))) const char*)p_; OPQ(a_); p_ = (decltype(p_))(__attribute__((address_space(3))) char*)(uintptr_t)a_; } while (0)
  auto m_setup = [&]() AI {
    tfp = smem + (lane == 0 ? O_CTL + c * 4 : O_JUNK + lane * 4);
    fp_ = smem + O_FAR + c * 4096 + lane * 16;
    qa = CTLA(C_Q) + (lane & 3) * 4; fa = CTLA(C_FAR + c); one = 1u;
    OPQ(qa); OPQ(fa); OPQ(one);
    LOPQ(tfp); LOPQ(fp_); LOPQ(erp); LOPQ(eup); LOPQ(egp); LOPQ(ub);
  };
#define WAITA(addr) do { u32 t_; int n_ = 1 << 20; asm volatile( \
    "1:\n\tds_read_b32 %0, %2\n\ts_sub_u32 %1, %1, 1\n\ts_cbranch_scc1 2f\n\ts_waitcnt lgkmcnt(0)\n\tv_cmp_eq_u32 vcc, 0, %0\n\ts_cbranch_vccnz 1b\n2:\n\ts_waitcnt lgkmcnt(0)" \
    : "=&v"(t_), "+s"(n_) : "v"(addr) : "vcc", "scc", "memory"); } while (0)
#define MF(d_, a_, b_) asm volatile("v_mfma_f32_16x16x32_bf16 %0, %1, %2, %0" : "+v"(d_) : "v"(a_), "v"(b_)); SB()
#define MF0(d_, a_, b_) asm volatile("v_mfma_f32_16x16x32_bf16 %0, %1, %2, 0" : "=&v"(d_) : "v"(a_), "v"(b_)); SB()
#define NOP(n_) asm volatile("s_nop " #n_); SB()
#define FL(i_, sg_) Fq[i_] = uld(sg_); SB()
#define RL(un_) ru[un_] = uld24(un_); SB()
#define AL(s_) atload(s_, s_ + 1); SB()
#define ULD(t_) euw[t_] = *(const u2v*)(eup + t_ * 512); SB()
#define GLD(t_) egv[t_] = *(const f4*)(egp + t_ * 1024); SB()
#define FLD(t_) efv[t_] = *(const f4*)(fp_ + (t_ - 2) * 1024); SB()
#define FST(un_) *(f4*)(fp_ + un_ * 1024) = acc[un_ + 2]; SB()
#define EUN(t_, e_) eu[t_][e_] = (e_ & 1) ? bfhi(euw[t_][e_ >> 1]) : bflo(euw[t_][e_ >> 1]); OPQ(eu[t_][e_]); ev[t_][e_] = eu[t_][e_]; SB()
#define EA1(t_, e_) ea[t_][e_] = fmaf(eu[t_][e_], dm1, acc[t_][e_]); OPQ(ea[t_][e_]); SB()
#define EFA(t_, e_) ev[t_][e_] = fmaf(egv[t_][e_], efv[t_][e_], eu[t_][e_]); OPQ(ev[t_][e_]); SB()
#define EA2(t_, e_) ea[t_][e_] = fmaf(egv[t_][e_], ea[t_][e_], ev[t_][e_]); OPQ(ea[t_][e_]); SB()
#define EPK(t_, k_) { u32 v_ = pk_bf(ea[t_][2 * k_], ea[t_][2 * k_ + 1]); OPQ(v_); eo[t_][k_] = v_; } SB()
#define OST(t_) *(u2v*)(erp + t_ * OT) = eo[t_]; SB()
#define WPK(un_, p_, j_) { u32 v_ = pk_bf(wq[un_][2 * p_ + j_ / 2][2 * (j_ & 1)], wq[un_][2 * p_ + j_ / 2][2 * (j_ & 1) + 1]); OPQ(v_); wa_[un_][p_][j_] = v_; } SB()
#define FLAG(w_) asm volatile("" ::: "memory"); *(u32*)(tfp + (w_) * 4) = one; SB()
#define PEEK(i_, a_) pk[i_] = __hip_atomic_load((__attribute__((address_space(3))) u32*)(uintptr_t)(a_), __ATOMIC_RELAXED, __HIP_MEMORY_SCOPE_WORKGROUP); SB()
#define CHECK(i_, a_) if (__builtin_expect(__builtin_amdgcn_ballot_w64(pk[i_] == 0u) != 0ull, 0)) WAITA(a_); asm volatile("" ::: "memory"); SB()
#define MIX2(nm, nd, nv) _Pragma("unroll") for (int r_ = 0; r_ < (nm); ++r_) { __builtin_amdgcn_sched_group_barrier(0x008, 1, 0); \
    if (nd) __builtin_amdgcn_sched_group_barrier(0x100, (nd), 0); if (nv) __builtin_amdgcn_sched_group_barrier(0x002, (nv), 0); }

  {
    ROLE("x");
    const int xw = w;
    char* img = ch_ + O_U + 512;
    STAMP(11);
    __builtin_amdgcn_s_setprio(2);
    { u32 zz = 0u; OPQ(zz); if (lane < 32) ctl[32 * xw + lane] = zz; }
    if (xi == 31 && w == 0) { const u32 th = later(ts0, now32() - DT_ARG); if (lane == 0) __builtin_amdgcn_raw_buffer_store_b64(u2v{th, ~th}, wrs, WS_HEAD + xg * 8, 0, 16); }
    BAR();
    STAMP(12);
    const char* wp; const char* fp; const char* tp0; const char* tp1; char* pp;
    {
      const int r = lane & 7, q = (lane >> 2) & 3;
      fp = img + r * 128 + ((((lane & 8) / 2 + g) ^ (r & 6)) * 16);
      wp = smem + O_FAR + xw * 1024 + (lane & 1) * 512 + (lane & 4) * 16 + g * 16;
      tp0 = img + q * 128 + (((2 * g + ((lane >> 1) & 1)) ^ (q & 2)) * 16) + (lane & 1) * 8;
      tp1 = img + 512 + q * 128 + (((2 * g + ((lane >> 1) & 1)) ^ ((q & 2) + 4)) * 16) + (lane & 1) * 8;
      pp = smem + ((lane >> 3) == 0 || (lane >> 3) == 3 ? O_PART + r * 64 + xw * 16 + g * 8 : O_JUNK + lane * 8);
    }
    const int so = WS_XT + (32 * xw + (lane >> 3)) * XT_PITCH + cg * 128 + (lane & 7) * 16, sstep = 8 * XT_PITCH;
    u32 lo_ = WS_XT + (cc >> 1) * XT_PITCH + (16 * xw + (lane >> 2)) * 128 + (cc & 1) * 64 + (lane & 3) * 16;
    OPQ(lo_);
    SB();
    asm volatile("s_waitcnt vmcnt(0)" ::: "memory");
    STAMP(1);
    {
      u4v wa[4], fb[4]; u2v ta[4], tb[4];
#pragma unroll
      for (int s = 0; s < 4; ++s) { wa[s] = *(const u4v*)(wp + s * 128); fb[s] = *(const u4v*)(fp + s * 1024); }
      SB();
      f4 r = {0.f, 0.f, 0.f, 0.f};
#pragma unroll
      for (int s = 0; s < 4; ++s) r = mma(wa[s], fb[s], r);
      *(f2*)pp = f2{r[0], r[1]};
      ARRIVE(C_PART);
      SB();
#pragma unroll
      for (int s = 0; s < 4; ++s) { ta[s] = tr_read(tp0 + s * 1024); tb[s] = tr_read(tp1 + s * 1024); }
      SB();
      WAITQ(C_GP, 1);
#pragma unroll
      for (int s = 0; s < 4; ++s) __builtin_amdgcn_raw_buffer_store_b128(u4v{ta[s][0], ta[s][1], tb[s][0], tb[s][1]}, wrs, so + s * sstep, 0, 16);
    }
    STAMP(2);
    const u32 dskw = ((gp16)kb[0])[d];
    conv_setup();
    band_setup();
    const u32 tag = par[0];
    ARRIVE(C_PUB);
    asm volatile("s_waitcnt vmcnt(0)" ::: "memory");
    if (lane == 0) __builtin_amdgcn_raw_buffer_store_b32(tag, wrs, WS_FLG + xw * 1024 + cg * 4, 0, 16);
    STAMP(3);
    dm1 = bflo(dskw) - 1.f;
    WAITS(C_TL, 1);
    const u32 tl0 = (u32)__builtin_amdgcn_readfirstlane((int)par[1]);
    u3v fq[2];
    auto rec_ld = [&]() AI {
#pragma unroll
      for (int k = 0; k < 2; ++k) fq[k] = __builtin_amdgcn_raw_buffer_load_b96(wrs, WS_REC + xw * 2048 + k * 1024 + lane * 16, 0, 16);
    };
    pace(tl0 + DT_REC);
    rec_ld();
    SB();
    {
#pragma unroll 1
      for (int spin = 0; spin < SPIN_MAX && __builtin_expect(__any(fq[0][2] != tag || fq[1][2] != tag), 0); ++spin) {
        __builtin_amdgcn_s_sleep(SLEEP_G);
        rec_ld();
        asm volatile("" ::: "memory");
      }
      char* gp = smem + O_GT + xw * 2048 + (lane & 3) * 256 + (lane >> 2) * 16;
#pragma unroll
      for (int k = 0; k < 2; ++k) *(f4*)(gp + k * 1024) = f4{bflo(fq[k][0]), bfhi(fq[k][0]), bflo(fq[k][1]), bfhi(fq[k][1])};
    }
    pace(tl0 + DT_FLG);
    u4v xq[4];
    bool late[4];
    {
      const u32 fl = __builtin_amdgcn_raw_buffer_load_b32(wrs, WS_FLG + (cc >> 6) * 1024 + xw * 64 + g * 256 + c16 * 4, 0, 16);
      const u64 bad = __builtin_amdgcn_ballot_w64(fl != tag);
      asm volatile("" ::: "memory");
#pragma unroll
      for (int k = 0; k < 4; ++k) {
        late[k] = ((bad >> (16 * k)) & 0xffffull) != 0ull;
        xq[k] = __builtin_amdgcn_raw_buffer_load_b128(wrs, lo_ | (late[k] ? 1u << 30 : 0u), k * 8192, 16);
        SB();
      }
    }
    ARRIVE(C_XL);
    STAMP(4);
    WAITQ(C_TAPS + c, 1);
    u4v Bi[2][3], Bo[4], As;
#pragma unroll
    for (int be = 0; be < 3; ++be) { Bi[0][be] = bin(0, be); Bi[1][be] = bin(1, be); }
#pragma unroll
    for (int be = 0; be < 4; ++be) Bo[be] = bout(be);
    As = asp();
    WAITQ(C_PUB, 4);
    char* dp = smem + (lane & 3) * CHB + O_U + 512 + xw * 256 + (lane >> 2) * 16;
    m_setup();
    auto stage = [&](int k) AI {
      SB();
      if (__builtin_expect(late[k], 0)) {
#pragma unroll 1
        for (int spin = 0; spin < SPIN_MAX; ++spin) {
          STAMP(14);
          const u32 f1 = __builtin_amdgcn_raw_buffer_load_b32(wrs, WS_FLG + (cc >> 6) * 1024 + xw * 64 + k * 256 + c16 * 4, 0, 16);
          if (!__any(f1 != tag)) break;
          __builtin_amdgcn_s_sleep(SLEEP_G);
          asm volatile("" ::: "memory");
        }
        asm volatile("" ::: "memory");
        xq[k] = __builtin_amdgcn_raw_buffer_load_b128(wrs, lo_, k * 8192, 16);
        asm volatile("s_waitcnt vmcnt(0)" : "+v"(xq[k]) :: "memory");
      }
      *(u4v*)(dp + k * 1024) = xq[k];
      FLAG(C_Q + k * 4);
    };
    stage(0);
    stage(1);
    STAMP(5);
    __builtin_amdgcn_s_setprio(0);
    WAITA(qa);
    SB();
    FL(0, 0); RL(0); FL(8, 8); RL(1);
    MF0(wq[0][0], Fq[0], band[0]);
    MF0(wq[0][1], Fq[0], band[1]);
    MF0(wq[0][2], ru[0], band[0]);
    MF0(wq[0][3], ru[0], band[1]);
    MF0(wq[0][4], Fq[0], band[2]);
    MF0(wq[0][5], ru[0], band[3]);
    MF0(acc[4], As, Fq[0]); PEEK(6, qa + 16);
    MF0(wq[1][0], Fq[8], band[0]); WPK(0, 0, 0); WPK(0, 0, 1);
    MF0(wq[1][1], Fq[8], band[1]); WPK(0, 0, 2); WPK(0, 0, 3);
    MF0(wq[1][2], ru[1], band[0]); WPK(0, 1, 0); WPK(0, 1, 1);
    MF0(wq[1][3], ru[1], band[1]); WPK(0, 1, 2); WPK(0, 1, 3);
    MF0(wq[1][4], Fq[8], band[2]); CHECK(6, qa + 16);
    MF0(wq[1][5], ru[1], band[3]); WPK(0, 2, 0); WPK(0, 2, 1);
    MF0(acc[5], As, Fq[8]); WPK(0, 2, 2); WPK(0, 2, 3);
    MF0(acc[2], wa_[0][0], Bi[0][0]); FL(16, 16); RL(2);
    MF(acc[2], wa_[0][1], Bi[1][0]); WPK(1, 0, 0); WPK(1, 0, 1);
    MF0(acc[3], wa_[0][0], Bi[0][1]); WPK(1, 0, 2); WPK(1, 0, 3);
    MF(acc[3], wa_[0][1], Bi[1][1]); WPK(1, 1, 0); WPK(1, 1, 1);
    MF(acc[4], wa_[0][0], Bi[0][2]); FST(0); FLAG(C_FAR + 0 * 4);
    MF(acc[4], wa_[0][1], Bi[1][2]); WPK(1, 1, 2); WPK(1, 1, 3);
    MF(acc[4], wa_[0][2], Bo[0]); WPK(1, 2, 0); WPK(1, 2, 1);
    MF(acc[5], wa_[0][2], Bo[1]); WPK(1, 2, 2); WPK(1, 2, 3);
    MF0(acc[6], wa_[0][2], Bo[2]);
    MF0(acc[7], wa_[0][2], Bo[3]); stage(2);
    MF0(wq[2][0], Fq[16], band[0]);
    MF0(wq[2][1], Fq[16], band[1]); stage(3);
    MF0(wq[2][2], ru[2], band[0]);
    MF0(wq[2][3], ru[2], band[1]);
    MF0(wq[2][4], Fq[16], band[2]); FL(24, 24); RL(3);
    MF0(wq[2][5], ru[2], band[3]); WPK(2, 0, 0); WPK(2, 0, 1);
    MF(acc[6], As, Fq[16]); WPK(2, 0, 2); WPK(2, 0, 3);
    MF(acc[3], wa_[1][0], Bi[0][0]); WPK(2, 1, 0); WPK(2, 1, 1);
    MF(acc[3], wa_[1][1], Bi[1][0]); WPK(2, 1, 2); WPK(2, 1, 3);
    MF(acc[4], wa_[1][0], Bi[0][1]); WPK(2, 2, 0); WPK(2, 2, 1);
    MF(acc[4], wa_[1][1], Bi[1][1]); WPK(2, 2, 2); WPK(2, 2, 3);
    MF(acc[5], wa_[1][0], Bi[0][2]); FST(1); FLAG(C_FAR + 1 * 4);
    MF(acc[5], wa_[1][1], Bi[1][2]); PEEK(7, qa + 32);
    MF(acc[5], wa_[1][2], Bo[0]);
    MF(acc[6], wa_[1][2], Bo[1]);
    MF(acc[7], wa_[1][2], Bo[2]);
    MF0(wq[3][0], Fq[24], band[0]);
    MF0(wq[3][1], Fq[24], band[1]); CHECK(7, qa + 32);
    MF0(wq[3][2], ru[3], band[0]);
    MF0(wq[3][3], ru[3], band[1]);
    MF0(wq[3][4], Fq[24], band[2]); FL(32, 32); RL(4);
    MF0(wq[3][5], ru[3], band[3]); WPK(3, 0, 0); WPK(3, 0, 1);
    MF(acc[7], As, Fq[24]); WPK(3, 0, 2); WPK(3, 0, 3);
    MF(acc[4], wa_[2][0], Bi[0][0]); WPK(3, 1, 0); WPK(3, 1, 1);
    MF(acc[4], wa_[2][1], Bi[1][0]); WPK(3, 1, 2); WPK(3, 1, 3);
    MF(acc[5], wa_[2][0], Bi[0][1]); WPK(3, 2, 0); WPK(3, 2, 1);
    MF(acc[5], wa_[2][1], Bi[1][1]); FL(40, 40); RL(5);
    MF(acc[6], wa_[2][0], Bi[0][2]); FST(2); FLAG(C_FAR + 2 * 4);
    MF(acc[6], wa_[2][1], Bi[1][2]); WPK(3, 2, 2); WPK(3, 2, 3);
    MF(acc[6], wa_[2][2], Bo[0]); AL(0);
    MF(acc[7], wa_[2][2], Bo[1]); AL(1);
    MF0(wq[4][0], Fq[32], band[0]); AL(2);
    MF0(wq[4][1], Fq[32], band[1]); AL(3);
    MF0(wq[4][2], ru[4], band[0]); AL(4);
    MF0(wq[4][3], ru[4], band[1]); WPK(4, 0, 0); WPK(4, 0, 1);
    MF(acc[5], wa_[3][0], Bi[0][0]); WPK(4, 0, 2); WPK(4, 0, 3);
    MF(acc[5], wa_[3][1], Bi[1][0]); WPK(4, 1, 0); WPK(4, 1, 1);
    MF(acc[6], wa_[3][0], Bi[0][1]); WPK(4, 1, 2); WPK(4, 1, 3);
    MF(acc[6], wa_[3][1], Bi[1][1]); AL(5);
    MF(acc[7], wa_[3][0], Bi[0][2]); FST(3); FLAG(C_FAR + 3 * 4);
    MF(acc[7], wa_[3][1], Bi[1][2]); AL(6);
    MF(acc[7], wa_[3][2], Bo[0]); FL(33, 33); AL(7);
    MF0(wq[5][0], Fq[40], band[0]); FL(34, 34); AL(8);
    MF0(wq[5][1], Fq[40], band[1]); FL(35, 35); AL(9);
    MF0(wq[5][2], ru[5], band[0]); WPK(5, 0, 0); WPK(5, 0, 1);
    MF0(wq[5][3], ru[5], band[1]); AL(10);
    MF(acc[6], wa_[4][0], Bi[0][0]); PEEK(8, qa + 48); FL(36, 36);
    MF(acc[6], wa_[4][1], Bi[1][0]); FL(37, 37); WPK(5, 0, 2);
    MF(acc[7], wa_[4][0], Bi[0][1]); FL(38, 38); WPK(5, 0, 3);
    MF(acc[7], wa_[4][1], Bi[1][1]); FL(39, 39); WPK(5, 1, 0);
    MF(acc[6], At[0], Fq[32]); WPK(5, 1, 1); WPK(5, 1, 2);
    MF(acc[6], At[1], Fq[33]); CHECK(8, qa + 48);
    MF(acc[6], At[2], Fq[34]); WPK(5, 1, 3); FL(41, 41);
    MF(acc[6], At[3], Fq[35]); AL(11);
    MF(acc[7], wa_[5][0], Bi[0][0]); FL(42, 42); AL(12);
    MF(acc[7], wa_[5][1], Bi[1][0]); AL(13);
    MF(acc[6], At[4], Fq[36]); FL(43, 43); AL(14);
    MF(acc[6], At[5], Fq[37]); AL(15);
    MF(acc[6], At[6], Fq[38]); FL(44, 44); AL(16);
    MF(acc[6], At[7], Fq[39]);
    MF(acc[6], At[8], Fq[40]); FL(45, 45);
    MF(acc[7], At[0], Fq[40]);
    MF(acc[6], At[9], Fq[41]); FL(46, 46);
    MF(acc[7], At[1], Fq[41]);
    MF(acc[6], At[10], Fq[42]); FL(47, 47);
    MF(acc[7], At[2], Fq[42]);
    MF(acc[6], At[11], Fq[43]); FL(48, 48);
    MF(acc[7], At[3], Fq[43]);
    MF(acc[6], At[12], Fq[44]); FL(49, 49); ULD(6);
    MF(acc[7], At[4], Fq[44]); FL(50, 50); GLD(6);
    MF(acc[6], At[13], Fq[45]); FL(51, 51);
    MF(acc[7], At[5], Fq[45]); FL(52, 52);
    MF(acc[6], At[14], Fq[46]); FL(53, 53);
    MF(acc[7], At[6], Fq[46]); FL(54, 54); EUN(6, 0);
    MF(acc[6], At[15], Fq[47]); FL(55, 55); EUN(6, 1);
    MF(acc[7], At[7], Fq[47]); FL(56, 56); EUN(6, 2);
    MF(acc[6], At[16], Fq[48]); EUN(6, 3);
    MF(acc[7], At[8], Fq[48]); ULD(7); GLD(7);
    MF(acc[7], At[9], Fq[49]);
    MF(acc[7], At[10], Fq[50]);
    MF(acc[7], At[11], Fq[51]);
    MF(acc[7], At[12], Fq[52]); EA1(6, 0); EA1(6, 1); EA1(6, 2); EA1(6, 3);
    MF(acc[7], At[13], Fq[53]); EA2(6, 0); EA2(6, 1); EA2(6, 2); EA2(6, 3);
    MF(acc[7], At[14], Fq[54]); EPK(6, 0); EPK(6, 1);
    MF(acc[7], At[15], Fq[55]); OST(6); FLAG(C_TILE + 6 * 4);
    MF(acc[7], At[16], Fq[56]); EUN(7, 0); EUN(7, 1); EUN(7, 2); EUN(7, 3); NOP(3); EA1(7, 0); EA1(7, 1); EA1(7, 2); EA1(7, 3); EA2(7, 0); EA2(7, 1); EA2(7, 2); EA2(7, 3); EPK(7, 0); EPK(7, 1); OST(7); FLAG(C_TILE + 7 * 4);
    STAMP(9);
    ROLE_END();
  }
  {
    ROLE("h");
    const int hw = w - 5;
    const int j = xg * 28 + xi;
    const int s1 = j >> 2, kq = j & 3;
    const bool own = xi < 28;
    const u64 wbase = hw == 0 ? kb[1] : kb[2];
    const int pitch = F * 2;
    auto hrs = __builtin_amdgcn_make_buffer_rsrc((void*)wbase, 0, own ? D * F * 2 : 0, 0x00020000);
    const int r8 = lane >> 3;
    const int vo = (256 * kq * F + 64 * s1) * 2 + (8 * (r8 >> 2) + (r8 & 3)) * pitch + (((lane & 7) * 16) ^ (((r8 >> 1) & 3) * 32));
    auto dma = [&](int h, int sB, int pt, u32 dst) AI {
      const int m = h >> 1;
#pragma unroll
      for (int i = 0; i < 2; ++i)
        asm volatile("s_mov_b32 m0, %3\n\ts_nop 0\n\tbuffer_load_dwordx4 %0, %1, %2 offen nt lds"
                     :: "v"(vo), "s"(hrs), "s"((m & 1) * 32 * pt + (m >> 1) * sB + (16 * i + 4 * (h & 1)) * pt), "s"(dst + i * 1024) : "memory");
    };
    const int q = c16 >> 2, p = c16 & 3;
    int tro[4];
#pragma unroll
    for (int ct = 0; ct < 4; ++ct) tro[ct] = (4 * g + q) * 128 + ((ct ^ (((4 * g + q) >> 1) & 3)) * 32) + p * 8;
    const int tok = lane & 7;
    u4v wd[2][8];
    auto wd_ld = [&](int k, int cb) AI {
#pragma unroll
      for (int i = 0; i < 8; ++i) {
        const char* pp = smem + O_WD + cb * 8192 + (i >> 2) * 4096 + tro[i & 3];
        const u2v lo = tr_read(pp), hi = tr_read(pp + 2048);
        wd[k][i] = u4v{lo[0], lo[1], hi[0], hi[1]};
      }
    };
    u32 p2o, p2k[4], hlo, hhi;
    auto f2_setup = [&](int cb0) AI {
      p2o = WS_P2 + ((64 * kq + 16 * cb0 + (lane & 8) / 2 + g) * 56 + s1) * 128 + tok * 16;
      hlo = lbase + O_HB + ((lane & 8) ? 512 : g * 128 + tok * 16);
      hhi = lbase + O_HB + ((lane & 8) ? g * 128 + tok * 16 : 512);
#pragma unroll
      for (int i = 0; i < (cb0 == 2 ? 4 : 2); ++i) { p2k[i] = p2o + i * 8 * 7168; OPQ(p2k[i]); }
      OPQ(hlo); OPQ(hhi);
    };
    auto f2_job = [&](int nb, u32 t10) AI {
      u4v hb[4];
      {
        u32 cnt; int n_ = 1 << 20;
        asm volatile("1:\n\tds_read_b32 %4, %6\n\tds_read_b128 %0, %7\n\tds_read_b128 %1, %8\n\tds_read_b128 %2, %7 offset:528\n\tds_read_b128 %3, %8 offset:528\n\t"
                     "s_sub_u32 %5, %5, 1\n\ts_cbranch_scc1 2f\n\ts_waitcnt lgkmcnt(0)\n\tv_cmp_gt_u32 vcc, 2, %4\n\ts_cbranch_vccnz 1b\n2:\n\ts_waitcnt lgkmcnt(0)"
                     : "=&v"(hb[0]), "=&v"(hb[1]), "=&v"(hb[2]), "=&v"(hb[3]), "=&v"(cnt), "+s"(n_) : "v"(CTLA(C_HRDY)), "v"(hlo), "v"(hhi) : "vcc", "scc", "memory");
      }
      STAMP(11);
      f4 r[4];
#pragma unroll
      for (int i = 0; i < 2 * nb; ++i) {
        const u4v* wp = &wd[i >> 1][(i & 1) * 2];
        r[i] = mma(wp[0], hb[0], f4{0.f, 0.f, 0.f, 0.f});
        r[i] = mma(wp[1], hb[1], r[i]);
        r[i] = mma(wp[4], hb[2], r[i]);
        r[i] = mma(wp[5], hb[3], r[i]);
      }
      SB();
#pragma unroll
      for (int i = 0; i < 2 * nb; ++i) {
        u4v v = __builtin_bit_cast(u4v, r[i]);
#pragma unroll
        for (int e = 0; e < 4; ++e) v[e] = __builtin_amdgcn_bitop3_b32(v[e], t10, TMASK, 0xd8);
        __builtin_amdgcn_raw_buffer_store_b128(v, wrs, p2k[i], 0, 16);
      }
      STAMP(12);
    };
    const u32 rb = lbase + O_RING + hw * (NSLOT * 2048);
    pace(ts0 + WDELAY);
    STAMP(2);
#pragma unroll
    for (int h = 0; h < WWIN; ++h) dma(h, 64 * F * 2, F * 2, rb + h * 2048);
    __builtin_amdgcn_s_setprio(3);
    STAMP(3);
    if (__builtin_expect(own, 1)) {
      u4v wv[32];
      f4 a1[4] = {f4{0.f, 0.f, 0.f, 0.f}, f4{0.f, 0.f, 0.f, 0.f}, f4{0.f, 0.f, 0.f, 0.f}, f4{0.f, 0.f, 0.f, 0.f}};
      u4v xb[8];
      auto f1 = [&](int k0, int k1) AI {
#pragma unroll
        for (int k = k0; k < k1; ++k)
#pragma unroll
          for (int ct = 0; ct < 4; ++ct) a1[ct] = mma(wv[4 * k + ct], xb[k], a1[ct]);
      };
      u32 xo = lbase + O_XS + tok * XS_PITCH + g * 16;
      u32 p1o = WS_P1 + s1 * 16384 + kq * 4096 + hw * 2048 + (lane & 8) * 64 + g * 128 + tok * 16;
      u32 t10, tpub;
      OPQ(xo); OPQ(p1o);
      constexpr int HX = (16 - WWIN + 1) & ~1;
#pragma unroll
      for (int h = 0; h < 16; ++h) {
        const int left = (h + WWIN < 16 ? WWIN : 16 - h) - 1;
        asm volatile("s_waitcnt vmcnt(%0)" :: "n"(2 * left) : "memory");
        if (h + WWIN < 16) {
          asm volatile("s_waitcnt lgkmcnt(0)" ::: "memory");
          dma(h + WWIN, 64 * F * 2, F * 2, rb + ((h + WWIN) % NSLOT) * 2048);
        }
#pragma unroll
        for (int ct = 0; ct < 4; ++ct) {
          const u2v t = tr_read(smem + O_RING + hw * (NSLOT * 2048) + (h % NSLOT) * 2048 + tro[ct]);
          wv[4 * (h >> 1) + ct][2 * (h & 1)] = t[0]; wv[4 * (h >> 1) + ct][2 * (h & 1) + 1] = t[1];
        }
        if (h == H2_AT) ARRIVE(C_WGU);
        if (h == HX - 1) {
          STAMP(4);
          WAITQ(C_XS, 1);
          STAMP(1);
#pragma unroll
          for (int k = 0; k < 8; ++k) xb[k] = *(const __attribute__((address_space(3))) u4v*)(uintptr_t)(xo + k * 64);
          t10 = par[0]; tpub = par[2];
          SB();
          f1(0, 2);
          SB();
        }
        if (h >= HX) { f1(h - HX + 2, h - HX + 3); SB(); }
      }
      STAMP(5);
      f1(18 - HX, 8);
      t10 &= TMASK;
#pragma unroll
      for (int a = 0; a < 2; ++a) {
        u4v v;
#pragma unroll
        for (int e = 0; e < 4; ++e)
          v[e] = ((u32)__builtin_amdgcn_update_dpp(__float_as_int(a1[2 * a][e]), __float_as_int(a1[2 * a + 1][e]), 0xe4, 0xf, 0xc, false) & ~TMASK) | t10;
        __builtin_amdgcn_raw_buffer_store_b128(v, wrs, p1o + a * 1024, 0, 16);
      }
      STAMP(6);
      const u32 tn = now32();
      u32 voff = WS_P1 + s1 * 16384 + hw * 1024 + lane * 16;
      u32 ho = lbase + O_HB + hw * 528 + g * 128 + tok * 16 + (lane & 8);
      float nl2e = -1.44269504088896341f;
      u32 wda = CTLA(C_WD) + hw * 4;
      OPQ(voff); OPQ(ho); OPQ(nl2e); OPQ(wda);
      f2_setup(hw & 1);
      pace2(tn, (u32)__builtin_amdgcn_readfirstlane((int)tpub) + DE_P1, DT_P1);
      u4v pb[8];
      auto p1_ld = [&]() AI {
#pragma unroll
        for (int i = 0; i < 8; ++i) { pb[i] = __builtin_amdgcn_raw_buffer_load_b128(wrs, voff + (i >> 2) * 2048, (i & 3) * 4096, 16); SB(); }
      };
      p1_ld();
      SB();
      f4 G, U;
      bool early = false;
#pragma unroll 1
      for (int spin = 0; spin < SPIN_MAX; ++spin) {
        u32 bad = 0;
        OPQ(bad);
        asm volatile("" :: "v"(pb[0]));
        const u32 cw = __hip_atomic_load((__attribute__((address_space(3))) u32*)(uintptr_t)wda, __ATOMIC_RELAXED, __HIP_MEMORY_SCOPE_WORKGROUP);
        G = tsum4(bad, t10, pb[0], pb[1], pb[2], pb[3]);
        early = __builtin_amdgcn_ballot_w64(cw == 0u) == 0ull;
        asm volatile("" ::: "memory");
        if (__builtin_expect(early, 1)) wd_ld(0, hw);
        SB();
        silu4(G, nl2e);
        U = tsum4(bad, t10, pb[4], pb[5], pb[6], pb[7]);
        if (__builtin_expect(!__any((bad & TMASK) != 0), 1)) break;
        STAMP(14);
        p1_ld();
        asm volatile("" ::: "memory");
      }
      STAMP(7);
      {
        float hv[4];
#pragma unroll
        for (int e = 0; e < 4; ++e) { hv[e] = G[e] * U[e]; asm("" : "+v"(hv[e])); }
        *(__attribute__((address_space(3))) u2v*)(uintptr_t)ho = u2v{pk_bf(hv[0], hv[1]), pk_bf(hv[2], hv[3])};
      }
      ARRIVE(C_HRDY);
      STAMP(8);
      if (__builtin_expect(!early, 0)) { WAITA(wda); wd_ld(0, hw); }
      f2_job(1, t10);
    }
    if (hw == 0) {
      WAITS(C_SEL, 1);
      const u32 t10 = par[0] & TMASK;
      const u32 tpub = (u32)__builtin_amdgcn_readfirstlane((int)par[2]);
      const u32 mykey = ((const u32*)(smem + O_SEL))[lane & 7];
      if (own) pace2(now32(), tpub + DE_P2, DT_P2); else pace(tpub + DE_P2 - 40);
      u4v pa[7];
      const int voff = WS_P2 + cc * 7168 + lane * 16;
#pragma unroll
      for (int k = 0; k < 7; ++k) pa[k] = __builtin_amdgcn_raw_buffer_load_b128(wrs, voff + (k & 3) * 1024, (k >> 2) * 4096, 16);
      SB();
      const int row = (int)(~mykey & 2047u);
      const float rsig = rb1(sigm(__uint_as_float((mykey ^ (~(u32)((int)mykey >> 31) | 0x80000000u)) & 0xffff0000u)));
      WAITA(CTLA(C_TILE) + (lane & 31) * 4);
      float cf_ = rsig * (1.f - *(const float*)(smem + O_GT + (row >> 8) * 1024 + ((row >> 2) & 3) * 256 + ((row >> 4) & 15) * 16 + (row & 3) * 4));
      float base[4];
#pragma unroll
      for (int e = 0; e < 4; ++e) base[e] = bflo(*(const u16*)(smem + e * CHB + O_OUT + (row >> 8) * OT + ((row >> 2) & 3) * 144 + ((row >> 4) & 15) * 8 + (row & 3) * 2));
      u64 op = kc[1] + (row * D + cc * 4) * 2;
      { float cq = cf_; OPQ(cq); cf_ = cq; OPQ(op); }
#pragma unroll
      for (int e = 0; e < 4; ++e) OPQ(base[e]);
      STAMP(13);
      f4 y;
#pragma unroll 1
      for (int spin = 0; spin < SPIN_MAX; ++spin) {
        u32 bad = 0;
        OPQ(bad);
        y = tsum4(bad, t10, pa[0], pa[1], pa[2], pa[3]);
        y = tsum3(bad, t10, y, pa[4], pa[5], pa[6]);
        if (__builtin_expect(!__any((bad & TMASK) != 0), 1)) break;
        STAMP(15);
#pragma unroll
        for (int k = 0; k < 7; ++k) pa[k] = __builtin_amdgcn_raw_buffer_load_b128(wrs, voff + (k & 3) * 1024, (k >> 2) * 4096, 16);
        asm volatile("" ::: "memory");
      }
      STAMP(9);
      float o[4];
#pragma unroll
      for (int e = 0; e < 4; ++e) {
        float v = y[e] + dpp_f<0x128>(y[e]);
        auto s16 = __builtin_amdgcn_permlane16_swap(__float_as_uint(v), __float_as_uint(v), false, false);
        v = __uint_as_float(s16[0]) + __uint_as_float(s16[1]);
        auto s32 = __builtin_amdgcn_permlane32_swap(__float_as_uint(v), __float_as_uint(v), false, false);
        o[e] = fmaf(__uint_as_float(s32[0]) + __uint_as_float(s32[1]), cf_, base[e]);
      }
      if (lane < 8) *(__attribute__((address_space(1))) u2v*)op = u2v{pk_bf(o[0], o[1]), pk_bf(o[2], o[3])};
    }
    ROLE_END();
  }
  {
    ROLE("g");
    const u32 pLA = __builtin_nontemporal_load((gp32)ka[3] + d * (NM / 2) + lane);
    const u32 pAI = __builtin_nontemporal_load((gp32)ka[4] + d * (NM / 2) + lane);
    const u32 pCR = __builtin_nontemporal_load((gp32)ka[5] + d * (NM / 2) + lane);
    const u32 pCI = __builtin_nontemporal_load((gp32)ka[6] + d * (NM / 2) + lane);
    const u32 ldt = ((gp16)ka[7])[d], dskw = ((gp16)kb[0])[d];
    u4v ixa[2];
#pragma unroll
    for (int r = 0; r < 2; ++r) ixa[r] = *(const u4v*)(g_tapidx.v + lane * 16 + r * 8);
    SB();
    asm volatile("s_mov_b32 m0, %0\n\ts_nop 0" :: "s"(lbase + c * CHB) : "memory");
#define PUT1(off, val) asm volatile("ds_write_addtid_b32 %0 offset:%1" :: "v"(val), "n"(off) : "memory")
#define PUTR(off, val) do { const ch v_ = (val); asm volatile("ds_write_addtid_b32 %0 offset:%2\n\tds_write_addtid_b32 %1 offset:%3" :: "v"(v_.r), "v"(v_.i), "n"(off), "n"((off) + 256) : "memory"); } while (0)
    const char* pf = ch_ + c16 * TROW + g * 16;
    PUT1(O_TAP, 0x3c003c00u); PUT1(O_TAP + 256, 0u);
    PUT1(O_U, 0u); PUT1(O_U + 256, 0u);
    SB();
    const float dt = rb1(exp_bf(bflo(ldt)));
    const f2 a2 = f2{-rb1(exp_bf(bflo(pLA))), -rb1(exp_bf(bfhi(pLA)))} * dt * 1.44269504088896341f;
    const f2 br = unbf(pAI) * dt * 0.15915494309189535f;
    STAMP(1);
    u4v Lf[2][8], Wf[8];
    f4 kk[2][2];
    float fk;
    {
      const cf z1 = cexp_p(a2, br, 1.f);
      const cf z2 = csq(z1), z4 = csq(z2), z8 = csq(z4), z16 = csq(z8), z32 = csq(z16), z64 = csq(z32);
      ch base[4], anc[4];
      base[1] = tohc(z8); base[2] = tohc(z16); base[3] = tohc(cmul(z16, z8));
      anc[1] = tohc(z32); anc[2] = tohc(z64); anc[3] = tohc(cmul(z64, z32));
      PUTR(O_OUT + 5 * TROW, tohc(csq(z64)));
#pragma unroll
      for (int j = 1; j < 16; ++j) {
        const int q = j >> 2, r = j & 3;
        PUTR(O_TAP + j * TROW, q == 0 ? base[r] : r == 0 ? anc[q] : hmul(anc[q], base[r]));
      }
#pragma unroll
      for (int s = 0; s < 8; ++s) Lf[0][s] = *(const u4v*)(pf + O_TAP + s * 64);
#pragma unroll
      for (int s = 0; s < 8; ++s) Lf[1][s] = *(const u4v*)(ch_ + O_OUT + 5 * TROW + g * 16 + s * 64);
      SB();
      STAMP(11);
      u32 mx = max(max(pCR << 17, pCR << 1), max(pCI << 17, pCI << 1));
      mx = wave_max_u32(mx);
      int ex = (int)(mx >> 24);
      ex = ex < 1 ? 1 : (ex > 253 ? 253 : ex);
      const float sdn = __uint_as_float((u32)(254 - ex) << 23);
      fk = 2.f * dt * __uint_as_float((u32)ex << 23);
      cf C; C.r = unbf(pCR) * sdn; C.i = unbf(pCI) * sdn;
      const cf E = cexp_p(a2, br, 256.f);
      const ch y1 = toh(z1), y2 = toh(z2), y4 = toh(z4), y6 = toh(cmul(z4, z2));
      {
        const ch R0 = toh(C), R2 = hmul(R0, y2), R4 = hmul(R0, y4), R6 = hmul(R0, y6);
        PUTR(O_TAP, R0); PUTR(O_TAP + 2 * TROW, R2); PUTR(O_TAP + 4 * TROW, R4); PUTR(O_TAP + 6 * TROW, R6);
        PUTR(O_TAP + 1 * TROW, hmul(R0, y1)); PUTR(O_TAP + 3 * TROW, hmul(R2, y1)); PUTR(O_TAP + 5 * TROW, hmul(R4, y1)); PUTR(O_TAP + 7 * TROW, hmul(R6, y1));
        C = cmul(C, E);
        const ch R8 = toh(C);
        PUTR(O_TAP + 8 * TROW, R8); PUTR(O_TAP + 9 * TROW, hmul(R8, y2)); PUTR(O_TAP + 10 * TROW, hmul(R8, y4)); PUTR(O_TAP + 11 * TROW, hmul(R8, y6));
        C = cmul(C, E);
        const ch R12 = toh(C);
        PUTR(O_TAP + 12 * TROW, R12); PUTR(O_TAP + 13 * TROW, hmul(R12, y4));
        C = cmul(C, E);
        const ch R14 = toh(C);
        PUTR(O_TAP + 14 * TROW, R14); PUTR(O_TAP + 15 * TROW, hmul(R14, y4));
      }
#pragma unroll
      for (int s = 0; s < 8; ++s) Wf[s] = *(const u4v*)(pf + O_TAP + s * 64);
      SB();
      STAMP(12);
#pragma unroll
      for (int i = 0; i < 5; ++i) { C = cmul(C, E); PUTR(O_OUT + i * TROW, toh(C)); }
      SB();
      auto mh = [&](u4v a, u4v bq, f4 cin) AI { return __builtin_amdgcn_mfma_f32_16x16x32_f16(as_h8(a), as_h8(bq), cin, 0, 0, 0); };
      kk[0][0] = f4{0.f, 0.f, 0.f, 0.f}; kk[0][1] = kk[0][0]; kk[1][0] = kk[0][0]; kk[1][1] = kk[0][0];
#pragma unroll
      for (int s = 0; s < 5; ++s) {
#pragma unroll
        for (int k = 0; k < 4; ++k) {
          if (k < 2) { if (s < 4) kk[0][0] = mh(Wf[s + 4 * k], Lf[0][s + 4 * k], kk[0][0]); }
          else if (s > 0) { const int u = s - 1 + 4 * (k - 2); kk[0][1] = mh(Wf[u], Lf[1][u], kk[0][1]); Wf[u] = *(const u4v*)(pf + O_OUT + u * 64); }
          SB();
          if (s < 4) {
            const ch o = hmul(ch{ash(Lf[0][s][k]), ash(Lf[0][s + 4][k])}, ch{ash(Lf[1][s][k]), ash(Lf[1][s + 4][k])});
            Lf[1][s][k] = __builtin_bit_cast(u32, o.r); Lf[1][s + 4][k] = __builtin_bit_cast(u32, o.i);
          }
          if (s == 3 && k == 3) asm volatile("s_nop 1");
          SB();
        }
      }
#pragma unroll
      for (int s = 0; s < 8; ++s) { kk[1][0] = mh(Wf[s], Lf[0][s], kk[1][0]); kk[1][1] = mh(Wf[s], Lf[1][s], kk[1][1]); }
    }
    {
      u32 ia[16];
      const u32 kb_ = lbase + c * CHB + O_OUT;
#pragma unroll
      for (int e = 0; e < 16; ++e) { const u32 iw = ixa[e >> 3][(e >> 1) & 3]; ia[e] = kb_ + ((e & 1) ? (iw >> 16) : (iw & 0xffffu)); }
      MIX2(16, 0, 1)
      SB();
      STAMP(2);
      char* ktp = ch_ + O_OUT + c16 * KT_PITCH + g * 8;
#pragma unroll
      for (int hf = 1; hf >= 0; --hf) {
#pragma unroll
        for (int ct = 0; ct < 2; ++ct) {
          const f4 v = kk[hf][ct] * fk;
          *(u32*)(ktp + ct * 16 * KT_PITCH + hf * 32) = pk_bf(v[0], v[1]);
          *(u32*)(ktp + ct * 16 * KT_PITCH + hf * 32 + 4) = pk_bf(v[2], v[3]);
        }
        asm volatile("" ::: "memory");
      }
      u32 zz = 0u; OPQ(zz);
      *(u32*)(ch_ + O_OUT + KT_ZERO + (lane & 7) * 4) = zz;
      asm volatile("" ::: "memory");
      u32 v[16], pe[9];
#pragma unroll
      for (int e = 15; e >= 0; --e) v[e] = *(const __attribute__((address_space(3))) u16*)(uintptr_t)ia[e];
      SB();
      pe[8] = zz;
#pragma unroll
      for (int i = 7; i >= 0; --i) {
        pe[i] = v[2 * i] | (v[2 * i + 1] << 16);
        const u32 nx = (u32)__builtin_amdgcn_update_dpp((int)dpp_u<0x134>(pe[i + 1]), (int)pe[i], 0x130, 0xf, 0xf, false);
        const u32 po = __builtin_amdgcn_alignbyte(nx, pe[i], 2);
        PUT1(O_TAP + i * 256, pe[i]); PUT1(O_TAP + 2 * TAP_PITCH - 4 + i * 256, pe[i]);
        PUT1(O_TAP + TAP_PITCH + i * 256, po); PUT1(O_TAP + 3 * TAP_PITCH - 4 + i * 256, po);
      }
      ARRIVE(C_TAPS + c);
    }
    STAMP(3);
    IDS();
    conv_setup();
    dm1 = bflo(dskw) - 1.f;
    atload(0, 17);
    m_setup();
    __builtin_amdgcn_s_setprio(1);
    SB();
    WAITA(qa);
    STAMP(4);
    SB();
    FL(0, -7); FL(1, -6); FL(2, -5); FL(3, -4); FL(4, -3); FL(5, -2); FL(6, -1); FL(7, 0); ULD(0); GLD(0);
    MF0(acc[0], At[9], Fq[0]); EUN(0, 0); EUN(0, 1);
    MF(acc[0], At[10], Fq[1]); EUN(0, 2); EUN(0, 3);
    MF(acc[0], At[11], Fq[2]);
    MF(acc[0], At[12], Fq[3]);
    MF(acc[0], At[13], Fq[4]);
    MF(acc[0], At[14], Fq[5]);
    MF(acc[0], At[15], Fq[6]); FL(8, 1);
    MF(acc[0], At[16], Fq[7]);
    MF0(acc[1], At[1], Fq[0]); FL(9, 2);
    MF(acc[1], At[2], Fq[1]);
    MF(acc[1], At[3], Fq[2]); FL(10, 3);
    MF(acc[1], At[4], Fq[3]);
    MF(acc[1], At[5], Fq[4]); FL(11, 4); EA1(0, 0);
    MF(acc[1], At[6], Fq[5]); EA1(0, 1); EA1(0, 2);
    MF(acc[1], At[7], Fq[6]); EA1(0, 3); PEEK(0, qa + 16);
    MF(acc[1], At[8], Fq[7]); FL(12, 5); EA2(0, 0);
    MF0(acc[2], At[0], Fq[7]); EA2(0, 1); EA2(0, 2);
    MF(acc[1], At[9], Fq[8]); EA2(0, 3); EPK(0, 0);
    MF(acc[2], At[1], Fq[8]); EPK(0, 1); FL(13, 6);
    MF(acc[1], At[10], Fq[9]); OST(0); FLAG(C_TILE + 0 * 4);
    MF(acc[2], At[2], Fq[9]); FL(14, 7); FL(15, 8);
    MF(acc[1], At[11], Fq[10]); CHECK(0, qa + 16);
    MF(acc[2], At[3], Fq[10]);
    MF(acc[1], At[12], Fq[11]); FL(16, 9);
    MF(acc[2], At[4], Fq[11]); ULD(1); GLD(1);
    MF(acc[1], At[13], Fq[12]); FL(17, 10);
    MF(acc[2], At[5], Fq[12]);
    MF(acc[1], At[14], Fq[13]); FL(18, 11);
    MF(acc[2], At[6], Fq[13]);
    MF(acc[1], At[15], Fq[14]); FL(19, 12); EUN(1, 0);
    MF(acc[2], At[7], Fq[14]); EUN(1, 1); EUN(1, 2);
    MF(acc[1], At[16], Fq[15]); FL(20, 13); EUN(1, 3);
    MF(acc[2], At[8], Fq[15]);
    MF0(acc[3], At[0], Fq[15]); FL(21, 14);
    MF(acc[2], At[9], Fq[16]);
    MF(acc[3], At[1], Fq[16]); FL(22, 15); EA1(1, 0);
    MF(acc[2], At[10], Fq[17]); EA1(1, 1); EA1(1, 2);
    MF(acc[3], At[2], Fq[17]); FL(23, 16); EA1(1, 3);
    MF(acc[2], At[11], Fq[18]); PEEK(2, fa + 0); EA2(1, 0);
    MF(acc[3], At[3], Fq[18]); EA2(1, 1); EA2(1, 2);
    MF(acc[2], At[12], Fq[19]); EA2(1, 3); EPK(1, 0);
    MF(acc[3], At[4], Fq[19]); EPK(1, 1); FL(24, 17);
    MF(acc[2], At[13], Fq[20]); OST(1); FLAG(C_TILE + 1 * 4);
    MF(acc[3], At[5], Fq[20]); FL(25, 18); CHECK(2, fa + 0);
    MF(acc[2], At[14], Fq[21]); FL(26, 19); ULD(2);
    MF(acc[3], At[6], Fq[21]); FLD(2); GLD(2);
    MF(acc[2], At[15], Fq[22]); FL(27, 20); PEEK(1, qa + 32);
    MF(acc[3], At[7], Fq[22]);
    MF(acc[2], At[16], Fq[23]); FL(28, 21);
    MF(acc[3], At[8], Fq[23]); EUN(2, 0); EUN(2, 1);
    MF0(acc[4], At[0], Fq[23]); FL(29, 22); EUN(2, 2);
    MF(acc[3], At[9], Fq[24]); EUN(2, 3); EA1(2, 0);
    MF(acc[4], At[1], Fq[24]); FL(30, 23); EA1(2, 1);
    MF(acc[3], At[10], Fq[25]); EA1(2, 2); EA1(2, 3);
    MF(acc[4], At[2], Fq[25]); FL(31, 24); EFA(2, 0);
    MF(acc[3], At[11], Fq[26]); EFA(2, 1); EFA(2, 2);
    MF(acc[4], At[3], Fq[26]); EFA(2, 3); PEEK(3, fa + 16);
    MF(acc[3], At[12], Fq[27]); EA2(2, 0); EA2(2, 1);
    MF(acc[4], At[4], Fq[27]); EA2(2, 2); EA2(2, 3);
    MF(acc[3], At[13], Fq[28]); CHECK(1, qa + 32); EPK(2, 0); EPK(2, 1);
    MF(acc[4], At[5], Fq[28]); FL(32, 25); OST(2);
    MF(acc[3], At[14], Fq[29]); FLAG(C_TILE + 2 * 4); FL(33, 26);
    MF(acc[4], At[6], Fq[29]); FL(34, 27); CHECK(3, fa + 16);
    MF(acc[3], At[15], Fq[30]); ULD(3); FL(35, 28);
    MF(acc[4], At[7], Fq[30]); FLD(3); GLD(3);
    MF(acc[3], At[16], Fq[31]); FL(36, 29);
    MF(acc[4], At[8], Fq[31]);
    MF0(acc[5], At[0], Fq[31]); FL(37, 30);
    MF(acc[4], At[9], Fq[32]); EUN(3, 0); EUN(3, 1);
    MF(acc[5], At[1], Fq[32]); PEEK(4, fa + 32); FL(38, 31);
    MF(acc[4], At[10], Fq[33]); EUN(3, 2); EUN(3, 3);
    MF(acc[5], At[2], Fq[33]); FL(39, 32); EA1(3, 0);
    MF(acc[4], At[11], Fq[34]); EA1(3, 1); EA1(3, 2);
    MF(acc[5], At[3], Fq[34]); EA1(3, 3); EFA(3, 0);
    MF(acc[4], At[12], Fq[35]); EFA(3, 1); EFA(3, 2); EFA(3, 3);
    MF(acc[5], At[4], Fq[35]); EA2(3, 0); EA2(3, 1); EA2(3, 2); EA2(3, 3);
    MF(acc[4], At[13], Fq[36]); FL(40, 33); EPK(3, 0); EPK(3, 1);
    MF(acc[5], At[5], Fq[36]); FL(41, 34); OST(3);
    MF(acc[4], At[14], Fq[37]); FL(42, 35); FLAG(C_TILE + 3 * 4); CHECK(4, fa + 32); ULD(4);
    MF(acc[5], At[6], Fq[37]); FL(43, 36); FLD(4); GLD(4); PEEK(5, fa + 48);
    MF(acc[4], At[15], Fq[38]); FL(44, 37); FL(45, 38);
    MF(acc[5], At[7], Fq[38]); FL(46, 39); FL(47, 40);
    MF(acc[4], At[16], Fq[39]);
    MF(acc[5], At[8], Fq[39]); EUN(4, 0); EUN(4, 1);
    MF(acc[5], At[9], Fq[40]); EUN(4, 2); EUN(4, 3);
    MF(acc[5], At[10], Fq[41]); EFA(4, 0); EA1(4, 0);
    MF(acc[5], At[11], Fq[42]); EA1(4, 1); EA1(4, 2);
    MF(acc[5], At[12], Fq[43]); EA1(4, 3); EFA(4, 1); EFA(4, 2); EFA(4, 3); CHECK(5, fa + 48); ULD(5);
    MF(acc[5], At[13], Fq[44]); EA2(4, 0); EA2(4, 1); EA2(4, 2); EA2(4, 3); FLD(5); GLD(5);
    MF(acc[5], At[14], Fq[45]); EPK(4, 0); EPK(4, 1);
    MF(acc[5], At[15], Fq[46]); OST(4); FLAG(C_TILE + 4 * 4);
    MF(acc[5], At[16], Fq[47]); EUN(5, 0); EUN(5, 1); EUN(5, 2); EUN(5, 3); EFA(5, 0); EFA(5, 1); EFA(5, 2); EFA(5, 3); EA1(5, 0); EA1(5, 1); EA1(5, 2); EA1(5, 3); EA2(5, 0); EA2(5, 1); EA2(5, 2); EA2(5, 3); EPK(5, 0); EPK(5, 1); OST(5); FLAG(C_TILE + 5 * 4);
    STAMP(8);
    ROLE_END();
  }
  {
    ROLE("c");
    __builtin_amdgcn_s_setprio(3);
    {
      u32 zz = 0u; OPQ(zz);
      if (lane < 2) *(u4v*)(smem + O_HB + 512 + lane * 528) = u4v{zz, zz, zz, zz};
    }
    u32* selw = (u32*)(smem + O_SEL);
    u32 tag;
    {
      const u32 pa = lbase + O_PART + (lane & 7) * 64;
      const u32 ib = 2047u - (u32)(cg * 8 + (lane & 7));
      f4 p0, p1, p2, p3; u32 cnt, ep; int n_ = 1 << 20; unsigned long long tk;
      asm volatile("1:\n\tds_read_b32 %4, %8\n\tds_read_b128 %0, %9\n\tds_read_b128 %1, %9 offset:16\n\tds_read_b128 %2, %9 offset:32\n\tds_read_b128 %3, %9 offset:48\n\t"
                   "ds_read_b32 %7, %10\n\ts_memrealtime %6\n\ts_sub_u32 %5, %5, 1\n\ts_cbranch_scc1 2f\n\ts_waitcnt lgkmcnt(0)\n\tv_cmp_gt_u32 vcc, 4, %4\n\ts_cbranch_vccnz 1b\n2:\n\ts_waitcnt lgkmcnt(0)"
                   : "=&v"(p0), "=&v"(p1), "=&v"(p2), "=&v"(p3), "=&v"(cnt), "+s"(n_), "=&s"(tk), "=&v"(ep) : "v"(CTLA(C_PART)), "v"(pa), "v"(lbase + O_XS) : "vcc", "scc", "memory");
      tag = (u32)__builtin_amdgcn_readfirstlane((int)ep) + 1u;
      tag += tag == 0u;
      const f4 s4_ = (p0 + p1) + (p2 + p3);
      const u32 rp = pk_bf(s4_[1] + s4_[3], s4_[0] + s4_[2]);
      const u32 key = ((rp ^ ((u32)((int)rp >> 31) | 0x80000000u)) & 0xffff0000u) | ib;
      const u32 kn = dpp_u<0xb1>(key);
      u32 m1 = max(key, kn), m2 = min(key, kn), m3;
      { const u32 o1 = dpp_u<0x4e>(m1), o2 = dpp_u<0x4e>(m2); const u32 lo = min(m1, o1), hb = max(m2, o2); m1 = max(m1, o1); m2 = max(lo, hb); m3 = min(lo, hb); }
      { const u32 o1 = dpp_u<0x141>(m1), o2 = dpp_u<0x141>(m2), o3 = dpp_u<0x141>(m3); const u32 lo = min(m1, o1), hb = max(m2, o2);
        m1 = max(m1, o1); m2 = max(lo, hb); m3 = max(min(lo, hb), max(m3, o3)); }
      const u32 wm = (((u32)tk + 8u) >> 2) & 0xffu;
      if (lane == 0) __builtin_amdgcn_raw_buffer_store_b128(u4v{m1, m2, (m3 & 0xffff0000u) | wm, tag}, wrs, WS_KEY + cg * 16, 0, 16);
      SB();
      STAMP(1);
      if (lane == 0) par[0] = tag;
      ARRIVE(C_GP);
      {
        const float gv = sigm(bflo(rp));
        const u32 g2 = pk_bf(gv, dpp_f<0xb1>(gv));
        u32 g3 = dpp_u<0x4e>(g2);
        OPQ(g3);
        if ((lane & 59) == 0) __builtin_amdgcn_raw_buffer_store_b128(u4v{g2, g3, tag, tag}, wrs, WS_REC + cg * 32 + lane * 4, 0, 16);
      }
      if ((lane & 57) == 0) __builtin_amdgcn_raw_buffer_store_b128(u4v{key, kn, 0u, tag}, wrs, WS_KALL + cg * 64 + lane * 8, 0, 16);
    }
    u32 l7 = (lane & 7) * 16, ko = WS_KEY + lane * 16;
    OPQ(l7); OPQ(ko);
    WAITQ(C_TL, 1);
    STAMP(5);
    const u32 tkey = (u32)__builtin_amdgcn_readfirstlane((int)par[1]) + DT_KEY;
    pace(tkey);
    u4v gr[4];
    u32 mykey;
    u4v xs[4];
    auto xrs = __builtin_amdgcn_make_buffer_rsrc((void*)(ka[0] + (xi & 3) * 512), 0, L * D * 2 - 1536, 0x00020000);
    asm volatile("" : "+s"(xrs));
    auto xs_ld = [&]() AI {
      const u32 vo = ((~mykey & 2047u) << 11) | l7;
#pragma unroll
      for (int k = 0; k < 4; ++k) xs[k] = __builtin_amdgcn_raw_buffer_load_b128(xrs, vo + k * 128, 0, 0);
    };
#pragma unroll
    for (int i = 0; i < 4; ++i) gr[i] = __builtin_amdgcn_raw_buffer_load_b128(wrs, ko + i * 1024, 0, 16);
#pragma unroll 1
    for (int spin = 0; spin < SPIN_MAX; ++spin) {
      const u32 bad = (gr[0][3] ^ tag) | (gr[1][3] ^ tag) | (gr[2][3] ^ tag) | (gr[3][3] ^ tag);
      if (__builtin_expect(!__any(bad != 0), 1)) break;
      STAMP(13);
      __builtin_amdgcn_s_sleep(SLEEP_AG1);
#pragma unroll
      for (int i = 0; i < 4; ++i)
        if (gr[i][3] != tag) gr[i] = __builtin_amdgcn_raw_buffer_load_b128(wrs, ko + i * 1024, 0, 16);
      asm volatile("" ::: "memory");
    }
    STAMP(2);
    {
      u32 m1 = gr[0][0], m2 = gr[0][1], m3 = 0, m4 = 0;
#pragma unroll
      for (int i = 2; i < 8; ++i) {
        const u32 k = gr[i >> 1][i & 1];
        if (i > 2) m4 = med3u(m3, m4, k);
        m3 = med3u(m2, m3, k);
        m2 = (i & 1) ? max(m2, k) : med3u(m1, m2, k);
        if (!(i & 1)) m1 = max(m1, k);
      }
      const u32 third = max(max(gr[0][2], gr[1][2]), max(gr[2][2], gr[3][2]));
      u32 W2;
      mykey = wave_top8(m1, m2, m3, m4, W2);
      xs_ld();
      SB();
      if (__builtin_expect(__any(m1 == 0u || (third | 0xffffu) > W2), 0)) {
        u4v ak[16];
#pragma unroll 1
        for (int spin = 0; spin < SPIN_MAX; ++spin) {
          u32 bad = 0;
#pragma unroll
          for (int i = 0; i < 16; ++i) {
            ak[i] = __builtin_amdgcn_raw_buffer_load_b128(wrs, WS_KALL + (i & 3) * 1024 + lane * 16, (i >> 2) * 4096, 16);
            bad |= ak[i][3] ^ tag;
          }
          if (!__any(bad != 0)) break;
          __builtin_amdgcn_s_sleep(8);
          asm volatile("" ::: "memory");
        }
        u32 prev = 0xffffffffu;
#pragma unroll 1
        for (int it = 0; it < 8; ++it) {
          u32 mm_ = 0;
#pragma unroll
          for (int i = 0; i < 32; ++i) {
            const u32 k = ak[i >> 1][i & 1];
            mm_ = max(mm_, k < prev ? k : 0u);
          }
          OPQ(mm_);
          prev = wave_max_u32(mm_);
          mykey = (lane >> 3) == it ? prev : mykey;
        }
        xs_ld();
      }
      OPQ(mykey);
    }
    SB();
    STAMP(3);
    if (l7 == 0) selw[lane >> 3] = mykey;
    {
      const u32 tn = now32();
      u32 yg = 0;
#pragma unroll
      for (int i = 0; i < 4; ++i) yg = max(yg, (gr[i][2] - (tn >> 2) - 1u) & 0xffu);
      const u32 tpub = tn - 4u * (255u - wave_max_u32(yg)) + DT_VIS;
      if (lane == 0) par[2] = later(tkey, tpub);
    }
    ARRIVE(C_SEL);
    if (b == 255 && lane == 0) __builtin_amdgcn_raw_buffer_store_b32(tag, wrs, WS_EPOCH, 0, 16);
    SB();
    {
      char* xp = smem + O_XS + (lane >> 3) * XS_PITCH + l7;
#pragma unroll
      for (int k = 0; k < 4; ++k) *(u4v*)(xp + k * 128) = xs[k];
      ARRIVE(C_XS);
      STAMP(6);
    }
    __builtin_amdgcn_s_setprio(2);
    auto ors = __builtin_amdgcn_make_buffer_rsrc((void*)kc[1], 0, L * D * 2, 0x00020000);
    const u32 rl = (lane + (xi & 15) * 4) & 63, un = (g * 4 + (lane & 3) + xi) & 15;
    u32 skip = 0;
#pragma unroll
    for (int k = 0; k < 8; ++k) {
      const u32 r = ~(u32)__builtin_amdgcn_readlane((int)mykey, 8 * k) & 2047u;
      skip |= (r & 63u) == rl ? 1u << (r >> 6) : 0u;
    }
    const char* op = smem + ((lane >> 2) & 3) * CHB + O_OUT + (un & 3) * 144 + (un >> 2) * 8;
    const u32 ob = rl * 2048 + cc * 8;
    const u32 tfast = (u32)__builtin_amdgcn_readfirstlane((int)par[2]) + DE_FAST;
#pragma unroll 1
    for (int t = 0; t < 8; ++t) {
      WAITA(CTLA(C_TILE) + t * 16 + (lane & 3) * 4);
      u2v yv[4];
#pragma unroll
      for (int e = 0; e < 4; ++e) yv[e] = tr_read(op + t * OT + e * 32);
      const bool slow = (int)(now32() - tfast) < 0;
#pragma unroll
      for (int e = 0; e < 4; ++e) {
        __builtin_amdgcn_raw_buffer_store_b64(yv[e], ors, ob | (((skip >> e) & 1u) << 30), t * 524288 + e * 131072, 0);
        if (slow) {
          SB(); __builtin_amdgcn_s_sleep(STSLP); SB();
          asm volatile("s_waitcnt vmcnt(%0)" :: "n"(STWIN - 1) : "memory");
        }
      }
      skip >>= 4;
    }
    STAMP(9);
    ROLE_END();
  }
  {
    ROLE("d");
        const int j = xg * 28 + xi;
    const int s1 = j >> 2, kq = j & 3;
    const bool own = xi < 28;
    const u64 wbase = kb[3];
    const int pitch = D * 2;
    auto hrs = __builtin_amdgcn_make_buffer_rsrc((void*)wbase, 0, own ? D * F * 2 : 0, 0x00020000);
    const int r8 = lane >> 3;
    const int vo = (64 * s1 * D + 256 * kq) * 2 + (8 * (r8 >> 2) + (r8 & 3)) * pitch + (((lane & 7) * 16) ^ (((r8 >> 1) & 3) * 32));
    auto dma = [&](int h, int sB, int pt, u32 dst) AI {
      const int m = h >> 1;
#pragma unroll
      for (int i = 0; i < 2; ++i)
        asm volatile("s_mov_b32 m0, %3\n\ts_nop 0\n\tbuffer_load_dwordx4 %0, %1, %2 offen nt lds"
                     :: "v"(vo), "s"(hrs), "s"((m & 1) * 32 * pt + (m >> 1) * sB + (16 * i + 4 * (h & 1)) * pt), "s"(dst + i * 1024) : "memory");
    };
    const int q = c16 >> 2, p = c16 & 3;
    int tro[4];
#pragma unroll
    for (int ct = 0; ct < 4; ++ct) tro[ct] = (4 * g + q) * 128 + ((ct ^ (((4 * g + q) >> 1) & 3)) * 32) + p * 8;
    const int tok = lane & 7;
    u4v wd[2][8];
    auto wd_ld = [&](int k, int cb) AI {
#pragma unroll
      for (int i = 0; i < 8; ++i) {
        const char* pp = smem + O_WD + cb * 8192 + (i >> 2) * 4096 + tro[i & 3];
        const u2v lo = tr_read(pp), hi = tr_read(pp + 2048);
        wd[k][i] = u4v{lo[0], lo[1], hi[0], hi[1]};
      }
    };
    u32 p2o, p2k[4], hlo, hhi;
    auto f2_setup = [&](int cb0) AI {
      p2o = WS_P2 + ((64 * kq + 16 * cb0 + (lane & 8) / 2 + g) * 56 + s1) * 128 + tok * 16;
      hlo = lbase + O_HB + ((lane & 8) ? 512 : g * 128 + tok * 16);
      hhi = lbase + O_HB + ((lane & 8) ? g * 128 + tok * 16 : 512);
#pragma unroll
      for (int i = 0; i < (cb0 == 2 ? 4 : 2); ++i) { p2k[i] = p2o + i * 8 * 7168; OPQ(p2k[i]); }
      OPQ(hlo); OPQ(hhi);
    };
    u4v hb[4];
    f4 r[4];
    auto hb_ld = [&]() AI {
      u32 cnt; int n_ = 1 << 20;
      asm volatile("1:\n\tds_read_b32 %4, %6\n\tds_read_b128 %0, %7\n\tds_read_b128 %1, %8\n\tds_read_b128 %2, %7 offset:528\n\tds_read_b128 %3, %8 offset:528\n\t"
                   "s_sub_u32 %5, %5, 1\n\ts_cbranch_scc1 2f\n\ts_waitcnt lgkmcnt(0)\n\tv_cmp_gt_u32 vcc, 2, %4\n\ts_cbranch_vccnz 1b\n2:\n\ts_waitcnt lgkmcnt(0)"
                   : "=&v"(hb[0]), "=&v"(hb[1]), "=&v"(hb[2]), "=&v"(hb[3]), "=&v"(cnt), "+s"(n_) : "v"(CTLA(C_HRDY)), "v"(hlo), "v"(hhi) : "vcc", "scc", "memory");
    };
    auto f2_mm = [&](int k) AI {
      const u4v* wp = wd[k];
      r[2 * k] = mma(wp[0], hb[0], f4{0.f, 0.f, 0.f, 0.f}); r[2 * k + 1] = mma(wp[2], hb[0], f4{0.f, 0.f, 0.f, 0.f});
      r[2 * k] = mma(wp[1], hb[1], r[2 * k]); r[2 * k + 1] = mma(wp[3], hb[1], r[2 * k + 1]);
      r[2 * k] = mma(wp[4], hb[2], r[2 * k]); r[2 * k + 1] = mma(wp[6], hb[2], r[2 * k + 1]);
      r[2 * k] = mma(wp[5], hb[3], r[2 * k]); r[2 * k + 1] = mma(wp[7], hb[3], r[2 * k + 1]);
    };
    auto f2_st = [&](int k, u32 t10) AI {
#pragma unroll
      for (int i = 2 * k; i < 2 * k + 2; ++i) {
        u4v v = __builtin_bit_cast(u4v, r[i]);
#pragma unroll
        for (int e = 0; e < 4; ++e) v[e] = __builtin_amdgcn_bitop3_b32(v[e], t10, TMASK, 0xd8);
        __builtin_amdgcn_raw_buffer_store_b128(v, wrs, p2k[i], 0, 16);
      }
    };
    {
      __builtin_amdgcn_s_setprio(3);
      const u32 ts = ts0 - 500u;
      u32 tl = ~0u;
      pace(later(ts0, now32() - DT_ARG - 15) + DT_HEAD);
      STAMP(3);
#pragma unroll 1
      for (int i = 0; i < 8 && tl >= 1500u; ++i) {
        const u2v hd = __builtin_amdgcn_raw_buffer_load_b64(wrs, WS_HEAD + (lane & 7) * 8, 0, 16);
        tl = wave_max_u32(hd[0] == ~hd[1] ? hd[0] - ts : ~0u);
        STAMP(14);
        asm volatile("" ::: "memory");
      }
      tl = tl < 1500u ? tl + ts : ts0;
      if (lane == 0) par[1] = tl;
      ARRIVE(C_TL);
      STAMP(2);
      if (own) {
        WAITS(C_WGU, 1);
#pragma unroll
        for (int h = 0; h < 16; ++h) {
          if (h == DSEL) WAITS(C_SEL, 1);
          if (h == DEV) WAITS(C_XL, 4);
          if (h >= DWIN) asm volatile("s_waitcnt vmcnt(%0)" :: "n"(2 * (DWIN - 1)) : "memory");
          if (h == 3 + DWIN) ARRIVE(C_WD);
          if (h == 7 + DWIN) ARRIVE(C_WD + 1);
          dma(h, 128, D * 2, lbase + O_WD + h * 2048);
        }
        f2_setup(2);
        const u32 t10 = par[0] & TMASK;
        asm volatile("s_waitcnt vmcnt(8)" ::: "memory");
        wd_ld(0, 2);
        const u32 hc = __hip_atomic_load(ctl + C_HRDY, __ATOMIC_RELAXED, __HIP_MEMORY_SCOPE_WORKGROUP);
        const bool early = __builtin_amdgcn_ballot_w64(hc < 2u) == 0ull;
        if (early) { hb_ld(); f2_mm(0); }
        asm volatile("s_waitcnt vmcnt(0)" ::: "memory");
        STAMP(5);
        wd_ld(1, 3);
        if (!early) { WAITS(C_HRDY, 1); hb_ld(); STAMP(11); f2_mm(0); }
        SB();
        f2_mm(1);
        f2_st(0, t10);
        SB();
        f2_st(1, t10);
        STAMP(12);
      }
      ROLE_END();
    }
  }
}

extern "C" void kernel_launch(void* const* d_in, const int* in_sizes, int n_in,
                              void* d_out, int out_size, void* d_ws, size_t ws_size,
                              hipStream_t stream) {
  k_main<<<256, 768, 0, stream>>>((const u16*)d_in[0], (const u16*)d_in[8], (const u16*)d_in[12],
                                  (const u16*)d_in[1], (const u16*)d_in[2], (const u16*)d_in[3],
                                  (const u16*)d_in[4], (const u16*)d_in[5], (const u16*)d_in[6],
                                  (const u16*)d_in[9], (const u16*)d_in[10], (const u16*)d_in[11],
                                  (char*)d_ws, (u16*)d_out);
}
```

```cpp
#include <hip/hip_runtime.h>
#include <stdint.h>

typedef unsigned short u16;
typedef unsigned u32;
typedef _Float16 h2 __attribute__((ext_vector_type(2)));
typedef _Float16 h8 __attribute__((ext_vector_type(8)));
typedef __bf16 b2 __attribute__((ext_vector_type(2)));
typedef __bf16 b8 __attribute__((ext_vector_type(8)));
typedef short s4 __attribute__((ext_vector_type(4)));
typedef float f2 __attribute__((ext_vector_type(2)));
typedef float f4 __attribute__((ext_vector_type(4)));
typedef u32 u2v __attribute__((ext_vector_type(2)));
typedef u32 u3v __attribute__((ext_vector_type(3)));
typedef u32 u4v __attribute__((ext_vector_type(4)));
typedef __attribute__((address_space(3))) s4* lds_s4p;

constexpr int L = 2048;
constexpr int D = 1024;
constexpr int NM = 128;
constexpr int F = 3584;
constexpr int NTAP = 1024;

constexpr int SEG_I = 576, SEG_O0 = 800, SEG_O1 = 880, SEG_SP = 896;

#define AI __attribute__((always_inline))
#define SB() __builtin_amdgcn_sched_barrier(0)
#define OPQ(x) asm volatile("" : "+v"(x))
#define STAMP0() do {} while (0)
#define STAMP(i)
#define STAMP_END()
#define STAMP_T0

__device__ __forceinline__ float bflo(u32 v) { return __uint_as_float(v << 16); }
__device__ __forceinline__ float bfhi(u32 v) { return __uint_as_float(v & 0xffff0000u); }
__device__ __forceinline__ u32 pk_bf(float a, float b) { f2 v = {a, b}; b2 r = __builtin_convertvector(v, b2); return __builtin_bit_cast(u32, r); }
__device__ __forceinline__ float rb1(float v) { return __uint_as_float(pk_bf(0.f, v)); }
__device__ __forceinline__ h8 as_h8(u4v v) { return __builtin_bit_cast(h8, v); }
__device__ __forceinline__ b8 as_b8(u4v v) { return __builtin_bit_cast(b8, v); }
__device__ __forceinline__ u2v tr_read(const char* p) {
  s4 t = __builtin_amdgcn_ds_read_tr16_b64_v4i16((lds_s4p)p);
  return __builtin_bit_cast(u2v, t);
}
template <int CTRL> __device__ __forceinline__ u32 dpp_u(u32 v) { return (u32)__builtin_amdgcn_update_dpp(0, (int)v, CTRL, 0xf, 0xf, false); }
template <int CTRL> __device__ __forceinline__ float dpp_f(float v) { return __uint_as_float(dpp_u<CTRL>(__float_as_uint(v))); }
__device__ __forceinline__ u32 wave_max_u32(u32 v) {
  v = max(v, dpp_u<0x111>(v)); v = max(v, dpp_u<0x112>(v)); v = max(v, dpp_u<0x114>(v)); v = max(v, dpp_u<0x118>(v));
  v = max(v, dpp_u<0x142>(v)); v = max(v, dpp_u<0x143>(v));
  return (u32)__builtin_amdgcn_readlane((int)v, 63);
}
#define DPM " row_mask:0xf bank_mask:0xf bound_ctrl:1\n\t"
#define T2S(HO, HI, LI, C) "v_max_u32_dpp " HO ", " HI ", " HI " " C DPM "v_min_u32_dpp %8, " HI ", " HI " " C DPM "v_max_u32_dpp %7, " LI ", " LI " " C DPM "v_max_u32 %7, %7, %8\n\t"
#define T2R(RM, REST) T2S("%5", "%0", "%1", "row_shr:1") T2S("%6", "%5", "%7", "row_shr:2") T2S("%5", "%6", "%7", "row_shr:4") T2S("%6", "%5", "%7", "row_shr:8") \
    T2S("%5", "%6", "%7", "row_bcast:15") T2S("%6", "%5", "%7", "row_bcast:31") \
    "v_readlane_b32 %9, %6, 63\n\tv_readlane_b32 %10, %7, 63\n\ts_nop 0\n\tv_mov_b32 %5, %9\n\tv_cmp_le_u32 %11, %10, %0\n\tv_cmp_le_u32 %12, %10, %1\n\tv_mov_b32 %6, %10\n\t" \
    "v_cndmask_b32 %0, %0, %1, %11\n\tv_mov_b32_dpp %4, %5 quad_perm:[0,1,2,3] row_mask:" RM " bank_mask:0x3\n\tv_cndmask_b32 %0, %0, %2, %12\n\t" \
    "v_mov_b32_dpp %4, %6 quad_perm:[0,1,2,3] row_mask:" RM " bank_mask:0xc\n\t" REST
#define T2P "v_cndmask_b32 %1, %1, %2, %11\n\tv_cndmask_b32 %2, %2, %3, %11\n\tv_cndmask_b32 %1, %1, %3, %12\n\tv_cndmask_b32 %3, %3, 0, %11\n\tv_cndmask_b32 %2, %2, 0, %12\n\t"
__device__ __forceinline__ u32 wave_top8(u32& m1, u32 m2, u32 m3, u32 m4, u32& w2) {
  u32 key, ta, tb, lo, t, w1;
  unsigned long long c1, c2;
  asm("s_nop 1\n\t" T2R("0x1", T2P) T2R("0x2", T2P) T2R("0x4", T2P) T2R("0x8", "") "s_nop 0"
      : "+v"(m1), "+v"(m2), "+v"(m3), "+v"(m4), "=&v"(key), "=&v"(ta), "=&v"(tb), "=&v"(lo), "=&v"(t), "=&s"(w1), "=&s"(w2), "=&s"(c1), "=&s"(c2));
  return key;
}

__device__ __forceinline__ float exp_bf(float v) {
  const float e = __builtin_amdgcn_exp2f(v * 1.44268798828125f);
  return fmaf(e, v * (7.0526077e-06f * 0.69314718f), e);
}
struct cf { f2 r, i; };
struct ch { h2 r, i; };
__device__ __forceinline__ cf cmul(cf a, cf b) { cf c; c.r = a.r * b.r - a.i * b.i; c.i = a.r * b.i + a.i * b.r; return c; }
__device__ __forceinline__ ch hmul(ch a, ch b) {
  ch c;
  asm("v_pk_mul_f16 %0, %2, %4\n\tv_pk_mul_f16 %1, %2, %5\n\tv_pk_fma_f16 %0, %3, %5, %0 neg_lo:[1,0,0] neg_hi:[1,0,0]\n\tv_pk_fma_f16 %1, %3, %4, %1"
      : "=&v"(c.r), "=&v"(c.i) : "v"(a.r), "v"(a.i), "v"(b.r), "v"(b.i));
  return c;
}
__device__ __forceinline__ cf csq(cf a) { cf c; c.r = a.r * a.r - a.i * a.i; c.i = (a.r + a.r) * a.i; return c; }
__device__ __forceinline__ ch toh(cf a) { ch c; c.r = __builtin_convertvector(a.r, h2); c.i = __builtin_convertvector(a.i, h2); return c; }
__device__ __forceinline__ ch tohc(cf a) { ch c; c.r = __builtin_convertvector(a.r, h2); c.i = __builtin_convertvector(-a.i, h2); return c; }
__device__ __forceinline__ h2 ash(u32 v) { return __builtin_bit_cast(h2, v); }
__device__ __forceinline__ cf cexp_p(f2 a2, f2 brev, float p) {
  f2 e = a2 * p, t = brev * p;
  f2 mag = {__builtin_amdgcn_exp2f(e[0]), __builtin_amdgcn_exp2f(e[1])};
  float r0 = __builtin_amdgcn_fractf(t[0]), r1 = __builtin_amdgcn_fractf(t[1]);
  cf c;
  c.r = mag * f2{__builtin_amdgcn_cosf(r0), __builtin_amdgcn_cosf(r1)};
  c.i = mag * f2{__builtin_amdgcn_sinf(r0), __builtin_amdgcn_sinf(r1)};
  return c;
}
__device__ __forceinline__ u32 med3u(u32 a, u32 b, u32 c) { u32 r; asm("v_med3_u32 %0, %1, %2, %3" : "=v"(r) : "v"(a), "v"(b), "v"(c)); return r; }
__device__ __forceinline__ f2 unbf(u32 v) { return f2{bflo(v), bfhi(v)}; }
__device__ __forceinline__ float sigm(float v) { return __builtin_amdgcn_rcpf(1.f + __builtin_amdgcn_exp2f(-1.44269504088896341f * v)); }

#define TS2(A, B, C, D) "v_xor_b32 %5, " A ", %9\n\tv_xor_b32 %6, " B ", %9\n\tv_xor_b32 %7, " C ", %9\n\tv_xor_b32 %8, " D ", %9\n\t" \
    "v_fma_f32 %1, %5, 1.0, %1\n\tv_fma_f32 %2, %6, 1.0, %2\n\tv_or3_b32 %0, %0, %5, %6\n\tv_fma_f32 %3, %7, 1.0, %3\n\tv_fma_f32 %4, %8, 1.0, %4\n\tv_or3_b32 %0, %0, %7, %8\n\t"
#define TSR TS2("%10", "%11", "%12", "%13") TS2("%14", "%15", "%16", "%17") TS2("%18", "%19", "%20", "%21")
#define TSI "=&v"(t0), "=&v"(t1), "=&v"(t2), "=&v"(t3) : "v"(t10), "v"(p[0]), "v"(p[1]), "v"(p[2]), "v"(p[3]), "v"(q[0]), "v"(q[1]), "v"(q[2]), "v"(q[3]), \
    "v"(r[0]), "v"(r[1]), "v"(r[2]), "v"(r[3])
__device__ __forceinline__ f4 tsum4(u32& bad, u32 t10, u4v s, u4v p, u4v q, u4v r) {
  float s0, s1, s2, s3; u32 t0, t1, t2, t3;
  asm volatile("v_xor_b32 %1, %22, %9\n\tv_xor_b32 %2, %23, %9\n\tv_xor_b32 %3, %24, %9\n\tv_xor_b32 %4, %25, %9\n\tv_or3_b32 %0, %0, %1, %2\n\tv_or3_b32 %0, %0, %3, %4\n\t" TSR
               : "+v"(bad), "=&v"(s0), "=&v"(s1), "=&v"(s2), "=&v"(s3), TSI, "v"(s[0]), "v"(s[1]), "v"(s[2]), "v"(s[3]));
  return f4{s0, s1, s2, s3};
}
__device__ __forceinline__ f4 tsum3(u32& bad, u32 t10, f4 s, u4v p, u4v q, u4v r) {
  float s0 = s[0], s1 = s[1], s2 = s[2], s3 = s[3]; u32 t0, t1, t2, t3;
  asm volatile(TSR : "+v"(bad), "+v"(s0), "+v"(s1), "+v"(s2), "+v"(s3), TSI);
  return f4{s0, s1, s2, s3};
}
__device__ __forceinline__ void silu4(f4& g, float nl2e) {
  float g0 = g[0], g1 = g[1], g2 = g[2], g3 = g[3], t0, t1, t2, t3;
  asm volatile("v_fma_f32 %4, %0, %8, 0\n\tv_fma_f32 %5, %1, %8, 0\n\tv_fma_f32 %6, %2, %8, 0\n\tv_fma_f32 %7, %3, %8, 0\n\t"
               "v_exp_f32 %4, %4\n\tv_exp_f32 %5, %5\n\tv_exp_f32 %6, %6\n\tv_exp_f32 %7, %7\n\t"
               "v_fma_f32 %4, %4, 1.0, 1.0\n\tv_fma_f32 %5, %5, 1.0, 1.0\n\tv_fma_f32 %6, %6, 1.0, 1.0\n\tv_fma_f32 %7, %7, 1.0, 1.0\n\t"
               "v_rcp_f32 %4, %4\n\tv_rcp_f32 %5, %5\n\tv_rcp_f32 %6, %6\n\tv_rcp_f32 %7, %7\n\t"
               "v_fma_f32 %0, %0, %4, 0\n\tv_fma_f32 %1, %1, %5, 0\n\tv_fma_f32 %2, %2, %6, 0\n\tv_fma_f32 %3, %3, %7, 0"
               : "+v"(g0), "+v"(g1), "+v"(g2), "+v"(g3), "=&v"(t0), "=&v"(t1), "=&v"(t2), "=&v"(t3) : "v"(nl2e));
  g = f4{g0, g1, g2, g3};
}

constexpr int KT_PITCH = 52;
constexpr int kt_off(int p) {
  int i = 20, j = 0;
  if (p < 256) { i = p & 7; j = p >> 3; }
  else if (p < 512) { int q = (p - 256) >> 1; i = 8 + (q & 3); j = q >> 2; }
  else if (p < 1024) { int q = (p - 512) >> 2; i = 12 + (q & 1) + 2 * (q >> 6); j = (q >> 1) & 31; }
  else if (p < 2048) { int q = (p - 1024) >> 3; i = 16 + (q >> 5); j = q & 31; }
  return j * KT_PITCH + i * 2;
}
constexpr int KT_ZERO = 32 * KT_PITCH + 16;
constexpr int rnd_lag(int m) {
  const int st = m < 256 ? 1 : m < 512 ? 2 : m < 1024 ? 4 : 8;
  int q = m / st;
  const int rem = m - q * st;
  if (2 * rem > st || (2 * rem == st && (q & 1))) ++q;
  return q * st;
}
constexpr int tap_entry(int e) {
  if (e < 544) {
    if (543 - e <= 525) return kt_off(rnd_lag(543 - e));
  } else if (e >= 560 && e < 752) {
    const int q = (e - 560) / 48, x = (e - 560) - q * 48;
    const int p = 512 + 16 * (31 - x) + 4 * (q + 1);
    if (x < 32 && p > 524) return kt_off(p);
  } else if (e >= 752 && e < 816) {
    return kt_off(1032 + 16 * (815 - e));
  } else if (e >= 832 && e < 896) {
    return kt_off(1040 + 16 * (895 - e));
  } else if (e == SEG_SP + 27 || e == SEG_SP + 28) {
    return kt_off(1024);
  }
  return KT_ZERO;
}
struct TapIdx { u16 v[NTAP]; };
constexpr TapIdx make_tapidx() {
  TapIdx t{};
  for (int e = 0; e < NTAP; ++e) t.v[e] = (u16)tap_entry(2 * ((e >> 4) + 64 * ((e >> 1) & 7)) + (e & 1));
  return t;
}
__device__ __attribute__((aligned(16))) const TapIdx g_tapidx = make_tapidx();

constexpr int TROW = 544;
constexpr int O_OUT = 0;
constexpr int OT = 576;
constexpr int O_U = O_OUT + 8 * OT;
constexpr int O_TAP = O_U + 4608;
constexpr int TAP_PITCH = 2 * NTAP + 64;
constexpr int CHB = O_TAP + 16 * TROW + 32;
static_assert(16 * TROW >= 4 * TAP_PITCH, "lds");
static_assert((CHB / 4) % 32 == 8, "banks");
#define DEV 3
#define DWIN 8
#define DSEL 0
constexpr int NSLOT = 5, WWIN = 4;
constexpr int O_GT = 4 * CHB;
constexpr int O_RING = O_GT + 8192;
constexpr int O_WD = O_RING + 2 * NSLOT * 2048;
constexpr int O_FAR = O_WD + 32768;
constexpr int O_XS = O_FAR + 16384;
constexpr int XS_PITCH = 544;
constexpr int O_HB = O_XS + 8 * XS_PITCH;
constexpr int O_PART = O_HB + 2 * 528;
constexpr int O_SEL = O_PART + 512;
constexpr int O_PAR = O_SEL + 32;
constexpr int O_CTL = O_PAR + 16;
constexpr int O_JUNK = O_CTL + 512;
constexpr int K_LDS = O_JUNK + 1024;
static_assert(DWIN == 8, "flags of the down tile");
static_assert(K_LDS <= 163840 && O_RING % 16 == 0, "lds");
enum { C_PART = 0, C_GP, C_PUB, C_SEL, C_XS, C_HRDY, C_WGU, C_TL, C_XL, C_WD = 10, C_TAPS = 12, C_Q = 16, C_FAR = 32, C_TILE = 48 };

#define CTLA(word) (lbase + O_CTL + (word) * 4)
#define ARRIVE(word) asm volatile("s_mov_b64 exec, 1\n\tds_add_u32 %0, %1\n\ts_mov_b64 exec, -1" :: "v"(CTLA(word)), "v"(1) : "memory")
#define WAITQ(word, target) do { u32 t_; int n_ = 1 << 20; asm volatile( \
    "1:\n\tds_read_b32 %0, %2\n\ts_sub_u32 %1, %1, 1\n\ts_cbranch_scc1 2f\n\ts_waitcnt lgkmcnt(0)\n\tv_cmp_gt_u32 vcc, %3, %0\n\ts_cbranch_vccnz 1b\n2:\n\ts_waitcnt lgkmcnt(0)" \
    : "=&v"(t_), "+s"(n_) : "v"(CTLA(word)), "n"(target) : "vcc", "scc", "memory"); } while (0)
#define WAITS(word, target) do { u32 t_; int n_ = 1 << 20; asm volatile( \
    "1:\n\tds_read_b32 %0, %2\n\ts_sub_u32 %1, %1, 1\n\ts_cbranch_scc1 2f\n\ts_waitcnt lgkmcnt(0)\n\tv_cmp_le_u32 vcc, %3, %0\n\ts_cbranch_vccnz 2f\n\ts_sleep 2\n\ts_branch 1b\n2:\n\ts_waitcnt lgkmcnt(0)" \
    : "=&v"(t_), "+s"(n_) : "v"(CTLA(word)), "n"(target) : "vcc", "scc", "memory"); } while (0)
#define BAR() asm volatile("s_waitcnt lgkmcnt(0)\n\ts_barrier" ::: "memory")
__device__ __forceinline__ u2v lds8(const char* p) { return __builtin_bit_cast(u2v, __hip_atomic_load((const unsigned long long*)p, __ATOMIC_RELAXED, __HIP_MEMORY_SCOPE_WORKGROUP)); }

constexpr int WS_EPOCH = 0;
constexpr int WS_HEAD = 1024;
constexpr int WS_REC = 4096;
constexpr int WS_FLG = 40960;
constexpr int WS_KALL = WS_REC + 8192;
constexpr int WS_KEY = 32768;
constexpr int WS_P1 = 65536;
constexpr int WS_P2 = WS_P1 + 56 * 16384;
constexpr int WS_XT = WS_P2 + 256 * 7168;
constexpr int XT_PITCH = 33280;
constexpr int WS_END = WS_XT + 128 * XT_PITCH;
constexpr int SPIN_MAX = 1 << 16;
constexpr u32 TMASK = 0x3ffu;

#define DT_HEAD 110
#define DT_ARG 45
#define WDELAY 60
#define DT_KEY 225
#define DT_GO 40
#define DT_REC 255
#define DT_FLG 365
#define DT_VIS 40
#define DE_P1 385
#define DE_P2 620
#define DE_FAST 605
#define DT_MIN 25
#define DT_P1 100
#define DT_P2 130
#define SLEEP_AG1 12
#define SLEEP_G 6
#define STSLP 2
#define STWIN 4
#define H2_AT 9
#define CODE_B 28400

#define ROLE_END() do { STAMP_END(); asm volatile("s_endpgm" ::: "memory"); } while (0)
__device__ __forceinline__ u32 now32() { return (u32)__builtin_amdgcn_s_memrealtime(); }
__device__ __forceinline__ u32 later(u32 a, u32 b) { return (int)(b - a) > 0 ? b : a; }
__device__ __forceinline__ void pace(u32 t) {
#pragma unroll 1
  for (int i = 0; i < 1024 && (int)(now32() - t) < 0; ++i) __builtin_amdgcn_s_sleep(1);
}
__device__ __forceinline__ void pace2(u32 n, u32 t, int dmax) {
  int d = (int)(t - n);
  d = d < DT_MIN ? DT_MIN : (d > dmax ? dmax : d);
  pace(n + (u32)d);
}

#define ALL_OTHER "vcc", "scc", "memory", \
    "s0","s1","s2","s5","s6","s7","s33","s34","s35","s44","s45","s46","s47","s48","s49","s50","s51","s52","s53","s54","s55","s56","s57","s58","s59", \
    "s60","s61","s62","s63","s64","s65","s66","s67","s68","s69","s70","s71","s72","s73","s74","s75","s76","s77","s78","s79","s80","s81","s82","s83","s84", \
    "s85","s86","s87","s88","s89","s90","s91","s92","s93","s94","s95","s96","s97","s98","s99", \
    "v0","v1","v2","v3","v4","v5","v6","v7","v8","v9","v10","v11","v12","v13","v14","v15","v16","v17","v18","v19","v20","v21","v22","v23","v24","v25", \
    "v26","v27","v28","v29","v30","v31","v32","v33","v34","v35","v36","v37","v38","v39","v40","v41","v42","v43","v44","v45","v46","v47","v48","v49","v50", \
    "v51","v52","v53","v54","v55","v56","v57","v58","v59","v60","v61","v62","v63","v64","v65","v66","v67","v68","v69","v70","v71","v72","v73","v74","v75", \
    "v76","v77","v78","v79","v80","v81","v82","v83","v84","v85","v86","v87","v88","v89","v90","v91","v92","v93","v94","v95","v96","v97","v98","v99", \
    "v100","v101","v102","v103","v104","v105","v106","v107","v108","v109","v110","v111","v112","v113","v114","v115","v116","v117","v118","v119","v120", \
    "v121","v122","v123","v124","v125","v126","v127","v128","v129","v130","v131","v132","v133","v134","v135","v136","v137","v138","v139","v140","v141", \
    "v142","v143","v144","v145","v146","v147","v148","v149","v150","v151","v152","v153","v154","v155","v156","v157","v158","v159","v160","v161","v162", \
    "v163","v164","v165","v166","v167"
typedef __attribute__((address_space(1))) const u16* gp16;
typedef __attribute__((address_space(1))) const u32* gp32;
typedef __attribute__((address_space(1))) const u4v* gp128;
typedef unsigned long long u64;
typedef u64 q2 __attribute__((ext_vector_type(2)));
typedef u64 q4 __attribute__((ext_vector_type(4)));
typedef u64 q8 __attribute__((ext_vector_type(8)));

__global__ void __launch_bounds__(768, 3) k_main(
    const u16* x_, const u16* w_r_, const u16* gate_w_, const u16* logA_, const u16* Aim_, const u16* Cre_, const u16* Cim_,
    const u16* log_dt_, const u16* Dskip_, const u16* w_gate_, const u16* w_up_, const u16* w_down_, char* wsp_, u16* out_) {
  __shared__ __attribute__((aligned(256))) char smem[K_LDS];
  const u32 lbase = (u32)(uintptr_t)(__attribute__((address_space(3))) char*)smem;
#define XDMA(OFF) "global_load_lds_dwordx4 v1, s[8:9] offset:" #OFF " nt\n\ts_addk_i32 m0, 896\n\t"
  asm volatile("s_load_dwordx16 s[8:23], %0, 0\n\tv_readfirstlane_b32 s3, %2\n\ts_load_dwordx8 s[24:31], %0, 64\n\ts_load_dwordx4 s[36:39], %0, 96\n\t"
               "s_memrealtime s[40:41]\n\t" STAMP_T0 "s_mov_b32 s4, %1\n\ts_lshr_b32 s3, s3, 6\n\t"
               "s_cmp_lt_u32 s3, 4\n\ts_cbranch_scc1 .Lrows\n\ts_barrier\n\t"
               "s_cmp_ge_u32 s3, 8\n\ts_cbranch_scc1 .Lrole_g\n\ts_cmp_eq_u32 s3, 7\n\ts_cbranch_scc1 .Lrole_d\n\t"
               "s_cmp_ge_u32 s3, 5\n\ts_cbranch_scc1 .Lrole_h\n\ts_branch .Lrole_c\n.Lrows:\n\t"
               "s_lshr_b32 s5, s4, 3\n\tv_lshlrev_b32 v3, 2, %2\n\ts_mov_b32 m0, %4\n\tv_lshl_or_b32 v3, s5, 10, v3\n\ts_getpc_b64 s[6:7]\n\t"
               "v_min_u32 v3, %3, v3\n\tv_lshrrev_b32 v1, 3, %2\n\tglobal_load_lds_dword v3, s[6:7]\n\t"
               "v_and_b32 v2, 6, v1\n\ts_and_b32 s34, s4, 7\n\tv_xor_b32 v2, v2, %2\n\ts_lshl_b32 s34, s34, 5\n\tv_and_b32 v2, 7, v2\n\ts_add_u32 s5, s5, s34\n\t"
               "v_and_b32 v1, 7, v1\n\ts_lshl_b32 s5, s5, 5\n\tv_lshlrev_b32 v1, 11, v1\n\ts_add_u32 s5, s5, s3\n\tv_lshl_or_b32 v1, v2, 4, v1\n\t"
               "s_mul_i32 s34, s3, %5\n\tv_lshl_add_u32 v1, s5, 9, v1\n\ts_add_u32 m0, s34, %6\n\tv_and_b32 v2, 31, %2\n\ts_waitcnt lgkmcnt(0)\n\t"
               XDMA(0) "s_lshl_b32 s5, s3, 5\n\t" XDMA(128) "v_add_lshl_u32 v2, s5, v2, 4\n\t" XDMA(256) "s_lshl_b32 s5, s3, 10\n\t"
               "global_load_lds_dwordx4 v1, s[8:9] offset:384 nt\n\ts_add_u32 m0, s5, %7\n\ts_mov_b32 exec_hi, 0\n\t"
               "global_load_lds_dwordx4 v2, s[10:11]\n\ts_addk_i32 m0, 512\n\ts_nop 0\n\tglobal_load_lds_dwordx4 v2, s[12:13]\n\t"
               "s_mov_b32 m0, %8\n\tv_mov_b32 v3, 0\n\tglobal_load_lds_dword v3, s[36:37]\n\ts_mov_b32 exec_hi, -1"
               :: "s"(__builtin_amdgcn_kernarg_segment_ptr()), "s"(blockIdx.x), "v"(threadIdx.x), "n"(CODE_B - 256), "n"(O_JUNK), "n"(CHB), "n"(O_U + 512), "n"(O_FAR), "n"(O_XS)
               : "s3", "s4", "s5", "s6", "s7", "s8", "s9", "s10", "s11", "s12", "s13", "s14", "s15", "s16", "s17", "s18", "s19", "s20", "s21", "s22", "s23",
                 "s24", "s25", "s26", "s27", "s28", "s29", "s30", "s31", "s34", "s36", "s37", "s38", "s39", "s40", "s41", "s42", "s43", "v1", "v2", "v3", "scc", "memory");
  int lane, c16, g, w, b, xg, xi, cg, cc, c, d;
  char* ch_;
  q8 ka; q4 kb; q2 kc, kt;
  u32* ctl = (u32*)(smem + O_CTL);
  u32* par = (u32*)(smem + O_PAR);
#define IDS() do { int z_ = 0; OPQ(z_); lane = __builtin_amdgcn_mbcnt_hi(~0u, __builtin_amdgcn_mbcnt_lo(~0u, z_)); c16 = lane & 15; g = lane >> 4; } while (0)
#define ROLE(name) \
  asm volatile(".Lrole_" name ":\n\ts_waitcnt lgkmcnt(0)" : "={s[8:23]}"(ka), "={s[24:31]}"(kb), "={s[36:39]}"(kc), "={s[40:43]}"(kt), "={s3}"(w), "={s4}"(b) :: ALL_OTHER); \
  if (__builtin_expect(w > 11, 0)) asm volatile("s_endpgm" ::: "memory");        \
  IDS(); \
  xg = b & 7; xi = b >> 3;                          \
  cg = xg * 32 + xi;                                \
  cc = (xi >> 4) * 128 + xg * 16 + (xi & 15);       \
  c = w & 3; ch_ = smem + c * CHB; d = cc * 4 + c; \
  const u32 ts0 = (u32)kt[0]; \
  auto wrs = __builtin_amdgcn_make_buffer_rsrc((void*)kc[0], 0, WS_END, 0x00020000); \
  STAMP0()

  const char* tapb;
  const char* ub;
  char* erp;
  const char* eup;
  const char* egp;
  u4v At[17], band[4];
  float dm1;
  f4 acc[8];
  auto conv_setup = [&]() AI {
    tapb = ch_ + O_TAP + ((15 - c16) & 3) * TAP_PITCH + ((15 - c16) & ~3) * 2;
    ub = ch_ + O_U - 32 + c16 * 32 + g * 16;
    erp = ch_ + O_OUT + g * 144 + c16 * 8;
    eup = ch_ + O_U + 512 + c16 * 32 + g * 8;
    egp = smem + O_GT + lane * 16;
  };
  auto band_setup = [&]() AI {
#pragma unroll
    for (int k = 0; k < 4; ++k) {
      const int ctr = k == 0 ? 12 : 8, hw = k + 1;
      const u32 m = ((1u << (2 * hw + 1)) - 1u) << (ctr - hw);
      const u32 bits = (m << c16) >> (8 * g);
#pragma unroll
      for (int j = 0; j < 4; ++j)
        band[k][j] = (__builtin_amdgcn_sbfe(bits, 2 * j, 1) & 0x3f80u) | (__builtin_amdgcn_sbfe(bits, 2 * j + 1, 1) & 0x3f800000u);
      OPQ(band[k]);
    }
  };
  auto frag2 = [&](const char* pa, const char* pb) AI { const u2v lo = lds8(pa), hi = lds8(pb); return u4v{lo[0], lo[1], hi[0], hi[1]}; };
  auto atload = [&](int s0, int s1) AI {
#pragma unroll
    for (int s = s0; s < s1; ++s) At[s] = frag2(tapb + 64 * s + g * 16, tapb + 64 * s + g * 16 + 8);
  };
  auto bin = [&](int pair, int be) AI { const char* p = tapb + g * 8 + (SEG_I + 96 * pair - 16 * be) * 2; return frag2(p, p + 96); };
  auto bout = [&](int be) AI { const char* p = tapb + g * 8 + (SEG_O0 - 16 * be) * 2; return frag2(p, p + (SEG_O1 - SEG_O0) * 2); };
  auto asp = [&]() AI { return frag2(tapb + SEG_SP * 2 + g * 16, tapb + SEG_SP * 2 + g * 16 + 8); };
  auto mma = [&](u4v a, u4v bq, f4 cin) AI { return __builtin_amdgcn_mfma_f32_16x16x32_bf16(as_b8(a), as_b8(bq), cin, 0, 0, 0); };
  auto uld = [&](int sg) AI { return *(const u4v*)(ub + 512 + sg * 64); };
  auto uld24 = [&](int un) AI { return *(const u4v*)(ub + 512 + un * 512 - 16); };
  u2v euw[8], eo[8]; f4 egv[8], efv[8];
  float eu[8][4], ev[8][4], ea[8][4];
  u4v Fq[57], ru[6], wa_[6][3];
  f4 wq[6][6];
  u32 pk[12], one, qa, fa;
  char* tfp; char* fp_;
#define LOPQ(p_) do { u32 a_ = (u32)(uintptr_t)(__attribute__((address_space(3))) const char*)p_; OPQ(a_); p_ = (decltype(p_))(__attribute__((address_space(3))) char*)(uintptr_t)a_; } while (0)
  auto m_setup = [&]() AI {
    tfp = smem + (lane == 0 ? O_CTL + c * 4 : O_JUNK + lane * 4);
    fp_ = smem + O_FAR + c * 4096 + lane * 16;
    qa = CTLA(C_Q) + (lane & 3) * 4; fa = CTLA(C_FAR + c); one = 1u;
    OPQ(qa); OPQ(fa); OPQ(one);
    LOPQ(tfp); LOPQ(fp_); LOPQ(erp); LOPQ(eup); LOPQ(egp); LOPQ(ub);
  };
#define WAITA(addr) do { u32 t_; int n_ = 1 << 20; asm volatile( \
    "1:\n\tds_read_b32 %0, %2\n\ts_sub_u32 %1, %1, 1\n\ts_cbranch_scc1 2f\n\ts_waitcnt lgkmcnt(0)\n\tv_cmp_eq_u32 vcc, 0, %0\n\ts_cbranch_vccnz 1b\n2:\n\ts_waitcnt lgkmcnt(0)" \
    : "=&v"(t_), "+s"(n_) : "v"(addr) : "vcc", "scc", "memory"); } while (0)
#define MF(d_, a_, b_) asm volatile("v_mfma_f32_16x16x32_bf16 %0, %1, %2, %0" : "+v"(d_) : "v"(a_), "v"(b_)); SB()
#define MF0(d_, a_, b_) asm volatile("v_mfma_f32_16x16x32_bf16 %0, %1, %2, 0" : "=&v"(d_) : "v"(a_), "v"(b_)); SB()
#define NOP(n_) asm volatile("s_nop " #n_); SB()
#define FL(i_, sg_) Fq[i_] = uld(sg_); SB()
#define RL(un_) ru[un_] = uld24(un_); SB()
#define AL(s_) atload(s_, s_ + 1); SB()
#define ULD(t_) euw[t_] = *(const u2v*)(eup + t_ * 512); SB()
#define GLD(t_) egv[t_] = *(const f4*)(egp + t_ * 1024); SB()
#define FLD(t_) efv[t_] = *(const f4*)(fp_ + (t_ - 2) * 1024); SB()
#define FST(un_) *(f4*)(fp_ + un_ * 1024) = acc[un_ + 2]; SB()
#define EUN(t_, e_) eu[t_][e_] = (e_ & 1) ? bfhi(euw[t_][e_ >> 1]) : bflo(euw[t_][e_ >> 1]); OPQ(eu[t_][e_]); ev[t_][e_] = eu[t_][e_]; SB()
#define EA1(t_, e_) ea[t_][e_] = fmaf(eu[t_][e_], dm1, acc[t_][e_]); OPQ(ea[t_][e_]); SB()
#define EFA(t_, e_) ev[t_][e_] = fmaf(egv[t_][e_], efv[t_][e_], eu[t_][e_]); OPQ(ev[t_][e_]); SB()
#define EA2(t_, e_) ea[t_][e_] = fmaf(egv[t_][e_], ea[t_][e_], ev[t_][e_]); OPQ(ea[t_][e_]); SB()
#define EPK(t_, k_) { u32 v_ = pk_bf(ea[t_][2 * k_], ea[t_][2 * k_ + 1]); OPQ(v_); eo[t_][k_] = v_; } SB()
#define OST(t_) *(u2v*)(erp + t_ * OT) = eo[t_]; SB()
#define WPK(un_, p_, j_) { u32 v_ = pk_bf(wq[un_][2 * p_ + j_ / 2][2 * (j_ & 1)], wq[un_][2 * p_ + j_ / 2][2 * (j_ & 1) + 1]); OPQ(v_); wa_[un_][p_][j_] = v_; } SB()
#define FLAG(w_) asm volatile("" ::: "memory"); *(u32*)(tfp + (w_) * 4) = one; SB()
#define PEEK(i_, a_) pk[i_] = __hip_atomic_load((__attribute__((address_space(3))) u32*)(uintptr_t)(a_), __ATOMIC_RELAXED, __HIP_MEMORY_SCOPE_WORKGROUP); SB()
#define CHECK(i_, a_) if (__builtin_expect(__builtin_amdgcn_ballot_w64(pk[i_] == 0u) != 0ull, 0)) WAITA(a_); asm volatile("" ::: "memory"); SB()
#define MIX2(nm, nd, nv) _Pragma("unroll") for (int r_ = 0; r_ < (nm); ++r_) { __builtin_amdgcn_sched_group_barrier(0x008, 1, 0); \
    if (nd) __builtin_amdgcn_sched_group_barrier(0x100, (nd), 0); if (nv) __builtin_amdgcn_sched_group_barrier(0x002, (nv), 0); }

  {
    ROLE("x");
    const int xw = w;
    char* img = ch_ + O_U + 512;
    STAMP(11);
    __builtin_amdgcn_s_setprio(2);
    { u32 zz = 0u; OPQ(zz); if (lane < 32) ctl[32 * xw + lane] = zz; }
    if (xi == 31 && w == 0) { const u32 th = later(ts0, now32() - DT_ARG); if (lane == 0) __builtin_amdgcn_raw_buffer_store_b64(u2v{th, ~th}, wrs, WS_HEAD + xg * 8, 0, 16); }
    BAR();
    STAMP(12);
    const char* wp; const char* fp; const char* tp0; const char* tp1; char* pp;
    {
      const int r = lane & 7, q = (lane >> 2) & 3;
      fp = img + r * 128 + ((((lane & 8) / 2 + g) ^ (r & 6)) * 16);
      wp = smem + O_FAR + xw * 1024 + (lane & 1) * 512 + (lane & 4) * 16 + g * 16;
      tp0 = img + q * 128 + (((2 * g + ((lane >> 1) & 1)) ^ (q & 2)) * 16) + (lane & 1) * 8;
      tp1 = img + 512 + q * 128 + (((2 * g + ((lane >> 1) & 1)) ^ ((q & 2) + 4)) * 16) + (lane & 1) * 8;
      pp = smem + ((lane >> 3) == 0 || (lane >> 3) == 3 ? O_PART + r * 64 + xw * 16 + g * 8 : O_JUNK + lane * 8);
    }
    const int so = WS_XT + (32 * xw + (lane >> 3)) * XT_PITCH + cg * 128 + (lane & 7) * 16, sstep = 8 * XT_PITCH;
    u32 lo_ = WS_XT + (cc >> 1) * XT_PITCH + (16 * xw + (lane >> 2)) * 128 + (cc & 1) * 64 + (lane & 3) * 16;
    OPQ(lo_);
    SB();
    asm volatile("s_waitcnt vmcnt(0)" ::: "memory");
    STAMP(1);
    {
      u4v wa[4], fb[4]; u2v ta[4], tb[4];
#pragma unroll
      for (int s = 0; s < 4; ++s) { wa[s] = *(const u4v*)(wp + s * 128); fb[s] = *(const u4v*)(fp + s * 1024); }
      SB();
      f4 r = {0.f, 0.f, 0.f, 0.f};
#pragma unroll
      for (int s = 0; s < 4; ++s) r = mma(wa[s], fb[s], r);
      *(f2*)pp = f2{r[0], r[1]};
      ARRIVE(C_PART);
      SB();
#pragma unroll
      for (int s = 0; s < 4; ++s) { ta[s] = tr_read(tp0 + s * 1024); tb[s] = tr_read(tp1 + s * 1024); }
      SB();
      WAITQ(C_GP, 1);
#pragma unroll
      for (int s = 0; s < 4; ++s) __builtin_amdgcn_raw_buffer_store_b128(u4v{ta[s][0], ta[s][1], tb[s][0], tb[s][1]}, wrs, so + s * sstep, 0, 16);
    }
    STAMP(2);
    const u32 dskw = ((gp16)kb[0])[d];
    conv_setup();
    band_setup();
    const u32 tag = par[0];
    ARRIVE(C_PUB);
    asm volatile("s_waitcnt vmcnt(0)" ::: "memory");
    if (lane == 0) __builtin_amdgcn_raw_buffer_store_b32(tag, wrs, WS_FLG + xw * 1024 + cg * 4, 0, 16);
    STAMP(3);
    dm1 = bflo(dskw) - 1.f;
    WAITS(C_TL, 1);
    const u32 tl0 = (u32)__builtin_amdgcn_readfirstlane((int)par[1]);
    u3v fq[2];
    auto rec_ld = [&]() AI {
#pragma unroll
      for (int k = 0; k < 2; ++k) fq[k] = __builtin_amdgcn_raw_buffer_load_b96(wrs, WS_REC + xw * 2048 + k * 1024 + lane * 16, 0, 16);
    };
    pace(tl0 + DT_REC);
    rec_ld();
    SB();
    pace(tl0 + DT_FLG);
    const u32 fl = __builtin_amdgcn_raw_buffer_load_b32(wrs, WS_FLG + (cc >> 6) * 1024 + xw * 64 + g * 256 + c16 * 4, 0, 16);
    SB();
    {
      if (__builtin_expect(__any(fq[0][2] != tag || fq[1][2] != tag), 0)) {
#pragma unroll 1
        for (int spin = 0; spin < SPIN_MAX; ++spin) {
          __builtin_amdgcn_s_sleep(SLEEP_G);
          rec_ld();
          if (!__any(fq[0][2] != tag || fq[1][2] != tag)) break;
          asm volatile("" ::: "memory");
        }
      }
      char* gp = smem + O_GT + xw * 2048 + (lane & 3) * 256 + (lane >> 2) * 16;
#pragma unroll
      for (int k = 0; k < 2; ++k) *(f4*)(gp + k * 1024) = f4{bflo(fq[k][0]), bfhi(fq[k][0]), bflo(fq[k][1]), bfhi(fq[k][1])};
    }
    SB();
    u4v xq[4];
    bool late[4];
    {
      const u64 bad = __builtin_amdgcn_ballot_w64(fl != tag);
      asm volatile("" ::: "memory");
#pragma unroll
      for (int k = 0; k < 4; ++k) {
        late[k] = ((bad >> (16 * k)) & 0xffffull) != 0ull;
        xq[k] = __builtin_amdgcn_raw_buffer_load_b128(wrs, lo_ | (late[k] ? 1u << 30 : 0u), k * 8192, 16);
        SB();
      }
    }
    ARRIVE(C_XL);
    STAMP(4);
    WAITQ(C_TAPS + c, 1);
    u4v Bi[2][3], Bo[4], As;
#pragma unroll
    for (int be = 0; be < 3; ++be) { Bi[0][be] = bin(0, be); Bi[1][be] = bin(1, be); }
#pragma unroll
    for (int be = 0; be < 4; ++be) Bo[be] = bout(be);
    As = asp();
    WAITQ(C_PUB, 4);
    char* dp = smem + (lane & 3) * CHB + O_U + 512 + xw * 256 + (lane >> 2) * 16;
    m_setup();
    auto stage = [&](int k) AI {
      SB();
      if (__builtin_expect(late[k], 0)) {
#pragma unroll 1
        for (int spin = 0; spin < SPIN_MAX; ++spin) {
          STAMP(14);
          const u32 f1 = __builtin_amdgcn_raw_buffer_load_b32(wrs, WS_FLG + (cc >> 6) * 1024 + xw * 64 + k * 256 + c16 * 4, 0, 16);
          if (!__any(f1 != tag)) break;
          __builtin_amdgcn_s_sleep(SLEEP_G);
          asm volatile("" ::: "memory");
        }
        asm volatile("" ::: "memory");
        xq[k] = __builtin_amdgcn_raw_buffer_load_b128(wrs, lo_, k * 8192, 16);
        asm volatile("s_waitcnt vmcnt(0)" : "+v"(xq[k]) :: "memory");
      }
      *(u4v*)(dp + k * 1024) = xq[k];
      FLAG(C_Q + k * 4);
    };
    stage(0);
    stage(1);
    STAMP(5);
    __builtin_amdgcn_s_setprio(0);
    WAITA(qa);
    SB();
    FL(0, 0); RL(0); FL(8, 8); RL(1);
    MF0(wq[0][0], Fq[0], band[0]);
    MF0(wq[0][1], Fq[0], band[1]);
    MF0(wq[0][2], ru[0], band[0]);
    MF0(wq[0][3], ru[0], band[1]);
    MF0(wq[0][4], Fq[0], band[2]);
    MF0(wq[0][5], ru[0], band[3]);
    MF0(acc[4], As, Fq[0]); PEEK(6, qa + 16);
    MF0(wq[1][0], Fq[8], band[0]); WPK(0, 0, 0); WPK(0, 0, 1);
    MF0(wq[1][1], Fq[8], band[1]); WPK(0, 0, 2); WPK(0, 0, 3);
    MF0(wq[1][2], ru[1], band[0]); WPK(0, 1, 0); WPK(0, 1, 1);
    MF0(wq[1][3], ru[1], band[1]); WPK(0, 1, 2); WPK(0, 1, 3);
    MF0(wq[1][4], Fq[8], band[2]); CHECK(6, qa + 16);
    MF0(wq[1][5], ru[1], band[3]); WPK(0, 2, 0); WPK(0, 2, 1);
    MF0(acc[5], As, Fq[8]); WPK(0, 2, 2); WPK(0, 2, 3);
    MF0(acc[2], wa_[0][0], Bi[0][0]); FL(16, 16); RL(2);
    MF(acc[2], wa_[0][1], Bi[1][0]); WPK(1, 0, 0); WPK(1, 0, 1);
    MF0(acc[3], wa_[0][0], Bi[0][1]); WPK(1, 0, 2); WPK(1, 0, 3);
    MF(acc[3], wa_[0][1], Bi[1][1]); WPK(1, 1, 0); WPK(1, 1, 1);
    MF(acc[4], wa_[0][0], Bi[0][2]); FST(0); FLAG(C_FAR + 0 * 4);
    MF(acc[4], wa_[0][1], Bi[1][2]); WPK(1, 1, 2); WPK(1, 1, 3);
    MF(acc[4], wa_[0][2], Bo[0]); WPK(1, 2, 0); WPK(1, 2, 1);
    MF(acc[5], wa_[0][2], Bo[1]); WPK(1, 2, 2); WPK(1, 2, 3);
    MF0(acc[6], wa_[0][2], Bo[2]);
    MF0(acc[7], wa_[0][2], Bo[3]); stage(2);
    MF0(wq[2][0], Fq[16], band[0]);
    MF0(wq[2][1], Fq[16], band[1]); stage(3);
    MF0(wq[2][2], ru[2], band[0]);
    MF0(wq[2][3], ru[2], band[1]);
    MF0(wq[2][4], Fq[16], band[2]); FL(24, 24); RL(3);
    MF0(wq[2][5], ru[2], band[3]); WPK(2, 0, 0); WPK(2, 0, 1);
    MF(acc[6], As, Fq[16]); WPK(2, 0, 2); WPK(2, 0, 3);
    MF(acc[3], wa_[1][0], Bi[0][0]); WPK(2, 1, 0); WPK(2, 1, 1);
    MF(acc[3], wa_[1][1], Bi[1][0]); WPK(2, 1, 2); WPK(2, 1, 3);
    MF(acc[4], wa_[1][0], Bi[0][1]); WPK(2, 2, 0); WPK(2, 2, 1);
    MF(acc[4], wa_[1][1], Bi[1][1]); WPK(2, 2, 2); WPK(2, 2, 3);
    MF(acc[5], wa_[1][0], Bi[0][2]); FST(1); FLAG(C_FAR + 1 * 4);
    MF(acc[5], wa_[1][1], Bi[1][2]); PEEK(7, qa + 32);
    MF(acc[5], wa_[1][2], Bo[0]);
    MF(acc[6], wa_[1][2], Bo[1]);
    MF(acc[7], wa_[1][2], Bo[2]);
    MF0(wq[3][0], Fq[24], band[0]);
    MF0(wq[3][1], Fq[24], band[1]); CHECK(7, qa + 32);
    MF0(wq[3][2], ru[3], band[0]);
    MF0(wq[3][3], ru[3], band[1]);
    MF0(wq[3][4], Fq[24], band[2]); FL(32, 32); RL(4);
    MF0(wq[3][5], ru[3], band[3]); WPK(3, 0, 0); WPK(3, 0, 1);
    MF(acc[7], As, Fq[24]); WPK(3, 0, 2); WPK(3, 0, 3);
    MF(acc[4], wa_[2][0], Bi[0][0]); WPK(3, 1, 0); WPK(3, 1, 1);
    MF(acc[4], wa_[2][1], Bi[1][0]); WPK(3, 1, 2); WPK(3, 1, 3);
    MF(acc[5], wa_[2][0], Bi[0][1]); WPK(3, 2, 0); WPK(3, 2, 1);
    MF(acc[5], wa_[2][1], Bi[1][1]); FL(40, 40); RL(5);
    MF(acc[6], wa_[2][0], Bi[0][2]); FST(2); FLAG(C_FAR + 2 * 4);
    MF(acc[6], wa_[2][1], Bi[1][2]); WPK(3, 2, 2); WPK(3, 2, 3);
    MF(acc[6], wa_[2][2], Bo[0]); AL(0);
    MF(acc[7], wa_[2][2], Bo[1]); AL(1);
    MF0(wq[4][0], Fq[32], band[0]); AL(2);
    MF0(wq[4][1], Fq[32], band[1]); AL(3);
    MF0(wq[4][2], ru[4], band[0]); AL(4);
    MF0(wq[4][3], ru[4], band[1]); WPK(4, 0, 0); WPK(4, 0, 1);
    MF(acc[5], wa_[3][0], Bi[0][0]); WPK(4, 0, 2); WPK(4, 0, 3);
    MF(acc[5], wa_[3][1], Bi[1][0]); WPK(4, 1, 0); WPK(4, 1, 1);
    MF(acc[6], wa_[3][0], Bi[0][1]); WPK(4, 1, 2); WPK(4, 1, 3);
    MF(acc[6], wa_[3][1], Bi[1][1]); AL(5);
    MF(acc[7], wa_[3][0], Bi[0][2]); FST(3); FLAG(C_FAR + 3 * 4);
    MF(acc[7], wa_[3][1], Bi[1][2]); AL(6);
    MF(acc[7], wa_[3][2], Bo[0]); FL(33, 33); AL(7);
    MF0(wq[5][0], Fq[40], band[0]); FL(34, 34); AL(8);
    MF0(wq[5][1], Fq[40], band[1]); FL(35, 35); AL(9);
    MF0(wq[5][2], ru[5], band[0]); WPK(5, 0, 0); WPK(5, 0, 1);
    MF0(wq[5][3], ru[5], band[1]); AL(10);
    MF(acc[6], wa_[4][0], Bi[0][0]); PEEK(8, qa + 48); FL(36, 36);
    MF(acc[6], wa_[4][1], Bi[1][0]); FL(37, 37); WPK(5, 0, 2);
    MF(acc[7], wa_[4][0], Bi[0][1]); FL(38, 38); WPK(5, 0, 3);
    MF(acc[7], wa_[4][1], Bi[1][1]); FL(39, 39); WPK(5, 1, 0);
    MF(acc[6], At[0], Fq[32]); WPK(5, 1, 1); WPK(5, 1, 2);
    MF(acc[6], At[1], Fq[33]); CHECK(8, qa + 48);
    MF(acc[6], At[2], Fq[34]); WPK(5, 1, 3); FL(41, 41);
    MF(acc[6], At[3], Fq[35]); AL(11);
    MF(acc[7], wa_[5][0], Bi[0][0]); FL(42, 42); AL(12);
    MF(acc[7], wa_[5][1], Bi[1][0]); AL(13);
    MF(acc[6], At[4], Fq[36]); FL(43, 43); AL(14);
    MF(acc[6], At[5], Fq[37]); AL(15);
    MF(acc[6], At[6], Fq[38]); FL(44, 44); AL(16);
    MF(acc[6], At[7], Fq[39]);
    MF(acc[6], At[8], Fq[40]); FL(45, 45);
    MF(acc[7], At[0], Fq[40]);
    MF(acc[6], At[9], Fq[41]); FL(46, 46);
    MF(acc[7], At[1], Fq[41]);
    MF(acc[6], At[10], Fq[42]); FL(47, 47);
    MF(acc[7], At[2], Fq[42]);
    MF(acc[6], At[11], Fq[43]); FL(48, 48);
    MF(acc[7], At[3], Fq[43]);
    MF(acc[6], At[12], Fq[44]); FL(49, 49); ULD(6);
    MF(acc[7], At[4], Fq[44]); FL(50, 50); GLD(6);
    MF(acc[6], At[13], Fq[45]); FL(51, 51);
    MF(acc[7], At[5], Fq[45]); FL(52, 52);
    MF(acc[6], At[14], Fq[46]); FL(53, 53);
    MF(acc[7], At[6], Fq[46]); FL(54, 54); EUN(6, 0);
    MF(acc[6], At[15], Fq[47]); FL(55, 55); EUN(6, 1);
    MF(acc[7], At[7], Fq[47]); FL(56, 56); EUN(6, 2);
    MF(acc[6], At[16], Fq[48]); EUN(6, 3);
    MF(acc[7], At[8], Fq[48]); ULD(7); GLD(7);
    MF(acc[7], At[9], Fq[49]);
    MF(acc[7], At[10], Fq[50]);
    MF(acc[7], At[11], Fq[51]);
    MF(acc[7], At[12], Fq[52]); EA1(6, 0); EA1(6, 1); EA1(6, 2); EA1(6, 3);
    MF(acc[7], At[13], Fq[53]); EA2(6, 0); EA2(6, 1); EA2(6, 2); EA2(6, 3);
    MF(acc[7], At[14], Fq[54]); EPK(6, 0); EPK(6, 1);
    MF(acc[7], At[15], Fq[55]); OST(6); FLAG(C_TILE + 6 * 4);
    MF(acc[7], At[16], Fq[56]); EUN(7, 0); EUN(7, 1); EUN(7, 2); EUN(7, 3); NOP(3); EA1(7, 0); EA1(7, 1); EA1(7, 2); EA1(7, 3); EA2(7, 0); EA2(7, 1); EA2(7, 2); EA2(7, 3); EPK(7, 0); EPK(7, 1); OST(7); FLAG(C_TILE + 7 * 4);
    STAMP(9);
    ROLE_END();
  }
  {
    ROLE("h");
    const int hw = w - 5;
    const int j = xg * 28 + xi;
    const int s1 = j >> 2, kq = j & 3;
    const bool own = xi < 28;
    const u64 wbase = hw == 0 ? kb[1] : kb[2];
    const int pitch = F * 2;
    auto hrs = __builtin_amdgcn_make_buffer_rsrc((void*)wbase, 0, own ? D * F * 2 : 0, 0x00020000);
    const int r8 = lane >> 3;
    const int vo = (256 * kq * F + 64 * s1) * 2 + (8 * (r8 >> 2) + (r8 & 3)) * pitch + (((lane & 7) * 16) ^ (((r8 >> 1) & 3) * 32));
    auto dma = [&](int h, int sB, int pt, u32 dst) AI {
      const int m = h >> 1;
#pragma unroll
      for (int i = 0; i < 2; ++i)
        asm volatile("s_mov_b32 m0, %3\n\ts_nop 0\n\tbuffer_load_dwordx4 %0, %1, %2 offen nt lds"
                     :: "v"(vo), "s"(hrs), "s"((m & 1) * 32 * pt + (m >> 1) * sB + (16 * i + 4 * (h & 1)) * pt), "s"(dst + i * 1024) : "memory");
    };
    const int q = c16 >> 2, p = c16 & 3;
    int tro[4];
#pragma unroll
    for (int ct = 0; ct < 4; ++ct) tro[ct] = (4 * g + q) * 128 + ((ct ^ (((4 * g + q) >> 1) & 3)) * 32) + p * 8;
    const int tok = lane & 7;
    u4v wd[2][8];
    auto wd_ld = [&](int k, int cb) AI {
#pragma unroll
      for (int i = 0; i < 8; ++i) {
        const char* pp = smem + O_WD + cb * 8192 + (i >> 2) * 4096 + tro[i & 3];
        const u2v lo = tr_read(pp), hi = tr_read(pp + 2048);
        wd[k][i] = u4v{lo[0], lo[1], hi[0], hi[1]};
      }
    };
    u32 p2o, p2k[4], hlo, hhi;
    auto f2_setup = [&](int cb0) AI {
      p2o = WS_P2 + ((64 * kq + 16 * cb0 + (lane & 8) / 2 + g) * 56 + s1) * 128 + tok * 16;
      hlo = lbase + O_HB + ((lane & 8) ? 512 : g * 128 + tok * 16);
      hhi = lbase + O_HB + ((lane & 8) ? g * 128 + tok * 16 : 512);
#pragma unroll
      for (int i = 0; i < (cb0 == 2 ? 4 : 2); ++i) { p2k[i] = p2o + i * 8 * 7168; OPQ(p2k[i]); }
      OPQ(hlo); OPQ(hhi);
    };
    auto f2_job = [&](int nb, u32 t10) AI {
      u4v hb[4];
      {
        u32 cnt; int n_ = 1 << 20;
        asm volatile("1:\n\tds_read_b32 %4, %6\n\tds_read_b128 %0, %7\n\tds_read_b128 %1, %8\n\tds_read_b128 %2, %7 offset:528\n\tds_read_b128 %3, %8 offset:528\n\t"
                     "s_sub_u32 %5, %5, 1\n\ts_cbranch_scc1 2f\n\ts_waitcnt lgkmcnt(0)\n\tv_cmp_gt_u32 vcc, 2, %4\n\ts_cbranch_vccnz 1b\n2:\n\ts_waitcnt lgkmcnt(0)"
                     : "=&v"(hb[0]), "=&v"(hb[1]), "=&v"(hb[2]), "=&v"(hb[3]), "=&v"(cnt), "+s"(n_) : "v"(CTLA(C_HRDY)), "v"(hlo), "v"(hhi) : "vcc", "scc", "memory");
      }
      STAMP(11);
      f4 r[4];
#pragma unroll
      for (int i = 0; i < 2 * nb; ++i) {
        const u4v* wp = &wd[i >> 1][(i & 1) * 2];
        r[i] = mma(wp[0], hb[0], f4{0.f, 0.f, 0.f, 0.f});
        r[i] = mma(wp[1], hb[1], r[i]);
        r[i] = mma(wp[4], hb[2], r[i]);
        r[i] = mma(wp[5], hb[3], r[i]);
      }
      SB();
#pragma unroll
      for (int i = 0; i < 2 * nb; ++i) {
        u4v v = __builtin_bit_cast(u4v, r[i]);
#pragma unroll
        for (int e = 0; e < 4; ++e) v[e] = __builtin_amdgcn_bitop3_b32(v[e], t10, TMASK, 0xd8);
        __builtin_amdgcn_raw_buffer_store_b128(v, wrs, p2k[i], 0, 16);
      }
      STAMP(12);
    };
    const u32 rb = lbase + O_RING + hw * (NSLOT * 2048);
    pace(ts0 + WDELAY);
    STAMP(2);
#pragma unroll
    for (int h = 0; h < WWIN; ++h) dma(h, 64 * F * 2, F * 2, rb + h * 2048);
    __builtin_amdgcn_s_setprio(3);
    STAMP(3);
    if (__builtin_expect(own, 1)) {
      u4v wv[32];
      f4 a1[4] = {f4{0.f, 0.f, 0.f, 0.f}, f4{0.f, 0.f, 0.f, 0.f}, f4{0.f, 0.f, 0.f, 0.f}, f4{0.f, 0.f, 0.f, 0.f}};
      u4v xb[8];
      auto f1 = [&](int k0, int k1) AI {
#pragma unroll
        for (int k = k0; k < k1; ++k)
#pragma unroll
          for (int ct = 0; ct < 4; ++ct) a1[ct] = mma(wv[4 * k + ct], xb[k], a1[ct]);
      };
      u32 xo = lbase + O_XS + tok * XS_PITCH + g * 16;
      u32 p1o = WS_P1 + s1 * 16384 + kq * 4096 + hw * 2048 + (lane & 8) * 64 + g * 128 + tok * 16;
      u32 t10, tpub;
      OPQ(xo); OPQ(p1o);
      constexpr int HX = (16 - WWIN + 1) & ~1;
#pragma unroll
      for (int h = 0; h < 16; ++h) {
        const int left = (h + WWIN < 16 ? WWIN : 16 - h) - 1;
        asm volatile("s_waitcnt vmcnt(%0)" :: "n"(2 * left) : "memory");
        if (h + WWIN < 16) {
          asm volatile("s_waitcnt lgkmcnt(0)" ::: "memory");
          dma(h + WWIN, 64 * F * 2, F * 2, rb + ((h + WWIN) % NSLOT) * 2048);
        }
#pragma unroll
        for (int ct = 0; ct < 4; ++ct) {
          const u2v t = tr_read(smem + O_RING + hw * (NSLOT * 2048) + (h % NSLOT) * 2048 + tro[ct]);
          wv[4 * (h >> 1) + ct][2 * (h & 1)] = t[0]; wv[4 * (h >> 1) + ct][2 * (h & 1) + 1] = t[1];
        }
        if (h == H2_AT) ARRIVE(C_WGU);
        if (h == HX - 1) {
          STAMP(4);
          WAITQ(C_XS, 1);
          STAMP(1);
#pragma unroll
          for (int k = 0; k < 8; ++k) xb[k] = *(const __attribute__((address_space(3))) u4v*)(uintptr_t)(xo + k * 64);
          t10 = par[0]; tpub = par[2];
          SB();
          f1(0, 2);
          SB();
        }
        if (h >= HX) { f1(h - HX + 2, h - HX + 3); SB(); }
      }
      STAMP(5);
      f1(18 - HX, 8);
      t10 &= TMASK;
#pragma unroll
      for (int a = 0; a < 2; ++a) {
        u4v v;
#pragma unroll
        for (int e = 0; e < 4; ++e)
          v[e] = ((u32)__builtin_amdgcn_update_dpp(__float_as_int(a1[2 * a][e]), __float_as_int(a1[2 * a + 1][e]), 0xe4, 0xf, 0xc, false) & ~TMASK) | t10;
        __builtin_amdgcn_raw_buffer_store_b128(v, wrs, p1o + a * 1024, 0, 16);
      }
      STAMP(6);
      const u32 tn = now32();
      u32 voff = WS_P1 + s1 * 16384 + hw * 1024 + lane * 16;
      u32 ho = lbase + O_HB + hw * 528 + g * 128 + tok * 16 + (lane & 8);
      float nl2e = -1.44269504088896341f;
      u32 wda = CTLA(C_WD) + hw * 4;
      OPQ(voff); OPQ(ho); OPQ(nl2e); OPQ(wda);
      f2_setup(hw & 1);
      pace2(tn, (u32)__builtin_amdgcn_readfirstlane((int)tpub) + DE_P1, DT_P1);
      u4v pb[8];
      auto p1_ld = [&]() AI {
#pragma unroll
        for (int i = 0; i < 8; ++i) { pb[i] = __builtin_amdgcn_raw_buffer_load_b128(wrs, voff + (i >> 2) * 2048, (i & 3) * 4096, 16); SB(); }
      };
      p1_ld();
      SB();
      f4 G, U;
      bool early = false;
#pragma unroll 1
      for (int spin = 0; spin < SPIN_MAX; ++spin) {
        u32 bad = 0;
        OPQ(bad);
        asm volatile("" :: "v"(pb[0]));
        const u32 cw = __hip_atomic_load((__attribute__((address_space(3))) u32*)(uintptr_t)wda, __ATOMIC_RELAXED, __HIP_MEMORY_SCOPE_WORKGROUP);
        G = tsum4(bad, t10, pb[0], pb[1], pb[2], pb[3]);
        early = __builtin_amdgcn_ballot_w64(cw == 0u) == 0ull;
        asm volatile("" ::: "memory");
        if (__builtin_expect(early, 1)) wd_ld(0, hw);
        SB();
        silu4(G, nl2e);
        U = tsum4(bad, t10, pb[4], pb[5], pb[6], pb[7]);
        if (__builtin_expect(!__any((bad & TMASK) != 0), 1)) break;
        STAMP(14);
        p1_ld();
        asm volatile("" ::: "memory");
      }
      STAMP(7);
      {
        float hv[4];
#pragma unroll
        for (int e = 0; e < 4; ++e) { hv[e] = G[e] * U[e]; asm("" : "+v"(hv[e])); }
        *(__attribute__((address_space(3))) u2v*)(uintptr_t)ho = u2v{pk_bf(hv[0], hv[1]), pk_bf(hv[2], hv[3])};
      }
      ARRIVE(C_HRDY);
      STAMP(8);
      if (__builtin_expect(!early, 0)) { WAITA(wda); wd_ld(0, hw); }
      f2_job(1, t10);
    }
    if (hw == 0) {
      WAITS(C_SEL, 1);
      const u32 t10 = par[0] & TMASK;
      const u32 tpub = (u32)__builtin_amdgcn_readfirstlane((int)par[2]);
      const u32 mykey = ((const u32*)(smem + O_SEL))[lane & 7];
      if (own) pace2(now32(), tpub + DE_P2, DT_P2); else pace(tpub + DE_P2 - 40);
      u4v pa[7];
      const int voff = WS_P2 + cc * 7168 + lane * 16;
#pragma unroll
      for (int k = 0; k < 7; ++k) pa[k] = __builtin_amdgcn_raw_buffer_load_b128(wrs, voff + (k & 3) * 1024, (k >> 2) * 4096, 16);
      SB();
      const int row = (int)(~mykey & 2047u);
      const float rsig = rb1(sigm(__uint_as_float((mykey ^ (~(u32)((int)mykey >> 31) | 0x80000000u)) & 0xffff0000u)));
      WAITA(CTLA(C_TILE) + (lane & 31) * 4);
      float cf_ = rsig * (1.f - *(const float*)(smem + O_GT + (row >> 8) * 1024 + ((row >> 2) & 3) * 256 + ((row >> 4) & 15) * 16 + (row & 3) * 4));
      float base[4];
#pragma unroll
      for (int e = 0; e < 4; ++e) base[e] = bflo(*(const u16*)(smem + e * CHB + O_OUT + (row >> 8) * OT + ((row >> 2) & 3) * 144 + ((row >> 4) & 15) * 8 + (row & 3) * 2));
      u64 op = kc[1] + (row * D + cc * 4) * 2;
      { float cq = cf_; OPQ(cq); cf_ = cq; OPQ(op); }
#pragma unroll
      for (int e = 0; e < 4; ++e) OPQ(base[e]);
      STAMP(13);
      f4 y;
#pragma unroll 1
      for (int spin = 0; spin < SPIN_MAX; ++spin) {
        u32 bad = 0;
        OPQ(bad);
        y = tsum4(bad, t10, pa[0], pa[1], pa[2], pa[3]);
        y = tsum3(bad, t10, y, pa[4], pa[5], pa[6]);
        if (__builtin_expect(!__any((bad & TMASK) != 0), 1)) break;
        STAMP(15);
#pragma unroll
        for (int k = 0; k < 7; ++k) pa[k] = __builtin_amdgcn_raw_buffer_load_b128(wrs, voff + (k & 3) * 1024, (k >> 2) * 4096, 16);
        asm volatile("" ::: "memory");
      }
      STAMP(9);
      float o[4];
#pragma unroll
      for (int e = 0; e < 4; ++e) {
        float v = y[e] + dpp_f<0x128>(y[e]);
        auto s16 = __builtin_amdgcn_permlane16_swap(__float_as_uint(v), __float_as_uint(v), false, false);
        v = __uint_as_float(s16[0]) + __uint_as_float(s16[1]);
        auto s32 = __builtin_amdgcn_permlane32_swap(__float_as_uint(v), __float_as_uint(v), false, false);
        o[e] = fmaf(__uint_as_float(s32[0]) + __uint_as_float(s32[1]), cf_, base[e]);
      }
      if (lane < 8) *(__attribute__((address_space(1))) u2v*)op = u2v{pk_bf(o[0], o[1]), pk_bf(o[2], o[3])};
    }
    ROLE_END();
  }
  {
    ROLE("g");
    const u32 pLA = __builtin_nontemporal_load((gp32)ka[3] + d * (NM / 2) + lane);
    const u32 pAI = __builtin_nontemporal_load((gp32)ka[4] + d * (NM / 2) + lane);
    const u32 pCR = __builtin_nontemporal_load((gp32)ka[5] + d * (NM / 2) + lane);
    const u32 pCI = __builtin_nontemporal_load((gp32)ka[6] + d * (NM / 2) + lane);
    const u32 ldt = ((gp16)ka[7])[d], dskw = ((gp16)kb[0])[d];
    u4v ixa[2];
#pragma unroll
    for (int r = 0; r < 2; ++r) ixa[r] = *(const u4v*)(g_tapidx.v + lane * 16 + r * 8);
    SB();
    asm volatile("s_mov_b32 m0, %0\n\ts_nop 0" :: "s"(lbase + c * CHB) : "memory");
#define PUT1(off, val) asm volatile("ds_write_addtid_b32 %0 offset:%1" :: "v"(val), "n"(off) : "memory")
#define PUTR(off, val) do { const ch v_ = (val); asm volatile("ds_write_addtid_b32 %0 offset:%2\n\tds_write_addtid_b32 %1 offset:%3" :: "v"(v_.r), "v"(v_.i), "n"(off), "n"((off) + 256) : "memory"); } while (0)
    const char* pf = ch_ + c16 * TROW + g * 16;
    PUT1(O_TAP, 0x3c003c00u); PUT1(O_TAP + 256, 0u);
    PUT1(O_U, 0u); PUT1(O_U + 256, 0u);
    SB();
    const float dt = rb1(exp_bf(bflo(ldt)));
    const f2 a2 = f2{-rb1(exp_bf(bflo(pLA))), -rb1(exp_bf(bfhi(pLA)))} * dt * 1.44269504088896341f;
    const f2 br = unbf(pAI) * dt * 0.15915494309189535f;
    STAMP(1);
    u4v Lf[2][8], Wf[8];
    f4 kk[2][2];
    float fk;
    {
      const cf z1 = cexp_p(a2, br, 1.f);
      const cf z2 = csq(z1), z4 = csq(z2), z8 = csq(z4), z16 = csq(z8), z32 = csq(z16), z64 = csq(z32);
      ch base[4], anc[4];
      base[1] = tohc(z8); base[2] = tohc(z16); base[3] = tohc(cmul(z16, z8));
      anc[1] = tohc(z32); anc[2] = tohc(z64); anc[3] = tohc(cmul(z64, z32));
      PUTR(O_OUT + 5 * TROW, tohc(csq(z64)));
#pragma unroll
      for (int j = 1; j < 16; ++j) {
        const int q = j >> 2, r = j & 3;
        PUTR(O_TAP + j * TROW, q == 0 ? base[r] : r == 0 ? anc[q] : hmul(anc[q], base[r]));
      }
#pragma unroll
      for (int s = 0; s < 8; ++s) Lf[0][s] = *(const u4v*)(pf + O_TAP + s * 64);
#pragma unroll
      for (int s = 0; s < 8; ++s) Lf[1][s] = *(const u4v*)(ch_ + O_OUT + 5 * TROW + g * 16 + s * 64);
      SB();
      STAMP(11);
      u32 mx = max(max(pCR << 17, pCR << 1), max(pCI << 17, pCI << 1));
      mx = wave_max_u32(mx);
      int ex = (int)(mx >> 24);
      ex = ex < 1 ? 1 : (ex > 253 ? 253 : ex);
      const float sdn = __uint_as_float((u32)(254 - ex) << 23);
      fk = 2.f * dt * __uint_as_float((u32)ex << 23);
      cf C; C.r = unbf(pCR) * sdn; C.i = unbf(pCI) * sdn;
      const cf E = cexp_p(a2, br, 256.f);
      const ch y1 = toh(z1), y2 = toh(z2), y4 = toh(z4), y6 = toh(cmul(z4, z2));
      {
        const ch R0 = toh(C), R2 = hmul(R0, y2), R4 = hmul(R0, y4), R6 = hmul(R0, y6);
        PUTR(O_TAP, R0); PUTR(O_TAP + 2 * TROW, R2); PUTR(O_TAP + 4 * TROW, R4); PUTR(O_TAP + 6 * TROW, R6);
        PUTR(O_TAP + 1 * TROW, hmul(R0, y1)); PUTR(O_TAP + 3 * TROW, hmul(R2, y1)); PUTR(O_TAP + 5 * TROW, hmul(R4, y1)); PUTR(O_TAP + 7 * TROW, hmul(R6, y1));
        C = cmul(C, E);
        const ch R8 = toh(C);
        PUTR(O_TAP + 8 * TROW, R8); PUTR(O_TAP + 9 * TROW, hmul(R8, y2)); PUTR(O_TAP + 10 * TROW, hmul(R8, y4)); PUTR(O_TAP + 11 * TROW, hmul(R8, y6));
        C = cmul(C, E);
        const ch R12 = toh(C);
        PUTR(O_TAP + 12 * TROW, R12); PUTR(O_TAP + 13 * TROW, hmul(R12, y4));
        C = cmul(C, E);
        const ch R14 = toh(C);
        PUTR(O_TAP + 14 * TROW, R14); PUTR(O_TAP + 15 * TROW, hmul(R14, y4));
      }
#pragma unroll
      for (int s = 0; s < 8; ++s) Wf[s] = *(const u4v*)(pf + O_TAP + s * 64);
      SB();
      STAMP(12);
#pragma unroll
      for (int i = 0; i < 5; ++i) { C = cmul(C, E); PUTR(O_OUT + i * TROW, toh(C)); }
      SB();
      auto mh = [&](u4v a, u4v bq, f4 cin) AI { return __builtin_amdgcn_mfma_f32_16x16x32_f16(as_h8(a), as_h8(bq), cin, 0, 0, 0); };
      kk[0][0] = f4{0.f, 0.f, 0.f, 0.f}; kk[0][1] = kk[0][0]; kk[1][0] = kk[0][0]; kk[1][1] = kk[0][0];
#pragma unroll
      for (int s = 0; s < 5; ++s) {
#pragma unroll
        for (int k = 0; k < 4; ++k) {
          if (k < 2) { if (s < 4) kk[0][0] = mh(Wf[s + 4 * k], Lf[0][s + 4 * k], kk[0][0]); }
          else if (s > 0) { const int u = s - 1 + 4 * (k - 2); kk[0][1] = mh(Wf[u], Lf[1][u], kk[0][1]); Wf[u] = *(const u4v*)(pf + O_OUT + u * 64); }
          SB();
          if (s < 4) {
            const ch o = hmul(ch{ash(Lf[0][s][k]), ash(Lf[0][s + 4][k])}, ch{ash(Lf[1][s][k]), ash(Lf[1][s + 4][k])});
            Lf[1][s][k] = __builtin_bit_cast(u32, o.r); Lf[1][s + 4][k] = __builtin_bit_cast(u32, o.i);
          }
          if (s == 3 && k == 3) asm volatile("s_nop 1");
          SB();
        }
      }
#pragma unroll
      for (int s = 0; s < 8; ++s) { kk[1][0] = mh(Wf[s], Lf[0][s], kk[1][0]); kk[1][1] = mh(Wf[s], Lf[1][s], kk[1][1]); }
    }
    {
      u32 ia[16];
      const u32 kb_ = lbase + c * CHB + O_OUT;
#pragma unroll
      for (int e = 0; e < 16; ++e) { const u32 iw = ixa[e >> 3][(e >> 1) & 3]; ia[e] = kb_ + ((e & 1) ? (iw >> 16) : (iw & 0xffffu)); }
      MIX2(16, 0, 1)
      SB();
      STAMP(2);
      char* ktp = ch_ + O_OUT + c16 * KT_PITCH + g * 8;
#pragma unroll
      for (int hf = 1; hf >= 0; --hf) {
#pragma unroll
        for (int ct = 0; ct < 2; ++ct) {
          const f4 v = kk[hf][ct] * fk;
          *(u32*)(ktp + ct * 16 * KT_PITCH + hf * 32) = pk_bf(v[0], v[1]);
          *(u32*)(ktp + ct * 16 * KT_PITCH + hf * 32 + 4) = pk_bf(v[2], v[3]);
        }
        asm volatile("" ::: "memory");
      }
      u32 zz = 0u; OPQ(zz);
      *(u32*)(ch_ + O_OUT + KT_ZERO + (lane & 7) * 4) = zz;
      asm volatile("" ::: "memory");
      u32 v[16], pe[9];
#pragma unroll
      for (int e = 15; e >= 0; --e) v[e] = *(const __attribute__((address_space(3))) u16*)(uintptr_t)ia[e];
      SB();
      pe[8] = zz;
#pragma unroll
      for (int i = 7; i >= 0; --i) {
        pe[i] = v[2 * i] | (v[2 * i + 1] << 16);
        const u32 nx = (u32)__builtin_amdgcn_update_dpp((int)dpp_u<0x134>(pe[i + 1]), (int)pe[i], 0x130, 0xf, 0xf, false);
        const u32 po = __builtin_amdgcn_alignbyte(nx, pe[i], 2);
        PUT1(O_TAP + i * 256, pe[i]); PUT1(O_TAP + 2 * TAP_PITCH - 4 + i * 256, pe[i]);
        PUT1(O_TAP + TAP_PITCH + i * 256, po); PUT1(O_TAP + 3 * TAP_PITCH - 4 + i * 256, po);
      }
      ARRIVE(C_TAPS + c);
    }
    STAMP(3);
    IDS();
    conv_setup();
    dm1 = bflo(dskw) - 1.f;
    atload(0, 17);
    m_setup();
    __builtin_amdgcn_s_setprio(1);
    SB();
    WAITA(qa);
    STAMP(4);
    SB();
    FL(0, -7); FL(1, -6); FL(2, -5); FL(3, -4); FL(4, -3); FL(5, -2); FL(6, -1); FL(7, 0); ULD(0); GLD(0);
    MF0(acc[0], At[9], Fq[0]); EUN(0, 0); EUN(0, 1);
    MF(acc[0], At[10], Fq[1]); EUN(0, 2); EUN(0, 3);
    MF(acc[0], At[11], Fq[2]);
    MF(acc[0], At[12], Fq[3]);
    MF(acc[0], At[13], Fq[4]);
    MF(acc[0], At[14], Fq[5]);
    MF(acc[0], At[15], Fq[6]); FL(8, 1);
    MF(acc[0], At[16], Fq[7]);
    MF0(acc[1], At[1], Fq[0]); FL(9, 2);
    MF(acc[1], At[2], Fq[1]);
    MF(acc[1], At[3], Fq[2]); FL(10, 3);
    MF(acc[1], At[4], Fq[3]);
    MF(acc[1], At[5], Fq[4]); FL(11, 4); EA1(0, 0);
    MF(acc[1], At[6], Fq[5]); EA1(0, 1); EA1(0, 2);
    MF(acc[1], At[7], Fq[6]); EA1(0, 3); PEEK(0, qa + 16);
    MF(acc[1], At[8], Fq[7]); FL(12, 5); EA2(0, 0);
    MF0(acc[2], At[0], Fq[7]); EA2(0, 1); EA2(0, 2);
    MF(acc[1], At[9], Fq[8]); EA2(0, 3); EPK(0, 0);
    MF(acc[2], At[1], Fq[8]); EPK(0, 1); FL(13, 6);
    MF(acc[1], At[10], Fq[9]); OST(0); FLAG(C_TILE + 0 * 4);
    MF(acc[2], At[2], Fq[9]); FL(14, 7); FL(15, 8);
    MF(acc[1], At[11], Fq[10]); CHECK(0, qa + 16);
    MF(acc[2], At[3], Fq[10]);
    MF(acc[1], At[12], Fq[11]); FL(16, 9);
    MF(acc[2], At[4], Fq[11]); ULD(1); GLD(1);
    MF(acc[1], At[13], Fq[12]); FL(17, 10);
    MF(acc[2], At[5], Fq[12]);
    MF(acc[1], At[14], Fq[13]); FL(18, 11);
    MF(acc[2], At[6], Fq[13]);
    MF(acc[1], At[15], Fq[14]); FL(19, 12); EUN(1, 0);
    MF(acc[2], At[7], Fq[14]); EUN(1, 1); EUN(1, 2);
    MF(acc[1], At[16], Fq[15]); FL(20, 13); EUN(1, 3);
    MF(acc[2], At[8], Fq[15]);
    MF0(acc[3], At[0], Fq[15]); FL(21, 14);
    MF(acc[2], At[9], Fq[16]);
    MF(acc[3], At[1], Fq[16]); FL(22, 15); EA1(1, 0);
    MF(acc[2], At[10], Fq[17]); EA1(1, 1); EA1(1, 2);
    MF(acc[3], At[2], Fq[17]); FL(23, 16); EA1(1, 3);
    MF(acc[2], At[11], Fq[18]); PEEK(2, fa + 0); EA2(1, 0);
    MF(acc[3], At[3], Fq[18]); EA2(1, 1); EA2(1, 2);
    MF(acc[2], At[12], Fq[19]); EA2(1, 3); EPK(1, 0);
    MF(acc[3], At[4], Fq[19]); EPK(1, 1); FL(24, 17);
    MF(acc[2], At[13], Fq[20]); OST(1); FLAG(C_TILE + 1 * 4);
    MF(acc[3], At[5], Fq[20]); FL(25, 18); CHECK(2, fa + 0);
    MF(acc[2], At[14], Fq[21]); FL(26, 19); ULD(2);
    MF(acc[3], At[6], Fq[21]); FLD(2); GLD(2);
    MF(acc[2], At[15], Fq[22]); FL(27, 20); PEEK(1, qa + 32);
    MF(acc[3], At[7], Fq[22]);
    MF(acc[2], At[16], Fq[23]); FL(28, 21);
    MF(acc[3], At[8], Fq[23]); EUN(2, 0); EUN(2, 1);
    MF0(acc[4], At[0], Fq[23]); FL(29, 22); EUN(2, 2);
    MF(acc[3], At[9], Fq[24]); EUN(2, 3); EA1(2, 0);
    MF(acc[4], At[1], Fq[24]); FL(30, 23); EA1(2, 1);
    MF(acc[3], At[10], Fq[25]); EA1(2, 2); EA1(2, 3);
    MF(acc[4], At[2], Fq[25]); FL(31, 24); EFA(2, 0);
    MF(acc[3], At[11], Fq[26]); EFA(2, 1); EFA(2, 2);
    MF(acc[4], At[3], Fq[26]); EFA(2, 3); PEEK(3, fa + 16);
    MF(acc[3], At[12], Fq[27]); EA2(2, 0); EA2(2, 1);
    MF(acc[4], At[4], Fq[27]); EA2(2, 2); EA2(2, 3);
    MF(acc[3], At[13], Fq[28]); CHECK(1, qa + 32); EPK(2, 0); EPK(2, 1);
    MF(acc[4], At[5], Fq[28]); FL(32, 25); OST(2);
    MF(acc[3], At[14], Fq[29]); FLAG(C_TILE + 2 * 4); FL(33, 26);
    MF(acc[4], At[6], Fq[29]); FL(34, 27); CHECK(3, fa + 16);
    MF(acc[3], At[15], Fq[30]); ULD(3); FL(35, 28);
    MF(acc[4], At[7], Fq[30]); FLD(3); GLD(3);
    MF(acc[3], At[16], Fq[31]); FL(36, 29);
    MF(acc[4], At[8], Fq[31]);
    MF0(acc[5], At[0], Fq[31]); FL(37, 30);
    MF(acc[4], At[9], Fq[32]); EUN(3, 0); EUN(3, 1);
    MF(acc[5], At[1], Fq[32]); PEEK(4, fa + 32); FL(38, 31);
    MF(acc[4], At[10], Fq[33]); EUN(3, 2); EUN(3, 3);
    MF(acc[5], At[2], Fq[33]); FL(39, 32); EA1(3, 0);
    MF(acc[4], At[11], Fq[34]); EA1(3, 1); EA1(3, 2);
    MF(acc[5], At[3], Fq[34]); EA1(3, 3); EFA(3, 0);
    MF(acc[4], At[12], Fq[35]); EFA(3, 1); EFA(3, 2); EFA(3, 3);
    MF(acc[5], At[4], Fq[35]); EA2(3, 0); EA2(3, 1); EA2(3, 2); EA2(3, 3);
    MF(acc[4], At[13], Fq[36]); FL(40, 33); EPK(3, 0); EPK(3, 1);
    MF(acc[5], At[5], Fq[36]); FL(41, 34); OST(3);
    MF(acc[4], At[14], Fq[37]); FL(42, 35); FLAG(C_TILE + 3 * 4); CHECK(4, fa + 32); ULD(4);
    MF(acc[5], At[6], Fq[37]); FL(43, 36); FLD(4); GLD(4); PEEK(5, fa + 48);
    MF(acc[4], At[15], Fq[38]); FL(44, 37); FL(45, 38);
    MF(acc[5], At[7], Fq[38]); FL(46, 39); FL(47, 40);
    MF(acc[4], At[16], Fq[39]);
    MF(acc[5], At[8], Fq[39]); EUN(4, 0); EUN(4, 1);
    MF(acc[5], At[9], Fq[40]); EUN(4, 2); EUN(4, 3);
    MF(acc[5], At[10], Fq[41]); EFA(4, 0); EA1(4, 0);
    MF(acc[5], At[11], Fq[42]); EA1(4, 1); EA1(4, 2);
    MF(acc[5], At[12], Fq[43]); EA1(4, 3); EFA(4, 1); EFA(4, 2); EFA(4, 3); CHECK(5, fa + 48); ULD(5);
    MF(acc[5], At[13], Fq[44]); EA2(4, 0); EA2(4, 1); EA2(4, 2); EA2(4, 3); FLD(5); GLD(5);
    MF(acc[5], At[14], Fq[45]); EPK(4, 0); EPK(4, 1);
    MF(acc[5], At[15], Fq[46]); OST(4); FLAG(C_TILE + 4 * 4);
    MF(acc[5], At[16], Fq[47]); EUN(5, 0); EUN(5, 1); EUN(5, 2); EUN(5, 3); EFA(5, 0); EFA(5, 1); EFA(5, 2); EFA(5, 3); EA1(5, 0); EA1(5, 1); EA1(5, 2); EA1(5, 3); EA2(5, 0); EA2(5, 1); EA2(5, 2); EA2(5, 3); EPK(5, 0); EPK(5, 1); OST(5); FLAG(C_TILE + 5 * 4);
    STAMP(8);
    ROLE_END();
  }
  {
    ROLE("c");
    __builtin_amdgcn_s_setprio(3);
    {
      u32 zz = 0u; OPQ(zz);
      if (lane < 2) *(u4v*)(smem + O_HB + 512 + lane * 528) = u4v{zz, zz, zz, zz};
    }
    u32* selw = (u32*)(smem + O_SEL);
    u32 tag;
    const u32 tgo = later(ts0, now32() - DT_ARG - 15) + (31 - xi) * 3 / 2 + DT_KEY + DT_GO;
    {
      const u32 pa = lbase + O_PART + (lane & 7) * 64;
      const u32 ib = 2047u - (u32)(cg * 8 + (lane & 7));
      f4 p0, p1, p2, p3; u32 cnt, ep; int n_ = 1 << 20; unsigned long long tk;
      asm volatile("1:\n\tds_read_b32 %4, %8\n\tds_read_b128 %0, %9\n\tds_read_b128 %1, %9 offset:16\n\tds_read_b128 %2, %9 offset:32\n\tds_read_b128 %3, %9 offset:48\n\t"
                   "ds_read_b32 %7, %10\n\ts_memrealtime %6\n\ts_sub_u32 %5, %5, 1\n\ts_cbranch_scc1 2f\n\ts_waitcnt lgkmcnt(0)\n\tv_cmp_gt_u32 vcc, 4, %4\n\ts_cbranch_vccnz 1b\n2:\n\ts_waitcnt lgkmcnt(0)"
                   : "=&v"(p0), "=&v"(p1), "=&v"(p2), "=&v"(p3), "=&v"(cnt), "+s"(n_), "=&s"(tk), "=&v"(ep) : "v"(CTLA(C_PART)), "v"(pa), "v"(lbase + O_XS) : "vcc", "scc", "memory");
      tag = (u32)__builtin_amdgcn_readfirstlane((int)ep) + 1u;
      tag += tag == 0u;
      const f4 s4_ = (p0 + p1) + (p2 + p3);
      const u32 rp = pk_bf(s4_[1] + s4_[3], s4_[0] + s4_[2]);
      const u32 key = ((rp ^ ((u32)((int)rp >> 31) | 0x80000000u)) & 0xffff0000u) | ib;
      const u32 kn = dpp_u<0xb1>(key);
      u32 m1 = max(key, kn), m2 = min(key, kn), m3;
      { const u32 o1 = dpp_u<0x4e>(m1), o2 = dpp_u<0x4e>(m2); const u32 lo = min(m1, o1), hb = max(m2, o2); m1 = max(m1, o1); m2 = max(lo, hb); m3 = min(lo, hb); }
      { const u32 o1 = dpp_u<0x141>(m1), o2 = dpp_u<0x141>(m2), o3 = dpp_u<0x141>(m3); const u32 lo = min(m1, o1), hb = max(m2, o2);
        m1 = max(m1, o1); m2 = max(lo, hb); m3 = max(min(lo, hb), max(m3, o3)); }
      const u32 wm = (((u32)tk + 8u) >> 2) & 0xffu;
      if (lane == 0) __builtin_amdgcn_raw_buffer_store_b128(u4v{m1, m2, (m3 & 0xffff0000u) | wm, tag}, wrs, WS_KEY + cg * 16, 0, 16);
      SB();
      STAMP(1);
      if (lane == 0) par[0] = tag;
      ARRIVE(C_GP);
      {
        const float gv = sigm(bflo(rp));
        const u32 g2 = pk_bf(gv, dpp_f<0xb1>(gv));
        u32 g3 = dpp_u<0x4e>(g2);
        OPQ(g3);
        if ((lane & 59) == 0) __builtin_amdgcn_raw_buffer_store_b128(u4v{g2, g3, tag, tag}, wrs, WS_REC + cg * 32 + lane * 4, 0, 16);
      }
      if ((lane & 57) == 0) __builtin_amdgcn_raw_buffer_store_b128(u4v{key, kn, 0u, tag}, wrs, WS_KALL + cg * 64 + lane * 8, 0, 16);
    }
    u32 l7 = (lane & 7) * 16, ko = WS_KEY + lane * 16;
    OPQ(l7); OPQ(ko);
#pragma unroll 1
    for (int i = 0; i < 4096; ++i) {
      const u32 have = __hip_atomic_load(ctl + C_TL, __ATOMIC_RELAXED, __HIP_MEMORY_SCOPE_WORKGROUP);
      const u32 tl = __hip_atomic_load(par + 1, __ATOMIC_RELAXED, __HIP_MEMORY_SCOPE_WORKGROUP), tn = now32();
      if (__builtin_amdgcn_readfirstlane((int)have)) { pace((u32)__builtin_amdgcn_readfirstlane((int)tl) + DT_KEY); break; }
      if ((int)(tn - tgo) >= 0) break;
      __builtin_amdgcn_s_sleep(1);
    }
    STAMP(5);
    u4v gr[4];
    u32 mykey;
    u4v xs[4];
    auto xrs = __builtin_amdgcn_make_buffer_rsrc((void*)(ka[0] + (xi & 3) * 512), 0, L * D * 2 - 1536, 0x00020000);
    asm volatile("" : "+s"(xrs));
    auto xs_ld = [&]() AI {
      const u32 vo = ((~mykey & 2047u) << 11) | l7;
#pragma unroll
      for (int k = 0; k < 4; ++k) xs[k] = __builtin_amdgcn_raw_buffer_load_b128(xrs, vo + k * 128, 0, 0);
    };
#pragma unroll
    for (int i = 0; i < 4; ++i) gr[i] = __builtin_amdgcn_raw_buffer_load_b128(wrs, ko + i * 1024, 0, 16);
#pragma unroll 1
    for (int spin = 0; spin < SPIN_MAX; ++spin) {
      const u32 bad = (gr[0][3] ^ tag) | (gr[1][3] ^ tag) | (gr[2][3] ^ tag) | (gr[3][3] ^ tag);
      if (__builtin_expect(!__any(bad != 0), 1)) break;
      STAMP(13);
      __builtin_amdgcn_s_sleep(SLEEP_AG1);
#pragma unroll
      for (int i = 0; i < 4; ++i)
        if (gr[i][3] != tag) gr[i] = __builtin_amdgcn_raw_buffer_load_b128(wrs, ko + i * 1024, 0, 16);
      asm volatile("" ::: "memory");
    }
    STAMP(2);
    {
      u32 m1 = gr[0][0], m2 = gr[0][1], m3 = 0, m4 = 0;
#pragma unroll
      for (int i = 2; i < 8; ++i) {
        const u32 k = gr[i >> 1][i & 1];
        if (i > 2) m4 = med3u(m3, m4, k);
        m3 = med3u(m2, m3, k);
        m2 = (i & 1) ? max(m2, k) : med3u(m1, m2, k);
        if (!(i & 1)) m1 = max(m1, k);
      }
      const u32 third = max(max(gr[0][2], gr[1][2]), max(gr[2][2], gr[3][2]));
      u32 W2;
      mykey = wave_top8(m1, m2, m3, m4, W2);
      xs_ld();
      SB();
      if (__builtin_expect(__any(m1 == 0u || (third | 0xffffu) > W2), 0)) {
        u4v ak[16];
#pragma unroll 1
        for (int spin = 0; spin < SPIN_MAX; ++spin) {
          u32 bad = 0;
#pragma unroll
          for (int i = 0; i < 16; ++i) {
            ak[i] = __builtin_amdgcn_raw_buffer_load_b128(wrs, WS_KALL + (i & 3) * 1024 + lane * 16, (i >> 2) * 4096, 16);
            bad |= ak[i][3] ^ tag;
          }
          if (!__any(bad != 0)) break;
          __builtin_amdgcn_s_sleep(8);
          asm volatile("" ::: "memory");
        }
        u32 prev = 0xffffffffu;
#pragma unroll 1
        for (int it = 0; it < 8; ++it) {
          u32 mm_ = 0;
#pragma unroll
          for (int i = 0; i < 32; ++i) {
            const u32 k = ak[i >> 1][i & 1];
            mm_ = max(mm_, k < prev ? k : 0u);
          }
          OPQ(mm_);
          prev = wave_max_u32(mm_);
          mykey = (lane >> 3) == it ? prev : mykey;
        }
        xs_ld();
      }
      OPQ(mykey);
    }
    SB();
    STAMP(3);
    if (l7 == 0) selw[lane >> 3] = mykey;
    {
      WAITQ(C_TL, 1);
      const u32 tn = now32(), tkey = par[1] + DT_KEY;
      u32 yg = 0;
#pragma unroll
      for (int i = 0; i < 4; ++i) yg = max(yg, (gr[i][2] - (tn >> 2) - 1u) & 0xffu);
      const u32 tpub = tn - 4u * (255u - wave_max_u32(yg)) + DT_VIS;
      if (lane == 0) par[2] = later(tkey, tpub);
    }
    ARRIVE(C_SEL);
    if (b == 255 && lane == 0) __builtin_amdgcn_raw_buffer_store_b32(tag, wrs, WS_EPOCH, 0, 16);
    SB();
    {
      char* xp = smem + O_XS + (lane >> 3) * XS_PITCH + l7;
#pragma unroll
      for (int k = 0; k < 4; ++k) *(u4v*)(xp + k * 128) = xs[k];
      ARRIVE(C_XS);
      STAMP(6);
    }
    __builtin_amdgcn_s_setprio(2);
    auto ors = __builtin_amdgcn_make_buffer_rsrc((void*)kc[1], 0, L * D * 2, 0x00020000);
    const u32 rl = (lane + (xi & 15) * 4) & 63, un = (g * 4 + (lane & 3) + xi) & 15;
    u32 skip = 0;
#pragma unroll
    for (int k = 0; k < 8; ++k) {
      const u32 r = ~(u32)__builtin_amdgcn_readlane((int)mykey, 8 * k) & 2047u;
      skip |= (r & 63u) == rl ? 1u << (r >> 6) : 0u;
    }
    const char* op = smem + ((lane >> 2) & 3) * CHB + O_OUT + (un & 3) * 144 + (un >> 2) * 8;
    const u32 ob = rl * 2048 + cc * 8;
    const u32 tfast = (u32)__builtin_amdgcn_readfirstlane((int)par[2]) + DE_FAST;
#pragma unroll 1
    for (int t = 0; t < 8; ++t) {
      WAITA(CTLA(C_TILE) + t * 16 + (lane & 3) * 4);
      u2v yv[4];
#pragma unroll
      for (int e = 0; e < 4; ++e) yv[e] = tr_read(op + t * OT + e * 32);
      const bool slow = (int)(now32() - tfast) < 0;
#pragma unroll
      for (int e = 0; e < 4; ++e) {
        __builtin_amdgcn_raw_buffer_store_b64(yv[e], ors, ob | (((skip >> e) & 1u) << 30), t * 524288 + e * 131072, 0);
        if (slow) {
          SB(); __builtin_amdgcn_s_sleep(STSLP); SB();
          asm volatile("s_waitcnt vmcnt(%0)" :: "n"(STWIN - 1) : "memory");
        }
      }
      skip >>= 4;
    }
    STAMP(9);
    ROLE_END();
  }
  {
    ROLE("d");
        const int j = xg * 28 + xi;
    const int s1 = j >> 2, kq = j & 3;
    const bool own = xi < 28;
    const u64 wbase = kb[3];
    const int pitch = D * 2;
    auto hrs = __builtin_amdgcn_make_buffer_rsrc((void*)wbase, 0, own ? D * F * 2 : 0, 0x00020000);
    const int r8 = lane >> 3;
    const int vo = (64 * s1 * D + 256 * kq) * 2 + (8 * (r8 >> 2) + (r8 & 3)) * pitch + (((lane & 7) * 16) ^ (((r8 >> 1) & 3) * 32));
    auto dma = [&](int h, int sB, int pt, u32 dst) AI {
      const int m = h >> 1;
#pragma unroll
      for (int i = 0; i < 2; ++i)
        asm volatile("s_mov_b32 m0, %3\n\ts_nop 0\n\tbuffer_load_dwordx4 %0, %1, %2 offen nt lds"
                     :: "v"(vo), "s"(hrs), "s"((m & 1) * 32 * pt + (m >> 1) * sB + (16 * i + 4 * (h & 1)) * pt), "s"(dst + i * 1024) : "memory");
    };
    const int q = c16 >> 2, p = c16 & 3;
    int tro[4];
#pragma unroll
    for (int ct = 0; ct < 4; ++ct) tro[ct] = (4 * g + q) * 128 + ((ct ^ (((4 * g + q) >> 1) & 3)) * 32) + p * 8;
    const int tok = lane & 7;
    u4v wd[2][8];
    auto wd_ld = [&](int k, int cb) AI {
#pragma unroll
      for (int i = 0; i < 8; ++i) {
        const char* pp = smem + O_WD + cb * 8192 + (i >> 2) * 4096 + tro[i & 3];
        const u2v lo = tr_read(pp), hi = tr_read(pp + 2048);
        wd[k][i] = u4v{lo[0], lo[1], hi[0], hi[1]};
      }
    };
    u32 p2o, p2k[4], hlo, hhi;
    auto f2_setup = [&](int cb0) AI {
      p2o = WS_P2 + ((64 * kq + 16 * cb0 + (lane & 8) / 2 + g) * 56 + s1) * 128 + tok * 16;
      hlo = lbase + O_HB + ((lane & 8) ? 512 : g * 128 + tok * 16);
      hhi = lbase + O_HB + ((lane & 8) ? g * 128 + tok * 16 : 512);
#pragma unroll
      for (int i = 0; i < (cb0 == 2 ? 4 : 2); ++i) { p2k[i] = p2o + i * 8 * 7168; OPQ(p2k[i]); }
      OPQ(hlo); OPQ(hhi);
    };
    u4v hb[4];
    f4 r[4];
    auto hb_ld = [&]() AI {
      u32 cnt; int n_ = 1 << 20;
      asm volatile("1:\n\tds_read_b32 %4, %6\n\tds_read_b128 %0, %7\n\tds_read_b128 %1, %8\n\tds_read_b128 %2, %7 offset:528\n\tds_read_b128 %3, %8 offset:528\n\t"
                   "s_sub_u32 %5, %5, 1\n\ts_cbranch_scc1 2f\n\ts_waitcnt lgkmcnt(0)\n\tv_cmp_gt_u32 vcc, 2, %4\n\ts_cbranch_vccnz 1b\n2:\n\ts_waitcnt lgkmcnt(0)"
                   : "=&v"(hb[0]), "=&v"(hb[1]), "=&v"(hb[2]), "=&v"(hb[3]), "=&v"(cnt), "+s"(n_) : "v"(CTLA(C_HRDY)), "v"(hlo), "v"(hhi) : "vcc", "scc", "memory");
    };
    auto f2_mm = [&](int k) AI {
      const u4v* wp = wd[k];
      r[2 * k] = mma(wp[0], hb[0], f4{0.f, 0.f, 0.f, 0.f}); r[2 * k + 1] = mma(wp[2], hb[0], f4{0.f, 0.f, 0.f, 0.f});
      r[2 * k] = mma(wp[1], hb[1], r[2 * k]); r[2 * k + 1] = mma(wp[3], hb[1], r[2 * k + 1]);
      r[2 * k] = mma(wp[4], hb[2], r[2 * k]); r[2 * k + 1] = mma(wp[6], hb[2], r[2 * k + 1]);
      r[2 * k] = mma(wp[5], hb[3], r[2 * k]); r[2 * k + 1] = mma(wp[7], hb[3], r[2 * k + 1]);
    };
    auto f2_st = [&](int k, u32 t10) AI {
#pragma unroll
      for (int i = 2 * k; i < 2 * k + 2; ++i) {
        u4v v = __builtin_bit_cast(u4v, r[i]);
#pragma unroll
        for (int e = 0; e < 4; ++e) v[e] = __builtin_amdgcn_bitop3_b32(v[e], t10, TMASK, 0xd8);
        __builtin_amdgcn_raw_buffer_store_b128(v, wrs, p2k[i], 0, 16);
      }
    };
    {
      __builtin_amdgcn_s_setprio(3);
      const u32 ts = ts0 - 500u;
      u32 tl = ~0u;
      pace(later(ts0, now32() - DT_ARG - 15) + DT_HEAD);
      STAMP(3);
#pragma unroll 1
      for (int i = 0; i < 8 && tl >= 1500u; ++i) {
        const u2v hd = __builtin_amdgcn_raw_buffer_load_b64(wrs, WS_HEAD + (lane & 7) * 8, 0, 16);
        tl = wave_max_u32(hd[0] == ~hd[1] ? hd[0] - ts : ~0u);
        STAMP(14);
        asm volatile("" ::: "memory");
      }
      tl = tl < 1500u ? tl + ts : ts0;
      if (lane == 0) par[1] = tl;
      ARRIVE(C_TL);
      STAMP(2);
      if (own) {
        WAITS(C_WGU, 1);
#pragma unroll
        for (int h = 0; h < 16; ++h) {
          if (h == DSEL) WAITS(C_SEL, 1);
          if (h == DEV) WAITS(C_XL, 4);
          if (h >= DWIN) asm volatile("s_waitcnt vmcnt(%0)" :: "n"(2 * (DWIN - 1)) : "memory");
          if (h == 3 + DWIN) ARRIVE(C_WD);
          if (h == 7 + DWIN) ARRIVE(C_WD + 1);
          dma(h, 128, D * 2, lbase + O_WD + h * 2048);
        }
        f2_setup(2);
        const u32 t10 = par[0] & TMASK;
        asm volatile("s_waitcnt vmcnt(8)" ::: "memory");
        wd_ld(0, 2);
        const u32 hc = __hip_atomic_load(ctl + C_HRDY, __ATOMIC_RELAXED, __HIP_MEMORY_SCOPE_WORKGROUP);
        const bool early = __builtin_amdgcn_ballot_w64(hc < 2u) == 0ull;
        if (early) { hb_ld(); f2_mm(0); }
        asm volatile("s_waitcnt vmcnt(0)" ::: "memory");
        STAMP(5);
        wd_ld(1, 3);
        if (!early) { WAITS(C_HRDY, 1); hb_ld(); STAMP(11); f2_mm(0); }
        SB();
        f2_mm(1);
        f2_st(0, t10);
        SB();
        f2_st(1, t10);
        STAMP(12);
      }
      ROLE_END();
    }
  }
}

extern "C" void kernel_launch(void* const* d_in, const int* in_sizes, int n_in,
                              void* d_out, int out_size, void* d_ws, size_t ws_size,
                              hipStream_t stream) {
  k_main<<<256, 768, 0, stream>>>((const u16*)d_in[0], (const u16*)d_in[8], (const u16*)d_in[12],
                                  (const u16*)d_in[1], (const u16*)d_in[2], (const u16*)d_in[3],
                                  (const u16*)d_in[4], (const u16*)d_in[5], (const u16*)d_in[6],
                                  (const u16*)d_in[9], (const u16*)d_in[10], (const u16*)d_in[11],
                                  (char*)d_ws, (u16*)d_out);
}
```

```cpp
#include <hip/hip_runtime.h>
#include <stdint.h>

typedef unsigned short u16;
typedef unsigned u32;
typedef _Float16 h2 __attribute__((ext_vector_type(2)));
typedef _Float16 h8 __attribute__((ext_vector_type(8)));
typedef __bf16 b2 __attribute__((ext_vector_type(2)));
typedef __bf16 b8 __attribute__((ext_vector_type(8)));
typedef short s4 __attribute__((ext_vector_type(4)));
typedef float f2 __attribute__((ext_vector_type(2)));
typedef float f4 __attribute__((ext_vector_type(4)));
typedef u32 u2v __attribute__((ext_vector_type(2)));
typedef u32 u3v __attribute__((ext_vector_type(3)));
typedef u32 u4v __attribute__((ext_vector_type(4)));
typedef __attribute__((address_space(3))) s4* lds_s4p;

constexpr int L = 2048;
constexpr int D = 1024;
constexpr int NM = 128;
constexpr int F = 3584;
constexpr int NTAP = 1024;

constexpr int SEG_I = 576, SEG_O0 = 800, SEG_O1 = 880, SEG_SP = 896;

#define AI __attribute__((always_inline))
#define SB() __builtin_amdgcn_sched_barrier(0)
#define OPQ(x) asm volatile("" : "+v"(x))
#define STAMP0() do {} while (0)
#define STAMP(i)
#define STAMP_END()
#define STAMP_T0

__device__ __forceinline__ float bflo(u32 v) { return __uint_as_float(v << 16); }
__device__ __forceinline__ float bfhi(u32 v) { return __uint_as_float(v & 0xffff0000u); }
__device__ __forceinline__ u32 pk_bf(float a, float b) { f2 v = {a, b}; b2 r = __builtin_convertvector(v, b2); return __builtin_bit_cast(u32, r); }
__device__ __forceinline__ float rb1(float v) { return __uint_as_float(pk_bf(0.f, v)); }
__device__ __forceinline__ h8 as_h8(u4v v) { return __builtin_bit_cast(h8, v); }
__device__ __forceinline__ b8 as_b8(u4v v) { return __builtin_bit_cast(b8, v); }
__device__ __forceinline__ u2v tr_read(const char* p) {
  s4 t = __builtin_amdgcn_ds_read_tr16_b64_v4i16((lds_s4p)p);
  return __builtin_bit_cast(u2v, t);
}
template <int CTRL> __device__ __forceinline__ u32 dpp_u(u32 v) { return (u32)__builtin_amdgcn_update_dpp(0, (int)v, CTRL, 0xf, 0xf, false); }
template <int CTRL> __device__ __forceinline__ float dpp_f(float v) { return __uint_as_float(dpp_u<CTRL>(__float_as_uint(v))); }
__device__ __forceinline__ u32 wave_max_u32(u32 v) {
  v = max(v, dpp_u<0x111>(v)); v = max(v, dpp_u<0x112>(v)); v = max(v, dpp_u<0x114>(v)); v = max(v, dpp_u<0x118>(v));
  v = max(v, dpp_u<0x142>(v)); v = max(v, dpp_u<0x143>(v));
  return (u32)__builtin_amdgcn_readlane((int)v, 63);
}
#define DPM " row_mask:0xf bank_mask:0xf bound_ctrl:1\n\t"
#define T2S(HO, HI, LI, C) "v_max_u32_dpp " HO ", " HI ", " HI " " C DPM "v_min_u32_dpp %8, " HI ", " HI " " C DPM "v_max_u32_dpp %7, " LI ", " LI " " C DPM "v_max_u32 %7, %7, %8\n\t"
#define T2R(RM, REST) T2S("%5", "%0", "%1", "row_shr:1") T2S("%6", "%5", "%7", "row_shr:2") T2S("%5", "%6", "%7", "row_shr:4") T2S("%6", "%5", "%7", "row_shr:8") \
    T2S("%5", "%6", "%7", "row_bcast:15") T2S("%6", "%5", "%7", "row_bcast:31") \
    "v_readlane_b32 %9, %6, 63\n\tv_readlane_b32 %10, %7, 63\n\ts_nop 0\n\tv_mov_b32 %5, %9\n\tv_cmp_le_u32 %11, %10, %0\n\tv_cmp_le_u32 %12, %10, %1\n\tv_mov_b32 %6, %10\n\t" \
    "v_cndmask_b32 %0, %0, %1, %11\n\tv_mov_b32_dpp %4, %5 quad_perm:[0,1,2,3] row_mask:" RM " bank_mask:0x3\n\tv_cndmask_b32 %0, %0, %2, %12\n\t" \
    "v_mov_b32_dpp %4, %6 quad_perm:[0,1,2,3] row_mask:" RM " bank_mask:0xc\n\t" REST
#define T2P "v_cndmask_b32 %1, %1, %2, %11\n\tv_cndmask_b32 %2, %2, %3, %11\n\tv_cndmask_b32 %1, %1, %3, %12\n\tv_cndmask_b32 %3, %3, 0, %11\n\tv_cndmask_b32 %2, %2, 0, %12\n\t"
__device__ __forceinline__ u32 wave_top8(u32& m1, u32 m2, u32 m3, u32 m4, u32& w2) {
  u32 key, ta, tb, lo, t, w1;
  unsigned long long c1, c2;
  asm("s_nop 1\n\t" T2R("0x1", T2P) T2R("0x2", T2P) T2R("0x4", T2P) T2R("0x8", "") "s_nop 0"
      : "+v"(m1), "+v"(m2), "+v"(m3), "+v"(m4), "=&v"(key), "=&v"(ta), "=&v"(tb), "=&v"(lo), "=&v"(t), "=&s"(w1), "=&s"(w2), "=&s"(c1), "=&s"(c2));
  return key;
}

__device__ __forceinline__ float exp_bf(float v) {
  const float e = __builtin_amdgcn_exp2f(v * 1.44268798828125f);
  return fmaf(e, v * (7.0526077e-06f * 0.69314718f), e);
}
struct cf { f2 r, i; };
struct ch { h2 r, i; };
__device__ __forceinline__ cf cmul(cf a, cf b) { cf c; c.r = a.r * b.r - a.i * b.i; c.i = a.r * b.i + a.i * b.r; return c; }
__device__ __forceinline__ ch hmul(ch a, ch b) {
  ch c;
  asm("v_pk_mul_f16 %0, %2, %4\n\tv_pk_mul_f16 %1, %2, %5\n\tv_pk_fma_f16 %0, %3, %5, %0 neg_lo:[1,0,0] neg_hi:[1,0,0]\n\tv_pk_fma_f16 %1, %3, %4, %1"
      : "=&v"(c.r), "=&v"(c.i) : "v"(a.r), "v"(a.i), "v"(b.r), "v"(b.i));
  return c;
}
__device__ __forceinline__ cf csq(cf a) { cf c; c.r = a.r * a.r - a.i * a.i; c.i = (a.r + a.r) * a.i; return c; }
__device__ __forceinline__ ch toh(cf a) { ch c; c.r = __builtin_convertvector(a.r, h2); c.i = __builtin_convertvector(a.i, h2); return c; }
__device__ __forceinline__ ch tohc(cf a) { ch c; c.r = __builtin_convertvector(a.r, h2); c.i = __builtin_convertvector(-a.i, h2); return c; }
__device__ __forceinline__ h2 ash(u32 v) { return __builtin_bit_cast(h2, v); }
__device__ __forceinline__ cf cexp_p(f2 a2, f2 brev, float p) {
  f2 e = a2 * p, t = brev * p;
  f2 mag = {__builtin_amdgcn_exp2f(e[0]), __builtin_amdgcn_exp2f(e[1])};
  float r0 = __builtin_amdgcn_fractf(t[0]), r1 = __builtin_amdgcn_fractf(t[1]);
  cf c;
  c.r = mag * f2{__builtin_amdgcn_cosf(r0), __builtin_amdgcn_cosf(r1)};
  c.i = mag * f2{__builtin_amdgcn_sinf(r0), __builtin_amdgcn_sinf(r1)};
  return c;
}
__device__ __forceinline__ u32 med3u(u32 a, u32 b, u32 c) { u32 r; asm("v_med3_u32 %0, %1, %2, %3" : "=v"(r) : "v"(a), "v"(b), "v"(c)); return r; }
__device__ __forceinline__ f2 unbf(u32 v) { return f2{bflo(v), bfhi(v)}; }
__device__ __forceinline__ float sigm(float v) { return __builtin_amdgcn_rcpf(1.f + __builtin_amdgcn_exp2f(-1.44269504088896341f * v)); }

#define TS2(A, B, C, D) "v_xor_b32 %5, " A ", %9\n\tv_xor_b32 %6, " B ", %9\n\tv_xor_b32 %7, " C ", %9\n\tv_xor_b32 %8, " D ", %9\n\t" \
    "v_fma_f32 %1, %5, 1.0, %1\n\tv_fma_f32 %2, %6, 1.0, %2\n\tv_or3_b32 %0, %0, %5, %6\n\tv_fma_f32 %3, %7, 1.0, %3\n\tv_fma_f32 %4, %8, 1.0, %4\n\tv_or3_b32 %0, %0, %7, %8\n\t"
#define TSR TS2("%10", "%11", "%12", "%13") TS2("%14", "%15", "%16", "%17") TS2("%18", "%19", "%20", "%21")
#define TSI "=&v"(t0), "=&v"(t1), "=&v"(t2), "=&v"(t3) : "v"(t10), "v"(p[0]), "v"(p[1]), "v"(p[2]), "v"(p[3]), "v"(q[0]), "v"(q[1]), "v"(q[2]), "v"(q[3]), \
    "v"(r[0]), "v"(r[1]), "v"(r[2]), "v"(r[3])
__device__ __forceinline__ f4 tsum4(u32& bad, u32 t10, u4v s, u4v p, u4v q, u4v r) {
  float s0, s1, s2, s3; u32 t0, t1, t2, t3;
  asm volatile("v_xor_b32 %1, %22, %9\n\tv_xor_b32 %2, %23, %9\n\tv_xor_b32 %3, %24, %9\n\tv_xor_b32 %4, %25, %9\n\tv_or3_b32 %0, %0, %1, %2\n\tv_or3_b32 %0, %0, %3, %4\n\t" TSR
               : "+v"(bad), "=&v"(s0), "=&v"(s1), "=&v"(s2), "=&v"(s3), TSI, "v"(s[0]), "v"(s[1]), "v"(s[2]), "v"(s[3]));
  return f4{s0, s1, s2, s3};
}
__device__ __forceinline__ f4 tsum3(u32& bad, u32 t10, f4 s, u4v p, u4v q, u4v r) {
  float s0 = s[0], s1 = s[1], s2 = s[2], s3 = s[3]; u32 t0, t1, t2, t3;
  asm volatile(TSR : "+v"(bad), "+v"(s0), "+v"(s1), "+v"(s2), "+v"(s3), TSI);
  return f4{s0, s1, s2, s3};
}
__device__ __forceinline__ void silu4(f4& g, float nl2e) {
  float g0 = g[0], g1 = g[1], g2 = g[2], g3 = g[3], t0, t1, t2, t3;
  asm volatile("v_fma_f32 %4, %0, %8, 0\n\tv_fma_f32 %5, %1, %8, 0\n\tv_fma_f32 %6, %2, %8, 0\n\tv_fma_f32 %7, %3, %8, 0\n\t"
               "v_exp_f32 %4, %4\n\tv_exp_f32 %5, %5\n\tv_exp_f32 %6, %6\n\tv_exp_f32 %7, %7\n\t"
               "v_fma_f32 %4, %4, 1.0, 1.0\n\tv_fma_f32 %5, %5, 1.0, 1.0\n\tv_fma_f32 %6, %6, 1.0, 1.0\n\tv_fma_f32 %7, %7, 1.0, 1.0\n\t"
               "v_rcp_f32 %4, %4\n\tv_rcp_f32 %5, %5\n\tv_rcp_f32 %6, %6\n\tv_rcp_f32 %7, %7\n\t"
               "v_fma_f32 %0, %0, %4, 0\n\tv_fma_f32 %1, %1, %5, 0\n\tv_fma_f32 %2, %2, %6, 0\n\tv_fma_f32 %3, %3, %7, 0"
               : "+v"(g0), "+v"(g1), "+v"(g2), "+v"(g3), "=&v"(t0), "=&v"(t1), "=&v"(t2), "=&v"(t3) : "v"(nl2e));
  g = f4{g0, g1, g2, g3};
}

constexpr int KT_PITCH = 52;
constexpr int kt_off(int p) {
  int i = 20, j = 0;
  if (p < 256) { i = p & 7; j = p >> 3; }
  else if (p < 512) { int q = (p - 256) >> 1; i = 8 + (q & 3); j = q >> 2; }
  else if (p < 1024) { int q = (p - 512) >> 2; i = 12 + (q & 1) + 2 * (q >> 6); j = (q >> 1) & 31; }
  else if (p < 2048) { int q = (p - 1024) >> 3; i = 16 + (q >> 5); j = q & 31; }
  return j * KT_PITCH + i * 2;
}
constexpr int KT_ZERO = 32 * KT_PITCH + 16;
constexpr int rnd_lag(int m) {
  const int st = m < 256 ? 1 : m < 512 ? 2 : m < 1024 ? 4 : 8;
  int q = m / st;
  const int rem = m - q * st;
  if (2 * rem > st || (2 * rem == st && (q & 1))) ++q;
  return q * st;
}
constexpr int tap_entry(int e) {
  if (e < 544) {
    if (543 - e <= 525) return kt_off(rnd_lag(543 - e));
  } else if (e >= 560 && e < 752) {
    const int q = (e - 560) / 48, x = (e - 560) - q * 48;
    const int p = 512 + 16 * (31 - x) + 4 * (q + 1);
    if (x < 32 && p > 524) return kt_off(p);
  } else if (e >= 752 && e < 816) {
    return kt_off(1032 + 16 * (815 - e));
  } else if (e >= 832 && e < 896) {
    return kt_off(1040 + 16 * (895 - e));
  } else if (e == SEG_SP + 27 || e == SEG_SP + 28) {
    return kt_off(1024);
  }
  return KT_ZERO;
}
struct TapIdx { u16 v[NTAP]; };
constexpr TapIdx make_tapidx() {
  TapIdx t{};
  for (int e = 0; e < NTAP; ++e) t.v[e] = (u16)tap_entry(2 * ((e >> 4) + 64 * ((e >> 1) & 7)) + (e & 1));
  return t;
}
__device__ __attribute__((aligned(16))) const TapIdx g_tapidx = make_tapidx();

constexpr int TROW = 544;
constexpr int O_OUT = 0;
constexpr int OT = 576;
constexpr int O_U = O_OUT + 8 * OT;
constexpr int O_TAP = O_U + 4608;
constexpr int TAP_PITCH = 2 * NTAP + 64;
constexpr int CHB = O_TAP + 16 * TROW + 32;
static_assert(16 * TROW >= 4 * TAP_PITCH, "lds");
static_assert((CHB / 4) % 32 == 8, "banks");
#define DEV 3
#define DWIN 8
#define DSEL 0
constexpr int NSLOT = 5, WWIN = 4;
constexpr int O_GT = 4 * CHB;
constexpr int O_RING = O_GT + 8192;
constexpr int O_WD = O_RING + 2 * NSLOT * 2048;
constexpr int O_FAR = O_WD + 32768;
constexpr int O_XS = O_FAR + 16384;
constexpr int XS_PITCH = 544;
constexpr int O_HB = O_XS + 8 * XS_PITCH;
constexpr int O_PART = O_HB + 2 * 528;
constexpr int O_SEL = O_PART + 512;
constexpr int O_PAR = O_SEL + 32;
constexpr int O_CTL = O_PAR + 16;
constexpr int O_JUNK = O_CTL + 512;
constexpr int K_LDS = O_JUNK + 1024;
static_assert(DWIN == 8, "flags of the down tile");
static_assert(K_LDS <= 163840 && O_RING % 16 == 0, "lds");
enum { C_PART = 0, C_GP, C_PUB, C_SEL, C_XS, C_HRDY, C_WGU, C_TL, C_XL, C_WD = 10, C_TAPS = 12, C_Q = 16, C_FAR = 32, C_TILE = 48 };

#define CTLA(word) (lbase + O_CTL + (word) * 4)
#define ARRIVE(word) asm volatile("s_mov_b64 exec, 1\n\tds_add_u32 %0, %1\n\ts_mov_b64 exec, -1" :: "v"(CTLA(word)), "v"(1) : "memory")
#define WAITQ(word, target) do { u32 t_; int n_ = 1 << 20; asm volatile( \
    "1:\n\tds_read_b32 %0, %2\n\ts_sub_u32 %1, %1, 1\n\ts_cbranch_scc1 2f\n\ts_waitcnt lgkmcnt(0)\n\tv_cmp_gt_u32 vcc, %3, %0\n\ts_cbranch_vccnz 1b\n2:\n\ts_waitcnt lgkmcnt(0)" \
    : "=&v"(t_), "+s"(n_) : "v"(CTLA(word)), "n"(target) : "vcc", "scc", "memory"); } while (0)
#define WAITS(word, target) do { u32 t_; int n_ = 1 << 20; asm volatile( \
    "1:\n\tds_read_b32 %0, %2\n\ts_sub_u32 %1, %1, 1\n\ts_cbranch_scc1 2f\n\ts_waitcnt lgkmcnt(0)\n\tv_cmp_le_u32 vcc, %3, %0\n\ts_cbranch_vccnz 2f\n\ts_sleep 2\n\ts_branch 1b\n2:\n\ts_waitcnt lgkmcnt(0)" \
    : "=&v"(t_), "+s"(n_) : "v"(CTLA(word)), "n"(target) : "vcc", "scc", "memory"); } while (0)
#define BAR() asm volatile("s_waitcnt lgkmcnt(0)\n\ts_barrier" ::: "memory")
__device__ __forceinline__ u2v lds8(const char* p) { return __builtin_bit_cast(u2v, __hip_atomic_load((const unsigned long long*)p, __ATOMIC_RELAXED, __HIP_MEMORY_SCOPE_WORKGROUP)); }

constexpr int WS_EPOCH = 0;
constexpr int WS_HEAD = 1024;
constexpr int WS_REC = 4096;
constexpr int WS_FLG = 40960;
constexpr int WS_KALL = WS_REC + 8192;
constexpr int WS_KEY = 32768;
constexpr int WS_P1 = 65536;
constexpr int WS_P2 = WS_P1 + 56 * 16384;
constexpr int WS_XT = WS_P2 + 256 * 7168;
constexpr int XT_PITCH = 33280;
constexpr int WS_END = WS_XT + 128 * XT_PITCH;
constexpr int SPIN_MAX = 1 << 16;
constexpr u32 TMASK = 0x3ffu;

#define DT_HEAD 110
#define DT_ARG 45
#define WDELAY 60
#define DT_KEY 225
#define DT_REC 255
#define DT_FLG 365
#define DT_VIS 40
#define DE_P1 385
#define DE_P2 620
#define DE_FAST 605
#define DT_MIN 25
#define DT_P1 100
#define DT_P2 130
#define SLEEP_AG1 12
#define SLEEP_G 6
#define STSLP 2
#define STWIN 4
#define H2_AT 9
#define CODE_B 28400

#define ROLE_END() do { STAMP_END(); asm volatile("s_endpgm" ::: "memory"); } while (0)
__device__ __forceinline__ u32 now32() { return (u32)__builtin_amdgcn_s_memrealtime(); }
__device__ __forceinline__ u32 later(u32 a, u32 b) { return (int)(b - a) > 0 ? b : a; }
__device__ __forceinline__ void pace(u32 t) {
#pragma unroll 1
  for (int i = 0; i < 1024 && (int)(now32() - t) < 0; ++i) __builtin_amdgcn_s_sleep(1);
}
__device__ __forceinline__ void pace2(u32 n, u32 t, int dmax) {
  int d = (int)(t - n);
  d = d < DT_MIN ? DT_MIN : (d > dmax ? dmax : d);
  pace(n + (u32)d);
}

#define ALL_OTHER "vcc", "scc", "memory", \
    "s0","s1","s2","s5","s6","s7","s33","s34","s35","s44","s45","s46","s47","s48","s49","s50","s51","s52","s53","s54","s55","s56","s57","s58","s59", \
    "s60","s61","s62","s63","s64","s65","s66","s67","s68","s69","s70","s71","s72","s73","s74","s75","s76","s77","s78","s79","s80","s81","s82","s83","s84", \
    "s85","s86","s87","s88","s89","s90","s91","s92","s93","s94","s95","s96","s97","s98","s99", \
    "v0","v1","v2","v3","v4","v5","v6","v7","v8","v9","v10","v11","v12","v13","v14","v15","v16","v17","v18","v19","v20","v21","v22","v23","v24","v25", \
    "v26","v27","v28","v29","v30","v31","v32","v33","v34","v35","v36","v37","v38","v39","v40","v41","v42","v43","v44","v45","v46","v47","v48","v49","v50", \
    "v51","v52","v53","v54","v55","v56","v57","v58","v59","v60","v61","v62","v63","v64","v65","v66","v67","v68","v69","v70","v71","v72","v73","v74","v75", \
    "v76","v77","v78","v79","v80","v81","v82","v83","v84","v85","v86","v87","v88","v89","v90","v91","v92","v93","v94","v95","v96","v97","v98","v99", \
    "v100","v101","v102","v103","v104","v105","v106","v107","v108","v109","v110","v111","v112","v113","v114","v115","v116","v117","v118","v119","v120", \
    "v121","v122","v123","v124","v125","v126","v127","v128","v129","v130","v131","v132","v133","v134","v135","v136","v137","v138","v139","v140","v141", \
    "v142","v143","v144","v145","v146","v147","v148","v149","v150","v151","v152","v153","v154","v155","v156","v157","v158","v159","v160","v161","v162", \
    "v163","v164","v165","v166","v167"
typedef __attribute__((address_space(1))) const u16* gp16;
typedef __attribute__((address_space(1))) const u32* gp32;
typedef __attribute__((address_space(1))) const u4v* gp128;
typedef unsigned long long u64;
typedef u64 q2 __attribute__((ext_vector_type(2)));
typedef u64 q4 __attribute__((ext_vector_type(4)));
typedef u64 q8 __attribute__((ext_vector_type(8)));

__global__ void __launch_bounds__(768, 3) k_main(
    const u16* x_, const u16* w_r_, const u16* gate_w_, const u16* logA_, const u16* Aim_, const u16* Cre_, const u16* Cim_,
    const u16* log_dt_, const u16* Dskip_, const u16* w_gate_, const u16* w_up_, const u16* w_down_, char* wsp_, u16* out_) {
  __shared__ __attribute__((aligned(256))) char smem[K_LDS];
  const u32 lbase = (u32)(uintptr_t)(__attribute__((address_space(3))) char*)smem;
#define XDMA(OFF) "global_load_lds_dwordx4 v1, s[8:9] offset:" #OFF " nt\n\ts_addk_i32 m0, 896\n\t"
  asm volatile("s_load_dwordx16 s[8:23], %0, 0\n\tv_readfirstlane_b32 s3, %2\n\ts_load_dwordx8 s[24:31], %0, 64\n\ts_load_dwordx4 s[36:39], %0, 96\n\t"
               "s_memrealtime s[40:41]\n\t" STAMP_T0 "s_mov_b32 s4, %1\n\ts_lshr_b32 s3, s3, 6\n\t"
               "s_cmp_lt_u32 s3, 4\n\ts_cbranch_scc1 .Lrows\n\ts_barrier\n\t"
               "s_cmp_ge_u32 s3, 8\n\ts_cbranch_scc1 .Lrole_g\n\ts_cmp_eq_u32 s3, 7\n\ts_cbranch_scc1 .Lrole_d\n\t"
               "s_cmp_ge_u32 s3, 5\n\ts_cbranch_scc1 .Lrole_h\n\ts_branch .Lrole_c\n.Lrows:\n\t"
               "s_lshr_b32 s5, s4, 3\n\tv_lshlrev_b32 v3, 2, %2\n\ts_mov_b32 m0, %4\n\tv_lshl_or_b32 v3, s5, 10, v3\n\ts_getpc_b64 s[6:7]\n\t"
               "v_min_u32 v3, %3, v3\n\tv_lshrrev_b32 v1, 3, %2\n\tglobal_load_lds_dword v3, s[6:7]\n\t"
               "v_and_b32 v2, 6, v1\n\ts_and_b32 s34, s4, 7\n\tv_xor_b32 v2, v2, %2\n\ts_lshl_b32 s34, s34, 5\n\tv_and_b32 v2, 7, v2\n\ts_add_u32 s5, s5, s34\n\t"
               "v_and_b32 v1, 7, v1\n\ts_lshl_b32 s5, s5, 5\n\tv_lshlrev_b32 v1, 11, v1\n\ts_add_u32 s5, s5, s3\n\tv_lshl_or_b32 v1, v2, 4, v1\n\t"
               "s_mul_i32 s34, s3, %5\n\tv_lshl_add_u32 v1, s5, 9, v1\n\ts_add_u32 m0, s34, %6\n\tv_and_b32 v2, 31, %2\n\ts_waitcnt lgkmcnt(0)\n\t"
               XDMA(0) "s_lshl_b32 s5, s3, 5\n\t" XDMA(128) "v_add_lshl_u32 v2, s5, v2, 4\n\t" XDMA(256) "s_lshl_b32 s5, s3, 10\n\t"
               "global_load_lds_dwordx4 v1, s[8:9] offset:384 nt\n\ts_add_u32 m0, s5, %7\n\ts_mov_b32 exec_hi, 0\n\t"
               "global_load_lds_dwordx4 v2, s[10:11]\n\ts_addk_i32 m0, 512\n\ts_nop 0\n\tglobal_load_lds_dwordx4 v2, s[12:13]\n\t"
               "s_mov_b32 m0, %8\n\tv_mov_b32 v3, 0\n\tglobal_load_lds_dword v3, s[36:37]\n\ts_mov_b32 exec_hi, -1"
               :: "s"(__builtin_amdgcn_kernarg_segment_ptr()), "s"(blockIdx.x), "v"(threadIdx.x), "n"(CODE_B - 256), "n"(O_JUNK), "n"(CHB), "n"(O_U + 512), "n"(O_FAR), "n"(O_XS)
               : "s3", "s4", "s5", "s6", "s7", "s8", "s9", "s10", "s11", "s12", "s13", "s14", "s15", "s16", "s17", "s18", "s19", "s20", "s21", "s22", "s23",
                 "s24", "s25", "s26", "s27", "s28", "s29", "s30", "s31", "s34", "s36", "s37", "s38", "s39", "s40", "s41", "s42", "s43", "v1", "v2", "v3", "scc", "memory");
  int lane, c16, g, w, b, xg, xi, cg, cc, c, d;
  char* ch_;
  q8 ka; q4 kb; q2 kc, kt;
  u32* ctl = (u32*)(smem + O_CTL);
  u32* par = (u32*)(smem + O_PAR);
#define IDS() do { int z_ = 0; OPQ(z_); lane = __builtin_amdgcn_mbcnt_hi(~0u, __builtin_amdgcn_mbcnt_lo(~0u, z_)); c16 = lane & 15; g = lane >> 4; } while (0)
#define ROLE(name) \
  asm volatile(".Lrole_" name ":\n\ts_waitcnt lgkmcnt(0)" : "={s[8:23]}"(ka), "={s[24:31]}"(kb), "={s[36:39]}"(kc), "={s[40:43]}"(kt), "={s3}"(w), "={s4}"(b) :: ALL_OTHER); \
  if (__builtin_expect(w > 11, 0)) asm volatile("s_endpgm" ::: "memory");        \
  IDS(); \
  xg = b & 7; xi = b >> 3;                          \
  cg = xg * 32 + xi;                                \
  cc = (xi >> 4) * 128 + xg * 16 + (xi & 15);       \
  c = w & 3; ch_ = smem + c * CHB; d = cc * 4 + c; \
  const u32 ts0 = (u32)kt[0]; \
  auto wrs = __builtin_amdgcn_make_buffer_rsrc((void*)kc[0], 0, WS_END, 0x00020000); \
  STAMP0()

  const char* tapb;
  const char* ub;
  char* erp;
  const char* eup;
  const char* egp;
  u4v At[17], band[4];
  float dm1;
  f4 acc[8];
  auto conv_setup = [&]() AI {
    tapb = ch_ + O_TAP + ((15 - c16) & 3) * TAP_PITCH + ((15 - c16) & ~3) * 2;
    ub = ch_ + O_U - 32 + c16 * 32 + g * 16;
    erp = ch_ + O_OUT + g * 144 + c16 * 8;
    eup = ch_ + O_U + 512 + c16 * 32 + g * 8;
    egp = smem + O_GT + lane * 16;
  };
  auto band_setup = [&]() AI {
#pragma unroll
    for (int k = 0; k < 4; ++k) {
      const int ctr = k == 0 ? 12 : 8, hw = k + 1;
      const u32 m = ((1u << (2 * hw + 1)) - 1u) << (ctr - hw);
      const u32 bits = (m << c16) >> (8 * g);
#pragma unroll
      for (int j = 0; j < 4; ++j)
        band[k][j] = (__builtin_amdgcn_sbfe(bits, 2 * j, 1) & 0x3f80u) | (__builtin_amdgcn_sbfe(bits, 2 * j + 1, 1) & 0x3f800000u);
      OPQ(band[k]);
    }
  };
  auto frag2 = [&](const char* pa, const char* pb) AI { const u2v lo = lds8(pa), hi = lds8(pb); return u4v{lo[0], lo[1], hi[0], hi[1]}; };
  auto atload = [&](int s0, int s1) AI {
#pragma unroll
    for (int s = s0; s < s1; ++s) At[s] = frag2(tapb + 64 * s + g * 16, tapb + 64 * s + g * 16 + 8);
  };
  auto bin = [&](int pair, int be) AI { const char* p = tapb + g * 8 + (SEG_I + 96 * pair - 16 * be) * 2; return frag2(p, p + 96); };
  auto bout = [&](int be) AI { const char* p = tapb + g * 8 + (SEG_O0 - 16 * be) * 2; return frag2(p, p + (SEG_O1 - SEG_O0) * 2); };
  auto asp = [&]() AI { return frag2(tapb + SEG_SP * 2 + g * 16, tapb + SEG_SP * 2 + g * 16 + 8); };
  auto mma = [&](u4v a, u4v bq, f4 cin) AI { return __builtin_amdgcn_mfma_f32_16x16x32_bf16(as_b8(a), as_b8(bq), cin, 0, 0, 0); };
  auto uld = [&](int sg) AI { return *(const u4v*)(ub + 512 + sg * 64); };
  auto uld24 = [&](int un) AI { return *(const u4v*)(ub + 512 + un * 512 - 16); };
  u2v euw[8], eo[8]; f4 egv[8], efv[8];
  float eu[8][4], ev[8][4], ea[8][4];
  u4v Fq[57], ru[6], wa_[6][3];
  f4 wq[6][6];
  u32 pk[12], one, qa, fa;
  char* tfp; char* fp_;
#define LOPQ(p_) do { u32 a_ = (u32)(uintptr_t)(__attribute__((address_space(3))) const char*)p_; OPQ(a_); p_ = (decltype(p_))(__attribute__((address_space(3))) char*)(uintptr_t)a_; } while (0)
  auto m_setup = [&]() AI {
    tfp = smem + (lane == 0 ? O_CTL + c * 4 : O_JUNK + lane * 4);
    fp_ = smem + O_FAR + c * 4096 + lane * 16;
    qa = CTLA(C_Q) + (lane & 3) * 4; fa = CTLA(C_FAR + c); one = 1u;
    OPQ(qa); OPQ(fa); OPQ(one);
    LOPQ(tfp); LOPQ(fp_); LOPQ(erp); LOPQ(eup); LOPQ(egp); LOPQ(ub);
  };
#define WAITA(addr) do { u32 t_; int n_ = 1 << 20; asm volatile( \
    "1:\n\tds_read_b32 %0, %2\n\ts_sub_u32 %1, %1, 1\n\ts_cbranch_scc1 2f\n\ts_waitcnt lgkmcnt(0)\n\tv_cmp_eq_u32 vcc, 0, %0\n\ts_cbranch_vccnz 1b\n2:\n\ts_waitcnt lgkmcnt(0)" \
    : "=&v"(t_), "+s"(n_) : "v"(addr) : "vcc", "scc", "memory"); } while (0)
#define MF(d_, a_, b_) asm volatile("v_mfma_f32_16x16x32_bf16 %0, %1, %2, %0" : "+v"(d_) : "v"(a_), "v"(b_)); SB()
#define MF0(d_, a_, b_) asm volatile("v_mfma_f32_16x16x32_bf16 %0, %1, %2, 0" : "=&v"(d_) : "v"(a_), "v"(b_)); SB()
#define NOP(n_) asm volatile("s_nop " #n_); SB()
#define FL(i_, sg_) Fq[i_] = uld(sg_); SB()
#define RL(un_) ru[un_] = uld24(un_); SB()
#define AL(s_) atload(s_, s_ + 1); SB()
#define ULD(t_) euw[t_] = *(const u2v*)(eup + t_ * 512); SB()
#define GLD(t_) egv[t_] = *(const f4*)(egp + t_ * 1024); SB()
#define FLD(t_) efv[t_] = *(const f4*)(fp_ + (t_ - 2) * 1024); SB()
#define FST(un_) *(f4*)(fp_ + un_ * 1024) = acc[un_ + 2]; SB()
#define EUN(t_, e_) eu[t_][e_] = (e_ & 1) ? bfhi(euw[t_][e_ >> 1]) : bflo(euw[t_][e_ >> 1]); OPQ(eu[t_][e_]); ev[t_][e_] = eu[t_][e_]; SB()
#define EA1(t_, e_) ea[t_][e_] = fmaf(eu[t_][e_], dm1, acc[t_][e_]); OPQ(ea[t_][e_]); SB()
#define EFA(t_, e_) ev[t_][e_] = fmaf(egv[t_][e_], efv[t_][e_], eu[t_][e_]); OPQ(ev[t_][e_]); SB()
#define EA2(t_, e_) ea[t_][e_] = fmaf(egv[t_][e_], ea[t_][e_], ev[t_][e_]); OPQ(ea[t_][e_]); SB()
#define EPK(t_, k_) { u32 v_ = pk_bf(ea[t_][2 * k_], ea[t_][2 * k_ + 1]); OPQ(v_); eo[t_][k_] = v_; } SB()
#define OST(t_) *(u2v*)(erp + t_ * OT) = eo[t_]; SB()
#define WPK(un_, p_, j_) { u32 v_ = pk_bf(wq[un_][2 * p_ + j_ / 2][2 * (j_ & 1)], wq[un_][2 * p_ + j_ / 2][2 * (j_ & 1) + 1]); OPQ(v_); wa_[un_][p_][j_] = v_; } SB()
#define FLAG(w_) asm volatile("" ::: "memory"); *(u32*)(tfp + (w_) * 4) = one; SB()
#define PEEK(i_, a_) pk[i_] = __hip_atomic_load((__attribute__((address_space(3))) u32*)(uintptr_t)(a_), __ATOMIC_RELAXED, __HIP_MEMORY_SCOPE_WORKGROUP); SB()
#define CHECK(i_, a_) if (__builtin_expect(__builtin_amdgcn_ballot_w64(pk[i_] == 0u) != 0ull, 0)) WAITA(a_); asm volatile("" ::: "memory"); SB()
#define MIX2(nm, nd, nv) _Pragma("unroll") for (int r_ = 0; r_ < (nm); ++r_) { __builtin_amdgcn_sched_group_barrier(0x008, 1, 0); \
    if (nd) __builtin_amdgcn_sched_group_barrier(0x100, (nd), 0); if (nv) __builtin_amdgcn_sched_group_barrier(0x002, (nv), 0); }

  {
    ROLE("x");
    const int xw = w;
    char* img = ch_ + O_U + 512;
    STAMP(11);
    __builtin_amdgcn_s_setprio(2);
    { u32 zz = 0u; OPQ(zz); if (lane < 32) ctl[32 * xw + lane] = zz; }
    if (xi == 31 && w == 0) { const u32 th = later(ts0, now32() - DT_ARG); if (lane == 0) __builtin_amdgcn_raw_buffer_store_b64(u2v{th, ~th}, wrs, WS_HEAD + xg * 8, 0, 16); }
    BAR();
    STAMP(12);
    const char* wp; const char* fp; const char* tp0; const char* tp1; char* pp;
    {
      const int r = lane & 7, q = (lane >> 2) & 3;
      fp = img + r * 128 + ((((lane & 8) / 2 + g) ^ (r & 6)) * 16);
      wp = smem + O_FAR + xw * 1024 + (lane & 1) * 512 + (lane & 4) * 16 + g * 16;
      tp0 = img + q * 128 + (((2 * g + ((lane >> 1) & 1)) ^ (q & 2)) * 16) + (lane & 1) * 8;
      tp1 = img + 512 + q * 128 + (((2 * g + ((lane >> 1) & 1)) ^ ((q & 2) + 4)) * 16) + (lane & 1) * 8;
      pp = smem + ((lane >> 3) == 0 || (lane >> 3) == 3 ? O_PART + r * 64 + xw * 16 + g * 8 : O_JUNK + lane * 8);
    }
    const int so = WS_XT + (32 * xw + (lane >> 3)) * XT_PITCH + cg * 128 + (lane & 7) * 16, sstep = 8 * XT_PITCH;
    u32 lo_ = WS_XT + (cc >> 1) * XT_PITCH + (16 * xw + (lane >> 2)) * 128 + (cc & 1) * 64 + (lane & 3) * 16;
    OPQ(lo_);
    SB();
    asm volatile("s_waitcnt vmcnt(0)" ::: "memory");
    STAMP(1);
    {
      u4v wa[4], fb[4]; u2v ta[4], tb[4];
#pragma unroll
      for (int s = 0; s < 4; ++s) { wa[s] = *(const u4v*)(wp + s * 128); fb[s] = *(const u4v*)(fp + s * 1024); }
      SB();
      f4 r = {0.f, 0.f, 0.f, 0.f};
#pragma unroll
      for (int s = 0; s < 4; ++s) r = mma(wa[s], fb[s], r);
      *(f2*)pp = f2{r[0], r[1]};
      ARRIVE(C_PART);
      SB();
#pragma unroll
      for (int s = 0; s < 4; ++s) { ta[s] = tr_read(tp0 + s * 1024); tb[s] = tr_read(tp1 + s * 1024); }
      SB();
      WAITQ(C_GP, 1);
#pragma unroll
      for (int s = 0; s < 4; ++s) __builtin_amdgcn_raw_buffer_store_b128(u4v{ta[s][0], ta[s][1], tb[s][0], tb[s][1]}, wrs, so + s * sstep, 0, 16);
    }
    STAMP(2);
    const u32 dskw = ((gp16)kb[0])[d];
    conv_setup();
    band_setup();
    const u32 tag = par[0];
    ARRIVE(C_PUB);
    asm volatile("s_waitcnt vmcnt(0)" ::: "memory");
    if (lane == 0) __builtin_amdgcn_raw_buffer_store_b32(tag, wrs, WS_FLG + xw * 1024 + cg * 4, 0, 16);
    STAMP(3);
    dm1 = bflo(dskw) - 1.f;
    WAITS(C_TL, 1);
    const u32 tl0 = (u32)__builtin_amdgcn_readfirstlane((int)par[1]);
    u3v fq[2];
    auto rec_ld = [&]() AI {
#pragma unroll
      for (int k = 0; k < 2; ++k) fq[k] = __builtin_amdgcn_raw_buffer_load_b96(wrs, WS_REC + xw * 2048 + k * 1024 + lane * 16, 0, 16);
    };
    pace(tl0 + DT_REC);
    rec_ld();
    SB();
    pace(tl0 + DT_FLG);
    const u32 fl = __builtin_amdgcn_raw_buffer_load_b32(wrs, WS_FLG + (cc >> 6) * 1024 + xw * 64 + g * 256 + c16 * 4, 0, 16);
    SB();
    {
      if (__builtin_expect(__any(fq[0][2] != tag || fq[1][2] != tag), 0)) {
#pragma unroll 1
        for (int spin = 0; spin < SPIN_MAX; ++spin) {
          __builtin_amdgcn_s_sleep(SLEEP_G);
          rec_ld();
          if (!__any(fq[0][2] != tag || fq[1][2] != tag)) break;
          asm volatile("" ::: "memory");
        }
      }
      char* gp = smem + O_GT + xw * 2048 + (lane & 3) * 256 + (lane >> 2) * 16;
#pragma unroll
      for (int k = 0; k < 2; ++k) *(f4*)(gp + k * 1024) = f4{bflo(fq[k][0]), bfhi(fq[k][0]), bflo(fq[k][1]), bfhi(fq[k][1])};
    }
    SB();
    u4v xq[4];
    bool late[4];
    {
      const u64 bad = __builtin_amdgcn_ballot_w64(fl != tag);
      asm volatile("" ::: "memory");
#pragma unroll
      for (int k = 0; k < 4; ++k) {
        late[k] = ((bad >> (16 * k)) & 0xffffull) != 0ull;
        xq[k] = __builtin_amdgcn_raw_buffer_load_b128(wrs, lo_ | (late[k] ? 1u << 30 : 0u), k * 8192, 16);
        SB();
      }
    }
    ARRIVE(C_XL);
    STAMP(4);
    WAITQ(C_TAPS + c, 1);
    u4v Bi[2][3], Bo[4], As;
#pragma unroll
    for (int be = 0; be < 3; ++be) { Bi[0][be] = bin(0, be); Bi[1][be] = bin(1, be); }
#pragma unroll
    for (int be = 0; be < 4; ++be) Bo[be] = bout(be);
    As = asp();
    WAITQ(C_PUB, 4);
    char* dp = smem + (lane & 3) * CHB + O_U + 512 + xw * 256 + (lane >> 2) * 16;
    m_setup();
    auto stage = [&](int k) AI {
      SB();
      if (__builtin_expect(late[k], 0)) {
#pragma unroll 1
        for (int spin = 0; spin < SPIN_MAX; ++spin) {
          STAMP(14);
          const u32 f1 = __builtin_amdgcn_raw_buffer_load_b32(wrs, WS_FLG + (cc >> 6) * 1024 + xw * 64 + k * 256 + c16 * 4, 0, 16);
          if (!__any(f1 != tag)) break;
          __builtin_amdgcn_s_sleep(SLEEP_G);
          asm volatile("" ::: "memory");
        }
        asm volatile("" ::: "memory");
        xq[k] = __builtin_amdgcn_raw_buffer_load_b128(wrs, lo_, k * 8192, 16);
        asm volatile("s_waitcnt vmcnt(0)" : "+v"(xq[k]) :: "memory");
      }
      *(u4v*)(dp + k * 1024) = xq[k];
      FLAG(C_Q + k * 4);
    };
    stage(0);
    stage(1);
    STAMP(5);
    __builtin_amdgcn_s_setprio(0);
    WAITA(qa);
    SB();
    FL(0, 0); RL(0); FL(8, 8); RL(1);
    MF0(wq[0][0], Fq[0], band[0]);
    MF0(wq[0][1], Fq[0], band[1]);
    MF0(wq[0][2], ru[0], band[0]);
    MF0(wq[0][3], ru[0], band[1]);
    MF0(wq[0][4], Fq[0], band[2]);
    MF0(wq[0][5], ru[0], band[3]);
    MF0(acc[4], As, Fq[0]); PEEK(6, qa + 16);
    MF0(wq[1][0], Fq[8], band[0]); WPK(0, 0, 0); WPK(0, 0, 1);
    MF0(wq[1][1], Fq[8], band[1]); WPK(0, 0, 2); WPK(0, 0, 3);
    MF0(wq[1][2], ru[1], band[0]); WPK(0, 1, 0); WPK(0, 1, 1);
    MF0(wq[1][3], ru[1], band[1]); WPK(0, 1, 2); WPK(0, 1, 3);
    MF0(wq[1][4], Fq[8], band[2]); CHECK(6, qa + 16);
    MF0(wq[1][5], ru[1], band[3]); WPK(0, 2, 0); WPK(0, 2, 1);
    MF0(acc[5], As, Fq[8]); WPK(0, 2, 2); WPK(0, 2, 3);
    MF0(acc[2], wa_[0][0], Bi[0][0]); FL(16, 16); RL(2);
    MF(acc[2], wa_[0][1], Bi[1][0]); WPK(1, 0, 0); WPK(1, 0, 1);
    MF0(acc[3], wa_[0][0], Bi[0][1]); WPK(1, 0, 2); WPK(1, 0, 3);
    MF(acc[3], wa_[0][1], Bi[1][1]); WPK(1, 1, 0); WPK(1, 1, 1);
    MF(acc[4], wa_[0][0], Bi[0][2]); FST(0); FLAG(C_FAR + 0 * 4);
    MF(acc[4], wa_[0][1], Bi[1][2]); WPK(1, 1, 2); WPK(1, 1, 3);
    MF(acc[4], wa_[0][2], Bo[0]); WPK(1, 2, 0); WPK(1, 2, 1);
    MF(acc[5], wa_[0][2], Bo[1]); WPK(1, 2, 2); WPK(1, 2, 3);
    MF0(acc[6], wa_[0][2], Bo[2]);
    MF0(acc[7], wa_[0][2], Bo[3]); stage(2);
    MF0(wq[2][0], Fq[16], band[0]);
    MF0(wq[2][1], Fq[16], band[1]); stage(3);
    MF0(wq[2][2], ru[2], band[0]);
    MF0(wq[2][3], ru[2], band[1]);
    MF0(wq[2][4], Fq[16], band[2]); FL(24, 24); RL(3);
    MF0(wq[2][5], ru[2], band[3]); WPK(2, 0, 0); WPK(2, 0, 1);
    MF(acc[6], As, Fq[16]); WPK(2, 0, 2); WPK(2, 0, 3);
    MF(acc[3], wa_[1][0], Bi[0][0]); WPK(2, 1, 0); WPK(2, 1, 1);
    MF(acc[3], wa_[1][1], Bi[1][0]); WPK(2, 1, 2); WPK(2, 1, 3);
    MF(acc[4], wa_[1][0], Bi[0][1]); WPK(2, 2, 0); WPK(2, 2, 1);
    MF(acc[4], wa_[1][1], Bi[1][1]); WPK(2, 2, 2); WPK(2, 2, 3);
    MF(acc[5], wa_[1][0], Bi[0][2]); FST(1); FLAG(C_FAR + 1 * 4);
    MF(acc[5], wa_[1][1], Bi[1][2]); PEEK(7, qa + 32);
    MF(acc[5], wa_[1][2], Bo[0]);
    MF(acc[6], wa_[1][2], Bo[1]);
    MF(acc[7], wa_[1][2], Bo[2]);
    MF0(wq[3][0], Fq[24], band[0]);
    MF0(wq[3][1], Fq[24], band[1]); CHECK(7, qa + 32);
    MF0(wq[3][2], ru[3], band[0]);
    MF0(wq[3][3], ru[3], band[1]);
    MF0(wq[3][4], Fq[24], band[2]); FL(32, 32); RL(4);
    MF0(wq[3][5], ru[3], band[3]); WPK(3, 0, 0); WPK(3, 0, 1);
    MF(acc[7], As, Fq[24]); WPK(3, 0, 2); WPK(3, 0, 3);
    MF(acc[4], wa_[2][0], Bi[0][0]); WPK(3, 1, 0); WPK(3, 1, 1);
    MF(acc[4], wa_[2][1], Bi[1][0]); WPK(3, 1, 2); WPK(3, 1, 3);
    MF(acc[5], wa_[2][0], Bi[0][1]); WPK(3, 2, 0); WPK(3, 2, 1);
    MF(acc[5], wa_[2][1], Bi[1][1]); FL(40, 40); RL(5);
    MF(acc[6], wa_[2][0], Bi[0][2]); FST(2); FLAG(C_FAR + 2 * 4);
    MF(acc[6], wa_[2][1], Bi[1][2]); WPK(3, 2, 2); WPK(3, 2, 3);
    MF(acc[6], wa_[2][2], Bo[0]); AL(0);
    MF(acc[7], wa_[2][2], Bo[1]); AL(1);
    MF0(wq[4][0], Fq[32], band[0]); AL(2);
    MF0(wq[4][1], Fq[32], band[1]); AL(3);
    MF0(wq[4][2], ru[4], band[0]); AL(4);
    MF0(wq[4][3], ru[4], band[1]); WPK(4, 0, 0); WPK(4, 0, 1);
    MF(acc[5], wa_[3][0], Bi[0][0]); WPK(4, 0, 2); WPK(4, 0, 3);
    MF(acc[5], wa_[3][1], Bi[1][0]); WPK(4, 1, 0); WPK(4, 1, 1);
    MF(acc[6], wa_[3][0], Bi[0][1]); WPK(4, 1, 2); WPK(4, 1, 3);
    MF(acc[6], wa_[3][1], Bi[1][1]); AL(5);
    MF(acc[7], wa_[3][0], Bi[0][2]); FST(3); FLAG(C_FAR + 3 * 4);
    MF(acc[7], wa_[3][1], Bi[1][2]); AL(6);
    MF(acc[7], wa_[3][2], Bo[0]); FL(33, 33); AL(7);
    MF0(wq[5][0], Fq[40], band[0]); FL(34, 34); AL(8);
    MF0(wq[5][1], Fq[40], band[1]); FL(35, 35); AL(9);
    MF0(wq[5][2], ru[5], band[0]); WPK(5, 0, 0); WPK(5, 0, 1);
    MF0(wq[5][3], ru[5], band[1]); AL(10);
    MF(acc[6], wa_[4][0], Bi[0][0]); PEEK(8, qa + 48); FL(36, 36);
    MF(acc[6], wa_[4][1], Bi[1][0]); FL(37, 37); WPK(5, 0, 2);
    MF(acc[7], wa_[4][0], Bi[0][1]); FL(38, 38); WPK(5, 0, 3);
    MF(acc[7], wa_[4][1], Bi[1][1]); FL(39, 39); WPK(5, 1, 0);
    MF(acc[6], At[0], Fq[32]); WPK(5, 1, 1); WPK(5, 1, 2);
    MF(acc[6], At[1], Fq[33]); CHECK(8, qa + 48);
    MF(acc[6], At[2], Fq[34]); WPK(5, 1, 3); FL(41, 41);
    MF(acc[6], At[3], Fq[35]); AL(11);
    MF(acc[7], wa_[5][0], Bi[0][0]); FL(42, 42); AL(12);
    MF(acc[7], wa_[5][1], Bi[1][0]); AL(13);
    MF(acc[6], At[4], Fq[36]); FL(43, 43); AL(14);
    MF(acc[6], At[5], Fq[37]); AL(15);
    MF(acc[6], At[6], Fq[38]); FL(44, 44); AL(16);
    MF(acc[6], At[7], Fq[39]);
    MF(acc[6], At[8], Fq[40]); FL(45, 45);
    MF(acc[7], At[0], Fq[40]);
    MF(acc[6], At[9], Fq[41]); FL(46, 46);
    MF(acc[7], At[1], Fq[41]);
    MF(acc[6], At[10], Fq[42]); FL(47, 47);
    MF(acc[7], At[2], Fq[42]);
    MF(acc[6], At[11], Fq[43]); FL(48, 48);
    MF(acc[7], At[3], Fq[43]);
    MF(acc[6], At[12], Fq[44]); FL(49, 49); ULD(6);
    MF(acc[7], At[4], Fq[44]); FL(50, 50); GLD(6);
    MF(acc[6], At[13], Fq[45]); FL(51, 51);
    MF(acc[7], At[5], Fq[45]); FL(52, 52);
    MF(acc[6], At[14], Fq[46]); FL(53, 53);
    MF(acc[7], At[6], Fq[46]); FL(54, 54); EUN(6, 0);
    MF(acc[6], At[15], Fq[47]); FL(55, 55); EUN(6, 1);
    MF(acc[7], At[7], Fq[47]); FL(56, 56); EUN(6, 2);
    MF(acc[6], At[16], Fq[48]); EUN(6, 3);
    MF(acc[7], At[8], Fq[48]); ULD(7); GLD(7);
    MF(acc[7], At[9], Fq[49]);
    MF(acc[7], At[10], Fq[50]);
    MF(acc[7], At[11], Fq[51]);
    MF(acc[7], At[12], Fq[52]); EA1(6, 0); EA1(6, 1); EA1(6, 2); EA1(6, 3);
    MF(acc[7], At[13], Fq[53]); EA2(6, 0); EA2(6, 1); EA2(6, 2); EA2(6, 3);
    MF(acc[7], At[14], Fq[54]); EPK(6, 0); EPK(6, 1);
    MF(acc[7], At[15], Fq[55]); OST(6); FLAG(C_TILE + 6 * 4);
    MF(acc[7], At[16], Fq[56]); EUN(7, 0); EUN(7, 1); EUN(7, 2); EUN(7, 3); NOP(3); EA1(7, 0); EA1(7, 1); EA1(7, 2); EA1(7, 3); EA2(7, 0); EA2(7, 1); EA2(7, 2); EA2(7, 3); EPK(7, 0); EPK(7, 1); OST(7); FLAG(C_TILE + 7 * 4);
    STAMP(9);
    ROLE_END();
  }
  {
    ROLE("h");
    const int hw = w - 5;
    const int j = xg * 28 + xi;
    const int s1 = j >> 2, kq = j & 3;
    const bool own = xi < 28;
    const u64 wbase = hw == 0 ? kb[1] : kb[2];
    const int pitch = F * 2;
    auto hrs = __builtin_amdgcn_make_buffer_rsrc((void*)wbase, 0, own ? D * F * 2 : 0, 0x00020000);
    const int r8 = lane >> 3;
    const int vo = (256 * kq * F + 64 * s1) * 2 + (8 * (r8 >> 2) + (r8 & 3)) * pitch + (((lane & 7) * 16) ^ (((r8 >> 1) & 3) * 32));
    auto dma = [&](int h, int sB, int pt, u32 dst) AI {
      const int m = h >> 1;
#pragma unroll
      for (int i = 0; i < 2; ++i)
        asm volatile("s_mov_b32 m0, %3\n\ts_nop 0\n\tbuffer_load_dwordx4 %0, %1, %2 offen nt lds"
                     :: "v"(vo), "s"(hrs), "s"((m & 1) * 32 * pt + (m >> 1) * sB + (16 * i + 4 * (h & 1)) * pt), "s"(dst + i * 1024) : "memory");
    };
    const int q = c16 >> 2, p = c16 & 3;
    int tro[4];
#pragma unroll
    for (int ct = 0; ct < 4; ++ct) tro[ct] = (4 * g + q) * 128 + ((ct ^ (((4 * g + q) >> 1) & 3)) * 32) + p * 8;
    const int tok = lane & 7;
    u4v wd[2][8];
    auto wd_ld = [&](int k, int cb) AI {
#pragma unroll
      for (int i = 0; i < 8; ++i) {
        const char* pp = smem + O_WD + cb * 8192 + (i >> 2) * 4096 + tro[i & 3];
        const u2v lo = tr_read(pp), hi = tr_read(pp + 2048);
        wd[k][i] = u4v{lo[0], lo[1], hi[0], hi[1]};
      }
    };
    u32 p2o, p2k[4], hlo, hhi;
    auto f2_setup = [&](int cb0) AI {
      p2o = WS_P2 + ((64 * kq + 16 * cb0 + (lane & 8) / 2 + g) * 56 + s1) * 128 + tok * 16;
      hlo = lbase + O_HB + ((lane & 8) ? 512 : g * 128 + tok * 16);
      hhi = lbase + O_HB + ((lane & 8) ? g * 128 + tok * 16 : 512);
#pragma unroll
      for (int i = 0; i < (cb0 == 2 ? 4 : 2); ++i) { p2k[i] = p2o + i * 8 * 7168; OPQ(p2k[i]); }
      OPQ(hlo); OPQ(hhi);
    };
    auto f2_job = [&](int nb, u32 t10) AI {
      u4v hb[4];
      {
        u32 cnt; int n_ = 1 << 20;
        asm volatile("1:\n\tds_read_b32 %4, %6\n\tds_read_b128 %0, %7\n\tds_read_b128 %1, %8\n\tds_read_b128 %2, %7 offset:528\n\tds_read_b128 %3, %8 offset:528\n\t"
                     "s_sub_u32 %5, %5, 1\n\ts_cbranch_scc1 2f\n\ts_waitcnt lgkmcnt(0)\n\tv_cmp_gt_u32 vcc, 2, %4\n\ts_cbranch_vccnz 1b\n2:\n\ts_waitcnt lgkmcnt(0)"
                     : "=&v"(hb[0]), "=&v"(hb[1]), "=&v"(hb[2]), "=&v"(hb[3]), "=&v"(cnt), "+s"(n_) : "v"(CTLA(C_HRDY)), "v"(hlo), "v"(hhi) : "vcc", "scc", "memory");
      }
      STAMP(11);
      f4 r[4];
#pragma unroll
      for (int i = 0; i < 2 * nb; ++i) {
        const u4v* wp = &wd[i >> 1][(i & 1) * 2];
        r[i] = mma(wp[0], hb[0], f4{0.f, 0.f, 0.f, 0.f});
        r[i] = mma(wp[1], hb[1], r[i]);
        r[i] = mma(wp[4], hb[2], r[i]);
        r[i] = mma(wp[5], hb[3], r[i]);
      }
      SB();
#pragma unroll
      for (int i = 0; i < 2 * nb; ++i) {
        u4v v = __builtin_bit_cast(u4v, r[i]);
#pragma unroll
        for (int e = 0; e < 4; ++e) v[e] = __builtin_amdgcn_bitop3_b32(v[e], t10, TMASK, 0xd8);
        __builtin_amdgcn_raw_buffer_store_b128(v, wrs, p2k[i], 0, 16);
      }
      STAMP(12);
    };
    const u32 rb = lbase + O_RING + hw * (NSLOT * 2048);
    pace(ts0 + WDELAY);
    STAMP(2);
#pragma unroll
    for (int h = 0; h < WWIN; ++h) dma(h, 64 * F * 2, F * 2, rb + h * 2048);
    __builtin_amdgcn_s_setprio(3);
    STAMP(3);
    if (__builtin_expect(own, 1)) {
      u4v wv[32];
      f4 a1[4] = {f4{0.f, 0.f, 0.f, 0.f}, f4{0.f, 0.f, 0.f, 0.f}, f4{0.f, 0.f, 0.f, 0.f}, f4{0.f, 0.f, 0.f, 0.f}};
      u4v xb[8];
      auto f1 = [&](int k0, int k1) AI {
#pragma unroll
        for (int k = k0; k < k1; ++k)
#pragma unroll
          for (int ct = 0; ct < 4; ++ct) a1[ct] = mma(wv[4 * k + ct], xb[k], a1[ct]);
      };
      u32 xo = lbase + O_XS + tok * XS_PITCH + g * 16;
      u32 p1o = WS_P1 + s1 * 16384 + kq * 4096 + hw * 2048 + (lane & 8) * 64 + g * 128 + tok * 16;
      u32 t10, tpub;
      OPQ(xo); OPQ(p1o);
      constexpr int HX = (16 - WWIN + 1) & ~1;
#pragma unroll
      for (int h = 0; h < 16; ++h) {
        const int left = (h + WWIN < 16 ? WWIN : 16 - h) - 1;
        asm volatile("s_waitcnt vmcnt(%0)" :: "n"(2 * left) : "memory");
        if (h + WWIN < 16) {
          asm volatile("s_waitcnt lgkmcnt(0)" ::: "memory");
          dma(h + WWIN, 64 * F * 2, F * 2, rb + ((h + WWIN) % NSLOT) * 2048);
        }
#pragma unroll
        for (int ct = 0; ct < 4; ++ct) {
          const u2v t = tr_read(smem + O_RING + hw * (NSLOT * 2048) + (h % NSLOT) * 2048 + tro[ct]);
          wv[4 * (h >> 1) + ct][2 * (h & 1)] = t[0]; wv[4 * (h >> 1) + ct][2 * (h & 1) + 1] = t[1];
        }
        if (h == H2_AT) ARRIVE(C_WGU);
        if (h == HX - 1) {
          STAMP(4);
          WAITQ(C_XS, 1);
          STAMP(1);
#pragma unroll
          for (int k = 0; k < 8; ++k) xb[k] = *(const __attribute__((address_space(3))) u4v*)(uintptr_t)(xo + k * 64);
          t10 = par[0]; tpub = par[2];
          SB();
          f1(0, 2);
          SB();
        }
        if (h >= HX) { f1(h - HX + 2, h - HX + 3); SB(); }
      }
      STAMP(5);
      f1(18 - HX, 8);
      t10 &= TMASK;
#pragma unroll
      for (int a = 0; a < 2; ++a) {
        u4v v;
#pragma unroll
        for (int e = 0; e < 4; ++e)
          v[e] = ((u32)__builtin_amdgcn_update_dpp(__float_as_int(a1[2 * a][e]), __float_as_int(a1[2 * a + 1][e]), 0xe4, 0xf, 0xc, false) & ~TMASK) | t10;
        __builtin_amdgcn_raw_buffer_store_b128(v, wrs, p1o + a * 1024, 0, 16);
      }
      STAMP(6);
      const u32 tn = now32();
      u32 voff = WS_P1 + s1 * 16384 + hw * 1024 + lane * 16;
      u32 ho = lbase + O_HB + hw * 528 + g * 128 + tok * 16 + (lane & 8);
      float nl2e = -1.44269504088896341f;
      u32 wda = CTLA(C_WD) + hw * 4;
      OPQ(voff); OPQ(ho); OPQ(nl2e); OPQ(wda);
      f2_setup(hw & 1);
      pace2(tn, (u32)__builtin_amdgcn_readfirstlane((int)tpub) + DE_P1, DT_P1);
      u4v pb[8];
      auto p1_ld = [&]() AI {
#pragma unroll
        for (int i = 0; i < 8; ++i) { pb[i] = __builtin_amdgcn_raw_buffer_load_b128(wrs, voff + (i >> 2) * 2048, (i & 3) * 4096, 16); SB(); }
      };
      p1_ld();
      SB();
      f4 G, U;
      bool early = false;
#pragma unroll 1
      for (int spin = 0; spin < SPIN_MAX; ++spin) {
        u32 bad = 0;
        OPQ(bad);
        asm volatile("" :: "v"(pb[0]));
        const u32 cw = __hip_atomic_load((__attribute__((address_space(3))) u32*)(uintptr_t)wda, __ATOMIC_RELAXED, __HIP_MEMORY_SCOPE_WORKGROUP);
        G = tsum4(bad, t10, pb[0], pb[1], pb[2], pb[3]);
        early = __builtin_amdgcn_ballot_w64(cw == 0u) == 0ull;
        asm volatile("" ::: "memory");
        if (__builtin_expect(early, 1)) wd_ld(0, hw);
        SB();
        silu4(G, nl2e);
        U = tsum4(bad, t10, pb[4], pb[5], pb[6], pb[7]);
        if (__builtin_expect(!__any((bad & TMASK) != 0), 1)) break;
        STAMP(14);
        p1_ld();
        asm volatile("" ::: "memory");
      }
      STAMP(7);
      {
        float hv[4];
#pragma unroll
        for (int e = 0; e < 4; ++e) { hv[e] = G[e] * U[e]; asm("" : "+v"(hv[e])); }
        *(__attribute__((address_space(3))) u2v*)(uintptr_t)ho = u2v{pk_bf(hv[0], hv[1]), pk_bf(hv[2], hv[3])};
      }
      ARRIVE(C_HRDY);
      STAMP(8);
      if (__builtin_expect(!early, 0)) { WAITA(wda); wd_ld(0, hw); }
      f2_job(1, t10);
    }
    if (hw == 0) {
      WAITS(C_SEL, 1);
      const u32 t10 = par[0] & TMASK;
      const u32 tpub = (u32)__builtin_amdgcn_readfirstlane((int)par[2]);
      const u32 mykey = ((const u32*)(smem + O_SEL))[lane & 7];
      if (own) pace2(now32(), tpub + DE_P2, DT_P2); else pace(tpub + DE_P2 - 40);
      u4v pa[7];
      const int voff = WS_P2 + cc * 7168 + lane * 16;
#pragma unroll
      for (int k = 0; k < 7; ++k) pa[k] = __builtin_amdgcn_raw_buffer_load_b128(wrs, voff + (k & 3) * 1024, (k >> 2) * 4096, 16);
      SB();
      const int row = (int)(~mykey & 2047u);
      const float rsig = rb1(sigm(__uint_as_float((mykey ^ (~(u32)((int)mykey >> 31) | 0x80000000u)) & 0xffff0000u)));
      WAITA(CTLA(C_TILE) + (lane & 31) * 4);
      float cf_ = rsig * (1.f - *(const float*)(smem + O_GT + (row >> 8) * 1024 + ((row >> 2) & 3) * 256 + ((row >> 4) & 15) * 16 + (row & 3) * 4));
      float base[4];
#pragma unroll
      for (int e = 0; e < 4; ++e) base[e] = bflo(*(const u16*)(smem + e * CHB + O_OUT + (row >> 8) * OT + ((row >> 2) & 3) * 144 + ((row >> 4) & 15) * 8 + (row & 3) * 2));
      u64 op = kc[1] + (row * D + cc * 4) * 2;
      { float cq = cf_; OPQ(cq); cf_ = cq; OPQ(op); }
#pragma unroll
      for (int e = 0; e < 4; ++e) OPQ(base[e]);
      STAMP(13);
      f4 y;
#pragma unroll 1
      for (int spin = 0; spin < SPIN_MAX; ++spin) {
        u32 bad = 0;
        OPQ(bad);
        y = tsum4(bad, t10, pa[0], pa[1], pa[2], pa[3]);
        y = tsum3(bad, t10, y, pa[4], pa[5], pa[6]);
        if (__builtin_expect(!__any((bad & TMASK) != 0), 1)) break;
        STAMP(15);
#pragma unroll
        for (int k = 0; k < 7; ++k) pa[k] = __builtin_amdgcn_raw_buffer_load_b128(wrs, voff + (k & 3) * 1024, (k >> 2) * 4096, 16);
        asm volatile("" ::: "memory");
      }
      STAMP(9);
      float o[4];
#pragma unroll
      for (int e = 0; e < 4; ++e) {
        float v = y[e] + dpp_f<0x128>(y[e]);
        auto s16 = __builtin_amdgcn_permlane16_swap(__float_as_uint(v), __float_as_uint(v), false, false);
        v = __uint_as_float(s16[0]) + __uint_as_float(s16[1]);
        auto s32 = __builtin_amdgcn_permlane32_swap(__float_as_uint(v), __float_as_uint(v), false, false);
        o[e] = fmaf(__uint_as_float(s32[0]) + __uint_as_float(s32[1]), cf_, base[e]);
      }
      if (lane < 8) *(__attribute__((address_space(1))) u2v*)op = u2v{pk_bf(o[0], o[1]), pk_bf(o[2], o[3])};
    }
    ROLE_END();
  }
  {
    ROLE("g");
    const u32 pLA = __builtin_nontemporal_load((gp32)ka[3] + d * (NM / 2) + lane);
    const u32 pAI = __builtin_nontemporal_load((gp32)ka[4] + d * (NM / 2) + lane);
    const u32 pCR = __builtin_nontemporal_load((gp32)ka[5] + d * (NM / 2) + lane);
    const u32 pCI = __builtin_nontemporal_load((gp32)ka[6] + d * (NM / 2) + lane);
    const u32 ldt = ((gp16)ka[7])[d], dskw = ((gp16)kb[0])[d];
    u4v ixa[2];
#pragma unroll
    for (int r = 0; r < 2; ++r) ixa[r] = *(const u4v*)(g_tapidx.v + lane * 16 + r * 8);
    SB();
    asm volatile("s_mov_b32 m0, %0\n\ts_nop 0" :: "s"(lbase + c * CHB) : "memory");
#define PUT1(off, val) asm volatile("ds_write_addtid_b32 %0 offset:%1" :: "v"(val), "n"(off) : "memory")
#define PUTR(off, val) do { const ch v_ = (val); asm volatile("ds_write_addtid_b32 %0 offset:%2\n\tds_write_addtid_b32 %1 offset:%3" :: "v"(v_.r), "v"(v_.i), "n"(off), "n"((off) + 256) : "memory"); } while (0)
    const char* pf = ch_ + c16 * TROW + g * 16;
    PUT1(O_TAP, 0x3c003c00u); PUT1(O_TAP + 256, 0u);
    PUT1(O_U, 0u); PUT1(O_U + 256, 0u);
    SB();
    const float dt = rb1(exp_bf(bflo(ldt)));
    const f2 a2 = f2{-rb1(exp_bf(bflo(pLA))), -rb1(exp_bf(bfhi(pLA)))} * dt * 1.44269504088896341f;
    const f2 br = unbf(pAI) * dt * 0.15915494309189535f;
    STAMP(1);
    u4v Lf[2][8], Wf[8];
    f4 kk[2][2];
    float fk;
    {
      const cf z1 = cexp_p(a2, br, 1.f);
      const cf z2 = csq(z1), z4 = csq(z2), z8 = csq(z4), z16 = csq(z8), z32 = csq(z16), z64 = csq(z32);
      ch base[4], anc[4];
      base[1] = tohc(z8); base[2] = tohc(z16); base[3] = tohc(cmul(z16, z8));
      anc[1] = tohc(z32); anc[2] = tohc(z64); anc[3] = tohc(cmul(z64, z32));
      PUTR(O_OUT + 5 * TROW, tohc(csq(z64)));
#pragma unroll
      for (int j = 1; j < 16; ++j) {
        const int q = j >> 2, r = j & 3;
        PUTR(O_TAP + j * TROW, q == 0 ? base[r] : r == 0 ? anc[q] : hmul(anc[q], base[r]));
      }
#pragma unroll
      for (int s = 0; s < 8; ++s) Lf[0][s] = *(const u4v*)(pf + O_TAP + s * 64);
#pragma unroll
      for (int s = 0; s < 8; ++s) Lf[1][s] = *(const u4v*)(ch_ + O_OUT + 5 * TROW + g * 16 + s * 64);
      SB();
      STAMP(11);
      u32 mx = max(max(pCR << 17, pCR << 1), max(pCI << 17, pCI << 1));
      mx = wave_max_u32(mx);
      int ex = (int)(mx >> 24);
      ex = ex < 1 ? 1 : (ex > 253 ? 253 : ex);
      const float sdn = __uint_as_float((u32)(254 - ex) << 23);
      fk = 2.f * dt * __uint_as_float((u32)ex << 23);
      cf C; C.r = unbf(pCR) * sdn; C.i = unbf(pCI) * sdn;
      const cf E = cexp_p(a2, br, 256.f);
      const ch y1 = toh(z1), y2 = toh(z2), y4 = toh(z4), y6 = toh(cmul(z4, z2));
      {
        const ch R0 = toh(C), R2 = hmul(R0, y2), R4 = hmul(R0, y4), R6 = hmul(R0, y6);
        PUTR(O_TAP, R0); PUTR(O_TAP + 2 * TROW, R2); PUTR(O_TAP + 4 * TROW, R4); PUTR(O_TAP + 6 * TROW, R6);
        PUTR(O_TAP + 1 * TROW, hmul(R0, y1)); PUTR(O_TAP + 3 * TROW, hmul(R2, y1)); PUTR(O_TAP + 5 * TROW, hmul(R4, y1)); PUTR(O_TAP + 7 * TROW, hmul(R6, y1));
        C = cmul(C, E);
        const ch R8 = toh(C);
        PUTR(O_TAP + 8 * TROW, R8); PUTR(O_TAP + 9 * TROW, hmul(R8, y2)); PUTR(O_TAP + 10 * TROW, hmul(R8, y4)); PUTR(O_TAP + 11 * TROW, hmul(R8, y6));
        C = cmul(C, E);
        const ch R12 = toh(C);
        PUTR(O_TAP + 12 * TROW, R12); PUTR(O_TAP + 13 * TROW, hmul(R12, y4));
        C = cmul(C, E);
        const ch R14 = toh(C);
        PUTR(O_TAP + 14 * TROW, R14); PUTR(O_TAP + 15 * TROW, hmul(R14, y4));
      }
#pragma unroll
      for (int s = 0; s < 8; ++s) Wf[s] = *(const u4v*)(pf + O_TAP + s * 64);
      SB();
      STAMP(12);
#pragma unroll
      for (int i = 0; i < 5; ++i) { C = cmul(C, E); PUTR(O_OUT + i * TROW, toh(C)); }
      SB();
      auto mh = [&](u4v a, u4v bq, f4 cin) AI { return __builtin_amdgcn_mfma_f32_16x16x32_f16(as_h8(a), as_h8(bq), cin, 0, 0, 0); };
      kk[0][0] = f4{0.f, 0.f, 0.f, 0.f}; kk[0][1] = kk[0][0]; kk[1][0] = kk[0][0]; kk[1][1] = kk[0][0];
#pragma unroll
      for (int s = 0; s < 5; ++s) {
#pragma unroll
        for (int k = 0; k < 4; ++k) {
          if (k < 2) { if (s < 4) kk[0][0] = mh(Wf[s + 4 * k], Lf[0][s + 4 * k], kk[0][0]); }
          else if (s > 0) { const int u = s - 1 + 4 * (k - 2); kk[0][1] = mh(Wf[u], Lf[1][u], kk[0][1]); Wf[u] = *(const u4v*)(pf + O_OUT + u * 64); }
          SB();
          if (s < 4) {
            const ch o = hmul(ch{ash(Lf[0][s][k]), ash(Lf[0][s + 4][k])}, ch{ash(Lf[1][s][k]), ash(Lf[1][s + 4][k])});
            Lf[1][s][k] = __builtin_bit_cast(u32, o.r); Lf[1][s + 4][k] = __builtin_bit_cast(u32, o.i);
          }
          if (s == 3 && k == 3) asm volatile("s_nop 1");
          SB();
        }
      }
#pragma unroll
      for (int s = 0; s < 8; ++s) { kk[1][0] = mh(Wf[s], Lf[0][s], kk[1][0]); kk[1][1] = mh(Wf[s], Lf[1][s], kk[1][1]); }
    }
    {
      u32 ia[16];
      const u32 kb_ = lbase + c * CHB + O_OUT;
#pragma unroll
      for (int e = 0; e < 16; ++e) { const u32 iw = ixa[e >> 3][(e >> 1) & 3]; ia[e] = kb_ + ((e & 1) ? (iw >> 16) : (iw & 0xffffu)); }
      MIX2(16, 0, 1)
      SB();
      STAMP(2);
      char* ktp = ch_ + O_OUT + c16 * KT_PITCH + g * 8;
#pragma unroll
      for (int hf = 1; hf >= 0; --hf) {
#pragma unroll
        for (int ct = 0; ct < 2; ++ct) {
          const f4 v = kk[hf][ct] * fk;
          *(u32*)(ktp + ct * 16 * KT_PITCH + hf * 32) = pk_bf(v[0], v[1]);
          *(u32*)(ktp + ct * 16 * KT_PITCH + hf * 32 + 4) = pk_bf(v[2], v[3]);
        }
        asm volatile("" ::: "memory");
      }
      u32 zz = 0u; OPQ(zz);
      *(u32*)(ch_ + O_OUT + KT_ZERO + (lane & 7) * 4) = zz;
      asm volatile("" ::: "memory");
      u32 v[16], pe[9];
#pragma unroll
      for (int e = 15; e >= 0; --e) v[e] = *(const __attribute__((address_space(3))) u16*)(uintptr_t)ia[e];
      SB();
      pe[8] = zz;
#pragma unroll
      for (int i = 7; i >= 0; --i) {
        pe[i] = v[2 * i] | (v[2 * i + 1] << 16);
        const u32 nx = (u32)__builtin_amdgcn_update_dpp((int)dpp_u<0x134>(pe[i + 1]), (int)pe[i], 0x130, 0xf, 0xf, false);
        const u32 po = __builtin_amdgcn_alignbyte(nx, pe[i], 2);
        PUT1(O_TAP + i * 256, pe[i]); PUT1(O_TAP + 2 * TAP_PITCH - 4 + i * 256, pe[i]);
        PUT1(O_TAP + TAP_PITCH + i * 256, po); PUT1(O_TAP + 3 * TAP_PITCH - 4 + i * 256, po);
      }
      ARRIVE(C_TAPS + c);
    }
    STAMP(3);
    IDS();
    conv_setup();
    dm1 = bflo(dskw) - 1.f;
    atload(0, 17);
    m_setup();
    __builtin_amdgcn_s_setprio(1);
    SB();
    WAITA(qa);
    STAMP(4);
    SB();
    FL(0, -7); FL(1, -6); FL(2, -5); FL(3, -4); FL(4, -3); FL(5, -2); FL(6, -1); FL(7, 0); ULD(0); GLD(0);
    MF0(acc[0], At[9], Fq[0]); EUN(0, 0); EUN(0, 1);
    MF(acc[0], At[10], Fq[1]); EUN(0, 2); EUN(0, 3);
    MF(acc[0], At[11], Fq[2]);
    MF(acc[0], At[12], Fq[3]);
    MF(acc[0], At[13], Fq[4]);
    MF(acc[0], At[14], Fq[5]);
    MF(acc[0], At[15], Fq[6]); FL(8, 1);
    MF(acc[0], At[16], Fq[7]);
    MF0(acc[1], At[1], Fq[0]); FL(9, 2);
    MF(acc[1], At[2], Fq[1]);
    MF(acc[1], At[3], Fq[2]); FL(10, 3);
    MF(acc[1], At[4], Fq[3]);
    MF(acc[1], At[5], Fq[4]); FL(11, 4); EA1(0, 0);
    MF(acc[1], At[6], Fq[5]); EA1(0, 1); EA1(0, 2);
    MF(acc[1], At[7], Fq[6]); EA1(0, 3); PEEK(0, qa + 16);
    MF(acc[1], At[8], Fq[7]); FL(12, 5); EA2(0, 0);
    MF0(acc[2], At[0], Fq[7]); EA2(0, 1); EA2(0, 2);
    MF(acc[1], At[9], Fq[8]); EA2(0, 3); EPK(0, 0);
    MF(acc[2], At[1], Fq[8]); EPK(0, 1); FL(13, 6);
    MF(acc[1], At[10], Fq[9]); OST(0); FLAG(C_TILE + 0 * 4);
    MF(acc[2], At[2], Fq[9]); FL(14, 7); FL(15, 8);
    MF(acc[1], At[11], Fq[10]); CHECK(0, qa + 16);
    MF(acc[2], At[3], Fq[10]);
    MF(acc[1], At[12], Fq[11]); FL(16, 9);
    MF(acc[2], At[4], Fq[11]); ULD(1); GLD(1);
    MF(acc[1], At[13], Fq[12]); FL(17, 10);
    MF(acc[2], At[5], Fq[12]);
    MF(acc[1], At[14], Fq[13]); FL(18, 11);
    MF(acc[2], At[6], Fq[13]);
    MF(acc[1], At[15], Fq[14]); FL(19, 12); EUN(1, 0);
    MF(acc[2], At[7], Fq[14]); EUN(1, 1); EUN(1, 2);
    MF(acc[1], At[16], Fq[15]); FL(20, 13); EUN(1, 3);
    MF(acc[2], At[8], Fq[15]);
    MF0(acc[3], At[0], Fq[15]); FL(21, 14);
    MF(acc[2], At[9], Fq[16]);
    MF(acc[3], At[1], Fq[16]); FL(22, 15); EA1(1, 0);
    MF(acc[2], At[10], Fq[17]); EA1(1, 1); EA1(1, 2);
    MF(acc[3], At[2], Fq[17]); FL(23, 16); EA1(1, 3);
    MF(acc[2], At[11], Fq[18]); PEEK(2, fa + 0); EA2(1, 0);
    MF(acc[3], At[3], Fq[18]); EA2(1, 1); EA2(1, 2);
    MF(acc[2], At[12], Fq[19]); EA2(1, 3); EPK(1, 0);
    MF(acc[3], At[4], Fq[19]); EPK(1, 1); FL(24, 17);
    MF(acc[2], At[13], Fq[20]); OST(1); FLAG(C_TILE + 1 * 4);
    MF(acc[3], At[5], Fq[20]); FL(25, 18); CHECK(2, fa + 0);
    MF(acc[2], At[14], Fq[21]); FL(26, 19); ULD(2);
    MF(acc[3], At[6], Fq[21]); FLD(2); GLD(2);
    MF(acc[2], At[15], Fq[22]); FL(27, 20); PEEK(1, qa + 32);
    MF(acc[3], At[7], Fq[22]);
    MF(acc[2], At[16], Fq[23]); FL(28, 21);
    MF(acc[3], At[8], Fq[23]); EUN(2, 0); EUN(2, 1);
    MF0(acc[4], At[0], Fq[23]); FL(29, 22); EUN(2, 2);
    MF(acc[3], At[9], Fq[24]); EUN(2, 3); EA1(2, 0);
    MF(acc[4], At[1], Fq[24]); FL(30, 23); EA1(2, 1);
    MF(acc[3], At[10], Fq[25]); EA1(2, 2); EA1(2, 3);
    MF(acc[4], At[2], Fq[25]); FL(31, 24); EFA(2, 0);
    MF(acc[3], At[11], Fq[26]); EFA(2, 1); EFA(2, 2);
    MF(acc[4], At[3], Fq[26]); EFA(2, 3); PEEK(3, fa + 16);
    MF(acc[3], At[12], Fq[27]); EA2(2, 0); EA2(2, 1);
    MF(acc[4], At[4], Fq[27]); EA2(2, 2); EA2(2, 3);
    MF(acc[3], At[13], Fq[28]); CHECK(1, qa + 32); EPK(2, 0); EPK(2, 1);
    MF(acc[4], At[5], Fq[28]); FL(32, 25); OST(2);
    MF(acc[3], At[14], Fq[29]); FLAG(C_TILE + 2 * 4); FL(33, 26);
    MF(acc[4], At[6], Fq[29]); FL(34, 27); CHECK(3, fa + 16);
    MF(acc[3], At[15], Fq[30]); ULD(3); FL(35, 28);
    MF(acc[4], At[7], Fq[30]); FLD(3); GLD(3);
    MF(acc[3], At[16], Fq[31]); FL(36, 29);
    MF(acc[4], At[8], Fq[31]);
    MF0(acc[5], At[0], Fq[31]); FL(37, 30);
    MF(acc[4], At[9], Fq[32]); EUN(3, 0); EUN(3, 1);
    MF(acc[5], At[1], Fq[32]); PEEK(4, fa + 32); FL(38, 31);
    MF(acc[4], At[10], Fq[33]); EUN(3, 2); EUN(3, 3);
    MF(acc[5], At[2], Fq[33]); FL(39, 32); EA1(3, 0);
    MF(acc[4], At[11], Fq[34]); EA1(3, 1); EA1(3, 2);
    MF(acc[5], At[3], Fq[34]); EA1(3, 3); EFA(3, 0);
    MF(acc[4], At[12], Fq[35]); EFA(3, 1); EFA(3, 2); EFA(3, 3);
    MF(acc[5], At[4], Fq[35]); EA2(3, 0); EA2(3, 1); EA2(3, 2); EA2(3, 3);
    MF(acc[4], At[13], Fq[36]); FL(40, 33); EPK(3, 0); EPK(3, 1);
    MF(acc[5], At[5], Fq[36]); FL(41, 34); OST(3);
    MF(acc[4], At[14], Fq[37]); FL(42, 35); FLAG(C_TILE + 3 * 4); CHECK(4, fa + 32); ULD(4);
    MF(acc[5], At[6], Fq[37]); FL(43, 36); FLD(4); GLD(4); PEEK(5, fa + 48);
    MF(acc[4], At[15], Fq[38]); FL(44, 37); FL(45, 38);
    MF(acc[5], At[7], Fq[38]); FL(46, 39); FL(47, 40);
    MF(acc[4], At[16], Fq[39]);
    MF(acc[5], At[8], Fq[39]); EUN(4, 0); EUN(4, 1);
    MF(acc[5], At[9], Fq[40]); EUN(4, 2); EUN(4, 3);
    MF(acc[5], At[10], Fq[41]); EFA(4, 0); EA1(4, 0);
    MF(acc[5], At[11], Fq[42]); EA1(4, 1); EA1(4, 2);
    MF(acc[5], At[12], Fq[43]); EA1(4, 3); EFA(4, 1); EFA(4, 2); EFA(4, 3); CHECK(5, fa + 48); ULD(5);
    MF(acc[5], At[13], Fq[44]); EA2(4, 0); EA2(4, 1); EA2(4, 2); EA2(4, 3); FLD(5); GLD(5);
    MF(acc[5], At[14], Fq[45]); EPK(4, 0); EPK(4, 1);
    MF(acc[5], At[15], Fq[46]); OST(4); FLAG(C_TILE + 4 * 4);
    MF(acc[5], At[16], Fq[47]); EUN(5, 0); EUN(5, 1); EUN(5, 2); EUN(5, 3); EFA(5, 0); EFA(5, 1); EFA(5, 2); EFA(5, 3); EA1(5, 0); EA1(5, 1); EA1(5, 2); EA1(5, 3); EA2(5, 0); EA2(5, 1); EA2(5, 2); EA2(5, 3); EPK(5, 0); EPK(5, 1); OST(5); FLAG(C_TILE + 5 * 4);
    STAMP(8);
    ROLE_END();
  }
  {
    ROLE("c");
    __builtin_amdgcn_s_setprio(3);
    {
      u32 zz = 0u; OPQ(zz);
      if (lane < 2) *(u4v*)(smem + O_HB + 512 + lane * 528) = u4v{zz, zz, zz, zz};
    }
    u32* selw = (u32*)(smem + O_SEL);
    u32 tag;
    {
      const u32 pa = lbase + O_PART + (lane & 7) * 64;
      const u32 ib = 2047u - (u32)(cg * 8 + (lane & 7));
      f4 p0, p1, p2, p3; u32 cnt, ep; int n_ = 1 << 20; unsigned long long tk;
      asm volatile("1:\n\tds_read_b32 %4, %8\n\tds_read_b128 %0, %9\n\tds_read_b128 %1, %9 offset:16\n\tds_read_b128 %2, %9 offset:32\n\tds_read_b128 %3, %9 offset:48\n\t"
                   "ds_read_b32 %7, %10\n\ts_memrealtime %6\n\ts_sub_u32 %5, %5, 1\n\ts_cbranch_scc1 2f\n\ts_waitcnt lgkmcnt(0)\n\tv_cmp_gt_u32 vcc, 4, %4\n\ts_cbranch_vccnz 1b\n2:\n\ts_waitcnt lgkmcnt(0)"
                   : "=&v"(p0), "=&v"(p1), "=&v"(p2), "=&v"(p3), "=&v"(cnt), "+s"(n_), "=&s"(tk), "=&v"(ep) : "v"(CTLA(C_PART)), "v"(pa), "v"(lbase + O_XS) : "vcc", "scc", "memory");
      tag = (u32)__builtin_amdgcn_readfirstlane((int)ep) + 1u;
      tag += tag == 0u;
      const f4 s4_ = (p0 + p1) + (p2 + p3);
      const u32 rp = pk_bf(s4_[1] + s4_[3], s4_[0] + s4_[2]);
      const u32 key = ((rp ^ ((u32)((int)rp >> 31) | 0x80000000u)) & 0xffff0000u) | ib;
      const u32 kn = dpp_u<0xb1>(key);
      u32 m1 = max(key, kn), m2 = min(key, kn), m3;
      { const u32 o1 = dpp_u<0x4e>(m1), o2 = dpp_u<0x4e>(m2); const u32 lo = min(m1, o1), hb = max(m2, o2); m1 = max(m1, o1); m2 = max(lo, hb); m3 = min(lo, hb); }
      { const u32 o1 = dpp_u<0x141>(m1), o2 = dpp_u<0x141>(m2), o3 = dpp_u<0x141>(m3); const u32 lo = min(m1, o1), hb = max(m2, o2);
        m1 = max(m1, o1); m2 = max(lo, hb); m3 = max(min(lo, hb), max(m3, o3)); }
      const u32 wm = (((u32)tk + 8u) >> 2) & 0xffu;
      if (lane == 0) __builtin_amdgcn_raw_buffer_store_b128(u4v{m1, m2, (m3 & 0xffff0000u) | wm, tag}, wrs, WS_KEY + cg * 16, 0, 16);
      SB();
      STAMP(1);
      if (lane == 0) par[0] = tag;
      ARRIVE(C_GP);
      {
        const float gv = sigm(bflo(rp));
        const u32 g2 = pk_bf(gv, dpp_f<0xb1>(gv));
        u32 g3 = dpp_u<0x4e>(g2);
        OPQ(g3);
        if ((lane & 59) == 0) __builtin_amdgcn_raw_buffer_store_b128(u4v{g2, g3, tag, tag}, wrs, WS_REC + cg * 32 + lane * 4, 0, 16);
      }
      if ((lane & 57) == 0) __builtin_amdgcn_raw_buffer_store_b128(u4v{key, kn, 0u, tag}, wrs, WS_KALL + cg * 64 + lane * 8, 0, 16);
    }
    u32 l7 = (lane & 7) * 16, ko = WS_KEY + lane * 16;
    OPQ(l7); OPQ(ko);
    WAITQ(C_TL, 1);
    STAMP(5);
    const u32 tkey = (u32)__builtin_amdgcn_readfirstlane((int)par[1]) + DT_KEY;
    pace(tkey);
    u4v gr[4];
    u32 mykey;
    u4v xs[4];
    auto xrs = __builtin_amdgcn_make_buffer_rsrc((void*)(ka[0] + (xi & 3) * 512), 0, L * D * 2 - 1536, 0x00020000);
    asm volatile("" : "+s"(xrs));
    auto xs_ld = [&]() AI {
      const u32 vo = ((~mykey & 2047u) << 11) | l7;
#pragma unroll
      for (int k = 0; k < 4; ++k) xs[k] = __builtin_amdgcn_raw_buffer_load_b128(xrs, vo + k * 128, 0, 0);
    };
#pragma unroll
    for (int i = 0; i < 4; ++i) gr[i] = __builtin_amdgcn_raw_buffer_load_b128(wrs, ko + i * 1024, 0, 16);
#pragma unroll 1
    for (int spin = 0; spin < SPIN_MAX; ++spin) {
      const u32 bad = (gr[0][3] ^ tag) | (gr[1][3] ^ tag) | (gr[2][3] ^ tag) | (gr[3][3] ^ tag);
      if (__builtin_expect(!__any(bad != 0), 1)) break;
      STAMP(13);
      __builtin_amdgcn_s_sleep(SLEEP_AG1);
#pragma unroll
      for (int i = 0; i < 4; ++i)
        if (gr[i][3] != tag) gr[i] = __builtin_amdgcn_raw_buffer_load_b128(wrs, ko + i * 1024, 0, 16);
      asm volatile("" ::: "memory");
    }
    STAMP(2);
    {
      u32 m1 = gr[0][0], m2 = gr[0][1], m3 = 0, m4 = 0;
#pragma unroll
      for (int i = 2; i < 8; ++i) {
        const u32 k = gr[i >> 1][i & 1];
        if (i > 2) m4 = med3u(m3, m4, k);
        m3 = med3u(m2, m3, k);
        m2 = (i & 1) ? max(m2, k) : med3u(m1, m2, k);
        if (!(i & 1)) m1 = max(m1, k);
      }
      const u32 third = max(max(gr[0][2], gr[1][2]), max(gr[2][2], gr[3][2]));
      u32 W2;
      mykey = wave_top8(m1, m2, m3, m4, W2);
      xs_ld();
      SB();
      if (__builtin_expect(__any(m1 == 0u || (third | 0xffffu) > W2), 0)) {
        u4v ak[16];
#pragma unroll 1
        for (int spin = 0; spin < SPIN_MAX; ++spin) {
          u32 bad = 0;
#pragma unroll
          for (int i = 0; i < 16; ++i) {
            ak[i] = __builtin_amdgcn_raw_buffer_load_b128(wrs, WS_KALL + (i & 3) * 1024 + lane * 16, (i >> 2) * 4096, 16);
            bad |= ak[i][3] ^ tag;
          }
          if (!__any(bad != 0)) break;
          __builtin_amdgcn_s_sleep(8);
          asm volatile("" ::: "memory");
        }
        u32 prev = 0xffffffffu;
#pragma unroll 1
        for (int it = 0; it < 8; ++it) {
          u32 mm_ = 0;
#pragma unroll
          for (int i = 0; i < 32; ++i) {
            const u32 k = ak[i >> 1][i & 1];
            mm_ = max(mm_, k < prev ? k : 0u);
          }
          OPQ(mm_);
          prev = wave_max_u32(mm_);
          mykey = (lane >> 3) == it ? prev : mykey;
        }
        xs_ld();
      }
      OPQ(mykey);
    }
    SB();
    STAMP(3);
    if (l7 == 0) selw[lane >> 3] = mykey;
    {
      const u32 tn = now32();
      u32 yg = 0;
#pragma unroll
      for (int i = 0; i < 4; ++i) yg = max(yg, (gr[i][2] - (tn >> 2) - 1u) & 0xffu);
      const u32 tpub = tn - 4u * (255u - wave_max_u32(yg)) + DT_VIS;
      if (lane == 0) par[2] = later(tkey, tpub);
    }
    ARRIVE(C_SEL);
    if (b == 255 && lane == 0) __builtin_amdgcn_raw_buffer_store_b32(tag, wrs, WS_EPOCH, 0, 16);
    SB();
    {
      char* xp = smem + O_XS + (lane >> 3) * XS_PITCH + l7;
#pragma unroll
      for (int k = 0; k < 4; ++k) *(u4v*)(xp + k * 128) = xs[k];
      ARRIVE(C_XS);
      STAMP(6);
    }
    __builtin_amdgcn_s_setprio(2);
    auto ors = __builtin_amdgcn_make_buffer_rsrc((void*)kc[1], 0, L * D * 2, 0x00020000);
    const u32 rl = (lane + (xi & 15) * 4) & 63, un = (g * 4 + (lane & 3) + xi) & 15;
    u32 skip = 0;
#pragma unroll
    for (int k = 0; k < 8; ++k) {
      const u32 r = ~(u32)__builtin_amdgcn_readlane((int)mykey, 8 * k) & 2047u;
      skip |= (r & 63u) == rl ? 1u << (r >> 6) : 0u;
    }
    const char* op = smem + ((lane >> 2) & 3) * CHB + O_OUT + (un & 3) * 144 + (un >> 2) * 8;
    const u32 ob = rl * 2048 + cc * 8;
    const u32 tfast = (u32)__builtin_amdgcn_readfirstlane((int)par[2]) + DE_FAST;
#pragma unroll 1
    for (int t = 0; t < 8; ++t) {
      WAITA(CTLA(C_TILE) + t * 16 + (lane & 3) * 4);
      u2v yv[4];
#pragma unroll
      for (int e = 0; e < 4; ++e) yv[e] = tr_read(op + t * OT + e * 32);
      const bool slow = (int)(now32() - tfast) < 0;
#pragma unroll
      for (int e = 0; e < 4; ++e) {
        __builtin_amdgcn_raw_buffer_store_b64(yv[e], ors, ob | (((skip >> e) & 1u) << 30), t * 524288 + e * 131072, 0);
        if (slow) {
          SB(); __builtin_amdgcn_s_sleep(STSLP); SB();
          asm volatile("s_waitcnt vmcnt(%0)" :: "n"(STWIN - 1) : "memory");
        }
      }
      skip >>= 4;
    }
    STAMP(9);
    ROLE_END();
  }
  {
    ROLE("d");
        const int j = xg * 28 + xi;
    const int s1 = j >> 2, kq = j & 3;
    const bool own = xi < 28;
    const u64 wbase = kb[3];
    const int pitch = D * 2;
    auto hrs = __builtin_amdgcn_make_buffer_rsrc((void*)wbase, 0, own ? D * F * 2 : 0, 0x00020000);
    const int r8 = lane >> 3;
    const int vo = (64 * s1 * D + 256 * kq) * 2 + (8 * (r8 >> 2) + (r8 & 3)) * pitch + (((lane & 7) * 16) ^ (((r8 >> 1) & 3) * 32));
    auto dma = [&](int h, int sB, int pt, u32 dst) AI {
      const int m = h >> 1;
#pragma unroll
      for (int i = 0; i < 2; ++i)
        asm volatile("s_mov_b32 m0, %3\n\ts_nop 0\n\tbuffer_load_dwordx4 %0, %1, %2 offen nt lds"
                     :: "v"(vo), "s"(hrs), "s"((m & 1) * 32 * pt + (m >> 1) * sB + (16 * i + 4 * (h & 1)) * pt), "s"(dst + i * 1024) : "memory");
    };
    const int q = c16 >> 2, p = c16 & 3;
    int tro[4];
#pragma unroll
    for (int ct = 0; ct < 4; ++ct) tro[ct] = (4 * g + q) * 128 + ((ct ^ (((4 * g + q) >> 1) & 3)) * 32) + p * 8;
    const int tok = lane & 7;
    u4v wd[2][8];
    auto wd_ld = [&](int k, int cb) AI {
#pragma unroll
      for (int i = 0; i < 8; ++i) {
        const char* pp = smem + O_WD + cb * 8192 + (i >> 2) * 4096 + tro[i & 3];
        const u2v lo = tr_read(pp), hi = tr_read(pp + 2048);
        wd[k][i] = u4v{lo[0], lo[1], hi[0], hi[1]};
      }
    };
    u32 p2o, p2k[4], hlo, hhi;
    auto f2_setup = [&](int cb0) AI {
      p2o = WS_P2 + ((64 * kq + 16 * cb0 + (lane & 8) / 2 + g) * 56 + s1) * 128 + tok * 16;
      hlo = lbase + O_HB + ((lane & 8) ? 512 : g * 128 + tok * 16);
      hhi = lbase + O_HB + ((lane & 8) ? g * 128 + tok * 16 : 512);
#pragma unroll
      for (int i = 0; i < (cb0 == 2 ? 4 : 2); ++i) { p2k[i] = p2o + i * 8 * 7168; OPQ(p2k[i]); }
      OPQ(hlo); OPQ(hhi);
    };
    u4v hb[4];
    f4 r[4];
    auto hb_ld = [&]() AI {
      u32 cnt; int n_ = 1 << 20;
      asm volatile("1:\n\tds_read_b32 %4, %6\n\tds_read_b128 %0, %7\n\tds_read_b128 %1, %8\n\tds_read_b128 %2, %7 offset:528\n\tds_read_b128 %3, %8 offset:528\n\t"
                   "s_sub_u32 %5, %5, 1\n\ts_cbranch_scc1 2f\n\ts_waitcnt lgkmcnt(0)\n\tv_cmp_gt_u32 vcc, 2, %4\n\ts_cbranch_vccnz 1b\n2:\n\ts_waitcnt lgkmcnt(0)"
                   : "=&v"(hb[0]), "=&v"(hb[1]), "=&v"(hb[2]), "=&v"(hb[3]), "=&v"(cnt), "+s"(n_) : "v"(CTLA(C_HRDY)), "v"(hlo), "v"(hhi) : "vcc", "scc", "memory");
    };
    auto f2_mm = [&](int k) AI {
      const u4v* wp = wd[k];
      r[2 * k] = mma(wp[0], hb[0], f4{0.f, 0.f, 0.f, 0.f}); r[2 * k + 1] = mma(wp[2], hb[0], f4{0.f, 0.f, 0.f, 0.f});
      r[2 * k] = mma(wp[1], hb[1], r[2 * k]); r[2 * k + 1] = mma(wp[3], hb[1], r[2 * k + 1]);
      r[2 * k] = mma(wp[4], hb[2], r[2 * k]); r[2 * k + 1] = mma(wp[6], hb[2], r[2 * k + 1]);
      r[2 * k] = mma(wp[5], hb[3], r[2 * k]); r[2 * k + 1] = mma(wp[7], hb[3], r[2 * k + 1]);
    };
    auto f2_st = [&](int k, u32 t10) AI {
#pragma unroll
      for (int i = 2 * k; i < 2 * k + 2; ++i) {
        u4v v = __builtin_bit_cast(u4v, r[i]);
#pragma unroll
        for (int e = 0; e < 4; ++e) v[e] = __builtin_amdgcn_bitop3_b32(v[e], t10, TMASK, 0xd8);
        __builtin_amdgcn_raw_buffer_store_b128(v, wrs, p2k[i], 0, 16);
      }
    };
    {
      __builtin_amdgcn_s_setprio(3);
      const u32 ts = ts0 - 500u;
      u32 tl = ~0u;
      pace(later(ts0, now32() - DT_ARG - 15) + DT_HEAD);
      STAMP(3);
#pragma unroll 1
      for (int i = 0; i < 8 && tl >= 1500u; ++i) {
        const u2v hd = __builtin_amdgcn_raw_buffer_load_b64(wrs, WS_HEAD + (lane & 7) * 8, 0, 16);
        tl = wave_max_u32(hd[0] == ~hd[1] ? hd[0] - ts : ~0u);
        STAMP(14);
        asm volatile("" ::: "memory");
      }
      tl = tl < 1500u ? tl + ts : ts0;
      if (lane == 0) par[1] = tl;
      ARRIVE(C_TL);
      STAMP(2);
      if (own) {
        WAITS(C_WGU, 1);
#pragma unroll
        for (int h = 0; h < 16; ++h) {
          if (h == DSEL) WAITS(C_SEL, 1);
          if (h == DEV) WAITS(C_XL, 4);
          if (h >= DWIN) asm volatile("s_waitcnt vmcnt(%0)" :: "n"(2 * (DWIN - 1)) : "memory");
          if (h == 3 + DWIN) ARRIVE(C_WD);
          if (h == 7 + DWIN) ARRIVE(C_WD + 1);
          dma(h, 128, D * 2, lbase + O_WD + h * 2048);
        }
        f2_setup(2);
        const u32 t10 = par[0] & TMASK;
        asm volatile("s_waitcnt vmcnt(8)" ::: "memory");
        wd_ld(0, 2);
        const u32 hc = __hip_atomic_load(ctl + C_HRDY, __ATOMIC_RELAXED, __HIP_MEMORY_SCOPE_WORKGROUP);
        const bool early = __builtin_amdgcn_ballot_w64(hc < 2u) == 0ull;
        if (early) { hb_ld(); f2_mm(0); }
        asm volatile("s_waitcnt vmcnt(0)" ::: "memory");
        STAMP(5);
        wd_ld(1, 3);
        if (!early) { WAITS(C_HRDY, 1); hb_ld(); STAMP(11); f2_mm(0); }
        SB();
        f2_mm(1);
        f2_st(0, t10);
        SB();
        f2_st(1, t10);
        STAMP(12);
      }
      ROLE_END();
    }
  }
}

extern "C" void kernel_launch(void* const* d_in, const int* in_sizes, int n_in,
                              void* d_out, int out_size, void* d_ws, size_t ws_size,
                              hipStream_t stream) {
  k_main<<<256, 768, 0, stream>>>((const u16*)d_in[0], (const u16*)d_in[8], (const u16*)d_in[12],
                                  (const u16*)d_in[1], (const u16*)d_in[2], (const u16*)d_in[3],
                                  (const u16*)d_in[4], (const u16*)d_in[5], (const u16*)d_in[6],
                                  (const u16*)d_in[9], (const u16*)d_in[10], (const u16*)d_in[11],
                                  (char*)d_ws, (u16*)d_out);
}
```

```cpp
#include <hip/hip_runtime.h>
#include <stdint.h>

typedef unsigned short u16;
typedef unsigned u32;
typedef _Float16 h2 __attribute__((ext_vector_type(2)));
typedef _Float16 h8 __attribute__((ext_vector_type(8)));
typedef __bf16 b2 __attribute__((ext_vector_type(2)));
typedef __bf16 b8 __attribute__((ext_vector_type(8)));
typedef short s4 __attribute__((ext_vector_type(4)));
typedef float f2 __attribute__((ext_vector_type(2)));
typedef float f4 __attribute__((ext_vector_type(4)));
typedef u32 u2v __attribute__((ext_vector_type(2)));
typedef u32 u3v __attribute__((ext_vector_type(3)));
typedef u32 u4v __attribute__((ext_vector_type(4)));
typedef __attribute__((address_space(3))) s4* lds_s4p;

constexpr int L = 2048;
constexpr int D = 1024;
constexpr int NM = 128;
constexpr int F = 3584;
constexpr int NTAP = 1024;

constexpr int SEG_I = 576, SEG_O0 = 800, SEG_O1 = 880, SEG_SP = 896;

#define AI __attribute__((always_inline))
#define SB() __builtin_amdgcn_sched_barrier(0)
#define OPQ(x) asm volatile("" : "+v"(x))
#define DIAG_K 4
#define DIAG_M 15
#define STAMP0() u32 dg_ = 0; const bool dgw_ = (DIAG_M >> w) & 1; if (DIAG_K == 0 && dgw_) dg_ = (u32)kt[0] | 1u
#define STAMP(i) do { if (i == DIAG_K && dgw_) { SB(); dg_ = (u32)__builtin_amdgcn_s_memrealtime() | 1u; SB(); } } while (0)
#define STAMP_END() do { if (DIAG_K == 99 && dgw_) { asm volatile("s_waitcnt vmcnt(0)" ::: "memory"); dg_ = (u32)__builtin_amdgcn_s_memrealtime() | 1u; } if (dg_) { while ((int)((u32)__builtin_amdgcn_s_memrealtime() - (dg_ + 4000u)) < 0) __builtin_amdgcn_s_sleep(2); } } while (0)

#define STAMP_T0

__device__ __forceinline__ float bflo(u32 v) { return __uint_as_float(v << 16); }
__device__ __forceinline__ float bfhi(u32 v) { return __uint_as_float(v & 0xffff0000u); }
__device__ __forceinline__ u32 pk_bf(float a, float b) { f2 v = {a, b}; b2 r = __builtin_convertvector(v, b2); return __builtin_bit_cast(u32, r); }
__device__ __forceinline__ float rb1(float v) { return __uint_as_float(pk_bf(0.f, v)); }
__device__ __forceinline__ h8 as_h8(u4v v) { return __builtin_bit_cast(h8, v); }
__device__ __forceinline__ b8 as_b8(u4v v) { return __builtin_bit_cast(b8, v); }
__device__ __forceinline__ u2v tr_read(const char* p) {
  s4 t = __builtin_amdgcn_ds_read_tr16_b64_v4i16((lds_s4p)p);
  return __builtin_bit_cast(u2v, t);
}
template <int CTRL> __device__ __forceinline__ u32 dpp_u(u32 v) { return (u32)__builtin_amdgcn_update_dpp(0, (int)v, CTRL, 0xf, 0xf, false); }
template <int CTRL> __device__ __forceinline__ float dpp_f(float v) { return __uint_as_float(dpp_u<CTRL>(__float_as_uint(v))); }
__device__ __forceinline__ u32 wave_max_u32(u32 v) {
  v = max(v, dpp_u<0x111>(v)); v = max(v, dpp_u<0x112>(v)); v = max(v, dpp_u<0x114>(v)); v = max(v, dpp_u<0x118>(v));
  v = max(v, dpp_u<0x142>(v)); v = max(v, dpp_u<0x143>(v));
  return (u32)__builtin_amdgcn_readlane((int)v, 63);
}
#define DPM " row_mask:0xf bank_mask:0xf bound_ctrl:1\n\t"
#define T2S(HO, HI, LI, C) "v_max_u32_dpp " HO ", " HI ", " HI " " C DPM "v_min_u32_dpp %8, " HI ", " HI " " C DPM "v_max_u32_dpp %7, " LI ", " LI " " C DPM "v_max_u32 %7, %7, %8\n\t"
#define T2R(RM, REST) T2S("%5", "%0", "%1", "row_shr:1") T2S("%6", "%5", "%7", "row_shr:2") T2S("%5", "%6", "%7", "row_shr:4") T2S("%6", "%5", "%7", "row_shr:8") \
    T2S("%5", "%6", "%7", "row_bcast:15") T2S("%6", "%5", "%7", "row_bcast:31") \
    "v_readlane_b32 %9, %6, 63\n\tv_readlane_b32 %10, %7, 63\n\ts_nop 0\n\tv_mov_b32 %5, %9\n\tv_cmp_le_u32 %11, %10, %0\n\tv_cmp_le_u32 %12, %10, %1\n\tv_mov_b32 %6, %10\n\t" \
    "v_cndmask_b32 %0, %0, %1, %11\n\tv_mov_b32_dpp %4, %5 quad_perm:[0,1,2,3] row_mask:" RM " bank_mask:0x3\n\tv_cndmask_b32 %0, %0, %2, %12\n\t" \
    "v_mov_b32_dpp %4, %6 quad_perm:[0,1,2,3] row_mask:" RM " bank_mask:0xc\n\t" REST
#define T2P "v_cndmask_b32 %1, %1, %2, %11\n\tv_cndmask_b32 %2, %2, %3, %11\n\tv_cndmask_b32 %1, %1, %3, %12\n\tv_cndmask_b32 %3, %3, 0, %11\n\tv_cndmask_b32 %2, %2, 0, %12\n\t"
__device__ __forceinline__ u32 wave_top8(u32& m1, u32 m2, u32 m3, u32 m4, u32& w2) {
  u32 key, ta, tb, lo, t, w1;
  unsigned long long c1, c2;
  asm("s_nop 1\n\t" T2R("0x1", T2P) T2R("0x2", T2P) T2R("0x4", T2P) T2R("0x8", "") "s_nop 0"
      : "+v"(m1), "+v"(m2), "+v"(m3), "+v"(m4), "=&v"(key), "=&v"(ta), "=&v"(tb), "=&v"(lo), "=&v"(t), "=&s"(w1), "=&s"(w2), "=&s"(c1), "=&s"(c2));
  return key;
}

__device__ __forceinline__ float exp_bf(float v) {
  const float e = __builtin_amdgcn_exp2f(v * 1.44268798828125f);
  return fmaf(e, v * (7.0526077e-06f * 0.69314718f), e);
}
struct cf { f2 r, i; };
struct ch { h2 r, i; };
__device__ __forceinline__ cf cmul(cf a, cf b) { cf c; c.r = a.r * b.r - a.i * b.i; c.i = a.r * b.i + a.i * b.r; return c; }
__device__ __forceinline__ ch hmul(ch a, ch b) {
  ch c;
  asm("v_pk_mul_f16 %0, %2, %4\n\tv_pk_mul_f16 %1, %2, %5\n\tv_pk_fma_f16 %0, %3, %5, %0 neg_lo:[1,0,0] neg_hi:[1,0,0]\n\tv_pk_fma_f16 %1, %3, %4, %1"
      : "=&v"(c.r), "=&v"(c.i) : "v"(a.r), "v"(a.i), "v"(b.r), "v"(b.i));
  return c;
}
__device__ __forceinline__ cf csq(cf a) { cf c; c.r = a.r * a.r - a.i * a.i; c.i = (a.r + a.r) * a.i; return c; }
__device__ __forceinline__ ch toh(cf a) { ch c; c.r = __builtin_convertvector(a.r, h2); c.i = __builtin_convertvector(a.i, h2); return c; }
__device__ __forceinline__ ch tohc(cf a) { ch c; c.r = __builtin_convertvector(a.r, h2); c.i = __builtin_convertvector(-a.i, h2); return c; }
__device__ __forceinline__ h2 ash(u32 v) { return __builtin_bit_cast(h2, v); }
__device__ __forceinline__ cf cexp_p(f2 a2, f2 brev, float p) {
  f2 e = a2 * p, t = brev * p;
  f2 mag = {__builtin_amdgcn_exp2f(e[0]), __builtin_amdgcn_exp2f(e[1])};
  float r0 = __builtin_amdgcn_fractf(t[0]), r1 = __builtin_amdgcn_fractf(t[1]);
  cf c;
  c.r = mag * f2{__builtin_amdgcn_cosf(r0), __builtin_amdgcn_cosf(r1)};
  c.i = mag * f2{__builtin_amdgcn_sinf(r0), __builtin_amdgcn_sinf(r1)};
  return c;
}
__device__ __forceinline__ u32 med3u(u32 a, u32 b, u32 c) { u32 r; asm("v_med3_u32 %0, %1, %2, %3" : "=v"(r) : "v"(a), "v"(b), "v"(c)); return r; }
__device__ __forceinline__ f2 unbf(u32 v) { return f2{bflo(v), bfhi(v)}; }
__device__ __forceinline__ float sigm(float v) { return __builtin_amdgcn_rcpf(1.f + __builtin_amdgcn_exp2f(-1.44269504088896341f * v)); }

#define TS2(A, B, C, D) "v_xor_b32 %5, " A ", %9\n\tv_xor_b32 %6, " B ", %9\n\tv_xor_b32 %7, " C ", %9\n\tv_xor_b32 %8, " D ", %9\n\t" \
    "v_fma_f32 %1, %5, 1.0, %1\n\tv_fma_f32 %2, %6, 1.0, %2\n\tv_or3_b32 %0, %0, %5, %6\n\tv_fma_f32 %3, %7, 1.0, %3\n\tv_fma_f32 %4, %8, 1.0, %4\n\tv_or3_b32 %0, %0, %7, %8\n\t"
#define TSR TS2("%10", "%11", "%12", "%13") TS2("%14", "%15", "%16", "%17") TS2("%18", "%19", "%20", "%21")
#define TSI "=&v"(t0), "=&v"(t1), "=&v"(t2), "=&v"(t3) : "v"(t10), "v"(p[0]), "v"(p[1]), "v"(p[2]), "v"(p[3]), "v"(q[0]), "v"(q[1]), "v"(q[2]), "v"(q[3]), \
    "v"(r[0]), "v"(r[1]), "v"(r[2]), "v"(r[3])
__device__ __forceinline__ f4 tsum4(u32& bad, u32 t10, u4v s, u4v p, u4v q, u4v r) {
  float s0, s1, s2, s3; u32 t0, t1, t2, t3;
  asm volatile("v_xor_b32 %1, %22, %9\n\tv_xor_b32 %2, %23, %9\n\tv_xor_b32 %3, %24, %9\n\tv_xor_b32 %4, %25, %9\n\tv_or3_b32 %0, %0, %1, %2\n\tv_or3_b32 %0, %0, %3, %4\n\t" TSR
               : "+v"(bad), "=&v"(s0), "=&v"(s1), "=&v"(s2), "=&v"(s3), TSI, "v"(s[0]), "v"(s[1]), "v"(s[2]), "v"(s[3]));
  return f4{s0, s1, s2, s3};
}
__device__ __forceinline__ f4 tsum3(u32& bad, u32 t10, f4 s, u4v p, u4v q, u4v r) {
  float s0 = s[0], s1 = s[1], s2 = s[2], s3 = s[3]; u32 t0, t1, t2, t3;
  asm volatile(TSR : "+v"(bad), "+v"(s0), "+v"(s1), "+v"(s2), "+v"(s3), TSI);
  return f4{s0, s1, s2, s3};
}
__device__ __forceinline__ void silu4(f4& g, float nl2e) {
  float g0 = g[0], g1 = g[1], g2 = g[2], g3 = g[3], t0, t1, t2, t3;
  asm volatile("v_fma_f32 %4, %0, %8, 0\n\tv_fma_f32 %5, %1, %8, 0\n\tv_fma_f32 %6, %2, %8, 0\n\tv_fma_f32 %7, %3, %8, 0\n\t"
               "v_exp_f32 %4, %4\n\tv_exp_f32 %5, %5\n\tv_exp_f32 %6, %6\n\tv_exp_f32 %7, %7\n\t"
               "v_fma_f32 %4, %4, 1.0, 1.0\n\tv_fma_f32 %5, %5, 1.0, 1.0\n\tv_fma_f32 %6, %6, 1.0, 1.0\n\tv_fma_f32 %7, %7, 1.0, 1.0\n\t"
               "v_rcp_f32 %4, %4\n\tv_rcp_f32 %5, %5\n\tv_rcp_f32 %6, %6\n\tv_rcp_f32 %7, %7\n\t"
               "v_fma_f32 %0, %0, %4, 0\n\tv_fma_f32 %1, %1, %5, 0\n\tv_fma_f32 %2, %2, %6, 0\n\tv_fma_f32 %3, %3, %7, 0"
               : "+v"(g0), "+v"(g1), "+v"(g2), "+v"(g3), "=&v"(t0), "=&v"(t1), "=&v"(t2), "=&v"(t3) : "v"(nl2e));
  g = f4{g0, g1, g2, g3};
}

constexpr int KT_PITCH = 52;
constexpr int kt_off(int p) {
  int i = 20, j = 0;
  if (p < 256) { i = p & 7; j = p >> 3; }
  else if (p < 512) { int q = (p - 256) >> 1; i = 8 + (q & 3); j = q >> 2; }
  else if (p < 1024) { int q = (p - 512) >> 2; i = 12 + (q & 1) + 2 * (q >> 6); j = (q >> 1) & 31; }
  else if (p < 2048) { int q = (p - 1024) >> 3; i = 16 + (q >> 5); j = q & 31; }
  return j * KT_PITCH + i * 2;
}
constexpr int KT_ZERO = 32 * KT_PITCH + 16;
constexpr int rnd_lag(int m) {
  const int st = m < 256 ? 1 : m < 512 ? 2 : m < 1024 ? 4 : 8;
  int q = m / st;
  const int rem = m - q * st;
  if (2 * rem > st || (2 * rem == st && (q & 1))) ++q;
  return q * st;
}
constexpr int tap_entry(int e) {
  if (e < 544) {
    if (543 - e <= 525) return kt_off(rnd_lag(543 - e));
  } else if (e >= 560 && e < 752) {
    const int q = (e - 560) / 48, x = (e - 560) - q * 48;
    const int p = 512 + 16 * (31 - x) + 4 * (q + 1);
    if (x < 32 && p > 524) return kt_off(p);
  } else if (e >= 752 && e < 816) {
    return kt_off(1032 + 16 * (815 - e));
  } else if (e >= 832 && e < 896) {
    return kt_off(1040 + 16 * (895 - e));
  } else if (e == SEG_SP + 27 || e == SEG_SP + 28) {
    return kt_off(1024);
  }
  return KT_ZERO;
}
struct TapIdx { u16 v[NTAP]; };
constexpr TapIdx make_tapidx() {
  TapIdx t{};
  for (int e = 0; e < NTAP; ++e) t.v[e] = (u16)tap_entry(2 * ((e >> 4) + 64 * ((e >> 1) & 7)) + (e & 1));
  return t;
}
__device__ __attribute__((aligned(16))) const TapIdx g_tapidx = make_tapidx();

constexpr int TROW = 544;
constexpr int O_OUT = 0;
constexpr int OT = 576;
constexpr int O_U = O_OUT + 8 * OT;
constexpr int O_TAP = O_U + 4608;
constexpr int TAP_PITCH = 2 * NTAP + 64;
constexpr int CHB = O_TAP + 16 * TROW + 32;
static_assert(16 * TROW >= 4 * TAP_PITCH, "lds");
static_assert((CHB / 4) % 32 == 8, "banks");
#define DEV 3
#define DWIN 8
#define DSEL 0
constexpr int NSLOT = 5, WWIN = 4;
constexpr int O_GT = 4 * CHB;
constexpr int O_RING = O_GT + 8192;
constexpr int O_WD = O_RING + 2 * NSLOT * 2048;
constexpr int O_FAR = O_WD + 32768;
constexpr int O_XS = O_FAR + 16384;
constexpr int XS_PITCH = 544;
constexpr int O_HB = O_XS + 8 * XS_PITCH;
constexpr int O_PART = O_HB + 2 * 528;
constexpr int O_SEL = O_PART + 512;
constexpr int O_PAR = O_SEL + 32;
constexpr int O_CTL = O_PAR + 16;
constexpr int O_JUNK = O_CTL + 512;
constexpr int K_LDS = O_JUNK + 1024;
static_assert(DWIN == 8, "flags of the down tile");
static_assert(K_LDS <= 163840 && O_RING % 16 == 0, "lds");
enum { C_PART = 0, C_GP, C_PUB, C_SEL, C_XS, C_HRDY, C_WGU, C_TL, C_XL, C_WD = 10, C_TAPS = 12, C_Q = 16, C_FAR = 32, C_TILE = 48 };

#define CTLA(word) (lbase + O_CTL + (word) * 4)
#define ARRIVE(word) asm volatile("s_mov_b64 exec, 1\n\tds_add_u32 %0, %1\n\ts_mov_b64 exec, -1" :: "v"(CTLA(word)), "v"(1) : "memory")
#define WAITQ(word, target) do { u32 t_; int n_ = 1 << 20; asm volatile( \
    "1:\n\tds_read_b32 %0, %2\n\ts_sub_u32 %1, %1, 1\n\ts_cbranch_scc1 2f\n\ts_waitcnt lgkmcnt(0)\n\tv_cmp_gt_u32 vcc, %3, %0\n\ts_cbranch_vccnz 1b\n2:\n\ts_waitcnt lgkmcnt(0)" \
    : "=&v"(t_), "+s"(n_) : "v"(CTLA(word)), "n"(target) : "vcc", "scc", "memory"); } while (0)
#define WAITS(word, target) do { u32 t_; int n_ = 1 << 20; asm volatile( \
    "1:\n\tds_read_b32 %0, %2\n\ts_sub_u32 %1, %1, 1\n\ts_cbranch_scc1 2f\n\ts_waitcnt lgkmcnt(0)\n\tv_cmp_le_u32 vcc, %3, %0\n\ts_cbranch_vccnz 2f\n\ts_sleep 2\n\ts_branch 1b\n2:\n\ts_waitcnt lgkmcnt(0)" \
    : "=&v"(t_), "+s"(n_) : "v"(CTLA(word)), "n"(target) : "vcc", "scc", "memory"); } while (0)
#define BAR() asm volatile("s_waitcnt lgkmcnt(0)\n\ts_barrier" ::: "memory")
__device__ __forceinline__ u2v lds8(const char* p) { return __builtin_bit_cast(u2v, __hip_atomic_load((const unsigned long long*)p, __ATOMIC_RELAXED, __HIP_MEMORY_SCOPE_WORKGROUP)); }

constexpr int WS_EPOCH = 0;
constexpr int WS_HEAD = 1024;
constexpr int WS_REC = 4096;
constexpr int WS_FLG = 40960;
constexpr int WS_KALL = WS_REC + 8192;
constexpr int WS_KEY = 32768;
constexpr int WS_P1 = 65536;
constexpr int WS_P2 = WS_P1 + 56 * 16384;
constexpr int WS_XT = WS_P2 + 256 * 7168;
constexpr int XT_PITCH = 33280;
constexpr int WS_END = WS_XT + 128 * XT_PITCH;
constexpr int SPIN_MAX = 1 << 16;
constexpr u32 TMASK = 0x3ffu;

#define DT_HEAD 110
#define DT_ARG 45
#define WDELAY 60
#define DT_KEY 225
#define DT_REC 255
#define DT_FLG 365
#define DT_VIS 40
#define DE_P1 385
#define DE_P2 620
#define DE_FAST 605
#define DT_MIN 25
#define DT_P1 100
#define DT_P2 130
#define SLEEP_AG1 12
#define SLEEP_G 6
#define STSLP 2
#define STWIN 4
#define H2_AT 9
#define CODE_B 28400

#define ROLE_END() do { STAMP_END(); asm volatile("s_endpgm" ::: "memory"); } while (0)
__device__ __forceinline__ u32 now32() { return (u32)__builtin_amdgcn_s_memrealtime(); }
__device__ __forceinline__ u32 later(u32 a, u32 b) { return (int)(b - a) > 0 ? b : a; }
__device__ __forceinline__ void pace(u32 t) {
#pragma unroll 1
  for (int i = 0; i < 1024 && (int)(now32() - t) < 0; ++i) __builtin_amdgcn_s_sleep(1);
}
__device__ __forceinline__ void pace2(u32 n, u32 t, int dmax) {
  int d = (int)(t - n);
  d = d < DT_MIN ? DT_MIN : (d > dmax ? dmax : d);
  pace(n + (u32)d);
}

#define ALL_OTHER "vcc", "scc", "memory", \
    "s0","s1","s2","s5","s6","s7","s33","s34","s35","s44","s45","s46","s47","s48","s49","s50","s51","s52","s53","s54","s55","s56","s57","s58","s59", \
    "s60","s61","s62","s63","s64","s65","s66","s67","s68","s69","s70","s71","s72","s73","s74","s75","s76","s77","s78","s79","s80","s81","s82","s83","s84", \
    "s85","s86","s87","s88","s89","s90","s91","s92","s93","s94","s95","s96","s97","s98","s99", \
    "v0","v1","v2","v3","v4","v5","v6","v7","v8","v9","v10","v11","v12","v13","v14","v15","v16","v17","v18","v19","v20","v21","v22","v23","v24","v25", \
    "v26","v27","v28","v29","v30","v31","v32","v33","v34","v35","v36","v37","v38","v39","v40","v41","v42","v43","v44","v45","v46","v47","v48","v49","v50", \
    "v51","v52","v53","v54","v55","v56","v57","v58","v59","v60","v61","v62","v63","v64","v65","v66","v67","v68","v69","v70","v71","v72","v73","v74","v75", \
    "v76","v77","v78","v79","v80","v81","v82","v83","v84","v85","v86","v87","v88","v89","v90","v91","v92","v93","v94","v95","v96","v97","v98","v99", \
    "v100","v101","v102","v103","v104","v105","v106","v107","v108","v109","v110","v111","v112","v113","v114","v115","v116","v117","v118","v119","v120", \
    "v121","v122","v123","v124","v125","v126","v127","v128","v129","v130","v131","v132","v133","v134","v135","v136","v137","v138","v139","v140","v141", \
    "v142","v143","v144","v145","v146","v147","v148","v149","v150","v151","v152","v153","v154","v155","v156","v157","v158","v159","v160","v161","v162", \
    "v163","v164","v165","v166","v167"
typedef __attribute__((address_space(1))) const u16* gp16;
typedef __attribute__((address_space(1))) const u32* gp32;
typedef __attribute__((address_space(1))) const u4v* gp128;
typedef unsigned long long u64;
typedef u64 q2 __attribute__((ext_vector_type(2)));
typedef u64 q4 __attribute__((ext_vector_type(4)));
typedef u64 q8 __attribute__((ext_vector_type(8)));

__global__ void __launch_bounds__(768, 3) k_main(
    const u16* x_, const u16* w_r_, const u16* gate_w_, const u16* logA_, const u16* Aim_, const u16* Cre_, const u16* Cim_,
    const u16* log_dt_, const u16* Dskip_, const u16* w_gate_, const u16* w_up_, const u16* w_down_, char* wsp_, u16* out_) {
  __shared__ __attribute__((aligned(256))) char smem[K_LDS];
  const u32 lbase = (u32)(uintptr_t)(__attribute__((address_space(3))) char*)smem;
#define XDMA(OFF) "global_load_lds_dwordx4 v1, s[8:9] offset:" #OFF " nt\n\ts_addk_i32 m0, 896\n\t"
  asm volatile("s_load_dwordx16 s[8:23], %0, 0\n\tv_readfirstlane_b32 s3, %2\n\ts_load_dwordx8 s[24:31], %0, 64\n\ts_load_dwordx4 s[36:39], %0, 96\n\t"
               "s_memrealtime s[40:41]\n\t" STAMP_T0 "s_mov_b32 s4, %1\n\ts_lshr_b32 s3, s3, 6\n\t"
               "s_cmp_lt_u32 s3, 4\n\ts_cbranch_scc1 .Lrows\n\ts_barrier\n\t"
               "s_cmp_ge_u32 s3, 8\n\ts_cbranch_scc1 .Lrole_g\n\ts_cmp_eq_u32 s3, 7\n\ts_cbranch_scc1 .Lrole_d\n\t"
               "s_cmp_ge_u32 s3, 5\n\ts_cbranch_scc1 .Lrole_h\n\ts_branch .Lrole_c\n.Lrows:\n\t"
               "s_lshr_b32 s5, s4, 3\n\tv_lshlrev_b32 v3, 2, %2\n\ts_mov_b32 m0, %4\n\tv_lshl_or_b32 v3, s5, 10, v3\n\ts_getpc_b64 s[6:7]\n\t"
               "v_min_u32 v3, %3, v3\n\tv_lshrrev_b32 v1, 3, %2\n\tglobal_load_lds_dword v3, s[6:7]\n\t"
               "v_and_b32 v2, 6, v1\n\ts_and_b32 s34, s4, 7\n\tv_xor_b32 v2, v2, %2\n\ts_lshl_b32 s34, s34, 5\n\tv_and_b32 v2, 7, v2\n\ts_add_u32 s5, s5, s34\n\t"
               "v_and_b32 v1, 7, v1\n\ts_lshl_b32 s5, s5, 5\n\tv_lshlrev_b32 v1, 11, v1\n\ts_add_u32 s5, s5, s3\n\tv_lshl_or_b32 v1, v2, 4, v1\n\t"
               "s_mul_i32 s34, s3, %5\n\tv_lshl_add_u32 v1, s5, 9, v1\n\ts_add_u32 m0, s34, %6\n\tv_and_b32 v2, 31, %2\n\ts_waitcnt lgkmcnt(0)\n\t"
               XDMA(0) "s_lshl_b32 s5, s3, 5\n\t" XDMA(128) "v_add_lshl_u32 v2, s5, v2, 4\n\t" XDMA(256) "s_lshl_b32 s5, s3, 10\n\t"
               "global_load_lds_dwordx4 v1, s[8:9] offset:384 nt\n\ts_add_u32 m0, s5, %7\n\ts_mov_b32 exec_hi, 0\n\t"
               "global_load_lds_dwordx4 v2, s[10:11]\n\ts_addk_i32 m0, 512\n\ts_nop 0\n\tglobal_load_lds_dwordx4 v2, s[12:13]\n\t"
               "s_mov_b32 m0, %8\n\tv_mov_b32 v3, 0\n\tglobal_load_lds_dword v3, s[36:37]\n\ts_mov_b32 exec_hi, -1"
               :: "s"(__builtin_amdgcn_kernarg_segment_ptr()), "s"(blockIdx.x), "v"(threadIdx.x), "n"(CODE_B - 256), "n"(O_JUNK), "n"(CHB), "n"(O_U + 512), "n"(O_FAR), "n"(O_XS)
               : "s3", "s4", "s5", "s6", "s7", "s8", "s9", "s10", "s11", "s12", "s13", "s14", "s15", "s16", "s17", "s18", "s19", "s20", "s21", "s22", "s23",
                 "s24", "s25", "s26", "s27", "s28", "s29", "s30", "s31", "s34", "s36", "s37", "s38", "s39", "s40", "s41", "s42", "s43", "v1", "v2", "v3", "scc", "memory");
  int lane, c16, g, w, b, xg, xi, cg, cc, c, d;
  char* ch_;
  q8 ka; q4 kb; q2 kc, kt;
  u32* ctl = (u32*)(smem + O_CTL);
  u32* par = (u32*)(smem + O_PAR);
#define IDS() do { int z_ = 0; OPQ(z_); lane = __builtin_amdgcn_mbcnt_hi(~0u, __builtin_amdgcn_mbcnt_lo(~0u, z_)); c16 = lane & 15; g = lane >> 4; } while (0)
#define ROLE(name) \
  asm volatile(".Lrole_" name ":\n\ts_waitcnt lgkmcnt(0)" : "={s[8:23]}"(ka), "={s[24:31]}"(kb), "={s[36:39]}"(kc), "={s[40:43]}"(kt), "={s3}"(w), "={s4}"(b) :: ALL_OTHER); \
  if (__builtin_expect(w > 11, 0)) asm volatile("s_endpgm" ::: "memory");        \
  IDS(); \
  xg = b & 7; xi = b >> 3;                          \
  cg = xg * 32 + xi;                                \
  cc = (xi >> 4) * 128 + xg * 16 + (xi & 15);       \
  c = w & 3; ch_ = smem + c * CHB; d = cc * 4 + c; \
  const u32 ts0 = (u32)kt[0]; \
  auto wrs = __builtin_amdgcn_make_buffer_rsrc((void*)kc[0], 0, WS_END, 0x00020000); \
  STAMP0()

  const char* tapb;
  const char* ub;
  char* erp;
  const char* eup;
  const char* egp;
  u4v At[17], band[4];
  float dm1;
  f4 acc[8];
  auto conv_setup = [&]() AI {
    tapb = ch_ + O_TAP + ((15 - c16) & 3) * TAP_PITCH + ((15 - c16) & ~3) * 2;
    ub = ch_ + O_U - 32 + c16 * 32 + g * 16;
    erp = ch_ + O_OUT + g * 144 + c16 * 8;
    eup = ch_ + O_U + 512 + c16 * 32 + g * 8;
    egp = smem + O_GT + lane * 16;
  };
  auto band_setup = [&]() AI {
#pragma unroll
    for (int k = 0; k < 4; ++k) {
      const int ctr = k == 0 ? 12 : 8, hw = k + 1;
      const u32 m = ((1u << (2 * hw + 1)) - 1u) << (ctr - hw);
      const u32 bits = (m << c16) >> (8 * g);
#pragma unroll
      for (int j = 0; j < 4; ++j)
        band[k][j] = (__builtin_amdgcn_sbfe(bits, 2 * j, 1) & 0x3f80u) | (__builtin_amdgcn_sbfe(bits, 2 * j + 1, 1) & 0x3f800000u);
      OPQ(band[k]);
    }
  };
  auto frag2 = [&](const char* pa, const char* pb) AI { const u2v lo = lds8(pa), hi = lds8(pb); return u4v{lo[0], lo[1], hi[0], hi[1]}; };
  auto atload = [&](int s0, int s1) AI {
#pragma unroll
    for (int s = s0; s < s1; ++s) At[s] = frag2(tapb + 64 * s + g * 16, tapb + 64 * s + g * 16 + 8);
  };
  auto bin = [&](int pair, int be) AI { const char* p = tapb + g * 8 + (SEG_I + 96 * pair - 16 * be) * 2; return frag2(p, p + 96); };
  auto bout = [&](int be) AI { const char* p = tapb + g * 8 + (SEG_O0 - 16 * be) * 2; return frag2(p, p + (SEG_O1 - SEG_O0) * 2); };
  auto asp = [&]() AI { return frag2(tapb + SEG_SP * 2 + g * 16, tapb + SEG_SP * 2 + g * 16 + 8); };
  auto mma = [&](u4v a, u4v bq, f4 cin) AI { return __builtin_amdgcn_mfma_f32_16x16x32_bf16(as_b8(a), as_b8(bq), cin, 0, 0, 0); };
  auto uld = [&](int sg) AI { return *(const u4v*)(ub + 512 + sg * 64); };
  auto uld24 = [&](int un) AI { return *(const u4v*)(ub + 512 + un * 512 - 16); };
  u2v euw[8], eo[8]; f4 egv[8], efv[8];
  float eu[8][4], ev[8][4], ea[8][4];
  u4v Fq[57], ru[6], wa_[6][3];
  f4 wq[6][6];
  u32 pk[12], one, qa, fa;
  char* tfp; char* fp_;
#define LOPQ(p_) do { u32 a_ = (u32)(uintptr_t)(__attribute__((address_space(3))) const char*)p_; OPQ(a_); p_ = (decltype(p_))(__attribute__((address_space(3))) char*)(uintptr_t)a_; } while (0)
  auto m_setup = [&]() AI {
    tfp = smem + (lane == 0 ? O_CTL + c * 4 : O_JUNK + lane * 4);
    fp_ = smem + O_FAR + c * 4096 + lane * 16;
    qa = CTLA(C_Q) + (lane & 3) * 4; fa = CTLA(C_FAR + c); one = 1u;
    OPQ(qa); OPQ(fa); OPQ(one);
    LOPQ(tfp); LOPQ(fp_); LOPQ(erp); LOPQ(eup); LOPQ(egp); LOPQ(ub);
  };
#define WAITA(addr) do { u32 t_; int n_ = 1 << 20; asm volatile( \
    "1:\n\tds_read_b32 %0, %2\n\ts_sub_u32 %1, %1, 1\n\ts_cbranch_scc1 2f\n\ts_waitcnt lgkmcnt(0)\n\tv_cmp_eq_u32 vcc, 0, %0\n\ts_cbranch_vccnz 1b\n2:\n\ts_waitcnt lgkmcnt(0)" \
    : "=&v"(t_), "+s"(n_) : "v"(addr) : "vcc", "scc", "memory"); } while (0)
#define MF(d_, a_, b_) asm volatile("v_mfma_f32_16x16x32_bf16 %0, %1, %2, %0" : "+v"(d_) : "v"(a_), "v"(b_)); SB()
#define MF0(d_, a_, b_) asm volatile("v_mfma_f32_16x16x32_bf16 %0, %1, %2, 0" : "=&v"(d_) : "v"(a_), "v"(b_)); SB()
#define NOP(n_) asm volatile("s_nop " #n_); SB()
#define FL(i_, sg_) Fq[i_] = uld(sg_); SB()
#define RL(un_) ru[un_] = uld24(un_); SB()
#define AL(s_) atload(s_, s_ + 1); SB()
#define ULD(t_) euw[t_] = *(const u2v*)(eup + t_ * 512); SB()
#define GLD(t_) egv[t_] = *(const f4*)(egp + t_ * 1024); SB()
#define FLD(t_) efv[t_] = *(const f4*)(fp_ + (t_ - 2) * 1024); SB()
#define FST(un_) *(f4*)(fp_ + un_ * 1024) = acc[un_ + 2]; SB()
#define EUN(t_, e_) eu[t_][e_] = (e_ & 1) ? bfhi(euw[t_][e_ >> 1]) : bflo(euw[t_][e_ >> 1]); OPQ(eu[t_][e_]); ev[t_][e_] = eu[t_][e_]; SB()
#define EA1(t_, e_) ea[t_][e_] = fmaf(eu[t_][e_], dm1, acc[t_][e_]); OPQ(ea[t_][e_]); SB()
#define EFA(t_, e_) ev[t_][e_] = fmaf(egv[t_][e_], efv[t_][e_], eu[t_][e_]); OPQ(ev[t_][e_]); SB()
#define EA2(t_, e_) ea[t_][e_] = fmaf(egv[t_][e_], ea[t_][e_], ev[t_][e_]); OPQ(ea[t_][e_]); SB()
#define EPK(t_, k_) { u32 v_ = pk_bf(ea[t_][2 * k_], ea[t_][2 * k_ + 1]); OPQ(v_); eo[t_][k_] = v_; } SB()
#define OST(t_) *(u2v*)(erp + t_ * OT) = eo[t_]; SB()
#define WPK(un_, p_, j_) { u32 v_ = pk_bf(wq[un_][2 * p_ + j_ / 2][2 * (j_ & 1)], wq[un_][2 * p_ + j_ / 2][2 * (j_ & 1) + 1]); OPQ(v_); wa_[un_][p_][j_] = v_; } SB()
#define FLAG(w_) asm volatile("" ::: "memory"); *(u32*)(tfp + (w_) * 4) = one; SB()
#define PEEK(i_, a_) pk[i_] = __hip_atomic_load((__attribute__((address_space(3))) u32*)(uintptr_t)(a_), __ATOMIC_RELAXED, __HIP_MEMORY_SCOPE_WORKGROUP); SB()
#define CHECK(i_, a_) if (__builtin_expect(__builtin_amdgcn_ballot_w64(pk[i_] == 0u) != 0ull, 0)) WAITA(a_); asm volatile("" ::: "memory"); SB()
#define MIX2(nm, nd, nv) _Pragma("unroll") for (int r_ = 0; r_ < (nm); ++r_) { __builtin_amdgcn_sched_group_barrier(0x008, 1, 0); \
    if (nd) __builtin_amdgcn_sched_group_barrier(0x100, (nd), 0); if (nv) __builtin_amdgcn_sched_group_barrier(0x002, (nv), 0); }

  {
    ROLE("x");
    const int xw = w;
    char* img = ch_ + O_U + 512;
    STAMP(11);
    __builtin_amdgcn_s_setprio(2);
    { u32 zz = 0u; OPQ(zz); if (lane < 32) ctl[32 * xw + lane] = zz; }
    if (xi == 31 && w == 0) { const u32 th = later(ts0, now32() - DT_ARG); if (lane == 0) __builtin_amdgcn_raw_buffer_store_b64(u2v{th, ~th}, wrs, WS_HEAD + xg * 8, 0, 16); }
    BAR();
    STAMP(12);
    const char* wp; const char* fp; const char* tp0; const char* tp1; char* pp;
    {
      const int r = lane & 7, q = (lane >> 2) & 3;
      fp = img + r * 128 + ((((lane & 8) / 2 + g) ^ (r & 6)) * 16);
      wp = smem + O_FAR + xw * 1024 + (lane & 1) * 512 + (lane & 4) * 16 + g * 16;
      tp0 = img + q * 128 + (((2 * g + ((lane >> 1) & 1)) ^ (q & 2)) * 16) + (lane & 1) * 8;
      tp1 = img + 512 + q * 128 + (((2 * g + ((lane >> 1) & 1)) ^ ((q & 2) + 4)) * 16) + (lane & 1) * 8;
      pp = smem + ((lane >> 3) == 0 || (lane >> 3) == 3 ? O_PART + r * 64 + xw * 16 + g * 8 : O_JUNK + lane * 8);
    }
    const int so = WS_XT + (32 * xw + (lane >> 3)) * XT_PITCH + cg * 128 + (lane & 7) * 16, sstep = 8 * XT_PITCH;
    u32 lo_ = WS_XT + (cc >> 1) * XT_PITCH + (16 * xw + (lane >> 2)) * 128 + (cc & 1) * 64 + (lane & 3) * 16;
    OPQ(lo_);
    SB();
    asm volatile("s_waitcnt vmcnt(0)" ::: "memory");
    STAMP(1);
    {
      u4v wa[4], fb[4]; u2v ta[4], tb[4];
#pragma unroll
      for (int s = 0; s < 4; ++s) { wa[s] = *(const u4v*)(wp + s * 128); fb[s] = *(const u4v*)(fp + s * 1024); }
      SB();
      f4 r = {0.f, 0.f, 0.f, 0.f};
#pragma unroll
      for (int s = 0; s < 4; ++s) r = mma(wa[s], fb[s], r);
      *(f2*)pp = f2{r[0], r[1]};
      ARRIVE(C_PART);
      SB();
#pragma unroll
      for (int s = 0; s < 4; ++s) { ta[s] = tr_read(tp0 + s * 1024); tb[s] = tr_read(tp1 + s * 1024); }
      SB();
      WAITQ(C_GP, 1);
#pragma unroll
      for (int s = 0; s < 4; ++s) __builtin_amdgcn_raw_buffer_store_b128(u4v{ta[s][0], ta[s][1], tb[s][0], tb[s][1]}, wrs, so + s * sstep, 0, 16);
    }
    STAMP(2);
    const u32 dskw = ((gp16)kb[0])[d];
    conv_setup();
    band_setup();
    const u32 tag = par[0];
    ARRIVE(C_PUB);
    asm volatile("s_waitcnt vmcnt(0)" ::: "memory");
    if (lane == 0) __builtin_amdgcn_raw_buffer_store_b32(tag, wrs, WS_FLG + xw * 1024 + cg * 4, 0, 16);
    STAMP(3);
    dm1 = bflo(dskw) - 1.f;
    WAITS(C_TL, 1);
    const u32 tl0 = (u32)__builtin_amdgcn_readfirstlane((int)par[1]);
    u3v fq[2];
    auto rec_ld = [&]() AI {
#pragma unroll
      for (int k = 0; k < 2; ++k) fq[k] = __builtin_amdgcn_raw_buffer_load_b96(wrs, WS_REC + xw * 2048 + k * 1024 + lane * 16, 0, 16);
    };
    pace(tl0 + DT_REC);
    rec_ld();
    SB();
    pace(tl0 + DT_FLG);
    const u32 fl = __builtin_amdgcn_raw_buffer_load_b32(wrs, WS_FLG + (cc >> 6) * 1024 + xw * 64 + g * 256 + c16 * 4, 0, 16);
    SB();
    {
      if (__builtin_expect(__any(fq[0][2] != tag || fq[1][2] != tag), 0)) {
#pragma unroll 1
        for (int spin = 0; spin < SPIN_MAX; ++spin) {
          __builtin_amdgcn_s_sleep(SLEEP_G);
          rec_ld();
          if (!__any(fq[0][2] != tag || fq[1][2] != tag)) break;
          asm volatile("" ::: "memory");
        }
      }
      char* gp = smem + O_GT + xw * 2048 + (lane & 3) * 256 + (lane >> 2) * 16;
#pragma unroll
      for (int k = 0; k < 2; ++k) *(f4*)(gp + k * 1024) = f4{bflo(fq[k][0]), bfhi(fq[k][0]), bflo(fq[k][1]), bfhi(fq[k][1])};
    }
    SB();
    u4v xq[4];
    bool late[4];
    {
      const u64 bad = __builtin_amdgcn_ballot_w64(fl != tag);
      asm volatile("" ::: "memory");
#pragma unroll
      for (int k = 0; k < 4; ++k) {
        late[k] = ((bad >> (16 * k)) & 0xffffull) != 0ull;
        xq[k] = __builtin_amdgcn_raw_buffer_load_b128(wrs, lo_ | (late[k] ? 1u << 30 : 0u), k * 8192, 16);
        SB();
      }
    }
    ARRIVE(C_XL);
    STAMP(4);
    WAITQ(C_TAPS + c, 1);
    u4v Bi[2][3], Bo[4], As;
#pragma unroll
    for (int be = 0; be < 3; ++be) { Bi[0][be] = bin(0, be); Bi[1][be] = bin(1, be); }
#pragma unroll
    for (int be = 0; be < 4; ++be) Bo[be] = bout(be);
    As = asp();
    WAITQ(C_PUB, 4);
    char* dp = smem + (lane & 3) * CHB + O_U + 512 + xw * 256 + (lane >> 2) * 16;
    m_setup();
    auto stage = [&](int k) AI {
      SB();
      if (__builtin_expect(late[k], 0)) {
#pragma unroll 1
        for (int spin = 0; spin < SPIN_MAX; ++spin) {
          STAMP(14);
          const u32 f1 = __builtin_amdgcn_raw_buffer_load_b32(wrs, WS_FLG + (cc >> 6) * 1024 + xw * 64 + k * 256 + c16 * 4, 0, 16);
          if (!__any(f1 != tag)) break;
          __builtin_amdgcn_s_sleep(SLEEP_G);
          asm volatile("" ::: "memory");
        }
        asm volatile("" ::: "memory");
        xq[k] = __builtin_amdgcn_raw_buffer_load_b128(wrs, lo_, k * 8192, 16);
        asm volatile("s_waitcnt vmcnt(0)" : "+v"(xq[k]) :: "memory");
      }
      *(u4v*)(dp + k * 1024) = xq[k];
      FLAG(C_Q + k * 4);
    };
    stage(0);
    stage(1);
    STAMP(5);
    __builtin_amdgcn_s_setprio(0);
    WAITA(qa);
    SB();
    FL(0, 0); RL(0); FL(8, 8); RL(1);
    MF0(wq[0][0], Fq[0], band[0]);
    MF0(wq[0][1], Fq[0], band[1]);
    MF0(wq[0][2], ru[0], band[0]);
    MF0(wq[0][3], ru[0], band[1]);
    MF0(wq[0][4], Fq[0], band[2]);
    MF0(wq[0][5], ru[0], band[3]);
    MF0(acc[4], As, Fq[0]); PEEK(6, qa + 16);
    MF0(wq[1][0], Fq[8], band[0]); WPK(0, 0, 0); WPK(0, 0, 1);
    MF0(wq[1][1], Fq[8], band[1]); WPK(0, 0, 2); WPK(0, 0, 3);
    MF0(wq[1][2], ru[1], band[0]); WPK(0, 1, 0); WPK(0, 1, 1);
    MF0(wq[1][3], ru[1], band[1]); WPK(0, 1, 2); WPK(0, 1, 3);
    MF0(wq[1][4], Fq[8], band[2]); CHECK(6, qa + 16);
    MF0(wq[1][5], ru[1], band[3]); WPK(0, 2, 0); WPK(0, 2, 1);
    MF0(acc[5], As, Fq[8]); WPK(0, 2, 2); WPK(0, 2, 3);
    MF0(acc[2], wa_[0][0], Bi[0][0]); FL(16, 16); RL(2);
    MF(acc[2], wa_[0][1], Bi[1][0]); WPK(1, 0, 0); WPK(1, 0, 1);
    MF0(acc[3], wa_[0][0], Bi[0][1]); WPK(1, 0, 2); WPK(1, 0, 3);
    MF(acc[3], wa_[0][1], Bi[1][1]); WPK(1, 1, 0); WPK(1, 1, 1);
    MF(acc[4], wa_[0][0], Bi[0][2]); FST(0); FLAG(C_FAR + 0 * 4);
    MF(acc[4], wa_[0][1], Bi[1][2]); WPK(1, 1, 2); WPK(1, 1, 3);
    MF(acc[4], wa_[0][2], Bo[0]); WPK(1, 2, 0); WPK(1, 2, 1);
    MF(acc[5], wa_[0][2], Bo[1]); WPK(1, 2, 2); WPK(1, 2, 3);
    MF0(acc[6], wa_[0][2], Bo[2]);
    MF0(acc[7], wa_[0][2], Bo[3]); stage(2);
    MF0(wq[2][0], Fq[16], band[0]);
    MF0(wq[2][1], Fq[16], band[1]); stage(3);
    MF0(wq[2][2], ru[2], band[0]);
    MF0(wq[2][3], ru[2], band[1]);
    MF0(wq[2][4], Fq[16], band[2]); FL(24, 24); RL(3);
    MF0(wq[2][5], ru[2], band[3]); WPK(2, 0, 0); WPK(2, 0, 1);
    MF(acc[6], As, Fq[16]); WPK(2, 0, 2); WPK(2, 0, 3);
    MF(acc[3], wa_[1][0], Bi[0][0]); WPK(2, 1, 0); WPK(2, 1, 1);
    MF(acc[3], wa_[1][1], Bi[1][0]); WPK(2, 1, 2); WPK(2, 1, 3);
    MF(acc[4], wa_[1][0], Bi[0][1]); WPK(2, 2, 0); WPK(2, 2, 1);
    MF(acc[4], wa_[1][1], Bi[1][1]); WPK(2, 2, 2); WPK(2, 2, 3);
    MF(acc[5], wa_[1][0], Bi[0][2]); FST(1); FLAG(C_FAR + 1 * 4);
    MF(acc[5], wa_[1][1], Bi[1][2]); PEEK(7, qa + 32);
    MF(acc[5], wa_[1][2], Bo[0]);
    MF(acc[6], wa_[1][2], Bo[1]);
    MF(acc[7], wa_[1][2], Bo[2]);
    MF0(wq[3][0], Fq[24], band[0]);
    MF0(wq[3][1], Fq[24], band[1]); CHECK(7, qa + 32);
    MF0(wq[3][2], ru[3], band[0]);
    MF0(wq[3][3], ru[3], band[1]);
    MF0(wq[3][4], Fq[24], band[2]); FL(32, 32); RL(4);
    MF0(wq[3][5], ru[3], band[3]); WPK(3, 0, 0); WPK(3, 0, 1);
    MF(acc[7], As, Fq[24]); WPK(3, 0, 2); WPK(3, 0, 3);
    MF(acc[4], wa_[2][0], Bi[0][0]); WPK(3, 1, 0); WPK(3, 1, 1);
    MF(acc[4], wa_[2][1], Bi[1][0]); WPK(3, 1, 2); WPK(3, 1, 3);
    MF(acc[5], wa_[2][0], Bi[0][1]); WPK(3, 2, 0); WPK(3, 2, 1);
    MF(acc[5], wa_[2][1], Bi[1][1]); FL(40, 40); RL(5);
    MF(acc[6], wa_[2][0], Bi[0][2]); FST(2); FLAG(C_FAR + 2 * 4);
    MF(acc[6], wa_[2][1], Bi[1][2]); WPK(3, 2, 2); WPK(3, 2, 3);
    MF(acc[6], wa_[2][2], Bo[0]); AL(0);
    MF(acc[7], wa_[2][2], Bo[1]); AL(1);
    MF0(wq[4][0], Fq[32], band[0]); AL(2);
    MF0(wq[4][1], Fq[32], band[1]); AL(3);
    MF0(wq[4][2], ru[4], band[0]); AL(4);
    MF0(wq[4][3], ru[4], band[1]); WPK(4, 0, 0); WPK(4, 0, 1);
    MF(acc[5], wa_[3][0], Bi[0][0]); WPK(4, 0, 2); WPK(4, 0, 3);
    MF(acc[5], wa_[3][1], Bi[1][0]); WPK(4, 1, 0); WPK(4, 1, 1);
    MF(acc[6], wa_[3][0], Bi[0][1]); WPK(4, 1, 2); WPK(4, 1, 3);
    MF(acc[6], wa_[3][1], Bi[1][1]); AL(5);
    MF(acc[7], wa_[3][0], Bi[0][2]); FST(3); FLAG(C_FAR + 3 * 4);
    MF(acc[7], wa_[3][1], Bi[1][2]); AL(6);
    MF(acc[7], wa_[3][2], Bo[0]); FL(33, 33); AL(7);
    MF0(wq[5][0], Fq[40], band[0]); FL(34, 34); AL(8);
    MF0(wq[5][1], Fq[40], band[1]); FL(35, 35); AL(9);
    MF0(wq[5][2], ru[5], band[0]); WPK(5, 0, 0); WPK(5, 0, 1);
    MF0(wq[5][3], ru[5], band[1]); AL(10);
    MF(acc[6], wa_[4][0], Bi[0][0]); PEEK(8, qa + 48); FL(36, 36);
    MF(acc[6], wa_[4][1], Bi[1][0]); FL(37, 37); WPK(5, 0, 2);
    MF(acc[7], wa_[4][0], Bi[0][1]); FL(38, 38); WPK(5, 0, 3);
    MF(acc[7], wa_[4][1], Bi[1][1]); FL(39, 39); WPK(5, 1, 0);
    MF(acc[6], At[0], Fq[32]); WPK(5, 1, 1); WPK(5, 1, 2);
    MF(acc[6], At[1], Fq[33]); CHECK(8, qa + 48);
    MF(acc[6], At[2], Fq[34]); WPK(5, 1, 3); FL(41, 41);
    MF(acc[6], At[3], Fq[35]); AL(11);
    MF(acc[7], wa_[5][0], Bi[0][0]); FL(42, 42); AL(12);
    MF(acc[7], wa_[5][1], Bi[1][0]); AL(13);
    MF(acc[6], At[4], Fq[36]); FL(43, 43); AL(14);
    MF(acc[6], At[5], Fq[37]); AL(15);
    MF(acc[6], At[6], Fq[38]); FL(44, 44); AL(16);
    MF(acc[6], At[7], Fq[39]);
    MF(acc[6], At[8], Fq[40]); FL(45, 45);
    MF(acc[7], At[0], Fq[40]);
    MF(acc[6], At[9], Fq[41]); FL(46, 46);
    MF(acc[7], At[1], Fq[41]);
    MF(acc[6], At[10], Fq[42]); FL(47, 47);
    MF(acc[7], At[2], Fq[42]);
    MF(acc[6], At[11], Fq[43]); FL(48, 48);
    MF(acc[7], At[3], Fq[43]);
    MF(acc[6], At[12], Fq[44]); FL(49, 49); ULD(6);
    MF(acc[7], At[4], Fq[44]); FL(50, 50); GLD(6);
    MF(acc[6], At[13], Fq[45]); FL(51, 51);
    MF(acc[7], At[5], Fq[45]); FL(52, 52);
    MF(acc[6], At[14], Fq[46]); FL(53, 53);
    MF(acc[7], At[6], Fq[46]); FL(54, 54); EUN(6, 0);
    MF(acc[6], At[15], Fq[47]); FL(55, 55); EUN(6, 1);
    MF(acc[7], At[7], Fq[47]); FL(56, 56); EUN(6, 2);
    MF(acc[6], At[16], Fq[48]); EUN(6, 3);
    MF(acc[7], At[8], Fq[48]); ULD(7); GLD(7);
    MF(acc[7], At[9], Fq[49]);
    MF(acc[7], At[10], Fq[50]);
    MF(acc[7], At[11], Fq[51]);
    MF(acc[7], At[12], Fq[52]); EA1(6, 0); EA1(6, 1); EA1(6, 2); EA1(6, 3);
    MF(acc[7], At[13], Fq[53]); EA2(6, 0); EA2(6, 1); EA2(6, 2); EA2(6, 3);
    MF(acc[7], At[14], Fq[54]); EPK(6, 0); EPK(6, 1);
    MF(acc[7], At[15], Fq[55]); OST(6); FLAG(C_TILE + 6 * 4);
    MF(acc[7], At[16], Fq[56]); EUN(7, 0); EUN(7, 1); EUN(7, 2); EUN(7, 3); NOP(3); EA1(7, 0); EA1(7, 1); EA1(7, 2); EA1(7, 3); EA2(7, 0); EA2(7, 1); EA2(7, 2); EA2(7, 3); EPK(7, 0); EPK(7, 1); OST(7); FLAG(C_TILE + 7 * 4);
    STAMP(9);
    ROLE_END();
  }
  {
    ROLE("h");
    const int hw = w - 5;
    const int j = xg * 28 + xi;
    const int s1 = j >> 2, kq = j & 3;
    const bool own = xi < 28;
    const u64 wbase = hw == 0 ? kb[1] : kb[2];
    const int pitch = F * 2;
    auto hrs = __builtin_amdgcn_make_buffer_rsrc((void*)wbase, 0, own ? D * F * 2 : 0, 0x00020000);
    const int r8 = lane >> 3;
    const int vo = (256 * kq * F + 64 * s1) * 2 + (8 * (r8 >> 2) + (r8 & 3)) * pitch + (((lane & 7) * 16) ^ (((r8 >> 1) & 3) * 32));
    auto dma = [&](int h, int sB, int pt, u32 dst) AI {
      const int m = h >> 1;
#pragma unroll
      for (int i = 0; i < 2; ++i)
        asm volatile("s_mov_b32 m0, %3\n\ts_nop 0\n\tbuffer_load_dwordx4 %0, %1, %2 offen nt lds"
                     :: "v"(vo), "s"(hrs), "s"((m & 1) * 32 * pt + (m >> 1) * sB + (16 * i + 4 * (h & 1)) * pt), "s"(dst + i * 1024) : "memory");
    };
    const int q = c16 >> 2, p = c16 & 3;
    int tro[4];
#pragma unroll
    for (int ct = 0; ct < 4; ++ct) tro[ct] = (4 * g + q) * 128 + ((ct ^ (((4 * g + q) >> 1) & 3)) * 32) + p * 8;
    const int tok = lane & 7;
    u4v wd[2][8];
    auto wd_ld = [&](int k, int cb) AI {
#pragma unroll
      for (int i = 0; i < 8; ++i) {
        const char* pp = smem + O_WD + cb * 8192 + (i >> 2) * 4096 + tro[i & 3];
        const u2v lo = tr_read(pp), hi = tr_read(pp + 2048);
        wd[k][i] = u4v{lo[0], lo[1], hi[0], hi[1]};
      }
    };
    u32 p2o, p2k[4], hlo, hhi;
    auto f2_setup = [&](int cb0) AI {
      p2o = WS_P2 + ((64 * kq + 16 * cb0 + (lane & 8) / 2 + g) * 56 + s1) * 128 + tok * 16;
      hlo = lbase + O_HB + ((lane & 8) ? 512 : g * 128 + tok * 16);
      hhi = lbase + O_HB + ((lane & 8) ? g * 128 + tok * 16 : 512);
#pragma unroll
      for (int i = 0; i < (cb0 == 2 ? 4 : 2); ++i) { p2k[i] = p2o + i * 8 * 7168; OPQ(p2k[i]); }
      OPQ(hlo); OPQ(hhi);
    };
    auto f2_job = [&](int nb, u32 t10) AI {
      u4v hb[4];
      {
        u32 cnt; int n_ = 1 << 20;
        asm volatile("1:\n\tds_read_b32 %4, %6\n\tds_read_b128 %0, %7\n\tds_read_b128 %1, %8\n\tds_read_b128 %2, %7 offset:528\n\tds_read_b128 %3, %8 offset:528\n\t"
                     "s_sub_u32 %5, %5, 1\n\ts_cbranch_scc1 2f\n\ts_waitcnt lgkmcnt(0)\n\tv_cmp_gt_u32 vcc, 2, %4\n\ts_cbranch_vccnz 1b\n2:\n\ts_waitcnt lgkmcnt(0)"
                     : "=&v"(hb[0]), "=&v"(hb[1]), "=&v"(hb[2]), "=&v"(hb[3]), "=&v"(cnt), "+s"(n_) : "v"(CTLA(C_HRDY)), "v"(hlo), "v"(hhi) : "vcc", "scc", "memory");
      }
      STAMP(11);
      f4 r[4];
#pragma unroll
      for (int i = 0; i < 2 * nb; ++i) {
        const u4v* wp = &wd[i >> 1][(i & 1) * 2];
        r[i] = mma(wp[0], hb[0], f4{0.f, 0.f, 0.f, 0.f});
        r[i] = mma(wp[1], hb[1], r[i]);
        r[i] = mma(wp[4], hb[2], r[i]);
        r[i] = mma(wp[5], hb[3], r[i]);
      }
      SB();
#pragma unroll
      for (int i = 0; i < 2 * nb; ++i) {
        u4v v = __builtin_bit_cast(u4v, r[i]);
#pragma unroll
        for (int e = 0; e < 4; ++e) v[e] = __builtin_amdgcn_bitop3_b32(v[e], t10, TMASK, 0xd8);
        __builtin_amdgcn_raw_buffer_store_b128(v, wrs, p2k[i], 0, 16);
      }
      STAMP(12);
    };
    const u32 rb = lbase + O_RING + hw * (NSLOT * 2048);
    pace(ts0 + WDELAY);
    STAMP(2);
#pragma unroll
    for (int h = 0; h < WWIN; ++h) dma(h, 64 * F * 2, F * 2, rb + h * 2048);
    __builtin_amdgcn_s_setprio(3);
    STAMP(3);
    if (__builtin_expect(own, 1)) {
      u4v wv[32];
      f4 a1[4] = {f4{0.f, 0.f, 0.f, 0.f}, f4{0.f, 0.f, 0.f, 0.f}, f4{0.f, 0.f, 0.f, 0.f}, f4{0.f, 0.f, 0.f, 0.f}};
      u4v xb[8];
      auto f1 = [&](int k0, int k1) AI {
#pragma unroll
        for (int k = k0; k < k1; ++k)
#pragma unroll
          for (int ct = 0; ct < 4; ++ct) a1[ct] = mma(wv[4 * k + ct], xb[k], a1[ct]);
      };
      u32 xo = lbase + O_XS + tok * XS_PITCH + g * 16;
      u32 p1o = WS_P1 + s1 * 16384 + kq * 4096 + hw * 2048 + (lane & 8) * 64 + g * 128 + tok * 16;
      u32 t10, tpub;
      OPQ(xo); OPQ(p1o);
      constexpr int HX = (16 - WWIN + 1) & ~1;
#pragma unroll
      for (int h = 0; h < 16; ++h) {
        const int left = (h + WWIN < 16 ? WWIN : 16 - h) - 1;
        asm volatile("s_waitcnt vmcnt(%0)" :: "n"(2 * left) : "memory");
        if (h + WWIN < 16) {
          asm volatile("s_waitcnt lgkmcnt(0)" ::: "memory");
          dma(h + WWIN, 64 * F * 2, F * 2, rb + ((h + WWIN) % NSLOT) * 2048);
        }
#pragma unroll
        for (int ct = 0; ct < 4; ++ct) {
          const u2v t = tr_read(smem + O_RING + hw * (NSLOT * 2048) + (h % NSLOT) * 2048 + tro[ct]);
          wv[4 * (h >> 1) + ct][2 * (h & 1)] = t[0]; wv[4 * (h >> 1) + ct][2 * (h & 1) + 1] = t[1];
        }
        if (h == H2_AT) ARRIVE(C_WGU);
        if (h == HX - 1) {
          STAMP(4);
          WAITQ(C_XS, 1);
          STAMP(1);
#pragma unroll
          for (int k = 0; k < 8; ++k) xb[k] = *(const __attribute__((address_space(3))) u4v*)(uintptr_t)(xo + k * 64);
          t10 = par[0]; tpub = par[2];
          SB();
          f1(0, 2);
          SB();
        }
        if (h >= HX) { f1(h - HX + 2, h - HX + 3); SB(); }
      }
      STAMP(5);
      f1(18 - HX, 8);
      t10 &= TMASK;
#pragma unroll
      for (int a = 0; a < 2; ++a) {
        u4v v;
#pragma unroll
        for (int e = 0; e < 4; ++e)
          v[e] = ((u32)__builtin_amdgcn_update_dpp(__float_as_int(a1[2 * a][e]), __float_as_int(a1[2 * a + 1][e]), 0xe4, 0xf, 0xc, false) & ~TMASK) | t10;
        __builtin_amdgcn_raw_buffer_store_b128(v, wrs, p1o + a * 1024, 0, 16);
      }
      STAMP(6);
      const u32 tn = now32();
      u32 voff = WS_P1 + s1 * 16384 + hw * 1024 + lane * 16;
      u32 ho = lbase + O_HB + hw * 528 + g * 128 + tok * 16 + (lane & 8);
      float nl2e = -1.44269504088896341f;
      u32 wda = CTLA(C_WD) + hw * 4;
      OPQ(voff); OPQ(ho); OPQ(nl2e); OPQ(wda);
      f2_setup(hw & 1);
      pace2(tn, (u32)__builtin_amdgcn_readfirstlane((int)tpub) + DE_P1, DT_P1);
      u4v pb[8];
      auto p1_ld = [&]() AI {
#pragma unroll
        for (int i = 0; i < 8; ++i) { pb[i] = __builtin_amdgcn_raw_buffer_load_b128(wrs, voff + (i >> 2) * 2048, (i & 3) * 4096, 16); SB(); }
      };
      p1_ld();
      SB();
      f4 G, U;
      bool early = false;
#pragma unroll 1
      for (int spin = 0; spin < SPIN_MAX; ++spin) {
        u32 bad = 0;
        OPQ(bad);
        asm volatile("" :: "v"(pb[0]));
        const u32 cw = __hip_atomic_load((__attribute__((address_space(3))) u32*)(uintptr_t)wda, __ATOMIC_RELAXED, __HIP_MEMORY_SCOPE_WORKGROUP);
        G = tsum4(bad, t10, pb[0], pb[1], pb[2], pb[3]);
        early = __builtin_amdgcn_ballot_w64(cw == 0u) == 0ull;
        asm volatile("" ::: "memory");
        if (__builtin_expect(early, 1)) wd_ld(0, hw);
        SB();
        silu4(G, nl2e);
        U = tsum4(bad, t10, pb[4], pb[5], pb[6], pb[7]);
        if (__builtin_expect(!__any((bad & TMASK) != 0), 1)) break;
        STAMP(14);
        p1_ld();
        asm volatile("" ::: "memory");
      }
      STAMP(7);
      {
        float hv[4];
#pragma unroll
        for (int e = 0; e < 4; ++e) { hv[e] = G[e] * U[e]; asm("" : "+v"(hv[e])); }
        *(__attribute__((address_space(3))) u2v*)(uintptr_t)ho = u2v{pk_bf(hv[0], hv[1]), pk_bf(hv[2], hv[3])};
      }
      ARRIVE(C_HRDY);
      STAMP(8);
      if (__builtin_expect(!early, 0)) { WAITA(wda); wd_ld(0, hw); }
      f2_job(1, t10);
    }
    if (hw == 0) {
      WAITS(C_SEL, 1);
      const u32 t10 = par[0] & TMASK;
      const u32 tpub = (u32)__builtin_amdgcn_readfirstlane((int)par[2]);
      const u32 mykey = ((const u32*)(smem + O_SEL))[lane & 7];
      if (own) pace2(now32(), tpub + DE_P2, DT_P2); else pace(tpub + DE_P2 - 40);
      u4v pa[7];
      const int voff = WS_P2 + cc * 7168 + lane * 16;
#pragma unroll
      for (int k = 0; k < 7; ++k) pa[k] = __builtin_amdgcn_raw_buffer_load_b128(wrs, voff + (k & 3) * 1024, (k >> 2) * 4096, 16);
      SB();
      const int row = (int)(~mykey & 2047u);
      const float rsig = rb1(sigm(__uint_as_float((mykey ^ (~(u32)((int)mykey >> 31) | 0x80000000u)) & 0xffff0000u)));
      WAITA(CTLA(C_TILE) + (lane & 31) * 4);
      float cf_ = rsig * (1.f - *(const float*)(smem + O_GT + (row >> 8) * 1024 + ((row >> 2) & 3) * 256 + ((row >> 4) & 15) * 16 + (row & 3) * 4));
      float base[4];
#pragma unroll
      for (int e = 0; e < 4; ++e) base[e] = bflo(*(const u16*)(smem + e * CHB + O_OUT + (row >> 8) * OT + ((row >> 2) & 3) * 144 + ((row >> 4) & 15) * 8 + (row & 3) * 2));
      u64 op = kc[1] + (row * D + cc * 4) * 2;
      { float cq = cf_; OPQ(cq); cf_ = cq; OPQ(op); }
#pragma unroll
      for (int e = 0; e < 4; ++e) OPQ(base[e]);
      STAMP(13);
      f4 y;
#pragma unroll 1
      for (int spin = 0; spin < SPIN_MAX; ++spin) {
        u32 bad = 0;
        OPQ(bad);
        y = tsum4(bad, t10, pa[0], pa[1], pa[2], pa[3]);
        y = tsum3(bad, t10, y, pa[4], pa[5], pa[6]);
        if (__builtin_expect(!__any((bad & TMASK) != 0), 1)) break;
        STAMP(15);
#pragma unroll
        for (int k = 0; k < 7; ++k) pa[k] = __builtin_amdgcn_raw_buffer_load_b128(wrs, voff + (k & 3) * 1024, (k >> 2) * 4096, 16);
        asm volatile("" ::: "memory");
      }
      STAMP(9);
      float o[4];
#pragma unroll
      for (int e = 0; e < 4; ++e) {
        float v = y[e] + dpp_f<0x128>(y[e]);
        auto s16 = __builtin_amdgcn_permlane16_swap(__float_as_uint(v), __float_as_uint(v), false, false);
        v = __uint_as_float(s16[0]) + __uint_as_float(s16[1]);
        auto s32 = __builtin_amdgcn_permlane32_swap(__float_as_uint(v), __float_as_uint(v), false, false);
        o[e] = fmaf(__uint_as_float(s32[0]) + __uint_as_float(s32[1]), cf_, base[e]);
      }
      if (lane < 8) *(__attribute__((address_space(1))) u2v*)op = u2v{pk_bf(o[0], o[1]), pk_bf(o[2], o[3])};
    }
    ROLE_END();
  }
  {
    ROLE("g");
    const u32 pLA = __builtin_nontemporal_load((gp32)ka[3] + d * (NM / 2) + lane);
    const u32 pAI = __builtin_nontemporal_load((gp32)ka[4] + d * (NM / 2) + lane);
    const u32 pCR = __builtin_nontemporal_load((gp32)ka[5] + d * (NM / 2) + lane);
    const u32 pCI = __builtin_nontemporal_load((gp32)ka[6] + d * (NM / 2) + lane);
    const u32 ldt = ((gp16)ka[7])[d], dskw = ((gp16)kb[0])[d];
    u4v ixa[2];
#pragma unroll
    for (int r = 0; r < 2; ++r) ixa[r] = *(const u4v*)(g_tapidx.v + lane * 16 + r * 8);
    SB();
    asm volatile("s_mov_b32 m0, %0\n\ts_nop 0" :: "s"(lbase + c * CHB) : "memory");
#define PUT1(off, val) asm volatile("ds_write_addtid_b32 %0 offset:%1" :: "v"(val), "n"(off) : "memory")
#define PUTR(off, val) do { const ch v_ = (val); asm volatile("ds_write_addtid_b32 %0 offset:%2\n\tds_write_addtid_b32 %1 offset:%3" :: "v"(v_.r), "v"(v_.i), "n"(off), "n"((off) + 256) : "memory"); } while (0)
    const char* pf = ch_ + c16 * TROW + g * 16;
    PUT1(O_TAP, 0x3c003c00u); PUT1(O_TAP + 256, 0u);
    PUT1(O_U, 0u); PUT1(O_U + 256, 0u);
    SB();
    const float dt = rb1(exp_bf(bflo(ldt)));
    const f2 a2 = f2{-rb1(exp_bf(bflo(pLA))), -rb1(exp_bf(bfhi(pLA)))} * dt * 1.44269504088896341f;
    const f2 br = unbf(pAI) * dt * 0.15915494309189535f;
    STAMP(1);
    u4v Lf[2][8], Wf[8];
    f4 kk[2][2];
    float fk;
    {
      const cf z1 = cexp_p(a2, br, 1.f);
      const cf z2 = csq(z1), z4 = csq(z2), z8 = csq(z4), z16 = csq(z8), z32 = csq(z16), z64 = csq(z32);
      ch base[4], anc[4];
      base[1] = tohc(z8); base[2] = tohc(z16); base[3] = tohc(cmul(z16, z8));
      anc[1] = tohc(z32); anc[2] = tohc(z64); anc[3] = tohc(cmul(z64, z32));
      PUTR(O_OUT + 5 * TROW, tohc(csq(z64)));
#pragma unroll
      for (int j = 1; j < 16; ++j) {
        const int q = j >> 2, r = j & 3;
        PUTR(O_TAP + j * TROW, q == 0 ? base[r] : r == 0 ? anc[q] : hmul(anc[q], base[r]));
      }
#pragma unroll
      for (int s = 0; s < 8; ++s) Lf[0][s] = *(const u4v*)(pf + O_TAP + s * 64);
#pragma unroll
      for (int s = 0; s < 8; ++s) Lf[1][s] = *(const u4v*)(ch_ + O_OUT + 5 * TROW + g * 16 + s * 64);
      SB();
      STAMP(11);
      u32 mx = max(max(pCR << 17, pCR << 1), max(pCI << 17, pCI << 1));
      mx = wave_max_u32(mx);
      int ex = (int)(mx >> 24);
      ex = ex < 1 ? 1 : (ex > 253 ? 253 : ex);
      const float sdn = __uint_as_float((u32)(254 - ex) << 23);
      fk = 2.f * dt * __uint_as_float((u32)ex << 23);
      cf C; C.r = unbf(pCR) * sdn; C.i = unbf(pCI) * sdn;
      const cf E = cexp_p(a2, br, 256.f);
      const ch y1 = toh(z1), y2 = toh(z2), y4 = toh(z4), y6 = toh(cmul(z4, z2));
      {
        const ch R0 = toh(C), R2 = hmul(R0, y2), R4 = hmul(R0, y4), R6 = hmul(R0, y6);
        PUTR(O_TAP, R0); PUTR(O_TAP + 2 * TROW, R2); PUTR(O_TAP + 4 * TROW, R4); PUTR(O_TAP + 6 * TROW, R6);
        PUTR(O_TAP + 1 * TROW, hmul(R0, y1)); PUTR(O_TAP + 3 * TROW, hmul(R2, y1)); PUTR(O_TAP + 5 * TROW, hmul(R4, y1)); PUTR(O_TAP + 7 * TROW, hmul(R6, y1));
        C = cmul(C, E);
        const ch R8 = toh(C);
        PUTR(O_TAP + 8 * TROW, R8); PUTR(O_TAP + 9 * TROW, hmul(R8, y2)); PUTR(O_TAP + 10 * TROW, hmul(R8, y4)); PUTR(O_TAP + 11 * TROW, hmul(R8, y6));
        C = cmul(C, E);
        const ch R12 = toh(C);
        PUTR(O_TAP + 12 * TROW, R12); PUTR(O_TAP + 13 * TROW, hmul(R12, y4));
        C = cmul(C, E);
        const ch R14 = toh(C);
        PUTR(O_TAP + 14 * TROW, R14); PUTR(O_TAP + 15 * TROW, hmul(R14, y4));
      }
#pragma unroll
      for (int s = 0; s < 8; ++s) Wf[s] = *(const u4v*)(pf + O_TAP + s * 64);
      SB();
      STAMP(12);
#pragma unroll
      for (int i = 0; i < 5; ++i) { C = cmul(C, E); PUTR(O_OUT + i * TROW, toh(C)); }
      SB();
      auto mh = [&](u4v a, u4v bq, f4 cin) AI { return __builtin_amdgcn_mfma_f32_16x16x32_f16(as_h8(a), as_h8(bq), cin, 0, 0, 0); };
      kk[0][0] = f4{0.f, 0.f, 0.f, 0.f}; kk[0][1] = kk[0][0]; kk[1][0] = kk[0][0]; kk[1][1] = kk[0][0];
#pragma unroll
      for (int s = 0; s < 5; ++s) {
#pragma unroll
        for (int k = 0; k < 4; ++k) {
          if (k < 2) { if (s < 4) kk[0][0] = mh(Wf[s + 4 * k], Lf[0][s + 4 * k], kk[0][0]); }
          else if (s > 0) { const int u = s - 1 + 4 * (k - 2); kk[0][1] = mh(Wf[u], Lf[1][u], kk[0][1]); Wf[u] = *(const u4v*)(pf + O_OUT + u * 64); }
          SB();
          if (s < 4) {
            const ch o = hmul(ch{ash(Lf[0][s][k]), ash(Lf[0][s + 4][k])}, ch{ash(Lf[1][s][k]), ash(Lf[1][s + 4][k])});
            Lf[1][s][k] = __builtin_bit_cast(u32, o.r); Lf[1][s + 4][k] = __builtin_bit_cast(u32, o.i);
          }
          if (s == 3 && k == 3) asm volatile("s_nop 1");
          SB();
        }
      }
#pragma unroll
      for (int s = 0; s < 8; ++s) { kk[1][0] = mh(Wf[s], Lf[0][s], kk[1][0]); kk[1][1] = mh(Wf[s], Lf[1][s], kk[1][1]); }
    }
    {
      u32 ia[16];
      const u32 kb_ = lbase + c * CHB + O_OUT;
#pragma unroll
      for (int e = 0; e < 16; ++e) { const u32 iw = ixa[e >> 3][(e >> 1) & 3]; ia[e] = kb_ + ((e & 1) ? (iw >> 16) : (iw & 0xffffu)); }
      MIX2(16, 0, 1)
      SB();
      STAMP(2);
      char* ktp = ch_ + O_OUT + c16 * KT_PITCH + g * 8;
#pragma unroll
      for (int hf = 1; hf >= 0; --hf) {
#pragma unroll
        for (int ct = 0; ct < 2; ++ct) {
          const f4 v = kk[hf][ct] * fk;
          *(u32*)(ktp + ct * 16 * KT_PITCH + hf * 32) = pk_bf(v[0], v[1]);
          *(u32*)(ktp + ct * 16 * KT_PITCH + hf * 32 + 4) = pk_bf(v[2], v[3]);
        }
        asm volatile("" ::: "memory");
      }
      u32 zz = 0u; OPQ(zz);
      *(u32*)(ch_ + O_OUT + KT_ZERO + (lane & 7) * 4) = zz;
      asm volatile("" ::: "memory");
      u32 v[16], pe[9];
#pragma unroll
      for (int e = 15; e >= 0; --e) v[e] = *(const __attribute__((address_space(3))) u16*)(uintptr_t)ia[e];
      SB();
      pe[8] = zz;
#pragma unroll
      for (int i = 7; i >= 0; --i) {
        pe[i] = v[2 * i] | (v[2 * i + 1] << 16);
        const u32 nx = (u32)__builtin_amdgcn_update_dpp((int)dpp_u<0x134>(pe[i + 1]), (int)pe[i], 0x130, 0xf, 0xf, false);
        const u32 po = __builtin_amdgcn_alignbyte(nx, pe[i], 2);
        PUT1(O_TAP + i * 256, pe[i]); PUT1(O_TAP + 2 * TAP_PITCH - 4 + i * 256, pe[i]);
        PUT1(O_TAP + TAP_PITCH + i * 256, po); PUT1(O_TAP + 3 * TAP_PITCH - 4 + i * 256, po);
      }
      ARRIVE(C_TAPS + c);
    }
    STAMP(3);
    IDS();
    conv_setup();
    dm1 = bflo(dskw) - 1.f;
    atload(0, 17);
    m_setup();
    __builtin_amdgcn_s_setprio(1);
    SB();
    WAITA(qa);
    STAMP(4);
    SB();
    FL(0, -7); FL(1, -6); FL(2, -5); FL(3, -4); FL(4, -3); FL(5, -2); FL(6, -1); FL(7, 0); ULD(0); GLD(0);
    MF0(acc[0], At[9], Fq[0]); EUN(0, 0); EUN(0, 1);
    MF(acc[0], At[10], Fq[1]); EUN(0, 2); EUN(0, 3);
    MF(acc[0], At[11], Fq[2]);
    MF(acc[0], At[12], Fq[3]);
    MF(acc[0], At[13], Fq[4]);
    MF(acc[0], At[14], Fq[5]);
    MF(acc[0], At[15], Fq[6]); FL(8, 1);
    MF(acc[0], At[16], Fq[7]);
    MF0(acc[1], At[1], Fq[0]); FL(9, 2);
    MF(acc[1], At[2], Fq[1]);
    MF(acc[1], At[3], Fq[2]); FL(10, 3);
    MF(acc[1], At[4], Fq[3]);
    MF(acc[1], At[5], Fq[4]); FL(11, 4); EA1(0, 0);
    MF(acc[1], At[6], Fq[5]); EA1(0, 1); EA1(0, 2);
    MF(acc[1], At[7], Fq[6]); EA1(0, 3); PEEK(0, qa + 16);
    MF(acc[1], At[8], Fq[7]); FL(12, 5); EA2(0, 0);
    MF0(acc[2], At[0], Fq[7]); EA2(0, 1); EA2(0, 2);
    MF(acc[1], At[9], Fq[8]); EA2(0, 3); EPK(0, 0);
    MF(acc[2], At[1], Fq[8]); EPK(0, 1); FL(13, 6);
    MF(acc[1], At[10], Fq[9]); OST(0); FLAG(C_TILE + 0 * 4);
    MF(acc[2], At[2], Fq[9]); FL(14, 7); FL(15, 8);
    MF(acc[1], At[11], Fq[10]); CHECK(0, qa + 16);
    MF(acc[2], At[3], Fq[10]);
    MF(acc[1], At[12], Fq[11]); FL(16, 9);
    MF(acc[2], At[4], Fq[11]); ULD(1); GLD(1);
    MF(acc[1], At[13], Fq[12]); FL(17, 10);
    MF(acc[2], At[5], Fq[12]);
    MF(acc[1], At[14], Fq[13]); FL(18, 11);
    MF(acc[2], At[6], Fq[13]);
    MF(acc[1], At[15], Fq[14]); FL(19, 12); EUN(1, 0);
    MF(acc[2], At[7], Fq[14]); EUN(1, 1); EUN(1, 2);
    MF(acc[1], At[16], Fq[15]); FL(20, 13); EUN(1, 3);
    MF(acc[2], At[8], Fq[15]);
    MF0(acc[3], At[0], Fq[15]); FL(21, 14);
    MF(acc[2], At[9], Fq[16]);
    MF(acc[3], At[1], Fq[16]); FL(22, 15); EA1(1, 0);
    MF(acc[2], At[10], Fq[17]); EA1(1, 1); EA1(1, 2);
    MF(acc[3], At[2], Fq[17]); FL(23, 16); EA1(1, 3);
    MF(acc[2], At[11], Fq[18]); PEEK(2, fa + 0); EA2(1, 0);
    MF(acc[3], At[3], Fq[18]); EA2(1, 1); EA2(1, 2);
    MF(acc[2], At[12], Fq[19]); EA2(1, 3); EPK(1, 0);
    MF(acc[3], At[4], Fq[19]); EPK(1, 1); FL(24, 17);
    MF(acc[2], At[13], Fq[20]); OST(1); FLAG(C_TILE + 1 * 4);
    MF(acc[3], At[5], Fq[20]); FL(25, 18); CHECK(2, fa + 0);
    MF(acc[2], At[14], Fq[21]); FL(26, 19); ULD(2);
    MF(acc[3], At[6], Fq[21]); FLD(2); GLD(2);
    MF(acc[2], At[15], Fq[22]); FL(27, 20); PEEK(1, qa + 32);
    MF(acc[3], At[7], Fq[22]);
    MF(acc[2], At[16], Fq[23]); FL(28, 21);
    MF(acc[3], At[8], Fq[23]); EUN(2, 0); EUN(2, 1);
    MF0(acc[4], At[0], Fq[23]); FL(29, 22); EUN(2, 2);
    MF(acc[3], At[9], Fq[24]); EUN(2, 3); EA1(2, 0);
    MF(acc[4], At[1], Fq[24]); FL(30, 23); EA1(2, 1);
    MF(acc[3], At[10], Fq[25]); EA1(2, 2); EA1(2, 3);
    MF(acc[4], At[2], Fq[25]); FL(31, 24); EFA(2, 0);
    MF(acc[3], At[11], Fq[26]); EFA(2, 1); EFA(2, 2);
    MF(acc[4], At[3], Fq[26]); EFA(2, 3); PEEK(3, fa + 16);
    MF(acc[3], At[12], Fq[27]); EA2(2, 0); EA2(2, 1);
    MF(acc[4], At[4], Fq[27]); EA2(2, 2); EA2(2, 3);
    MF(acc[3], At[13], Fq[28]); CHECK(1, qa + 32); EPK(2, 0); EPK(2, 1);
    MF(acc[4], At[5], Fq[28]); FL(32, 25); OST(2);
    MF(acc[3], At[14], Fq[29]); FLAG(C_TILE + 2 * 4); FL(33, 26);
    MF(acc[4], At[6], Fq[29]); FL(34, 27); CHECK(3, fa + 16);
    MF(acc[3], At[15], Fq[30]); ULD(3); FL(35, 28);
    MF(acc[4], At[7], Fq[30]); FLD(3); GLD(3);
    MF(acc[3], At[16], Fq[31]); FL(36, 29);
    MF(acc[4], At[8], Fq[31]);
    MF0(acc[5], At[0], Fq[31]); FL(37, 30);
    MF(acc[4], At[9], Fq[32]); EUN(3, 0); EUN(3, 1);
    MF(acc[5], At[1], Fq[32]); PEEK(4, fa + 32); FL(38, 31);
    MF(acc[4], At[10], Fq[33]); EUN(3, 2); EUN(3, 3);
    MF(acc[5], At[2], Fq[33]); FL(39, 32); EA1(3, 0);
    MF(acc[4], At[11], Fq[34]); EA1(3, 1); EA1(3, 2);
    MF(acc[5], At[3], Fq[34]); EA1(3, 3); EFA(3, 0);
    MF(acc[4], At[12], Fq[35]); EFA(3, 1); EFA(3, 2); EFA(3, 3);
    MF(acc[5], At[4], Fq[35]); EA2(3, 0); EA2(3, 1); EA2(3, 2); EA2(3, 3);
    MF(acc[4], At[13], Fq[36]); FL(40, 33); EPK(3, 0); EPK(3, 1);
    MF(acc[5], At[5], Fq[36]); FL(41, 34); OST(3);
    MF(acc[4], At[14], Fq[37]); FL(42, 35); FLAG(C_TILE + 3 * 4); CHECK(4, fa + 32); ULD(4);
    MF(acc[5], At[6], Fq[37]); FL(43, 36); FLD(4); GLD(4); PEEK(5, fa + 48);
    MF(acc[4], At[15], Fq[38]); FL(44, 37); FL(45, 38);
    MF(acc[5], At[7], Fq[38]); FL(46, 39); FL(47, 40);
    MF(acc[4], At[16], Fq[39]);
    MF(acc[5], At[8], Fq[39]); EUN(4, 0); EUN(4, 1);
    MF(acc[5], At[9], Fq[40]); EUN(4, 2); EUN(4, 3);
    MF(acc[5], At[10], Fq[41]); EFA(4, 0); EA1(4, 0);
    MF(acc[5], At[11], Fq[42]); EA1(4, 1); EA1(4, 2);
    MF(acc[5], At[12], Fq[43]); EA1(4, 3); EFA(4, 1); EFA(4, 2); EFA(4, 3); CHECK(5, fa + 48); ULD(5);
    MF(acc[5], At[13], Fq[44]); EA2(4, 0); EA2(4, 1); EA2(4, 2); EA2(4, 3); FLD(5); GLD(5);
    MF(acc[5], At[14], Fq[45]); EPK(4, 0); EPK(4, 1);
    MF(acc[5], At[15], Fq[46]); OST(4); FLAG(C_TILE + 4 * 4);
    MF(acc[5], At[16], Fq[47]); EUN(5, 0); EUN(5, 1); EUN(5, 2); EUN(5, 3); EFA(5, 0); EFA(5, 1); EFA(5, 2); EFA(5, 3); EA1(5, 0); EA1(5, 1); EA1(5, 2); EA1(5, 3); EA2(5, 0); EA2(5, 1); EA2(5, 2); EA2(5, 3); EPK(5, 0); EPK(5, 1); OST(5); FLAG(C_TILE + 5 * 4);
    STAMP(8);
    ROLE_END();
  }
  {
    ROLE("c");
    __builtin_amdgcn_s_setprio(3);
    {
      u32 zz = 0u; OPQ(zz);
      if (lane < 2) *(u4v*)(smem + O_HB + 512 + lane * 528) = u4v{zz, zz, zz, zz};
    }
    u32* selw = (u32*)(smem + O_SEL);
    u32 tag;
    {
      const u32 pa = lbase + O_PART + (lane & 7) * 64;
      const u32 ib = 2047u - (u32)(cg * 8 + (lane & 7));
      f4 p0, p1, p2, p3; u32 cnt, ep; int n_ = 1 << 20; unsigned long long tk;
      asm volatile("1:\n\tds_read_b32 %4, %8\n\tds_read_b128 %0, %9\n\tds_read_b128 %1, %9 offset:16\n\tds_read_b128 %2, %9 offset:32\n\tds_read_b128 %3, %9 offset:48\n\t"
                   "ds_read_b32 %7, %10\n\ts_memrealtime %6\n\ts_sub_u32 %5, %5, 1\n\ts_cbranch_scc1 2f\n\ts_waitcnt lgkmcnt(0)\n\tv_cmp_gt_u32 vcc, 4, %4\n\ts_cbranch_vccnz 1b\n2:\n\ts_waitcnt lgkmcnt(0)"
                   : "=&v"(p0), "=&v"(p1), "=&v"(p2), "=&v"(p3), "=&v"(cnt), "+s"(n_), "=&s"(tk), "=&v"(ep) : "v"(CTLA(C_PART)), "v"(pa), "v"(lbase + O_XS) : "vcc", "scc", "memory");
      tag = (u32)__builtin_amdgcn_readfirstlane((int)ep) + 1u;
      tag += tag == 0u;
      const f4 s4_ = (p0 + p1) + (p2 + p3);
      const u32 rp = pk_bf(s4_[1] + s4_[3], s4_[0] + s4_[2]);
      const u32 key = ((rp ^ ((u32)((int)rp >> 31) | 0x80000000u)) & 0xffff0000u) | ib;
      const u32 kn = dpp_u<0xb1>(key);
      u32 m1 = max(key, kn), m2 = min(key, kn), m3;
      { const u32 o1 = dpp_u<0x4e>(m1), o2 = dpp_u<0x4e>(m2); const u32 lo = min(m1, o1), hb = max(m2, o2); m1 = max(m1, o1); m2 = max(lo, hb); m3 = min(lo, hb); }
      { const u32 o1 = dpp_u<0x141>(m1), o2 = dpp_u<0x141>(m2), o3 = dpp_u<0x141>(m3); const u32 lo = min(m1, o1), hb = max(m2, o2);
        m1 = max(m1, o1); m2 = max(lo, hb); m3 = max(min(lo, hb), max(m3, o3)); }
      const u32 wm = (((u32)tk + 8u) >> 2) & 0xffu;
      if (lane == 0) __builtin_amdgcn_raw_buffer_store_b128(u4v{m1, m2, (m3 & 0xffff0000u) | wm, tag}, wrs, WS_KEY + cg * 16, 0, 16);
      SB();
      STAMP(1);
      if (lane == 0) par[0] = tag;
      ARRIVE(C_GP);
      {
        const float gv = sigm(bflo(rp));
        const u32 g2 = pk_bf(gv, dpp_f<0xb1>(gv));
        u32 g3 = dpp_u<0x4e>(g2);
        OPQ(g3);
        if ((lane & 59) == 0) __builtin_amdgcn_raw_buffer_store_b128(u4v{g2, g3, tag, tag}, wrs, WS_REC + cg * 32 + lane * 4, 0, 16);
      }
      if ((lane & 57) == 0) __builtin_amdgcn_raw_buffer_store_b128(u4v{key, kn, 0u, tag}, wrs, WS_KALL + cg * 64 + lane * 8, 0, 16);
    }
    u32 l7 = (lane & 7) * 16, ko = WS_KEY + lane * 16;
    OPQ(l7); OPQ(ko);
    WAITQ(C_TL, 1);
    STAMP(5);
    const u32 tkey = (u32)__builtin_amdgcn_readfirstlane((int)par[1]) + DT_KEY;
    pace(tkey);
    u4v gr[4];
    u32 mykey;
    u4v xs[4];
    auto xrs = __builtin_amdgcn_make_buffer_rsrc((void*)(ka[0] + (xi & 3) * 512), 0, L * D * 2 - 1536, 0x00020000);
    asm volatile("" : "+s"(xrs));
    auto xs_ld = [&]() AI {
      const u32 vo = ((~mykey & 2047u) << 11) | l7;
#pragma unroll
      for (int k = 0; k < 4; ++k) xs[k] = __builtin_amdgcn_raw_buffer_load_b128(xrs, vo + k * 128, 0, 0);
    };
#pragma unroll
    for (int i = 0; i < 4; ++i) gr[i] = __builtin_amdgcn_raw_buffer_load_b128(wrs, ko + i * 1024, 0, 16);
#pragma unroll 1
    for (int spin = 0; spin < SPIN_MAX; ++spin) {
      const u32 bad = (gr[0][3] ^ tag) | (gr[1][3] ^ tag) | (gr[2][3] ^ tag) | (gr[3][3] ^ tag);
      if (__builtin_expect(!__any(bad != 0), 1)) break;
      STAMP(13);
      __builtin_amdgcn_s_sleep(SLEEP_AG1);
#pragma unroll
      for (int i = 0; i < 4; ++i)
        if (gr[i][3] != tag) gr[i] = __builtin_amdgcn_raw_buffer_load_b128(wrs, ko + i * 1024, 0, 16);
      asm volatile("" ::: "memory");
    }
    STAMP(2);
    {
      u32 m1 = gr[0][0], m2 = gr[0][1], m3 = 0, m4 = 0;
#pragma unroll
      for (int i = 2; i < 8; ++i) {
        const u32 k = gr[i >> 1][i & 1];
        if (i > 2) m4 = med3u(m3, m4, k);
        m3 = med3u(m2, m3, k);
        m2 = (i & 1) ? max(m2, k) : med3u(m1, m2, k);
        if (!(i & 1)) m1 = max(m1, k);
      }
      const u32 third = max(max(gr[0][2], gr[1][2]), max(gr[2][2], gr[3][2]));
      u32 W2;
      mykey = wave_top8(m1, m2, m3, m4, W2);
      xs_ld();
      SB();
      if (__builtin_expect(__any(m1 == 0u || (third | 0xffffu) > W2), 0)) {
        u4v ak[16];
#pragma unroll 1
        for (int spin = 0; spin < SPIN_MAX; ++spin) {
          u32 bad = 0;
#pragma unroll
          for (int i = 0; i < 16; ++i) {
            ak[i] = __builtin_amdgcn_raw_buffer_load_b128(wrs, WS_KALL + (i & 3) * 1024 + lane * 16, (i >> 2) * 4096, 16);
            bad |= ak[i][3] ^ tag;
          }
          if (!__any(bad != 0)) break;
          __builtin_amdgcn_s_sleep(8);
          asm volatile("" ::: "memory");
        }
        u32 prev = 0xffffffffu;
#pragma unroll 1
        for (int it = 0; it < 8; ++it) {
          u32 mm_ = 0;
#pragma unroll
          for (int i = 0; i < 32; ++i) {
            const u32 k = ak[i >> 1][i & 1];
            mm_ = max(mm_, k < prev ? k : 0u);
          }
          OPQ(mm_);
          prev = wave_max_u32(mm_);
          mykey = (lane >> 3) == it ? prev : mykey;
        }
        xs_ld();
      }
      OPQ(mykey);
    }
    SB();
    STAMP(3);
    if (l7 == 0) selw[lane >> 3] = mykey;
    {
      const u32 tn = now32();
      u32 yg = 0;
#pragma unroll
      for (int i = 0; i < 4; ++i) yg = max(yg, (gr[i][2] - (tn >> 2) - 1u) & 0xffu);
      const u32 tpub = tn - 4u * (255u - wave_max_u32(yg)) + DT_VIS;
      if (lane == 0) par[2] = later(tkey, tpub);
    }
    ARRIVE(C_SEL);
    if (b == 255 && lane == 0) __builtin_amdgcn_raw_buffer_store_b32(tag, wrs, WS_EPOCH, 0, 16);
    SB();
    {
      char* xp = smem + O_XS + (lane >> 3) * XS_PITCH + l7;
#pragma unroll
      for (int k = 0; k < 4; ++k) *(u4v*)(xp + k * 128) = xs[k];
      ARRIVE(C_XS);
      STAMP(6);
    }
    __builtin_amdgcn_s_setprio(2);
    auto ors = __builtin_amdgcn_make_buffer_rsrc((void*)kc[1], 0, L * D * 2, 0x00020000);
    const u32 rl = (lane + (xi & 15) * 4) & 63, un = (g * 4 + (lane & 3) + xi) & 15;
    u32 skip = 0;
#pragma unroll
    for (int k = 0; k < 8; ++k) {
      const u32 r = ~(u32)__builtin_amdgcn_readlane((int)mykey, 8 * k) & 2047u;
      skip |= (r & 63u) == rl ? 1u << (r >> 6) : 0u;
    }
    const char* op = smem + ((lane >> 2) & 3) * CHB + O_OUT + (un & 3) * 144 + (un >> 2) * 8;
    const u32 ob = rl * 2048 + cc * 8;
    const u32 tfast = (u32)__builtin_amdgcn_readfirstlane((int)par[2]) + DE_FAST;
#pragma unroll 1
    for (int t = 0; t < 8; ++t) {
      WAITA(CTLA(C_TILE) + t * 16 + (lane & 3) * 4);
      u2v yv[4];
#pragma unroll
      for (int e = 0; e < 4; ++e) yv[e] = tr_read(op + t * OT + e * 32);
      const bool slow = (int)(now32() - tfast) < 0;
#pragma unroll
      for (int e = 0; e < 4; ++e) {
        __builtin_amdgcn_raw_buffer_store_b64(yv[e], ors, ob | (((skip >> e) & 1u) << 30), t * 524288 + e * 131072, 0);
        if (slow) {
          SB(); __builtin_amdgcn_s_sleep(STSLP); SB();
          asm volatile("s_waitcnt vmcnt(%0)" :: "n"(STWIN - 1) : "memory");
        }
      }
      skip >>= 4;
    }
    STAMP(9);
    ROLE_END();
  }
  {
    ROLE("d");
        const int j = xg * 28 + xi;
    const int s1 = j >> 2, kq = j & 3;
    const bool own = xi < 28;
    const u64 wbase = kb[3];
    const int pitch = D * 2;
    auto hrs = __builtin_amdgcn_make_buffer_rsrc((void*)wbase, 0, own ? D * F * 2 : 0, 0x00020000);
    const int r8 = lane >> 3;
    const int vo = (64 * s1 * D + 256 * kq) * 2 + (8 * (r8 >> 2) + (r8 & 3)) * pitch + (((lane & 7) * 16) ^ (((r8 >> 1) & 3) * 32));
    auto dma = [&](int h, int sB, int pt, u32 dst) AI {
      const int m = h >> 1;
#pragma unroll
      for (int i = 0; i < 2; ++i)
        asm volatile("s_mov_b32 m0, %3\n\ts_nop 0\n\tbuffer_load_dwordx4 %0, %1, %2 offen nt lds"
                     :: "v"(vo), "s"(hrs), "s"((m & 1) * 32 * pt + (m >> 1) * sB + (16 * i + 4 * (h & 1)) * pt), "s"(dst + i * 1024) : "memory");
    };
    const int q = c16 >> 2, p = c16 & 3;
    int tro[4];
#pragma unroll
    for (int ct = 0; ct < 4; ++ct) tro[ct] = (4 * g + q) * 128 + ((ct ^ (((4 * g + q) >> 1) & 3)) * 32) + p * 8;
    const int tok = lane & 7;
    u4v wd[2][8];
    auto wd_ld = [&](int k, int cb) AI {
#pragma unroll
      for (int i = 0; i < 8; ++i) {
        const char* pp = smem + O_WD + cb * 8192 + (i >> 2) * 4096 + tro[i & 3];
        const u2v lo = tr_read(pp), hi = tr_read(pp + 2048);
        wd[k][i] = u4v{lo[0], lo[1], hi[0], hi[1]};
      }
    };
    u32 p2o, p2k[4], hlo, hhi;
    auto f2_setup = [&](int cb0) AI {
      p2o = WS_P2 + ((64 * kq + 16 * cb0 + (lane & 8) / 2 + g) * 56 + s1) * 128 + tok * 16;
      hlo = lbase + O_HB + ((lane & 8) ? 512 : g * 128 + tok * 16);
      hhi = lbase + O_HB + ((lane & 8) ? g * 128 + tok * 16 : 512);
#pragma unroll
      for (int i = 0; i < (cb0 == 2 ? 4 : 2); ++i) { p2k[i] = p2o + i * 8 * 7168; OPQ(p2k[i]); }
      OPQ(hlo); OPQ(hhi);
    };
    u4v hb[4];
    f4 r[4];
    auto hb_ld = [&]() AI {
      u32 cnt; int n_ = 1 << 20;
      asm volatile("1:\n\tds_read_b32 %4, %6\n\tds_read_b128 %0, %7\n\tds_read_b128 %1, %8\n\tds_read_b128 %2, %7 offset:528\n\tds_read_b128 %3, %8 offset:528\n\t"
                   "s_sub_u32 %5, %5, 1\n\ts_cbranch_scc1 2f\n\ts_waitcnt lgkmcnt(0)\n\tv_cmp_gt_u32 vcc, 2, %4\n\ts_cbranch_vccnz 1b\n2:\n\ts_waitcnt lgkmcnt(0)"
                   : "=&v"(hb[0]), "=&v"(hb[1]), "=&v"(hb[2]), "=&v"(hb[3]), "=&v"(cnt), "+s"(n_) : "v"(CTLA(C_HRDY)), "v"(hlo), "v"(hhi) : "vcc", "scc", "memory");
    };
    auto f2_mm = [&](int k) AI {
      const u4v* wp = wd[k];
      r[2 * k] = mma(wp[0], hb[0], f4{0.f, 0.f, 0.f, 0.f}); r[2 * k + 1] = mma(wp[2], hb[0], f4{0.f, 0.f, 0.f, 0.f});
      r[2 * k] = mma(wp[1], hb[1], r[2 * k]); r[2 * k + 1] = mma(wp[3], hb[1], r[2 * k + 1]);
      r[2 * k] = mma(wp[4], hb[2], r[2 * k]); r[2 * k + 1] = mma(wp[6], hb[2], r[2 * k + 1]);
      r[2 * k] = mma(wp[5], hb[3], r[2 * k]); r[2 * k + 1] = mma(wp[7], hb[3], r[2 * k + 1]);
    };
    auto f2_st = [&](int k, u32 t10) AI {
#pragma unroll
      for (int i = 2 * k; i < 2 * k + 2; ++i) {
        u4v v = __builtin_bit_cast(u4v, r[i]);
#pragma unroll
        for (int e = 0; e < 4; ++e) v[e] = __builtin_amdgcn_bitop3_b32(v[e], t10, TMASK, 0xd8);
        __builtin_amdgcn_raw_buffer_store_b128(v, wrs, p2k[i], 0, 16);
      }
    };
    {
      __builtin_amdgcn_s_setprio(3);
      const u32 ts = ts0 - 500u;
      u32 tl = ~0u;
      pace(later(ts0, now32() - DT_ARG - 15) + DT_HEAD);
      STAMP(3);
#pragma unroll 1
      for (int i = 0; i < 8 && tl >= 1500u; ++i) {
        const u2v hd = __builtin_amdgcn_raw_buffer_load_b64(wrs, WS_HEAD + (lane & 7) * 8, 0, 16);
        tl = wave_max_u32(hd[0] == ~hd[1] ? hd[0] - ts : ~0u);
        STAMP(14);
        asm volatile("" ::: "memory");
      }
      tl = tl < 1500u ? tl + ts : ts0;
      if (lane == 0) par[1] = tl;
      ARRIVE(C_TL);
      STAMP(2);
      if (own) {
        WAITS(C_WGU, 1);
#pragma unroll
        for (int h = 0; h < 16; ++h) {
          if (h == DSEL) WAITS(C_SEL, 1);
          if (h == DEV) WAITS(C_XL, 4);
          if (h >= DWIN) asm volatile("s_waitcnt vmcnt(%0)" :: "n"(2 * (DWIN - 1)) : "memory");
          if (h == 3 + DWIN) ARRIVE(C_WD);
          if (h == 7 + DWIN) ARRIVE(C_WD + 1);
          dma(h, 128, D * 2, lbase + O_WD + h * 2048);
        }
        f2_setup(2);
        const u32 t10 = par[0] & TMASK;
        asm volatile("s_waitcnt vmcnt(8)" ::: "memory");
        wd_ld(0, 2);
        const u32 hc = __hip_atomic_load(ctl + C_HRDY, __ATOMIC_RELAXED, __HIP_MEMORY_SCOPE_WORKGROUP);
        const bool early = __builtin_amdgcn_ballot_w64(hc < 2u) == 0ull;
        if (early) { hb_ld(); f2_mm(0); }
        asm volatile("s_waitcnt vmcnt(0)" ::: "memory");
        STAMP(5);
        wd_ld(1, 3);
        if (!early) { WAITS(C_HRDY, 1); hb_ld(); STAMP(11); f2_mm(0); }
        SB();
        f2_mm(1);
        f2_st(0, t10);
        SB();
        f2_st(1, t10);
        STAMP(12);
      }
      ROLE_END();
    }
  }
}

extern "C" void kernel_launch(void* const* d_in, const int* in_sizes, int n_in,
                              void* d_out, int out_size, void* d_ws, size_t ws_size,
                              hipStream_t stream) {
  k_main<<<256, 768, 0, stream>>>((const u16*)d_in[0], (const u16*)d_in[8], (const u16*)d_in[12],
                                  (const u16*)d_in[1], (const u16*)d_in[2], (const u16*)d_in[3],
                                  (const u16*)d_in[4], (const u16*)d_in[5], (const u16*)d_in[6],
                                  (const u16*)d_in[9], (const u16*)d_in[10], (const u16*)d_in[11],
                                  (char*)d_ws, (u16*)d_out);
}
```

```cpp
#include <hip/hip_runtime.h>
#include <stdint.h>

typedef unsigned short u16;
typedef unsigned u32;
typedef _Float16 h2 __attribute__((ext_vector_type(2)));
typedef _Float16 h8 __attribute__((ext_vector_type(8)));
typedef __bf16 b2 __attribute__((ext_vector_type(2)));
typedef __bf16 b8 __attribute__((ext_vector_type(8)));
typedef short s4 __attribute__((ext_vector_type(4)));
typedef float f2 __attribute__((ext_vector_type(2)));
typedef float f4 __attribute__((ext_vector_type(4)));
typedef u32 u2v __attribute__((ext_vector_type(2)));
typedef u32 u3v __attribute__((ext_vector_type(3)));
typedef u32 u4v __attribute__((ext_vector_type(4)));
typedef __attribute__((address_space(3))) s4* lds_s4p;

constexpr int L = 2048;
constexpr int D = 1024;
constexpr int NM = 128;
constexpr int F = 3584;
constexpr int NTAP = 1024;

constexpr int SEG_I = 576, SEG_O0 = 800, SEG_O1 = 880, SEG_SP = 896;

#define AI __attribute__((always_inline))
#define SB() __builtin_amdgcn_sched_barrier(0)
#define OPQ(x) asm volatile("" : "+v"(x))
#define STAMP0() do {} while (0)
#define STAMP(i)
#define STAMP_END()
#define STAMP_T0

__device__ __forceinline__ float bflo(u32 v) { return __uint_as_float(v << 16); }
__device__ __forceinline__ float bfhi(u32 v) { return __uint_as_float(v & 0xffff0000u); }
__device__ __forceinline__ u32 pk_bf(float a, float b) { f2 v = {a, b}; b2 r = __builtin_convertvector(v, b2); return __builtin_bit_cast(u32, r); }
__device__ __forceinline__ float rb1(float v) { return __uint_as_float(pk_bf(0.f, v)); }
__device__ __forceinline__ h8 as_h8(u4v v) { return __builtin_bit_cast(h8, v); }
__device__ __forceinline__ b8 as_b8(u4v v) { return __builtin_bit_cast(b8, v); }
__device__ __forceinline__ u2v tr_read(const char* p) {
  s4 t = __builtin_amdgcn_ds_read_tr16_b64_v4i16((lds_s4p)p);
  return __builtin_bit_cast(u2v, t);
}
template <int CTRL> __device__ __forceinline__ u32 dpp_u(u32 v) { return (u32)__builtin_amdgcn_update_dpp(0, (int)v, CTRL, 0xf, 0xf, false); }
template <int CTRL> __device__ __forceinline__ float dpp_f(float v) { return __uint_as_float(dpp_u<CTRL>(__float_as_uint(v))); }
__device__ __forceinline__ u32 wave_max_u32(u32 v) {
  v = max(v, dpp_u<0x111>(v)); v = max(v, dpp_u<0x112>(v)); v = max(v, dpp_u<0x114>(v)); v = max(v, dpp_u<0x118>(v));
  v = max(v, dpp_u<0x142>(v)); v = max(v, dpp_u<0x143>(v));
  return (u32)__builtin_amdgcn_readlane((int)v, 63);
}
#define DPM " row_mask:0xf bank_mask:0xf bound_ctrl:1\n\t"
#define T2S(HO, HI, LI, C) "v_max_u32_dpp " HO ", " HI ", " HI " " C DPM "v_min_u32_dpp %8, " HI ", " HI " " C DPM "v_max_u32_dpp %7, " LI ", " LI " " C DPM "v_max_u32 %7, %7, %8\n\t"
#define T2R(RM, REST) T2S("%5", "%0", "%1", "row_shr:1") T2S("%6", "%5", "%7", "row_shr:2") T2S("%5", "%6", "%7", "row_shr:4") T2S("%6", "%5", "%7", "row_shr:8") \
    T2S("%5", "%6", "%7", "row_bcast:15") T2S("%6", "%5", "%7", "row_bcast:31") \
    "v_readlane_b32 %9, %6, 63\n\tv_readlane_b32 %10, %7, 63\n\ts_nop 0\n\tv_mov_b32 %5, %9\n\tv_cmp_le_u32 %11, %10, %0\n\tv_cmp_le_u32 %12, %10, %1\n\tv_mov_b32 %6, %10\n\t" \
    "v_cndmask_b32 %0, %0, %1, %11\n\tv_mov_b32_dpp %4, %5 quad_perm:[0,1,2,3] row_mask:" RM " bank_mask:0x3\n\tv_cndmask_b32 %0, %0, %2, %12\n\t" \
    "v_mov_b32_dpp %4, %6 quad_perm:[0,1,2,3] row_mask:" RM " bank_mask:0xc\n\t" REST
#define T2P "v_cndmask_b32 %1, %1, %2, %11\n\tv_cndmask_b32 %2, %2, %3, %11\n\tv_cndmask_b32 %1, %1, %3, %12\n\tv_cndmask_b32 %3, %3, 0, %11\n\tv_cndmask_b32 %2, %2, 0, %12\n\t"
__device__ __forceinline__ u32 wave_top8(u32& m1, u32 m2, u32 m3, u32 m4, u32& w2) {
  u32 key, ta, tb, lo, t, w1;
  unsigned long long c1, c2;
  asm("s_nop 1\n\t" T2R("0x1", T2P) T2R("0x2", T2P) T2R("0x4", T2P) T2R("0x8", "") "s_nop 0"
      : "+v"(m1), "+v"(m2), "+v"(m3), "+v"(m4), "=&v"(key), "=&v"(ta), "=&v"(tb), "=&v"(lo), "=&v"(t), "=&s"(w1), "=&s"(w2), "=&s"(c1), "=&s"(c2));
  return key;
}

__device__ __forceinline__ float exp_bf(float v) {
  const float e = __builtin_amdgcn_exp2f(v * 1.44268798828125f);
  return fmaf(e, v * (7.0526077e-06f * 0.69314718f), e);
}
struct cf { f2 r, i; };
struct ch { h2 r, i; };
__device__ __forceinline__ cf cmul(cf a, cf b) { cf c; c.r = a.r * b.r - a.i * b.i; c.i = a.r * b.i + a.i * b.r; return c; }
__device__ __forceinline__ ch hmul(ch a, ch b) {
  ch c;
  asm("v_pk_mul_f16 %0, %2, %4\n\tv_pk_mul_f16 %1, %2, %5\n\tv_pk_fma_f16 %0, %3, %5, %0 neg_lo:[1,0,0] neg_hi:[1,0,0]\n\tv_pk_fma_f16 %1, %3, %4, %1"
      : "=&v"(c.r), "=&v"(c.i) : "v"(a.r), "v"(a.i), "v"(b.r), "v"(b.i));
  return c;
}
__device__ __forceinline__ cf csq(cf a) { cf c; c.r = a.r * a.r - a.i * a.i; c.i = (a.r + a.r) * a.i; return c; }
__device__ __forceinline__ ch toh(cf a) { ch c; c.r = __builtin_convertvector(a.r, h2); c.i = __builtin_convertvector(a.i, h2); return c; }
__device__ __forceinline__ ch tohc(cf a) { ch c; c.r = __builtin_convertvector(a.r, h2); c.i = __builtin_convertvector(-a.i, h2); return c; }
__device__ __forceinline__ h2 ash(u32 v) { return __builtin_bit_cast(h2, v); }
__device__ __forceinline__ cf cexp_p(f2 a2, f2 brev, float p) {
  f2 e = a2 * p, t = brev * p;
  f2 mag = {__builtin_amdgcn_exp2f(e[0]), __builtin_amdgcn_exp2f(e[1])};
  float r0 = __builtin_amdgcn_fractf(t[0]), r1 = __builtin_amdgcn_fractf(t[1]);
  cf c;
  c.r = mag * f2{__builtin_amdgcn_cosf(r0), __builtin_amdgcn_cosf(r1)};
  c.i = mag * f2{__builtin_amdgcn_sinf(r0), __builtin_amdgcn_sinf(r1)};
  return c;
}
__device__ __forceinline__ u32 med3u(u32 a, u32 b, u32 c) { u32 r; asm("v_med3_u32 %0, %1, %2, %3" : "=v"(r) : "v"(a), "v"(b), "v"(c)); return r; }
__device__ __forceinline__ f2 unbf(u32 v) { return f2{bflo(v), bfhi(v)}; }
__device__ __forceinline__ float sigm(float v) { return __builtin_amdgcn_rcpf(1.f + __builtin_amdgcn_exp2f(-1.44269504088896341f * v)); }

#define TS2(A, B, C, D) "v_xor_b32 %5, " A ", %9\n\tv_xor_b32 %6, " B ", %9\n\tv_xor_b32 %7, " C ", %9\n\tv_xor_b32 %8, " D ", %9\n\t" \
    "v_fma_f32 %1, %5, 1.0, %1\n\tv_fma_f32 %2, %6, 1.0, %2\n\tv_or3_b32 %0, %0, %5, %6\n\tv_fma_f32 %3, %7, 1.0, %3\n\tv_fma_f32 %4, %8, 1.0, %4\n\tv_or3_b32 %0, %0, %7, %8\n\t"
#define TSR TS2("%10", "%11", "%12", "%13") TS2("%14", "%15", "%16", "%17") TS2("%18", "%19", "%20", "%21")
#define TSI "=&v"(t0), "=&v"(t1), "=&v"(t2), "=&v"(t3) : "v"(t10), "v"(p[0]), "v"(p[1]), "v"(p[2]), "v"(p[3]), "v"(q[0]), "v"(q[1]), "v"(q[2]), "v"(q[3]), \
    "v"(r[0]), "v"(r[1]), "v"(r[2]), "v"(r[3])
__device__ __forceinline__ f4 tsum4(u32& bad, u32 t10, u4v s, u4v p, u4v q, u4v r) {
  float s0, s1, s2, s3; u32 t0, t1, t2, t3;
  asm volatile("v_xor_b32 %1, %22, %9\n\tv_xor_b32 %2, %23, %9\n\tv_xor_b32 %3, %24, %9\n\tv_xor_b32 %4, %25, %9\n\tv_or3_b32 %0, %0, %1, %2\n\tv_or3_b32 %0, %0, %3, %4\n\t" TSR
               : "+v"(bad), "=&v"(s0), "=&v"(s1), "=&v"(s2), "=&v"(s3), TSI, "v"(s[0]), "v"(s[1]), "v"(s[2]), "v"(s[3]));
  return f4{s0, s1, s2, s3};
}
__device__ __forceinline__ f4 tsum3(u32& bad, u32 t10, f4 s, u4v p, u4v q, u4v r) {
  float s0 = s[0], s1 = s[1], s2 = s[2], s3 = s[3]; u32 t0, t1, t2, t3;
  asm volatile(TSR : "+v"(bad), "+v"(s0), "+v"(s1), "+v"(s2), "+v"(s3), TSI);
  return f4{s0, s1, s2, s3};
}
__device__ __forceinline__ void silu4(f4& g, float nl2e) {
  float g0 = g[0], g1 = g[1], g2 = g[2], g3 = g[3], t0, t1, t2, t3;
  asm volatile("v_fma_f32 %4, %0, %8, 0\n\tv_fma_f32 %5, %1, %8, 0\n\tv_fma_f32 %6, %2, %8, 0\n\tv_fma_f32 %7, %3, %8, 0\n\t"
               "v_exp_f32 %4, %4\n\tv_exp_f32 %5, %5\n\tv_exp_f32 %6, %6\n\tv_exp_f32 %7, %7\n\t"
               "v_fma_f32 %4, %4, 1.0, 1.0\n\tv_fma_f32 %5, %5, 1.0, 1.0\n\tv_fma_f32 %6, %6, 1.0, 1.0\n\tv_fma_f32 %7, %7, 1.0, 1.0\n\t"
               "v_rcp_f32 %4, %4\n\tv_rcp_f32 %5, %5\n\tv_rcp_f32 %6, %6\n\tv_rcp_f32 %7, %7\n\t"
               "v_fma_f32 %0, %0, %4, 0\n\tv_fma_f32 %1, %1, %5, 0\n\tv_fma_f32 %2, %2, %6, 0\n\tv_fma_f32 %3, %3, %7, 0"
               : "+v"(g0), "+v"(g1), "+v"(g2), "+v"(g3), "=&v"(t0), "=&v"(t1), "=&v"(t2), "=&v"(t3) : "v"(nl2e));
  g = f4{g0, g1, g2, g3};
}

constexpr int KT_PITCH = 52;
constexpr int kt_off(int p) {
  int i = 20, j = 0;
  if (p < 256) { i = p & 7; j = p >> 3; }
  else if (p < 512) { int q = (p - 256) >> 1; i = 8 + (q & 3); j = q >> 2; }
  else if (p < 1024) { int q = (p - 512) >> 2; i = 12 + (q & 1) + 2 * (q >> 6); j = (q >> 1) & 31; }
  else if (p < 2048) { int q = (p - 1024) >> 3; i = 16 + (q >> 5); j = q & 31; }
  return j * KT_PITCH + i * 2;
}
constexpr int KT_ZERO = 32 * KT_PITCH + 16;
constexpr int rnd_lag(int m) {
  const int st = m < 256 ? 1 : m < 512 ? 2 : m < 1024 ? 4 : 8;
  int q = m / st;
  const int rem = m - q * st;
  if (2 * rem > st || (2 * rem == st && (q & 1))) ++q;
  return q * st;
}
constexpr int tap_entry(int e) {
  if (e < 544) {
    if (543 - e <= 525) return kt_off(rnd_lag(543 - e));
  } else if (e >= 560 && e < 752) {
    const int q = (e - 560) / 48, x = (e - 560) - q * 48;
    const int p = 512 + 16 * (31 - x) + 4 * (q + 1);
    if (x < 32 && p > 524) return kt_off(p);
  } else if (e >= 752 && e < 816) {
    return kt_off(1032 + 16 * (815 - e));
  } else if (e >= 832 && e < 896) {
    return kt_off(1040 + 16 * (895 - e));
  } else if (e == SEG_SP + 27 || e == SEG_SP + 28) {
    return kt_off(1024);
  }
  return KT_ZERO;
}
struct TapIdx { u16 v[NTAP]; };
constexpr TapIdx make_tapidx() {
  TapIdx t{};
  for (int e = 0; e < NTAP; ++e) t.v[e] = (u16)tap_entry(2 * ((e >> 4) + 64 * ((e >> 1) & 7)) + (e & 1));
  return t;
}
__device__ __attribute__((aligned(16))) const TapIdx g_tapidx = make_tapidx();

constexpr int TROW = 544;
constexpr int O_OUT = 0;
constexpr int OT = 576;
constexpr int O_U = O_OUT + 8 * OT;
constexpr int O_TAP = O_U + 4608;
constexpr int TAP_PITCH = 2 * NTAP + 64;
constexpr int CHB = O_TAP + 16 * TROW + 32;
static_assert(16 * TROW >= 4 * TAP_PITCH, "lds");
static_assert((CHB / 4) % 32 == 8, "banks");
#define DEV 3
#define DWIN 8
#define DSEL 0
constexpr int NSLOT = 5, WWIN = 4;
constexpr int O_GT = 4 * CHB;
constexpr int O_RING = O_GT + 8192;
constexpr int O_WD = O_RING + 2 * NSLOT * 2048;
constexpr int O_FAR = O_WD + 32768;
constexpr int O_XS = O_FAR + 16384;
constexpr int XS_PITCH = 544;
constexpr int O_HB = O_XS + 8 * XS_PITCH;
constexpr int O_PART = O_HB + 2 * 528;
constexpr int O_SEL = O_PART + 512;
constexpr int O_PAR = O_SEL + 32;
constexpr int O_CTL = O_PAR + 16;
constexpr int O_JUNK = O_CTL + 512;
constexpr int K_LDS = O_JUNK + 1024;
static_assert(DWIN == 8, "flags of the down tile");
static_assert(K_LDS <= 163840 && O_RING % 16 == 0, "lds");
enum { C_PART = 0, C_GP, C_PUB, C_SEL, C_XS, C_HRDY, C_WGU, C_TL, C_XL, C_WD = 10, C_TAPS = 12, C_Q = 16, C_FAR = 32, C_TILE = 48 };

#define CTLA(word) (lbase + O_CTL + (word) * 4)
#define ARRIVE(word) asm volatile("s_mov_b64 exec, 1\n\tds_add_u32 %0, %1\n\ts_mov_b64 exec, -1" :: "v"(CTLA(word)), "v"(1) : "memory")
#define WAITQ(word, target) do { u32 t_; int n_ = 1 << 20; asm volatile( \
    "1:\n\tds_read_b32 %0, %2\n\ts_sub_u32 %1, %1, 1\n\ts_cbranch_scc1 2f\n\ts_waitcnt lgkmcnt(0)\n\tv_cmp_gt_u32 vcc, %3, %0\n\ts_cbranch_vccnz 1b\n2:\n\ts_waitcnt lgkmcnt(0)" \
    : "=&v"(t_), "+s"(n_) : "v"(CTLA(word)), "n"(target) : "vcc", "scc", "memory"); } while (0)
#define WAITS(word, target) do { u32 t_; int n_ = 1 << 20; asm volatile( \
    "1:\n\tds_read_b32 %0, %2\n\ts_sub_u32 %1, %1, 1\n\ts_cbranch_scc1 2f\n\ts_waitcnt lgkmcnt(0)\n\tv_cmp_le_u32 vcc, %3, %0\n\ts_cbranch_vccnz 2f\n\ts_sleep 2\n\ts_branch 1b\n2:\n\ts_waitcnt lgkmcnt(0)" \
    : "=&v"(t_), "+s"(n_) : "v"(CTLA(word)), "n"(target) : "vcc", "scc", "memory"); } while (0)
#define BAR() asm volatile("s_waitcnt lgkmcnt(0)\n\ts_barrier" ::: "memory")
__device__ __forceinline__ u2v lds8(const char* p) { return __builtin_bit_cast(u2v, __hip_atomic_load((const unsigned long long*)p, __ATOMIC_RELAXED, __HIP_MEMORY_SCOPE_WORKGROUP)); }

constexpr int WS_EPOCH = 0;
constexpr int WS_HEAD = 1024;
constexpr int WS_REC = 4096;
constexpr int WS_FLG = 40960;
constexpr int WS_KALL = WS_REC + 8192;
constexpr int WS_KEY = 32768;
constexpr int WS_P1 = 65536;
constexpr int WS_P2 = WS_P1 + 56 * 16384;
constexpr int WS_XT = WS_P2 + 256 * 7168;
constexpr int XT_PITCH = 33280;
constexpr int WS_END = WS_XT + 128 * XT_PITCH;
constexpr int SPIN_MAX = 1 << 16;
constexpr u32 TMASK = 0x3ffu;

#define DT_HEAD 110
#define DT_ARG 45
#define WDELAY 60
#define DT_KEY 225
#define DT_REC 255
#define DT_FLG 365
#define DT_DRN 60
#define DT_VIS 40
#define DE_P1 385
#define DE_P2 620
#define DE_FAST 605
#define DT_MIN 25
#define DT_P1 100
#define DT_P2 130
#define SLEEP_AG1 12
#define SLEEP_G 6
#define STSLP 2
#define STWIN 4
#define H2_AT 9
#define CODE_B 28400

#define ROLE_END() do { STAMP_END(); asm volatile("s_endpgm" ::: "memory"); } while (0)
__device__ __forceinline__ u32 now32() { return (u32)__builtin_amdgcn_s_memrealtime(); }
__device__ __forceinline__ u32 later(u32 a, u32 b) { return (int)(b - a) > 0 ? b : a; }
__device__ __forceinline__ void pace(u32 t) {
#pragma unroll 1
  for (int i = 0; i < 1024 && (int)(now32() - t) < 0; ++i) __builtin_amdgcn_s_sleep(1);
}
__device__ __forceinline__ void pace2(u32 n, u32 t, int dmax) {
  int d = (int)(t - n);
  d = d < DT_MIN ? DT_MIN : (d > dmax ? dmax : d);
  pace(n + (u32)d);
}

#define ALL_OTHER "vcc", "scc", "memory", \
    "s0","s1","s2","s5","s6","s7","s33","s34","s35","s44","s45","s46","s47","s48","s49","s50","s51","s52","s53","s54","s55","s56","s57","s58","s59", \
    "s60","s61","s62","s63","s64","s65","s66","s67","s68","s69","s70","s71","s72","s73","s74","s75","s76","s77","s78","s79","s80","s81","s82","s83","s84", \
    "s85","s86","s87","s88","s89","s90","s91","s92","s93","s94","s95","s96","s97","s98","s99", \
    "v0","v1","v2","v3","v4","v5","v6","v7","v8","v9","v10","v11","v12","v13","v14","v15","v16","v17","v18","v19","v20","v21","v22","v23","v24","v25", \
    "v26","v27","v28","v29","v30","v31","v32","v33","v34","v35","v36","v37","v38","v39","v40","v41","v42","v43","v44","v45","v46","v47","v48","v49","v50", \
    "v51","v52","v53","v54","v55","v56","v57","v58","v59","v60","v61","v62","v63","v64","v65","v66","v67","v68","v69","v70","v71","v72","v73","v74","v75", \
    "v76","v77","v78","v79","v80","v81","v82","v83","v84","v85","v86","v87","v88","v89","v90","v91","v92","v93","v94","v95","v96","v97","v98","v99", \
    "v100","v101","v102","v103","v104","v105","v106","v107","v108","v109","v110","v111","v112","v113","v114","v115","v116","v117","v118","v119","v120", \
    "v121","v122","v123","v124","v125","v126","v127","v128","v129","v130","v131","v132","v133","v134","v135","v136","v137","v138","v139","v140","v141", \
    "v142","v143","v144","v145","v146","v147","v148","v149","v150","v151","v152","v153","v154","v155","v156","v157","v158","v159","v160","v161","v162", \
    "v163","v164","v165","v166","v167"
typedef __attribute__((address_space(1))) const u16* gp16;
typedef __attribute__((address_space(1))) const u32* gp32;
typedef __attribute__((address_space(1))) const u4v* gp128;
typedef unsigned long long u64;
typedef u64 q2 __attribute__((ext_vector_type(2)));
typedef u64 q4 __attribute__((ext_vector_type(4)));
typedef u64 q8 __attribute__((ext_vector_type(8)));

__global__ void __launch_bounds__(768, 3) k_main(
    const u16* x_, const u16* w_r_, const u16* gate_w_, const u16* logA_, const u16* Aim_, const u16* Cre_, const u16* Cim_,
    const u16* log_dt_, const u16* Dskip_, const u16* w_gate_, const u16* w_up_, const u16* w_down_, char* wsp_, u16* out_) {
  __shared__ __attribute__((aligned(256))) char smem[K_LDS];
  const u32 lbase = (u32)(uintptr_t)(__attribute__((address_space(3))) char*)smem;
#define XDMA(OFF) "global_load_lds_dwordx4 v1, s[8:9] offset:" #OFF " nt\n\ts_addk_i32 m0, 896\n\t"
  asm volatile("s_load_dwordx16 s[8:23], %0, 0\n\tv_readfirstlane_b32 s3, %2\n\ts_load_dwordx8 s[24:31], %0, 64\n\ts_load_dwordx4 s[36:39], %0, 96\n\t"
               "s_memrealtime s[40:41]\n\t" STAMP_T0 "s_mov_b32 s4, %1\n\ts_lshr_b32 s3, s3, 6\n\t"
               "s_cmp_lt_u32 s3, 4\n\ts_cbranch_scc1 .Lrows\n\ts_barrier\n\t"
               "s_cmp_ge_u32 s3, 8\n\ts_cbranch_scc1 .Lrole_g\n\ts_cmp_eq_u32 s3, 7\n\ts_cbranch_scc1 .Lrole_d\n\t"
               "s_cmp_ge_u32 s3, 5\n\ts_cbranch_scc1 .Lrole_h\n\ts_branch .Lrole_c\n.Lrows:\n\t"
               "s_lshr_b32 s5, s4, 3\n\tv_lshlrev_b32 v3, 2, %2\n\ts_mov_b32 m0, %4\n\tv_lshl_or_b32 v3, s5, 10, v3\n\ts_getpc_b64 s[6:7]\n\t"
               "v_min_u32 v3, %3, v3\n\tv_lshrrev_b32 v1, 3, %2\n\tglobal_load_lds_dword v3, s[6:7]\n\t"
               "v_and_b32 v2, 6, v1\n\ts_and_b32 s34, s4, 7\n\tv_xor_b32 v2, v2, %2\n\ts_lshl_b32 s34, s34, 5\n\tv_and_b32 v2, 7, v2\n\ts_add_u32 s5, s5, s34\n\t"
               "v_and_b32 v1, 7, v1\n\ts_lshl_b32 s5, s5, 5\n\tv_lshlrev_b32 v1, 11, v1\n\ts_add_u32 s5, s5, s3\n\tv_lshl_or_b32 v1, v2, 4, v1\n\t"
               "s_mul_i32 s34, s3, %5\n\tv_lshl_add_u32 v1, s5, 9, v1\n\ts_add_u32 m0, s34, %6\n\tv_and_b32 v2, 31, %2\n\ts_waitcnt lgkmcnt(0)\n\t"
               XDMA(0) "s_lshl_b32 s5, s3, 5\n\t" XDMA(128) "v_add_lshl_u32 v2, s5, v2, 4\n\t" XDMA(256) "s_lshl_b32 s5, s3, 10\n\t"
               "global_load_lds_dwordx4 v1, s[8:9] offset:384 nt\n\ts_add_u32 m0, s5, %7\n\ts_mov_b32 exec_hi, 0\n\t"
               "global_load_lds_dwordx4 v2, s[10:11]\n\ts_addk_i32 m0, 512\n\ts_nop 0\n\tglobal_load_lds_dwordx4 v2, s[12:13]\n\t"
               "s_mov_b32 m0, %8\n\tv_mov_b32 v3, 0\n\tglobal_load_lds_dword v3, s[36:37]\n\ts_mov_b32 exec_hi, -1"
               :: "s"(__builtin_amdgcn_kernarg_segment_ptr()), "s"(blockIdx.x), "v"(threadIdx.x), "n"(CODE_B - 256), "n"(O_JUNK), "n"(CHB), "n"(O_U + 512), "n"(O_FAR), "n"(O_XS)
               : "s3", "s4", "s5", "s6", "s7", "s8", "s9", "s10", "s11", "s12", "s13", "s14", "s15", "s16", "s17", "s18", "s19", "s20", "s21", "s22", "s23",
                 "s24", "s25", "s26", "s27", "s28", "s29", "s30", "s31", "s34", "s36", "s37", "s38", "s39", "s40", "s41", "s42", "s43", "v1", "v2", "v3", "scc", "memory");
  int lane, c16, g, w, b, xg, xi, cg, cc, c, d;
  char* ch_;
  q8 ka; q4 kb; q2 kc, kt;
  u32* ctl = (u32*)(smem + O_CTL);
  u32* par = (u32*)(smem + O_PAR);
#define IDS() do { int z_ = 0; OPQ(z_); lane = __builtin_amdgcn_mbcnt_hi(~0u, __builtin_amdgcn_mbcnt_lo(~0u, z_)); c16 = lane & 15; g = lane >> 4; } while (0)
#define ROLE(name) \
  asm volatile(".Lrole_" name ":\n\ts_waitcnt lgkmcnt(0)" : "={s[8:23]}"(ka), "={s[24:31]}"(kb), "={s[36:39]}"(kc), "={s[40:43]}"(kt), "={s3}"(w), "={s4}"(b) :: ALL_OTHER); \
  if (__builtin_expect(w > 11, 0)) asm volatile("s_endpgm" ::: "memory");        \
  IDS(); \
  xg = b & 7; xi = b >> 3;                          \
  cg = xg * 32 + xi;                                \
  cc = (xi >> 4) * 128 + xg * 16 + (xi & 15);       \
  c = w & 3; ch_ = smem + c * CHB; d = cc * 4 + c; \
  const u32 ts0 = (u32)kt[0]; \
  auto wrs = __builtin_amdgcn_make_buffer_rsrc((void*)kc[0], 0, WS_END, 0x00020000); \
  STAMP0()

  const char* tapb;
  const char* ub;
  char* erp;
  const char* eup;
  const char* egp;
  u4v At[17], band[4];
  float dm1;
  f4 acc[8];
  auto conv_setup = [&]() AI {
    tapb = ch_ + O_TAP + ((15 - c16) & 3) * TAP_PITCH + ((15 - c16) & ~3) * 2;
    ub = ch_ + O_U - 32 + c16 * 32 + g * 16;
    erp = ch_ + O_OUT + g * 144 + c16 * 8;
    eup = ch_ + O_U + 512 + c16 * 32 + g * 8;
    egp = smem + O_GT + lane * 16;
  };
  auto band_setup = [&]() AI {
#pragma unroll
    for (int k = 0; k < 4; ++k) {
      const int ctr = k == 0 ? 12 : 8, hw = k + 1;
      const u32 m = ((1u << (2 * hw + 1)) - 1u) << (ctr - hw);
      const u32 bits = (m << c16) >> (8 * g);
#pragma unroll
      for (int j = 0; j < 4; ++j)
        band[k][j] = (__builtin_amdgcn_sbfe(bits, 2 * j, 1) & 0x3f80u) | (__builtin_amdgcn_sbfe(bits, 2 * j + 1, 1) & 0x3f800000u);
      OPQ(band[k]);
    }
  };
  auto frag2 = [&](const char* pa, const char* pb) AI { const u2v lo = lds8(pa), hi = lds8(pb); return u4v{lo[0], lo[1], hi[0], hi[1]}; };
  auto atload = [&](int s0, int s1) AI {
#pragma unroll
    for (int s = s0; s < s1; ++s) At[s] = frag2(tapb + 64 * s + g * 16, tapb + 64 * s + g * 16 + 8);
  };
  auto bin = [&](int pair, int be) AI { const char* p = tapb + g * 8 + (SEG_I + 96 * pair - 16 * be) * 2; return frag2(p, p + 96); };
  auto bout = [&](int be) AI { const char* p = tapb + g * 8 + (SEG_O0 - 16 * be) * 2; return frag2(p, p + (SEG_O1 - SEG_O0) * 2); };
  auto asp = [&]() AI { return frag2(tapb + SEG_SP * 2 + g * 16, tapb + SEG_SP * 2 + g * 16 + 8); };
  auto mma = [&](u4v a, u4v bq, f4 cin) AI { return __builtin_amdgcn_mfma_f32_16x16x32_bf16(as_b8(a), as_b8(bq), cin, 0, 0, 0); };
  auto uld = [&](int sg) AI { return *(const u4v*)(ub + 512 + sg * 64); };
  auto uld24 = [&](int un) AI { return *(const u4v*)(ub + 512 + un * 512 - 16); };
  u2v euw[8], eo[8]; f4 egv[8], efv[8];
  float eu[8][4], ev[8][4], ea[8][4];
  u4v Fq[57], ru[6], wa_[6][3];
  f4 wq[6][6];
  u32 pk[12], one, qa, fa;
  char* tfp; char* fp_;
#define LOPQ(p_) do { u32 a_ = (u32)(uintptr_t)(__attribute__((address_space(3))) const char*)p_; OPQ(a_); p_ = (decltype(p_))(__attribute__((address_space(3))) char*)(uintptr_t)a_; } while (0)
  auto m_setup = [&]() AI {
    tfp = smem + (lane == 0 ? O_CTL + c * 4 : O_JUNK + lane * 4);
    fp_ = smem + O_FAR + c * 4096 + lane * 16;
    qa = CTLA(C_Q) + (lane & 3) * 4; fa = CTLA(C_FAR + c); one = 1u;
    OPQ(qa); OPQ(fa); OPQ(one);
    LOPQ(tfp); LOPQ(fp_); LOPQ(erp); LOPQ(eup); LOPQ(egp); LOPQ(ub);
  };
#define WAITA(addr) do { u32 t_; int n_ = 1 << 20; asm volatile( \
    "1:\n\tds_read_b32 %0, %2\n\ts_sub_u32 %1, %1, 1\n\ts_cbranch_scc1 2f\n\ts_waitcnt lgkmcnt(0)\n\tv_cmp_eq_u32 vcc, 0, %0\n\ts_cbranch_vccnz 1b\n2:\n\ts_waitcnt lgkmcnt(0)" \
    : "=&v"(t_), "+s"(n_) : "v"(addr) : "vcc", "scc", "memory"); } while (0)
#define MF(d_, a_, b_) asm volatile("v_mfma_f32_16x16x32_bf16 %0, %1, %2, %0" : "+v"(d_) : "v"(a_), "v"(b_)); SB()
#define MF0(d_, a_, b_) asm volatile("v_mfma_f32_16x16x32_bf16 %0, %1, %2, 0" : "=&v"(d_) : "v"(a_), "v"(b_)); SB()
#define NOP(n_) asm volatile("s_nop " #n_); SB()
#define FL(i_, sg_) Fq[i_] = uld(sg_); SB()
#define RL(un_) ru[un_] = uld24(un_); SB()
#define AL(s_) atload(s_, s_ + 1); SB()
#define ULD(t_) euw[t_] = *(const u2v*)(eup + t_ * 512); SB()
#define GLD(t_) egv[t_] = *(const f4*)(egp + t_ * 1024); SB()
#define FLD(t_) efv[t_] = *(const f4*)(fp_ + (t_ - 2) * 1024); SB()
#define FST(un_) *(f4*)(fp_ + un_ * 1024) = acc[un_ + 2]; SB()
#define EUN(t_, e_) eu[t_][e_] = (e_ & 1) ? bfhi(euw[t_][e_ >> 1]) : bflo(euw[t_][e_ >> 1]); OPQ(eu[t_][e_]); ev[t_][e_] = eu[t_][e_]; SB()
#define EA1(t_, e_) ea[t_][e_] = fmaf(eu[t_][e_], dm1, acc[t_][e_]); OPQ(ea[t_][e_]); SB()
#define EFA(t_, e_) ev[t_][e_] = fmaf(egv[t_][e_], efv[t_][e_], eu[t_][e_]); OPQ(ev[t_][e_]); SB()
#define EA2(t_, e_) ea[t_][e_] = fmaf(egv[t_][e_], ea[t_][e_], ev[t_][e_]); OPQ(ea[t_][e_]); SB()
#define EPK(t_, k_) { u32 v_ = pk_bf(ea[t_][2 * k_], ea[t_][2 * k_ + 1]); OPQ(v_); eo[t_][k_] = v_; } SB()
#define OST(t_) *(u2v*)(erp + t_ * OT) = eo[t_]; SB()
#define WPK(un_, p_, j_) { u32 v_ = pk_bf(wq[un_][2 * p_ + j_ / 2][2 * (j_ & 1)], wq[un_][2 * p_ + j_ / 2][2 * (j_ & 1) + 1]); OPQ(v_); wa_[un_][p_][j_] = v_; } SB()
#define FLAG(w_) asm volatile("" ::: "memory"); *(u32*)(tfp + (w_) * 4) = one; SB()
#define PEEK(i_, a_) pk[i_] = __hip_atomic_load((__attribute__((address_space(3))) u32*)(uintptr_t)(a_), __ATOMIC_RELAXED, __HIP_MEMORY_SCOPE_WORKGROUP); SB()
#define CHECK(i_, a_) if (__builtin_expect(__builtin_amdgcn_ballot_w64(pk[i_] == 0u) != 0ull, 0)) WAITA(a_); asm volatile("" ::: "memory"); SB()
#define MIX2(nm, nd, nv) _Pragma("unroll") for (int r_ = 0; r_ < (nm); ++r_) { __builtin_amdgcn_sched_group_barrier(0x008, 1, 0); \
    if (nd) __builtin_amdgcn_sched_group_barrier(0x100, (nd), 0); if (nv) __builtin_amdgcn_sched_group_barrier(0x002, (nv), 0); }

  {
    ROLE("x");
    const int xw = w;
    char* img = ch_ + O_U + 512;
    STAMP(11);
    __builtin_amdgcn_s_setprio(2);
    { u32 zz = 0u; OPQ(zz); if (lane < 32) ctl[32 * xw + lane] = zz; }
    if (xi == 31 && w == 0) { const u32 th = later(ts0, now32() - DT_ARG); if (lane == 0) __builtin_amdgcn_raw_buffer_store_b64(u2v{th, ~th}, wrs, WS_HEAD + xg * 8, 0, 16); }
    BAR();
    STAMP(12);
    const char* wp; const char* fp; const char* tp0; const char* tp1; char* pp;
    {
      const int r = lane & 7, q = (lane >> 2) & 3;
      fp = img + r * 128 + ((((lane & 8) / 2 + g) ^ (r & 6)) * 16);
      wp = smem + O_FAR + xw * 1024 + (lane & 1) * 512 + (lane & 4) * 16 + g * 16;
      tp0 = img + q * 128 + (((2 * g + ((lane >> 1) & 1)) ^ (q & 2)) * 16) + (lane & 1) * 8;
      tp1 = img + 512 + q * 128 + (((2 * g + ((lane >> 1) & 1)) ^ ((q & 2) + 4)) * 16) + (lane & 1) * 8;
      pp = smem + ((lane >> 3) == 0 || (lane >> 3) == 3 ? O_PART + r * 64 + xw * 16 + g * 8 : O_JUNK + lane * 8);
    }
    const int so = WS_XT + (32 * xw + (lane >> 3)) * XT_PITCH + cg * 128 + (lane & 7) * 16, sstep = 8 * XT_PITCH;
    u32 lo_ = WS_XT + (cc >> 1) * XT_PITCH + (16 * xw + (lane >> 2)) * 128 + (cc & 1) * 64 + (lane & 3) * 16;
    OPQ(lo_);
    SB();
    asm volatile("s_waitcnt vmcnt(0)" ::: "memory");
    STAMP(1);
    {
      u4v wa[4], fb[4]; u2v ta[4], tb[4];
#pragma unroll
      for (int s = 0; s < 4; ++s) { wa[s] = *(const u4v*)(wp + s * 128); fb[s] = *(const u4v*)(fp + s * 1024); }
      SB();
      f4 r = {0.f, 0.f, 0.f, 0.f};
#pragma unroll
      for (int s = 0; s < 4; ++s) r = mma(wa[s], fb[s], r);
      *(f2*)pp = f2{r[0], r[1]};
      ARRIVE(C_PART);
      SB();
#pragma unroll
      for (int s = 0; s < 4; ++s) { ta[s] = tr_read(tp0 + s * 1024); tb[s] = tr_read(tp1 + s * 1024); }
      SB();
      WAITQ(C_GP, 1);
#pragma unroll
      for (int s = 0; s < 4; ++s) __builtin_amdgcn_raw_buffer_store_b128(u4v{ta[s][0], ta[s][1], tb[s][0], tb[s][1]}, wrs, so + s * sstep, 0, 16);
    }
    STAMP(2);
    const u32 dskw = ((gp16)kb[0])[d];
    conv_setup();
    band_setup();
    const u32 tag = par[0];
    ARRIVE(C_PUB);
    asm volatile("s_waitcnt vmcnt(0)" ::: "memory");
    if (lane == 0) __builtin_amdgcn_raw_buffer_store_b32(tag, wrs, WS_FLG + xw * 1024 + cg * 4, 0, 16);
    STAMP(3);
    const u32 tdr = now32();
    dm1 = bflo(dskw) - 1.f;
    WAITS(C_TL, 1);
    const u32 tl0 = (u32)__builtin_amdgcn_readfirstlane((int)par[1]);
    u3v fq[2];
    auto rec_ld = [&]() AI {
#pragma unroll
      for (int k = 0; k < 2; ++k) fq[k] = __builtin_amdgcn_raw_buffer_load_b96(wrs, WS_REC + xw * 2048 + k * 1024 + lane * 16, 0, 16);
    };
    pace(tl0 + DT_REC);
    rec_ld();
    SB();
    pace(later(tl0 + DT_FLG, tdr + DT_DRN));
    const u32 fl = __builtin_amdgcn_raw_buffer_load_b32(wrs, WS_FLG + (cc >> 6) * 1024 + xw * 64 + g * 256 + c16 * 4, 0, 16);
    SB();
    {
      if (__builtin_expect(__any(fq[0][2] != tag || fq[1][2] != tag), 0)) {
#pragma unroll 1
        for (int spin = 0; spin < SPIN_MAX; ++spin) {
          __builtin_amdgcn_s_sleep(SLEEP_G);
          rec_ld();
          if (!__any(fq[0][2] != tag || fq[1][2] != tag)) break;
          asm volatile("" ::: "memory");
        }
      }
      char* gp = smem + O_GT + xw * 2048 + (lane & 3) * 256 + (lane >> 2) * 16;
#pragma unroll
      for (int k = 0; k < 2; ++k) *(f4*)(gp + k * 1024) = f4{bflo(fq[k][0]), bfhi(fq[k][0]), bflo(fq[k][1]), bfhi(fq[k][1])};
    }
    SB();
    u4v xq[4];
    bool late[4];
    {
      const u64 bad = __builtin_amdgcn_ballot_w64(fl != tag);
      asm volatile("" ::: "memory");
#pragma unroll
      for (int k = 0; k < 4; ++k) {
        late[k] = ((bad >> (16 * k)) & 0xffffull) != 0ull;
        xq[k] = __builtin_amdgcn_raw_buffer_load_b128(wrs, lo_ | (late[k] ? 1u << 30 : 0u), k * 8192, 16);
        SB();
      }
    }
    ARRIVE(C_XL);
    STAMP(4);
    WAITQ(C_TAPS + c, 1);
    u4v Bi[2][3], Bo[4], As;
#pragma unroll
    for (int be = 0; be < 3; ++be) { Bi[0][be] = bin(0, be); Bi[1][be] = bin(1, be); }
#pragma unroll
    for (int be = 0; be < 4; ++be) Bo[be] = bout(be);
    As = asp();
    WAITQ(C_PUB, 4);
    char* dp = smem + (lane & 3) * CHB + O_U + 512 + xw * 256 + (lane >> 2) * 16;
    m_setup();
    auto stage = [&](int k) AI {
      SB();
      if (__builtin_expect(late[k], 0)) {
#pragma unroll 1
        for (int spin = 0; spin < SPIN_MAX; ++spin) {
          STAMP(14);
          const u32 f1 = __builtin_amdgcn_raw_buffer_load_b32(wrs, WS_FLG + (cc >> 6) * 1024 + xw * 64 + k * 256 + c16 * 4, 0, 16);
          if (!__any(f1 != tag)) break;
          __builtin_amdgcn_s_sleep(SLEEP_G);
          asm volatile("" ::: "memory");
        }
        asm volatile("" ::: "memory");
        xq[k] = __builtin_amdgcn_raw_buffer_load_b128(wrs, lo_, k * 8192, 16);
        asm volatile("s_waitcnt vmcnt(0)" : "+v"(xq[k]) :: "memory");
      }
      *(u4v*)(dp + k * 1024) = xq[k];
      FLAG(C_Q + k * 4);
    };
    stage(0);
    stage(1);
    STAMP(5);
    __builtin_amdgcn_s_setprio(0);
    WAITA(qa);
    SB();
    FL(0, 0); RL(0); FL(8, 8); RL(1);
    MF0(wq[0][0], Fq[0], band[0]);
    MF0(wq[0][1], Fq[0], band[1]);
    MF0(wq[0][2], ru[0], band[0]);
    MF0(wq[0][3], ru[0], band[1]);
    MF0(wq[0][4], Fq[0], band[2]);
    MF0(wq[0][5], ru[0], band[3]);
    MF0(acc[4], As, Fq[0]); PEEK(6, qa + 16);
    MF0(wq[1][0], Fq[8], band[0]); WPK(0, 0, 0); WPK(0, 0, 1);
    MF0(wq[1][1], Fq[8], band[1]); WPK(0, 0, 2); WPK(0, 0, 3);
    MF0(wq[1][2], ru[1], band[0]); WPK(0, 1, 0); WPK(0, 1, 1);
    MF0(wq[1][3], ru[1], band[1]); WPK(0, 1, 2); WPK(0, 1, 3);
    MF0(wq[1][4], Fq[8], band[2]); CHECK(6, qa + 16);
    MF0(wq[1][5], ru[1], band[3]); WPK(0, 2, 0); WPK(0, 2, 1);
    MF0(acc[5], As, Fq[8]); WPK(0, 2, 2); WPK(0, 2, 3);
    MF0(acc[2], wa_[0][0], Bi[0][0]); FL(16, 16); RL(2);
    MF(acc[2], wa_[0][1], Bi[1][0]); WPK(1, 0, 0); WPK(1, 0, 1);
    MF0(acc[3], wa_[0][0], Bi[0][1]); WPK(1, 0, 2); WPK(1, 0, 3);
    MF(acc[3], wa_[0][1], Bi[1][1]); WPK(1, 1, 0); WPK(1, 1, 1);
    MF(acc[4], wa_[0][0], Bi[0][2]); FST(0); FLAG(C_FAR + 0 * 4);
    MF(acc[4], wa_[0][1], Bi[1][2]); WPK(1, 1, 2); WPK(1, 1, 3);
    MF(acc[4], wa_[0][2], Bo[0]); WPK(1, 2, 0); WPK(1, 2, 1);
    MF(acc[5], wa_[0][2], Bo[1]); WPK(1, 2, 2); WPK(1, 2, 3);
    MF0(acc[6], wa_[0][2], Bo[2]);
    MF0(acc[7], wa_[0][2], Bo[3]); stage(2);
    MF0(wq[2][0], Fq[16], band[0]);
    MF0(wq[2][1], Fq[16], band[1]); stage(3);
    MF0(wq[2][2], ru[2], band[0]);
    MF0(wq[2][3], ru[2], band[1]);
    MF0(wq[2][4], Fq[16], band[2]); FL(24, 24); RL(3);
    MF0(wq[2][5], ru[2], band[3]); WPK(2, 0, 0); WPK(2, 0, 1);
    MF(acc[6], As, Fq[16]); WPK(2, 0, 2); WPK(2, 0, 3);
    MF(acc[3], wa_[1][0], Bi[0][0]); WPK(2, 1, 0); WPK(2, 1, 1);
    MF(acc[3], wa_[1][1], Bi[1][0]); WPK(2, 1, 2); WPK(2, 1, 3);
    MF(acc[4], wa_[1][0], Bi[0][1]); WPK(2, 2, 0); WPK(2, 2, 1);
    MF(acc[4], wa_[1][1], Bi[1][1]); WPK(2, 2, 2); WPK(2, 2, 3);
    MF(acc[5], wa_[1][0], Bi[0][2]); FST(1); FLAG(C_FAR + 1 * 4);
    MF(acc[5], wa_[1][1], Bi[1][2]); PEEK(7, qa + 32);
    MF(acc[5], wa_[1][2], Bo[0]);
    MF(acc[6], wa_[1][2], Bo[1]);
    MF(acc[7], wa_[1][2], Bo[2]);
    MF0(wq[3][0], Fq[24], band[0]);
    MF0(wq[3][1], Fq[24], band[1]); CHECK(7, qa + 32);
    MF0(wq[3][2], ru[3], band[0]);
    MF0(wq[3][3], ru[3], band[1]);
    MF0(wq[3][4], Fq[24], band[2]); FL(32, 32); RL(4);
    MF0(wq[3][5], ru[3], band[3]); WPK(3, 0, 0); WPK(3, 0, 1);
    MF(acc[7], As, Fq[24]); WPK(3, 0, 2); WPK(3, 0, 3);
    MF(acc[4], wa_[2][0], Bi[0][0]); WPK(3, 1, 0); WPK(3, 1, 1);
    MF(acc[4], wa_[2][1], Bi[1][0]); WPK(3, 1, 2); WPK(3, 1, 3);
    MF(acc[5], wa_[2][0], Bi[0][1]); WPK(3, 2, 0); WPK(3, 2, 1);
    MF(acc[5], wa_[2][1], Bi[1][1]); FL(40, 40); RL(5);
    MF(acc[6], wa_[2][0], Bi[0][2]); FST(2); FLAG(C_FAR + 2 * 4);
    MF(acc[6], wa_[2][1], Bi[1][2]); WPK(3, 2, 2); WPK(3, 2, 3);
    MF(acc[6], wa_[2][2], Bo[0]); AL(0);
    MF(acc[7], wa_[2][2], Bo[1]); AL(1);
    MF0(wq[4][0], Fq[32], band[0]); AL(2);
    MF0(wq[4][1], Fq[32], band[1]); AL(3);
    MF0(wq[4][2], ru[4], band[0]); AL(4);
    MF0(wq[4][3], ru[4], band[1]); WPK(4, 0, 0); WPK(4, 0, 1);
    MF(acc[5], wa_[3][0], Bi[0][0]); WPK(4, 0, 2); WPK(4, 0, 3);
    MF(acc[5], wa_[3][1], Bi[1][0]); WPK(4, 1, 0); WPK(4, 1, 1);
    MF(acc[6], wa_[3][0], Bi[0][1]); WPK(4, 1, 2); WPK(4, 1, 3);
    MF(acc[6], wa_[3][1], Bi[1][1]); AL(5);
    MF(acc[7], wa_[3][0], Bi[0][2]); FST(3); FLAG(C_FAR + 3 * 4);
    MF(acc[7], wa_[3][1], Bi[1][2]); AL(6);
    MF(acc[7], wa_[3][2], Bo[0]); FL(33, 33); AL(7);
    MF0(wq[5][0], Fq[40], band[0]); FL(34, 34); AL(8);
    MF0(wq[5][1], Fq[40], band[1]); FL(35, 35); AL(9);
    MF0(wq[5][2], ru[5], band[0]); WPK(5, 0, 0); WPK(5, 0, 1);
    MF0(wq[5][3], ru[5], band[1]); AL(10);
    MF(acc[6], wa_[4][0], Bi[0][0]); PEEK(8, qa + 48); FL(36, 36);
    MF(acc[6], wa_[4][1], Bi[1][0]); FL(37, 37); WPK(5, 0, 2);
    MF(acc[7], wa_[4][0], Bi[0][1]); FL(38, 38); WPK(5, 0, 3);
    MF(acc[7], wa_[4][1], Bi[1][1]); FL(39, 39); WPK(5, 1, 0);
    MF(acc[6], At[0], Fq[32]); WPK(5, 1, 1); WPK(5, 1, 2);
    MF(acc[6], At[1], Fq[33]); CHECK(8, qa + 48);
    MF(acc[6], At[2], Fq[34]); WPK(5, 1, 3); FL(41, 41);
    MF(acc[6], At[3], Fq[35]); AL(11);
    MF(acc[7], wa_[5][0], Bi[0][0]); FL(42, 42); AL(12);
    MF(acc[7], wa_[5][1], Bi[1][0]); AL(13);
    MF(acc[6], At[4], Fq[36]); FL(43, 43); AL(14);
    MF(acc[6], At[5], Fq[37]); AL(15);
    MF(acc[6], At[6], Fq[38]); FL(44, 44); AL(16);
    MF(acc[6], At[7], Fq[39]);
    MF(acc[6], At[8], Fq[40]); FL(45, 45);
    MF(acc[7], At[0], Fq[40]);
    MF(acc[6], At[9], Fq[41]); FL(46, 46);
    MF(acc[7], At[1], Fq[41]);
    MF(acc[6], At[10], Fq[42]); FL(47, 47);
    MF(acc[7], At[2], Fq[42]);
    MF(acc[6], At[11], Fq[43]); FL(48, 48);
    MF(acc[7], At[3], Fq[43]);
    MF(acc[6], At[12], Fq[44]); FL(49, 49); ULD(6);
    MF(acc[7], At[4], Fq[44]); FL(50, 50); GLD(6);
    MF(acc[6], At[13], Fq[45]); FL(51, 51);
    MF(acc[7], At[5], Fq[45]); FL(52, 52);
    MF(acc[6], At[14], Fq[46]); FL(53, 53);
    MF(acc[7], At[6], Fq[46]); FL(54, 54); EUN(6, 0);
    MF(acc[6], At[15], Fq[47]); FL(55, 55); EUN(6, 1);
    MF(acc[7], At[7], Fq[47]); FL(56, 56); EUN(6, 2);
    MF(acc[6], At[16], Fq[48]); EUN(6, 3);
    MF(acc[7], At[8], Fq[48]); ULD(7); GLD(7);
    MF(acc[7], At[9], Fq[49]);
    MF(acc[7], At[10], Fq[50]);
    MF(acc[7], At[11], Fq[51]);
    MF(acc[7], At[12], Fq[52]); EA1(6, 0); EA1(6, 1); EA1(6, 2); EA1(6, 3);
    MF(acc[7], At[13], Fq[53]); EA2(6, 0); EA2(6, 1); EA2(6, 2); EA2(6, 3);
    MF(acc[7], At[14], Fq[54]); EPK(6, 0); EPK(6, 1);
    MF(acc[7], At[15], Fq[55]); OST(6); FLAG(C_TILE + 6 * 4);
    MF(acc[7], At[16], Fq[56]); EUN(7, 0); EUN(7, 1); EUN(7, 2); EUN(7, 3); NOP(3); EA1(7, 0); EA1(7, 1); EA1(7, 2); EA1(7, 3); EA2(7, 0); EA2(7, 1); EA2(7, 2); EA2(7, 3); EPK(7, 0); EPK(7, 1); OST(7); FLAG(C_TILE + 7 * 4);
    STAMP(9);
    ROLE_END();
  }
  {
    ROLE("h");
    const int hw = w - 5;
    const int j = xg * 28 + xi;
    const int s1 = j >> 2, kq = j & 3;
    const bool own = xi < 28;
    const u64 wbase = hw == 0 ? kb[1] : kb[2];
    const int pitch = F * 2;
    auto hrs = __builtin_amdgcn_make_buffer_rsrc((void*)wbase, 0, own ? D * F * 2 : 0, 0x00020000);
    const int r8 = lane >> 3;
    const int vo = (256 * kq * F + 64 * s1) * 2 + (8 * (r8 >> 2) + (r8 & 3)) * pitch + (((lane & 7) * 16) ^ (((r8 >> 1) & 3) * 32));
    auto dma = [&](int h, int sB, int pt, u32 dst) AI {
      const int m = h >> 1;
#pragma unroll
      for (int i = 0; i < 2; ++i)
        asm volatile("s_mov_b32 m0, %3\n\ts_nop 0\n\tbuffer_load_dwordx4 %0, %1, %2 offen nt lds"
                     :: "v"(vo), "s"(hrs), "s"((m & 1) * 32 * pt + (m >> 1) * sB + (16 * i + 4 * (h & 1)) * pt), "s"(dst + i * 1024) : "memory");
    };
    const int q = c16 >> 2, p = c16 & 3;
    int tro[4];
#pragma unroll
    for (int ct = 0; ct < 4; ++ct) tro[ct] = (4 * g + q) * 128 + ((ct ^ (((4 * g + q) >> 1) & 3)) * 32) + p * 8;
    const int tok = lane & 7;
    u4v wd[2][8];
    auto wd_ld = [&](int k, int cb) AI {
#pragma unroll
      for (int i = 0; i < 8; ++i) {
        const char* pp = smem + O_WD + cb * 8192 + (i >> 2) * 4096 + tro[i & 3];
        const u2v lo = tr_read(pp), hi = tr_read(pp + 2048);
        wd[k][i] = u4v{lo[0], lo[1], hi[0], hi[1]};
      }
    };
    u32 p2o, p2k[4], hlo, hhi;
    auto f2_setup = [&](int cb0) AI {
      p2o = WS_P2 + ((64 * kq + 16 * cb0 + (lane & 8) / 2 + g) * 56 + s1) * 128 + tok * 16;
      hlo = lbase + O_HB + ((lane & 8) ? 512 : g * 128 + tok * 16);
      hhi = lbase + O_HB + ((lane & 8) ? g * 128 + tok * 16 : 512);
#pragma unroll
      for (int i = 0; i < (cb0 == 2 ? 4 : 2); ++i) { p2k[i] = p2o + i * 8 * 7168; OPQ(p2k[i]); }
      OPQ(hlo); OPQ(hhi);
    };
    auto f2_job = [&](int nb, u32 t10) AI {
      u4v hb[4];
      {
        u32 cnt; int n_ = 1 << 20;
        asm volatile("1:\n\tds_read_b32 %4, %6\n\tds_read_b128 %0, %7\n\tds_read_b128 %1, %8\n\tds_read_b128 %2, %7 offset:528\n\tds_read_b128 %3, %8 offset:528\n\t"
                     "s_sub_u32 %5, %5, 1\n\ts_cbranch_scc1 2f\n\ts_waitcnt lgkmcnt(0)\n\tv_cmp_gt_u32 vcc, 2, %4\n\ts_cbranch_vccnz 1b\n2:\n\ts_waitcnt lgkmcnt(0)"
                     : "=&v"(hb[0]), "=&v"(hb[1]), "=&v"(hb[2]), "=&v"(hb[3]), "=&v"(cnt), "+s"(n_) : "v"(CTLA(C_HRDY)), "v"(hlo), "v"(hhi) : "vcc", "scc", "memory");
      }
      STAMP(11);
      f4 r[4];
#pragma unroll
      for (int i = 0; i < 2 * nb; ++i) {
        const u4v* wp = &wd[i >> 1][(i & 1) * 2];
        r[i] = mma(wp[0], hb[0], f4{0.f, 0.f, 0.f, 0.f});
        r[i] = mma(wp[1], hb[1], r[i]);
        r[i] = mma(wp[4], hb[2], r[i]);
        r[i] = mma(wp[5], hb[3], r[i]);
      }
      SB();
#pragma unroll
      for (int i = 0; i < 2 * nb; ++i) {
        u4v v = __builtin_bit_cast(u4v, r[i]);
#pragma unroll
        for (int e = 0; e < 4; ++e) v[e] = __builtin_amdgcn_bitop3_b32(v[e], t10, TMASK, 0xd8);
        __builtin_amdgcn_raw_buffer_store_b128(v, wrs, p2k[i], 0, 16);
      }
      STAMP(12);
    };
    const u32 rb = lbase + O_RING + hw * (NSLOT * 2048);
    pace(ts0 + WDELAY);
    STAMP(2);
#pragma unroll
    for (int h = 0; h < WWIN; ++h) dma(h, 64 * F * 2, F * 2, rb + h * 2048);
    __builtin_amdgcn_s_setprio(3);
    STAMP(3);
    if (__builtin_expect(own, 1)) {
      u4v wv[32];
      f4 a1[4] = {f4{0.f, 0.f, 0.f, 0.f}, f4{0.f, 0.f, 0.f, 0.f}, f4{0.f, 0.f, 0.f, 0.f}, f4{0.f, 0.f, 0.f, 0.f}};
      u4v xb[8];
      auto f1 = [&](int k0, int k1) AI {
#pragma unroll
        for (int k = k0; k < k1; ++k)
#pragma unroll
          for (int ct = 0; ct < 4; ++ct) a1[ct] = mma(wv[4 * k + ct], xb[k], a1[ct]);
      };
      u32 xo = lbase + O_XS + tok * XS_PITCH + g * 16;
      u32 p1o = WS_P1 + s1 * 16384 + kq * 4096 + hw * 2048 + (lane & 8) * 64 + g * 128 + tok * 16;
      u32 t10, tpub;
      OPQ(xo); OPQ(p1o);
      constexpr int HX = (16 - WWIN + 1) & ~1;
#pragma unroll
      for (int h = 0; h < 16; ++h) {
        const int left = (h + WWIN < 16 ? WWIN : 16 - h) - 1;
        asm volatile("s_waitcnt vmcnt(%0)" :: "n"(2 * left) : "memory");
        if (h + WWIN < 16) {
          asm volatile("s_waitcnt lgkmcnt(0)" ::: "memory");
          dma(h + WWIN, 64 * F * 2, F * 2, rb + ((h + WWIN) % NSLOT) * 2048);
        }
#pragma unroll
        for (int ct = 0; ct < 4; ++ct) {
          const u2v t = tr_read(smem + O_RING + hw * (NSLOT * 2048) + (h % NSLOT) * 2048 + tro[ct]);
          wv[4 * (h >> 1) + ct][2 * (h & 1)] = t[0]; wv[4 * (h >> 1) + ct][2 * (h & 1) + 1] = t[1];
        }
        if (h == H2_AT) ARRIVE(C_WGU);
        if (h == HX - 1) {
          STAMP(4);
          WAITQ(C_XS, 1);
          STAMP(1);
#pragma unroll
          for (int k = 0; k < 8; ++k) xb[k] = *(const __attribute__((address_space(3))) u4v*)(uintptr_t)(xo + k * 64);
          t10 = par[0]; tpub = par[2];
          SB();
          f1(0, 2);
          SB();
        }
        if (h >= HX) { f1(h - HX + 2, h - HX + 3); SB(); }
      }
      STAMP(5);
      f1(18 - HX, 8);
      t10 &= TMASK;
#pragma unroll
      for (int a = 0; a < 2; ++a) {
        u4v v;
#pragma unroll
        for (int e = 0; e < 4; ++e)
          v[e] = ((u32)__builtin_amdgcn_update_dpp(__float_as_int(a1[2 * a][e]), __float_as_int(a1[2 * a + 1][e]), 0xe4, 0xf, 0xc, false) & ~TMASK) | t10;
        __builtin_amdgcn_raw_buffer_store_b128(v, wrs, p1o + a * 1024, 0, 16);
      }
      STAMP(6);
      const u32 tn = now32();
      u32 voff = WS_P1 + s1 * 16384 + hw * 1024 + lane * 16;
      u32 ho = lbase + O_HB + hw * 528 + g * 128 + tok * 16 + (lane & 8);
      float nl2e = -1.44269504088896341f;
      u32 wda = CTLA(C_WD) + hw * 4;
      OPQ(voff); OPQ(ho); OPQ(nl2e); OPQ(wda);
      f2_setup(hw & 1);
      pace2(tn, (u32)__builtin_amdgcn_readfirstlane((int)tpub) + DE_P1, DT_P1);
      u4v pb[8];
      auto p1_ld = [&]() AI {
#pragma unroll
        for (int i = 0; i < 8; ++i) { pb[i] = __builtin_amdgcn_raw_buffer_load_b128(wrs, voff + (i >> 2) * 2048, (i & 3) * 4096, 16); SB(); }
      };
      p1_ld();
      SB();
      f4 G, U;
      bool early = false;
#pragma unroll 1
      for (int spin = 0; spin < SPIN_MAX; ++spin) {
        u32 bad = 0;
        OPQ(bad);
        asm volatile("" :: "v"(pb[0]));
        const u32 cw = __hip_atomic_load((__attribute__((address_space(3))) u32*)(uintptr_t)wda, __ATOMIC_RELAXED, __HIP_MEMORY_SCOPE_WORKGROUP);
        G = tsum4(bad, t10, pb[0], pb[1], pb[2], pb[3]);
        early = __builtin_amdgcn_ballot_w64(cw == 0u) == 0ull;
        asm volatile("" ::: "memory");
        if (__builtin_expect(early, 1)) wd_ld(0, hw);
        SB();
        silu4(G, nl2e);
        U = tsum4(bad, t10, pb[4], pb[5], pb[6], pb[7]);
        if (__builtin_expect(!__any((bad & TMASK) != 0), 1)) break;
        STAMP(14);
        p1_ld();
        asm volatile("" ::: "memory");
      }
      STAMP(7);
      {
        float hv[4];
#pragma unroll
        for (int e = 0; e < 4; ++e) { hv[e] = G[e] * U[e]; asm("" : "+v"(hv[e])); }
        *(__attribute__((address_space(3))) u2v*)(uintptr_t)ho = u2v{pk_bf(hv[0], hv[1]), pk_bf(hv[2], hv[3])};
      }
      ARRIVE(C_HRDY);
      STAMP(8);
      if (__builtin_expect(!early, 0)) { WAITA(wda); wd_ld(0, hw); }
      f2_job(1, t10);
    }
    if (hw == 0) {
      WAITS(C_SEL, 1);
      const u32 t10 = par[0] & TMASK;
      const u32 tpub = (u32)__builtin_amdgcn_readfirstlane((int)par[2]);
      const u32 mykey = ((const u32*)(smem + O_SEL))[lane & 7];
      if (own) pace2(now32(), tpub + DE_P2, DT_P2); else pace(tpub + DE_P2 - 40);
      u4v pa[7];
      const int voff = WS_P2 + cc * 7168 + lane * 16;
#pragma unroll
      for (int k = 0; k < 7; ++k) pa[k] = __builtin_amdgcn_raw_buffer_load_b128(wrs, voff + (k & 3) * 1024, (k >> 2) * 4096, 16);
      SB();
      const int row = (int)(~mykey & 2047u);
      const float rsig = rb1(sigm(__uint_as_float((mykey ^ (~(u32)((int)mykey >> 31) | 0x80000000u)) & 0xffff0000u)));
      WAITA(CTLA(C_TILE) + (lane & 31) * 4);
      float cf_ = rsig * (1.f - *(const float*)(smem + O_GT + (row >> 8) * 1024 + ((row >> 2) & 3) * 256 + ((row >> 4) & 15) * 16 + (row & 3) * 4));
      float base[4];
#pragma unroll
      for (int e = 0; e < 4; ++e) base[e] = bflo(*(const u16*)(smem + e * CHB + O_OUT + (row >> 8) * OT + ((row >> 2) & 3) * 144 + ((row >> 4) & 15) * 8 + (row & 3) * 2));
      u64 op = kc[1] + (row * D + cc * 4) * 2;
      { float cq = cf_; OPQ(cq); cf_ = cq; OPQ(op); }
#pragma unroll
      for (int e = 0; e < 4; ++e) OPQ(base[e]);
      STAMP(13);
      f4 y;
#pragma unroll 1
      for (int spin = 0; spin < SPIN_MAX; ++spin) {
        u32 bad = 0;
        OPQ(bad);
        y = tsum4(bad, t10, pa[0], pa[1], pa[2], pa[3]);
        y = tsum3(bad, t10, y, pa[4], pa[5], pa[6]);
        if (__builtin_expect(!__any((bad & TMASK) != 0), 1)) break;
        STAMP(15);
#pragma unroll
        for (int k = 0; k < 7; ++k) pa[k] = __builtin_amdgcn_raw_buffer_load_b128(wrs, voff + (k & 3) * 1024, (k >> 2) * 4096, 16);
        asm volatile("" ::: "memory");
      }
      STAMP(9);
      float o[4];
#pragma unroll
      for (int e = 0; e < 4; ++e) {
        float v = y[e] + dpp_f<0x128>(y[e]);
        auto s16 = __builtin_amdgcn_permlane16_swap(__float_as_uint(v), __float_as_uint(v), false, false);
        v = __uint_as_float(s16[0]) + __uint_as_float(s16[1]);
        auto s32 = __builtin_amdgcn_permlane32_swap(__float_as_uint(v), __float_as_uint(v), false, false);
        o[e] = fmaf(__uint_as_float(s32[0]) + __uint_as_float(s32[1]), cf_, base[e]);
      }
      if (lane < 8) *(__attribute__((address_space(1))) u2v*)op = u2v{pk_bf(o[0], o[1]), pk_bf(o[2], o[3])};
    }
    ROLE_END();
  }
  {
    ROLE("g");
    const u32 pLA = __builtin_nontemporal_load((gp32)ka[3] + d * (NM / 2) + lane);
    const u32 pAI = __builtin_nontemporal_load((gp32)ka[4] + d * (NM / 2) + lane);
    const u32 pCR = __builtin_nontemporal_load((gp32)ka[5] + d * (NM / 2) + lane);
    const u32 pCI = __builtin_nontemporal_load((gp32)ka[6] + d * (NM / 2) + lane);
    const u32 ldt = ((gp16)ka[7])[d], dskw = ((gp16)kb[0])[d];
    u4v ixa[2];
#pragma unroll
    for (int r = 0; r < 2; ++r) ixa[r] = *(const u4v*)(g_tapidx.v + lane * 16 + r * 8);
    SB();
    asm volatile("s_mov_b32 m0, %0\n\ts_nop 0" :: "s"(lbase + c * CHB) : "memory");
#define PUT1(off, val) asm volatile("ds_write_addtid_b32 %0 offset:%1" :: "v"(val), "n"(off) : "memory")
#define PUTR(off, val) do { const ch v_ = (val); asm volatile("ds_write_addtid_b32 %0 offset:%2\n\tds_write_addtid_b32 %1 offset:%3" :: "v"(v_.r), "v"(v_.i), "n"(off), "n"((off) + 256) : "memory"); } while (0)
    const char* pf = ch_ + c16 * TROW + g * 16;
    PUT1(O_TAP, 0x3c003c00u); PUT1(O_TAP + 256, 0u);
    PUT1(O_U, 0u); PUT1(O_U + 256, 0u);
    SB();
    const float dt = rb1(exp_bf(bflo(ldt)));
    const f2 a2 = f2{-rb1(exp_bf(bflo(pLA))), -rb1(exp_bf(bfhi(pLA)))} * dt * 1.44269504088896341f;
    const f2 br = unbf(pAI) * dt * 0.15915494309189535f;
    STAMP(1);
    u4v Lf[2][8], Wf[8];
    f4 kk[2][2];
    float fk;
    {
      const cf z1 = cexp_p(a2, br, 1.f);
      const cf z2 = csq(z1), z4 = csq(z2), z8 = csq(z4), z16 = csq(z8), z32 = csq(z16), z64 = csq(z32);
      ch base[4], anc[4];
      base[1] = tohc(z8); base[2] = tohc(z16); base[3] = tohc(cmul(z16, z8));
      anc[1] = tohc(z32); anc[2] = tohc(z64); anc[3] = tohc(cmul(z64, z32));
      PUTR(O_OUT + 5 * TROW, tohc(csq(z64)));
#pragma unroll
      for (int j = 1; j < 16; ++j) {
        const int q = j >> 2, r = j & 3;
        PUTR(O_TAP + j * TROW, q == 0 ? base[r] : r == 0 ? anc[q] : hmul(anc[q], base[r]));
      }
#pragma unroll
      for (int s = 0; s < 8; ++s) Lf[0][s] = *(const u4v*)(pf + O_TAP + s * 64);
#pragma unroll
      for (int s = 0; s < 8; ++s) Lf[1][s] = *(const u4v*)(ch_ + O_OUT + 5 * TROW + g * 16 + s * 64);
      SB();
      STAMP(11);
      u32 mx = max(max(pCR << 17, pCR << 1), max(pCI << 17, pCI << 1));
      mx = wave_max_u32(mx);
      int ex = (int)(mx >> 24);
      ex = ex < 1 ? 1 : (ex > 253 ? 253 : ex);
      const float sdn = __uint_as_float((u32)(254 - ex) << 23);
      fk = 2.f * dt * __uint_as_float((u32)ex << 23);
      cf C; C.r = unbf(pCR) * sdn; C.i = unbf(pCI) * sdn;
      const cf E = cexp_p(a2, br, 256.f);
      const ch y1 = toh(z1), y2 = toh(z2), y4 = toh(z4), y6 = toh(cmul(z4, z2));
      {
        const ch R0 = toh(C), R2 = hmul(R0, y2), R4 = hmul(R0, y4), R6 = hmul(R0, y6);
        PUTR(O_TAP, R0); PUTR(O_TAP + 2 * TROW, R2); PUTR(O_TAP + 4 * TROW, R4); PUTR(O_TAP + 6 * TROW, R6);
        PUTR(O_TAP + 1 * TROW, hmul(R0, y1)); PUTR(O_TAP + 3 * TROW, hmul(R2, y1)); PUTR(O_TAP + 5 * TROW, hmul(R4, y1)); PUTR(O_TAP + 7 * TROW, hmul(R6, y1));
        C = cmul(C, E);
        const ch R8 = toh(C);
        PUTR(O_TAP + 8 * TROW, R8); PUTR(O_TAP + 9 * TROW, hmul(R8, y2)); PUTR(O_TAP + 10 * TROW, hmul(R8, y4)); PUTR(O_TAP + 11 * TROW, hmul(R8, y6));
        C = cmul(C, E);
        const ch R12 = toh(C);
        PUTR(O_TAP + 12 * TROW, R12); PUTR(O_TAP + 13 * TROW, hmul(R12, y4));
        C = cmul(C, E);
        const ch R14 = toh(C);
        PUTR(O_TAP + 14 * TROW, R14); PUTR(O_TAP + 15 * TROW, hmul(R14, y4));
      }
#pragma unroll
      for (int s = 0; s < 8; ++s) Wf[s] = *(const u4v*)(pf + O_TAP + s * 64);
      SB();
      STAMP(12);
#pragma unroll
      for (int i = 0; i < 5; ++i) { C = cmul(C, E); PUTR(O_OUT + i * TROW, toh(C)); }
      SB();
      auto mh = [&](u4v a, u4v bq, f4 cin) AI { return __builtin_amdgcn_mfma_f32_16x16x32_f16(as_h8(a), as_h8(bq), cin, 0, 0, 0); };
      kk[0][0] = f4{0.f, 0.f, 0.f, 0.f}; kk[0][1] = kk[0][0]; kk[1][0] = kk[0][0]; kk[1][1] = kk[0][0];
#pragma unroll
      for (int s = 0; s < 5; ++s) {
#pragma unroll
        for (int k = 0; k < 4; ++k) {
          if (k < 2) { if (s < 4) kk[0][0] = mh(Wf[s + 4 * k], Lf[0][s + 4 * k], kk[0][0]); }
          else if (s > 0) { const int u = s - 1 + 4 * (k - 2); kk[0][1] = mh(Wf[u], Lf[1][u], kk[0][1]); Wf[u] = *(const u4v*)(pf + O_OUT + u * 64); }
          SB();
          if (s < 4) {
            const ch o = hmul(ch{ash(Lf[0][s][k]), ash(Lf[0][s + 4][k])}, ch{ash(Lf[1][s][k]), ash(Lf[1][s + 4][k])});
            Lf[1][s][k] = __builtin_bit_cast(u32, o.r); Lf[1][s + 4][k] = __builtin_bit_cast(u32, o.i);
          }
          if (s == 3 && k == 3) asm volatile("s_nop 1");
          SB();
        }
      }
#pragma unroll
      for (int s = 0; s < 8; ++s) { kk[1][0] = mh(Wf[s], Lf[0][s], kk[1][0]); kk[1][1] = mh(Wf[s], Lf[1][s], kk[1][1]); }
    }
    {
      u32 ia[16];
      const u32 kb_ = lbase + c * CHB + O_OUT;
#pragma unroll
      for (int e = 0; e < 16; ++e) { const u32 iw = ixa[e >> 3][(e >> 1) & 3]; ia[e] = kb_ + ((e & 1) ? (iw >> 16) : (iw & 0xffffu)); }
      MIX2(16, 0, 1)
      SB();
      STAMP(2);
      char* ktp = ch_ + O_OUT + c16 * KT_PITCH + g * 8;
#pragma unroll
      for (int hf = 1; hf >= 0; --hf) {
#pragma unroll
        for (int ct = 0; ct < 2; ++ct) {
          const f4 v = kk[hf][ct] * fk;
          *(u32*)(ktp + ct * 16 * KT_PITCH + hf * 32) = pk_bf(v[0], v[1]);
          *(u32*)(ktp + ct * 16 * KT_PITCH + hf * 32 + 4) = pk_bf(v[2], v[3]);
        }
        asm volatile("" ::: "memory");
      }
      u32 zz = 0u; OPQ(zz);
      *(u32*)(ch_ + O_OUT + KT_ZERO + (lane & 7) * 4) = zz;
      asm volatile("" ::: "memory");
      u32 v[16], pe[9];
#pragma unroll
      for (int e = 15; e >= 0; --e) v[e] = *(const __attribute__((address_space(3))) u16*)(uintptr_t)ia[e];
      SB();
      pe[8] = zz;
#pragma unroll
      for (int i = 7; i >= 0; --i) {
        pe[i] = v[2 * i] | (v[2 * i + 1] << 16);
        const u32 nx = (u32)__builtin_amdgcn_update_dpp((int)dpp_u<0x134>(pe[i + 1]), (int)pe[i], 0x130, 0xf, 0xf, false);
        const u32 po = __builtin_amdgcn_alignbyte(nx, pe[i], 2);
        PUT1(O_TAP + i * 256, pe[i]); PUT1(O_TAP + 2 * TAP_PITCH - 4 + i * 256, pe[i]);
        PUT1(O_TAP + TAP_PITCH + i * 256, po); PUT1(O_TAP + 3 * TAP_PITCH - 4 + i * 256, po);
      }
      ARRIVE(C_TAPS + c);
    }
    STAMP(3);
    IDS();
    conv_setup();
    dm1 = bflo(dskw) - 1.f;
    atload(0, 17);
    m_setup();
    __builtin_amdgcn_s_setprio(1);
    SB();
    WAITA(qa);
    STAMP(4);
    SB();
    FL(0, -7); FL(1, -6); FL(2, -5); FL(3, -4); FL(4, -3); FL(5, -2); FL(6, -1); FL(7, 0); ULD(0); GLD(0);
    MF0(acc[0], At[9], Fq[0]); EUN(0, 0); EUN(0, 1);
    MF(acc[0], At[10], Fq[1]); EUN(0, 2); EUN(0, 3);
    MF(acc[0], At[11], Fq[2]);
    MF(acc[0], At[12], Fq[3]);
    MF(acc[0], At[13], Fq[4]);
    MF(acc[0], At[14], Fq[5]);
    MF(acc[0], At[15], Fq[6]); FL(8, 1);
    MF(acc[0], At[16], Fq[7]);
    MF0(acc[1], At[1], Fq[0]); FL(9, 2);
    MF(acc[1], At[2], Fq[1]);
    MF(acc[1], At[3], Fq[2]); FL(10, 3);
    MF(acc[1], At[4], Fq[3]);
    MF(acc[1], At[5], Fq[4]); FL(11, 4); EA1(0, 0);
    MF(acc[1], At[6], Fq[5]); EA1(0, 1); EA1(0, 2);
    MF(acc[1], At[7], Fq[6]); EA1(0, 3); PEEK(0, qa + 16);
    MF(acc[1], At[8], Fq[7]); FL(12, 5); EA2(0, 0);
    MF0(acc[2], At[0], Fq[7]); EA2(0, 1); EA2(0, 2);
    MF(acc[1], At[9], Fq[8]); EA2(0, 3); EPK(0, 0);
    MF(acc[2], At[1], Fq[8]); EPK(0, 1); FL(13, 6);
    MF(acc[1], At[10], Fq[9]); OST(0); FLAG(C_TILE + 0 * 4);
    MF(acc[2], At[2], Fq[9]); FL(14, 7); FL(15, 8);
    MF(acc[1], At[11], Fq[10]); CHECK(0, qa + 16);
    MF(acc[2], At[3], Fq[10]);
    MF(acc[1], At[12], Fq[11]); FL(16, 9);
    MF(acc[2], At[4], Fq[11]); ULD(1); GLD(1);
    MF(acc[1], At[13], Fq[12]); FL(17, 10);
    MF(acc[2], At[5], Fq[12]);
    MF(acc[1], At[14], Fq[13]); FL(18, 11);
    MF(acc[2], At[6], Fq[13]);
    MF(acc[1], At[15], Fq[14]); FL(19, 12); EUN(1, 0);
    MF(acc[2], At[7], Fq[14]); EUN(1, 1); EUN(1, 2);
    MF(acc[1], At[16], Fq[15]); FL(20, 13); EUN(1, 3);
    MF(acc[2], At[8], Fq[15]);
    MF0(acc[3], At[0], Fq[15]); FL(21, 14);
    MF(acc[2], At[9], Fq[16]);
    MF(acc[3], At[1], Fq[16]); FL(22, 15); EA1(1, 0);
    MF(acc[2], At[10], Fq[17]); EA1(1, 1); EA1(1, 2);
    MF(acc[3], At[2], Fq[17]); FL(23, 16); EA1(1, 3);
    MF(acc[2], At[11], Fq[18]); PEEK(2, fa + 0); EA2(1, 0);
    MF(acc[3], At[3], Fq[18]); EA2(1, 1); EA2(1, 2);
    MF(acc[2], At[12], Fq[19]); EA2(1, 3); EPK(1, 0);
    MF(acc[3], At[4], Fq[19]); EPK(1, 1); FL(24, 17);
    MF(acc[2], At[13], Fq[20]); OST(1); FLAG(C_TILE + 1 * 4);
    MF(acc[3], At[5], Fq[20]); FL(25, 18); CHECK(2, fa + 0);
    MF(acc[2], At[14], Fq[21]); FL(26, 19); ULD(2);
    MF(acc[3], At[6], Fq[21]); FLD(2); GLD(2);
    MF(acc[2], At[15], Fq[22]); FL(27, 20); PEEK(1, qa + 32);
    MF(acc[3], At[7], Fq[22]);
    MF(acc[2], At[16], Fq[23]); FL(28, 21);
    MF(acc[3], At[8], Fq[23]); EUN(2, 0); EUN(2, 1);
    MF0(acc[4], At[0], Fq[23]); FL(29, 22); EUN(2, 2);
    MF(acc[3], At[9], Fq[24]); EUN(2, 3); EA1(2, 0);
    MF(acc[4], At[1], Fq[24]); FL(30, 23); EA1(2, 1);
    MF(acc[3], At[10], Fq[25]); EA1(2, 2); EA1(2, 3);
    MF(acc[4], At[2], Fq[25]); FL(31, 24); EFA(2, 0);
    MF(acc[3], At[11], Fq[26]); EFA(2, 1); EFA(2, 2);
    MF(acc[4], At[3], Fq[26]); EFA(2, 3); PEEK(3, fa + 16);
    MF(acc[3], At[12], Fq[27]); EA2(2, 0); EA2(2, 1);
    MF(acc[4], At[4], Fq[27]); EA2(2, 2); EA2(2, 3);
    MF(acc[3], At[13], Fq[28]); CHECK(1, qa + 32); EPK(2, 0); EPK(2, 1);
    MF(acc[4], At[5], Fq[28]); FL(32, 25); OST(2);
    MF(acc[3], At[14], Fq[29]); FLAG(C_TILE + 2 * 4); FL(33, 26);
    MF(acc[4], At[6], Fq[29]); FL(34, 27); CHECK(3, fa + 16);
    MF(acc[3], At[15], Fq[30]); ULD(3); FL(35, 28);
    MF(acc[4], At[7], Fq[30]); FLD(3); GLD(3);
    MF(acc[3], At[16], Fq[31]); FL(36, 29);
    MF(acc[4], At[8], Fq[31]);
    MF0(acc[5], At[0], Fq[31]); FL(37, 30);
    MF(acc[4], At[9], Fq[32]); EUN(3, 0); EUN(3, 1);
    MF(acc[5], At[1], Fq[32]); PEEK(4, fa + 32); FL(38, 31);
    MF(acc[4], At[10], Fq[33]); EUN(3, 2); EUN(3, 3);
    MF(acc[5], At[2], Fq[33]); FL(39, 32); EA1(3, 0);
    MF(acc[4], At[11], Fq[34]); EA1(3, 1); EA1(3, 2);
    MF(acc[5], At[3], Fq[34]); EA1(3, 3); EFA(3, 0);
    MF(acc[4], At[12], Fq[35]); EFA(3, 1); EFA(3, 2); EFA(3, 3);
    MF(acc[5], At[4], Fq[35]); EA2(3, 0); EA2(3, 1); EA2(3, 2); EA2(3, 3);
    MF(acc[4], At[13], Fq[36]); FL(40, 33); EPK(3, 0); EPK(3, 1);
    MF(acc[5], At[5], Fq[36]); FL(41, 34); OST(3);
    MF(acc[4], At[14], Fq[37]); FL(42, 35); FLAG(C_TILE + 3 * 4); CHECK(4, fa + 32); ULD(4);
    MF(acc[5], At[6], Fq[37]); FL(43, 36); FLD(4); GLD(4); PEEK(5, fa + 48);
    MF(acc[4], At[15], Fq[38]); FL(44, 37); FL(45, 38);
    MF(acc[5], At[7], Fq[38]); FL(46, 39); FL(47, 40);
    MF(acc[4], At[16], Fq[39]);
    MF(acc[5], At[8], Fq[39]); EUN(4, 0); EUN(4, 1);
    MF(acc[5], At[9], Fq[40]); EUN(4, 2); EUN(4, 3);
    MF(acc[5], At[10], Fq[41]); EFA(4, 0); EA1(4, 0);
    MF(acc[5], At[11], Fq[42]); EA1(4, 1); EA1(4, 2);
    MF(acc[5], At[12], Fq[43]); EA1(4, 3); EFA(4, 1); EFA(4, 2); EFA(4, 3); CHECK(5, fa + 48); ULD(5);
    MF(acc[5], At[13], Fq[44]); EA2(4, 0); EA2(4, 1); EA2(4, 2); EA2(4, 3); FLD(5); GLD(5);
    MF(acc[5], At[14], Fq[45]); EPK(4, 0); EPK(4, 1);
    MF(acc[5], At[15], Fq[46]); OST(4); FLAG(C_TILE + 4 * 4);
    MF(acc[5], At[16], Fq[47]); EUN(5, 0); EUN(5, 1); EUN(5, 2); EUN(5, 3); EFA(5, 0); EFA(5, 1); EFA(5, 2); EFA(5, 3); EA1(5, 0); EA1(5, 1); EA1(5, 2); EA1(5, 3); EA2(5, 0); EA2(5, 1); EA2(5, 2); EA2(5, 3); EPK(5, 0); EPK(5, 1); OST(5); FLAG(C_TILE + 5 * 4);
    STAMP(8);
    ROLE_END();
  }
  {
    ROLE("c");
    __builtin_amdgcn_s_setprio(3);
    {
      u32 zz = 0u; OPQ(zz);
      if (lane < 2) *(u4v*)(smem + O_HB + 512 + lane * 528) = u4v{zz, zz, zz, zz};
    }
    u32* selw = (u32*)(smem + O_SEL);
    u32 tag;
    {
      const u32 pa = lbase + O_PART + (lane & 7) * 64;
      const u32 ib = 2047u - (u32)(cg * 8 + (lane & 7));
      f4 p0, p1, p2, p3; u32 cnt, ep; int n_ = 1 << 20; unsigned long long tk;
      asm volatile("1:\n\tds_read_b32 %4, %8\n\tds_read_b128 %0, %9\n\tds_read_b128 %1, %9 offset:16\n\tds_read_b128 %2, %9 offset:32\n\tds_read_b128 %3, %9 offset:48\n\t"
                   "ds_read_b32 %7, %10\n\ts_memrealtime %6\n\ts_sub_u32 %5, %5, 1\n\ts_cbranch_scc1 2f\n\ts_waitcnt lgkmcnt(0)\n\tv_cmp_gt_u32 vcc, 4, %4\n\ts_cbranch_vccnz 1b\n2:\n\ts_waitcnt lgkmcnt(0)"
                   : "=&v"(p0), "=&v"(p1), "=&v"(p2), "=&v"(p3), "=&v"(cnt), "+s"(n_), "=&s"(tk), "=&v"(ep) : "v"(CTLA(C_PART)), "v"(pa), "v"(lbase + O_XS) : "vcc", "scc", "memory");
      tag = (u32)__builtin_amdgcn_readfirstlane((int)ep) + 1u;
      tag += tag == 0u;
      const f4 s4_ = (p0 + p1) + (p2 + p3);
      const u32 rp = pk_bf(s4_[1] + s4_[3], s4_[0] + s4_[2]);
      const u32 key = ((rp ^ ((u32)((int)rp >> 31) | 0x80000000u)) & 0xffff0000u) | ib;
      const u32 kn = dpp_u<0xb1>(key);
      u32 m1 = max(key, kn), m2 = min(key, kn), m3;
      { const u32 o1 = dpp_u<0x4e>(m1), o2 = dpp_u<0x4e>(m2); const u32 lo = min(m1, o1), hb = max(m2, o2); m1 = max(m1, o1); m2 = max(lo, hb); m3 = min(lo, hb); }
      { const u32 o1 = dpp_u<0x141>(m1), o2 = dpp_u<0x141>(m2), o3 = dpp_u<0x141>(m3); const u32 lo = min(m1, o1), hb = max(m2, o2);
        m1 = max(m1, o1); m2 = max(lo, hb); m3 = max(min(lo, hb), max(m3, o3)); }
      const u32 wm = (((u32)tk + 8u) >> 2) & 0xffu;
      if (lane == 0) __builtin_amdgcn_raw_buffer_store_b128(u4v{m1, m2, (m3 & 0xffff0000u) | wm, tag}, wrs, WS_KEY + cg * 16, 0, 16);
      SB();
      STAMP(1);
      if (lane == 0) par[0] = tag;
      ARRIVE(C_GP);
      {
        const float gv = sigm(bflo(rp));
        const u32 g2 = pk_bf(gv, dpp_f<0xb1>(gv));
        u32 g3 = dpp_u<0x4e>(g2);
        OPQ(g3);
        if ((lane & 59) == 0) __builtin_amdgcn_raw_buffer_store_b128(u4v{g2, g3, tag, tag}, wrs, WS_REC + cg * 32 + lane * 4, 0, 16);
      }
      if ((lane & 57) == 0) __builtin_amdgcn_raw_buffer_store_b128(u4v{key, kn, 0u, tag}, wrs, WS_KALL + cg * 64 + lane * 8, 0, 16);
    }
    u32 l7 = (lane & 7) * 16, ko = WS_KEY + lane * 16;
    OPQ(l7); OPQ(ko);
    WAITQ(C_TL, 1);
    STAMP(5);
    const u32 tkey = (u32)__builtin_amdgcn_readfirstlane((int)par[1]) + DT_KEY;
    pace(tkey);
    u4v gr[4];
    u32 mykey;
    u4v xs[4];
    auto xrs = __builtin_amdgcn_make_buffer_rsrc((void*)(ka[0] + (xi & 3) * 512), 0, L * D * 2 - 1536, 0x00020000);
    asm volatile("" : "+s"(xrs));
    auto xs_ld = [&]() AI {
      const u32 vo = ((~mykey & 2047u) << 11) | l7;
#pragma unroll
      for (int k = 0; k < 4; ++k) xs[k] = __builtin_amdgcn_raw_buffer_load_b128(xrs, vo + k * 128, 0, 0);
    };
#pragma unroll
    for (int i = 0; i < 4; ++i) gr[i] = __builtin_amdgcn_raw_buffer_load_b128(wrs, ko + i * 1024, 0, 16);
#pragma unroll 1
    for (int spin = 0; spin < SPIN_MAX; ++spin) {
      const u32 bad = (gr[0][3] ^ tag) | (gr[1][3] ^ tag) | (gr[2][3] ^ tag) | (gr[3][3] ^ tag);
      if (__builtin_expect(!__any(bad != 0), 1)) break;
      STAMP(13);
      __builtin_amdgcn_s_sleep(SLEEP_AG1);
#pragma unroll
      for (int i = 0; i < 4; ++i)
        if (gr[i][3] != tag) gr[i] = __builtin_amdgcn_raw_buffer_load_b128(wrs, ko + i * 1024, 0, 16);
      asm volatile("" ::: "memory");
    }
    STAMP(2);
    {
      u32 m1 = gr[0][0], m2 = gr[0][1], m3 = 0, m4 = 0;
#pragma unroll
      for (int i = 2; i < 8; ++i) {
        const u32 k = gr[i >> 1][i & 1];
        if (i > 2) m4 = med3u(m3, m4, k);
        m3 = med3u(m2, m3, k);
        m2 = (i & 1) ? max(m2, k) : med3u(m1, m2, k);
        if (!(i & 1)) m1 = max(m1, k);
      }
      const u32 third = max(max(gr[0][2], gr[1][2]), max(gr[2][2], gr[3][2]));
      u32 W2;
      mykey = wave_top8(m1, m2, m3, m4, W2);
      xs_ld();
      SB();
      if (__builtin_expect(__any(m1 == 0u || (third | 0xffffu) > W2), 0)) {
        u4v ak[16];
#pragma unroll 1
        for (int spin = 0; spin < SPIN_MAX; ++spin) {
          u32 bad = 0;
#pragma unroll
          for (int i = 0; i < 16; ++i) {
            ak[i] = __builtin_amdgcn_raw_buffer_load_b128(wrs, WS_KALL + (i & 3) * 1024 + lane * 16, (i >> 2) * 4096, 16);
            bad |= ak[i][3] ^ tag;
          }
          if (!__any(bad != 0)) break;
          __builtin_amdgcn_s_sleep(8);
          asm volatile("" ::: "memory");
        }
        u32 prev = 0xffffffffu;
#pragma unroll 1
        for (int it = 0; it < 8; ++it) {
          u32 mm_ = 0;
#pragma unroll
          for (int i = 0; i < 32; ++i) {
            const u32 k = ak[i >> 1][i & 1];
            mm_ = max(mm_, k < prev ? k : 0u);
          }
          OPQ(mm_);
          prev = wave_max_u32(mm_);
          mykey = (lane >> 3) == it ? prev : mykey;
        }
        xs_ld();
      }
      OPQ(mykey);
    }
    SB();
    STAMP(3);
    if (l7 == 0) selw[lane >> 3] = mykey;
    {
      const u32 tn = now32();
      u32 yg = 0;
#pragma unroll
      for (int i = 0; i < 4; ++i) yg = max(yg, (gr[i][2] - (tn >> 2) - 1u) & 0xffu);
      const u32 tpub = tn - 4u * (255u - wave_max_u32(yg)) + DT_VIS;
      if (lane == 0) par[2] = later(tkey, tpub);
    }
    ARRIVE(C_SEL);
    if (b == 255 && lane == 0) __builtin_amdgcn_raw_buffer_store_b32(tag, wrs, WS_EPOCH, 0, 16);
    SB();
    {
      char* xp = smem + O_XS + (lane >> 3) * XS_PITCH + l7;
#pragma unroll
      for (int k = 0; k < 4; ++k) *(u4v*)(xp + k * 128) = xs[k];
      ARRIVE(C_XS);
      STAMP(6);
    }
    __builtin_amdgcn_s_setprio(2);
    auto ors = __builtin_amdgcn_make_buffer_rsrc((void*)kc[1], 0, L * D * 2, 0x00020000);
    const u32 rl = (lane + (xi & 15) * 4) & 63, un = (g * 4 + (lane & 3) + xi) & 15;
    u32 skip = 0;
#pragma unroll
    for (int k = 0; k < 8; ++k) {
      const u32 r = ~(u32)__builtin_amdgcn_readlane((int)mykey, 8 * k) & 2047u;
      skip |= (r & 63u) == rl ? 1u << (r >> 6) : 0u;
    }
    const char* op = smem + ((lane >> 2) & 3) * CHB + O_OUT + (un & 3) * 144 + (un >> 2) * 8;
    const u32 ob = rl * 2048 + cc * 8;
    const u32 tfast = (u32)__builtin_amdgcn_readfirstlane((int)par[2]) + DE_FAST;
#pragma unroll 1
    for (int t = 0; t < 8; ++t) {
      WAITA(CTLA(C_TILE) + t * 16 + (lane & 3) * 4);
      u2v yv[4];
#pragma unroll
      for (int e = 0; e < 4; ++e) yv[e] = tr_read(op + t * OT + e * 32);
      const bool slow = (int)(now32() - tfast) < 0;
#pragma unroll
      for (int e = 0; e < 4; ++e) {
        __builtin_amdgcn_raw_buffer_store_b64(yv[e], ors, ob | (((skip >> e) & 1u) << 30), t * 524288 + e * 131072, 0);
        if (slow) {
          SB(); __builtin_amdgcn_s_sleep(STSLP); SB();
          asm volatile("s_waitcnt vmcnt(%0)" :: "n"(STWIN - 1) : "memory");
        }
      }
      skip >>= 4;
    }
    STAMP(9);
    ROLE_END();
  }
  {
    ROLE("d");
        const int j = xg * 28 + xi;
    const int s1 = j >> 2, kq = j & 3;
    const bool own = xi < 28;
    const u64 wbase = kb[3];
    const int pitch = D * 2;
    auto hrs = __builtin_amdgcn_make_buffer_rsrc((void*)wbase, 0, own ? D * F * 2 : 0, 0x00020000);
    const int r8 = lane >> 3;
    const int vo = (64 * s1 * D + 256 * kq) * 2 + (8 * (r8 >> 2) + (r8 & 3)) * pitch + (((lane & 7) * 16) ^ (((r8 >> 1) & 3) * 32));
    auto dma = [&](int h, int sB, int pt, u32 dst) AI {
      const int m = h >> 1;
#pragma unroll
      for (int i = 0; i < 2; ++i)
        asm volatile("s_mov_b32 m0, %3\n\ts_nop 0\n\tbuffer_load_dwordx4 %0, %1, %2 offen nt lds"
                     :: "v"(vo), "s"(hrs), "s"((m & 1) * 32 * pt + (m >> 1) * sB + (16 * i + 4 * (h & 1)) * pt), "s"(dst + i * 1024) : "memory");
    };
    const int q = c16 >> 2, p = c16 & 3;
    int tro[4];
#pragma unroll
    for (int ct = 0; ct < 4; ++ct) tro[ct] = (4 * g + q) * 128 + ((ct ^ (((4 * g + q) >> 1) & 3)) * 32) + p * 8;
    const int tok = lane & 7;
    u4v wd[2][8];
    auto wd_ld = [&](int k, int cb) AI {
#pragma unroll
      for (int i = 0; i < 8; ++i) {
        const char* pp = smem + O_WD + cb * 8192 + (i >> 2) * 4096 + tro[i & 3];
        const u2v lo = tr_read(pp), hi = tr_read(pp + 2048);
        wd[k][i] = u4v{lo[0], lo[1], hi[0], hi[1]};
      }
    };
    u32 p2o, p2k[4], hlo, hhi;
    auto f2_setup = [&](int cb0) AI {
      p2o = WS_P2 + ((64 * kq + 16 * cb0 + (lane & 8) / 2 + g) * 56 + s1) * 128 + tok * 16;
      hlo = lbase + O_HB + ((lane & 8) ? 512 : g * 128 + tok * 16);
      hhi = lbase + O_HB + ((lane & 8) ? g * 128 + tok * 16 : 512);
#pragma unroll
      for (int i = 0; i < (cb0 == 2 ? 4 : 2); ++i) { p2k[i] = p2o + i * 8 * 7168; OPQ(p2k[i]); }
      OPQ(hlo); OPQ(hhi);
    };
    u4v hb[4];
    f4 r[4];
    auto hb_ld = [&]() AI {
      u32 cnt; int n_ = 1 << 20;
      asm volatile("1:\n\tds_read_b32 %4, %6\n\tds_read_b128 %0, %7\n\tds_read_b128 %1, %8\n\tds_read_b128 %2, %7 offset:528\n\tds_read_b128 %3, %8 offset:528\n\t"
                   "s_sub_u32 %5, %5, 1\n\ts_cbranch_scc1 2f\n\ts_waitcnt lgkmcnt(0)\n\tv_cmp_gt_u32 vcc, 2, %4\n\ts_cbranch_vccnz 1b\n2:\n\ts_waitcnt lgkmcnt(0)"
                   : "=&v"(hb[0]), "=&v"(hb[1]), "=&v"(hb[2]), "=&v"(hb[3]), "=&v"(cnt), "+s"(n_) : "v"(CTLA(C_HRDY)), "v"(hlo), "v"(hhi) : "vcc", "scc", "memory");
    };
    auto f2_mm = [&](int k) AI {
      const u4v* wp = wd[k];
      r[2 * k] = mma(wp[0], hb[0], f4{0.f, 0.f, 0.f, 0.f}); r[2 * k + 1] = mma(wp[2], hb[0], f4{0.f, 0.f, 0.f, 0.f});
      r[2 * k] = mma(wp[1], hb[1], r[2 * k]); r[2 * k + 1] = mma(wp[3], hb[1], r[2 * k + 1]);
      r[2 * k] = mma(wp[4], hb[2], r[2 * k]); r[2 * k + 1] = mma(wp[6], hb[2], r[2 * k + 1]);
      r[2 * k] = mma(wp[5], hb[3], r[2 * k]); r[2 * k + 1] = mma(wp[7], hb[3], r[2 * k + 1]);
    };
    auto f2_st = [&](int k, u32 t10) AI {
#pragma unroll
      for (int i = 2 * k; i < 2 * k + 2; ++i) {
        u4v v = __builtin_bit_cast(u4v, r[i]);
#pragma unroll
        for (int e = 0; e < 4; ++e) v[e] = __builtin_amdgcn_bitop3_b32(v[e], t10, TMASK, 0xd8);
        __builtin_amdgcn_raw_buffer_store_b128(v, wrs, p2k[i], 0, 16);
      }
    };
    {
      __builtin_amdgcn_s_setprio(3);
      const u32 ts = ts0 - 500u;
      u32 tl = ~0u;
      pace(later(ts0, now32() - DT_ARG - 15) + DT_HEAD);
      STAMP(3);
#pragma unroll 1
      for (int i = 0; i < 8 && tl >= 1500u; ++i) {
        const u2v hd = __builtin_amdgcn_raw_buffer_load_b64(wrs, WS_HEAD + (lane & 7) * 8, 0, 16);
        tl = wave_max_u32(hd[0] == ~hd[1] ? hd[0] - ts : ~0u);
        STAMP(14);
        asm volatile("" ::: "memory");
      }
      tl = tl < 1500u ? tl + ts : ts0;
      if (lane == 0) par[1] = tl;
      ARRIVE(C_TL);
      STAMP(2);
      if (own) {
        WAITS(C_WGU, 1);
#pragma unroll
        for (int h = 0; h < 16; ++h) {
          if (h == DSEL) WAITS(C_SEL, 1);
          if (h == DEV) WAITS(C_XL, 4);
          if (h >= DWIN) asm volatile("s_waitcnt vmcnt(%0)" :: "n"(2 * (DWIN - 1)) : "memory");
          if (h == 3 + DWIN) ARRIVE(C_WD);
          if (h == 7 + DWIN) ARRIVE(C_WD + 1);
          dma(h, 128, D * 2, lbase + O_WD + h * 2048);
        }
        f2_setup(2);
        const u32 t10 = par[0] & TMASK;
        asm volatile("s_waitcnt vmcnt(8)" ::: "memory");
        wd_ld(0, 2);
        const u32 hc = __hip_atomic_load(ctl + C_HRDY, __ATOMIC_RELAXED, __HIP_MEMORY_SCOPE_WORKGROUP);
        const bool early = __builtin_amdgcn_ballot_w64(hc < 2u) == 0ull;
        if (early) { hb_ld(); f2_mm(0); }
        asm volatile("s_waitcnt vmcnt(0)" ::: "memory");
        STAMP(5);
        wd_ld(1, 3);
        if (!early) { WAITS(C_HRDY, 1); hb_ld(); STAMP(11); f2_mm(0); }
        SB();
        f2_mm(1);
        f2_st(0, t10);
        SB();
        f2_st(1, t10);
        STAMP(12);
      }
      ROLE_END();
    }
  }
}

extern "C" void kernel_launch(void* const* d_in, const int* in_sizes, int n_in,
                              void* d_out, int out_size, void* d_ws, size_t ws_size,
                              hipStream_t stream) {
  k_main<<<256, 768, 0, stream>>>((const u16*)d_in[0], (const u16*)d_in[8], (const u16*)d_in[12],
                                  (const u16*)d_in[1], (const u16*)d_in[2], (const u16*)d_in[3],
                                  (const u16*)d_in[4], (const u16*)d_in[5], (const u16*)d_in[6],
                                  (const u16*)d_in[9], (const u16*)d_in[10], (const u16*)d_in[11],
                                  (char*)d_ws, (u16*)d_out);
}
```
